```python
import torch

def _softmax(x, axis=-1):
    return torch.softmax(x, dim=axis)
def _gelu(x):
    return torch.nn.functional.gelu(x, approximate="tanh")
def _silu(x):
    return torch.nn.functional.silu(x)
def _sigmoid(x):
    return torch.sigmoid(x)
def _layernorm(x, axis=-1, eps=1e-5):
    m = x.mean(dim=axis, keepdim=True)
    v = x.var(dim=axis, keepdim=True, unbiased=False)
    return (x - m) / torch.sqrt(v + eps)
def _scatter_add(tgt, idx, src, axis=0):
    return tgt.clone().index_add_(axis, idx, src)
def _segment_sum(src, seg, n):
    out = torch.zeros((n,) + src.shape[1:], dtype=src.dtype)
    return out.index_add_(0, seg, src)
def _pad(x, pw):
    flat = tuple(v for pair in reversed(pw) for v in pair)
    return torch.nn.functional.pad(x, flat)

kernel_name = 'parallel_sum__compressed_kv_cla__glu_gated_swiglu__decode'

def setup_inputs():

    _w = (lambda a: (lambda t: t.to(torch.bfloat16) if t.is_floating_point() else t)(torch.as_tensor(a)))
    def _A_setup():
        B, H, Sq, Sk, D, M = 4, 32, 1, 4096, 128, 576
        Dc = 4 * D
        return {
            "x": _w((lambda *s: torch.randn(*s, dtype=torch.bfloat16) * 0.05)(B, Sq, M)),
            "c_kv": _w((lambda *s: torch.randn(*s, dtype=torch.bfloat16) * 0.8)(B, Sk, Dc)),
            "w_q": _w((lambda *s: torch.randn(*s, dtype=torch.bfloat16) * 1.6)(M, H, D) * (M ** -0.5)),
            "w_uk": _w((lambda *s: torch.randn(*s, dtype=torch.bfloat16) * 1.6)(Dc, H, D) * (Dc ** -0.5)),
            "w_uv": _w((lambda *s: torch.randn(*s, dtype=torch.bfloat16) * 0.8)(Dc, H, D) * (Dc ** -0.5)),
            "w_o": _w((lambda *s: torch.randn(*s, dtype=torch.bfloat16) * 0.8)(H, D, M) * ((H * D) ** -0.5)),
        }
    _A_in = _A_setup()

    def _B_setup():
        B, S, D, F = 4, 1, 576, 4608
        return {
            "x": _w((lambda *s: torch.randn(*s, dtype=torch.bfloat16) * 0.05)(B, S, D)),
            "w1": _w((lambda *s: torch.randn(*s, dtype=torch.bfloat16) * 0.4)(D, F) * (2.0 / D) ** 0.5),
            "w3": _w((lambda *s: torch.randn(*s, dtype=torch.bfloat16) * 0.4)(D, F) * (2.0 / D) ** 0.5),
            "w2": _w((lambda *s: torch.randn(*s, dtype=torch.bfloat16) * 0.4)(F, D) * (2.0 / F) ** 0.5),
        }
    _B_in = _B_setup()

    return {
        "A_x": _A_in["x"],
        "A_c_kv": _A_in["c_kv"],
        "A_w_q": _A_in["w_q"],
        "A_w_uk": _A_in["w_uk"],
        "A_w_uv": _A_in["w_uv"],
        "A_w_o": _A_in["w_o"],
        "B_x": _B_in["x"],
        "B_w1": _B_in["w1"],
        "B_w3": _B_in["w3"],
        "B_w2": _B_in["w2"],
    }

def reference(A_x, A_c_kv, A_w_q, A_w_uk, A_w_uv, A_w_o, B_x, B_w1, B_w3, B_w2):
    def _A_ref(x, c_kv, w_q, w_uk, w_uv, w_o):
        B, Sq, M = x.shape[0], x.shape[1], x.shape[2]
        Sk = c_kv.shape[1]
        H, D = w_q.shape[1], w_q.shape[2]
        q = torch.einsum("bqm,mhd->bhqd", x, w_q)
        k = torch.einsum("bkc,chd->bhkd", c_kv, w_uk)
        v = torch.einsum("bkc,chd->bhkd", c_kv, w_uv)
        scores = torch.einsum("bhqd,bhkd->bhqk", q, k) * (D ** -0.5)
        off = Sk - Sq
        qi = torch.arange(Sq)[:, None]
        kj = torch.arange(Sk)[None, :]
        cmask = kj <= (qi + off)
        scores = torch.where(cmask[None, None], scores, -1e9)
        p = _softmax(scores, axis=-1)
        ctx = torch.einsum("bhqk,bhkd->bhqd", p, v)
        return torch.einsum("bhqd,hdm->bqm", ctx, w_o)

    def _B_ref(x, w1, w3, w2):
        gate = torch.einsum("bsd,df->bsf", x, w1)
        up = torch.einsum("bsd,df->bsf", x, w3)
        h = _silu(gate) * up
        out = torch.einsum("bsf,fd->bsd", h, w2)
        return out

    x = A_x
    xn = _layernorm(x, axis=-1)
    return x + _A_ref(xn, A_c_kv, A_w_q, A_w_uk, A_w_uv, A_w_o) + _B_ref(xn, B_w1, B_w3, B_w2)
```

```cpp
#include <hip/hip_runtime.h>
#include <hip/hip_bf16.h>
#include <stdint.h>

typedef unsigned short u16;
__device__ __forceinline__ float bf2f(u16 v){ return __uint_as_float(((unsigned)v)<<16); }
__device__ __forceinline__ u16 f2bf(float f){ unsigned u=__float_as_uint(f); u += 0x7fffu + ((u>>16)&1u); return (u16)(u>>16); }

constexpr int B=4,H=32,SK=4096,D=128,M=576,DC=512,F=4608;

constexpr int OFF_XN=0;
constexpr int OFF_Q=4096;
constexpr int OFF_QA=OFF_Q+16384;
constexpr int OFF_S=OFF_QA+65536;
constexpr int OFF_CC=OFF_S+524288;
constexpr int OFF_CTX=OFF_CC+65536;
constexpr int OFF_H=OFF_CTX+16384;

__global__ void k_ln(const u16* x, float* ws){
  int b=blockIdx.x; int t=threadIdx.x;
  float v[9]; float s=0;
  for(int i=0;i<9;i++){ v[i]=bf2f(x[b*M+t+64*i]); s+=v[i]; }
  for(int o=32;o>0;o>>=1) s+=__shfl_xor(s,o);
  float m=s/M; float q=0;
  for(int i=0;i<9;i++){ float d=v[i]-m; q+=d*d; }
  for(int o=32;o>0;o>>=1) q+=__shfl_xor(q,o);
  float r=1.0f/sqrtf(q/M+1e-5f);
  for(int i=0;i<9;i++) ws[OFF_XN+b*M+t+64*i]=(v[i]-m)*r;
}
__global__ void k_q(const u16* wq, const u16* w1, const u16* w3, float* ws){
  int n=blockIdx.x*blockDim.x+threadIdx.x;
  const float* xn=ws+OFF_XN;
  if(n<4096){
    float a[4]={0,0,0,0};
    for(int m=0;m<M;m++){ float w=bf2f(wq[m*4096+n]); for(int b=0;b<4;b++) a[b]+=xn[b*M+m]*w; }
    for(int b=0;b<4;b++) ws[OFF_Q+b*4096+n]=a[b];
  } else if(n<4096+F){
    int f=n-4096;
    float g[4]={0,0,0,0},u[4]={0,0,0,0};
    for(int m=0;m<M;m++){ float a=bf2f(w1[m*F+f]), c=bf2f(w3[m*F+f]); for(int b=0;b<4;b++){ g[b]+=xn[b*M+m]*a; u[b]+=xn[b*M+m]*c; } }
    for(int b=0;b<4;b++) ws[OFF_H+b*F+f]=g[b]/(1.0f+__expf(-g[b]))*u[b];
  }
}
__global__ void k_qa(const u16* wuk, float* ws){
  int n=blockIdx.x*blockDim.x+threadIdx.x;
  int h=n/512,c=n%512;
  float a[4]={0,0,0,0};
  for(int d=0;d<D;d++){ float w=bf2f(wuk[(c*H+h)*D+d]); for(int b=0;b<4;b++) a[b]+=ws[OFF_Q+b*4096+h*D+d]*w; }
  for(int b=0;b<4;b++) ws[OFF_QA+(b*H+h)*DC+c]=a[b]*0.08838834764831845f;
}
__global__ void k_s(const u16* ckv, float* ws){
  int n=blockIdx.x*blockDim.x+threadIdx.x;
  int k=n%SK, bh=n/SK, b=bh/H;
  float a=0;
  for(int c=0;c<DC;c++) a+=ws[OFF_QA+bh*DC+c]*bf2f(ckv[((size_t)b*SK+k)*DC+c]);
  ws[OFF_S+n]=a;
}
__global__ void k_sm(float* ws){
  int bh=blockIdx.x; int t=threadIdx.x;
  float* s=ws+OFF_S+bh*SK;
  float m=-1e30f;
  for(int i=t;i<SK;i+=64) m=fmaxf(m,s[i]);
  for(int o=32;o>0;o>>=1) m=fmaxf(m,__shfl_xor(m,o));
  float l=0;
  for(int i=t;i<SK;i+=64){ float e=__expf(s[i]-m); s[i]=e; l+=e; }
  for(int o=32;o>0;o>>=1) l+=__shfl_xor(l,o);
  float r=1.0f/l;
  for(int i=t;i<SK;i+=64) s[i]*=r;
}
__global__ void k_pv(const u16* ckv, float* ws){
  int n=blockIdx.x*blockDim.x+threadIdx.x;
  int c=n%DC,bh=n/DC,b=bh/H;
  float a=0;
  for(int k=0;k<SK;k++) a+=ws[OFF_S+bh*SK+k]*bf2f(ckv[((size_t)b*SK+k)*DC+c]);
  ws[OFF_CC+n]=a;
}
__global__ void k_uv(const u16* wuv, float* ws){
  int n=blockIdx.x*blockDim.x+threadIdx.x;
  int h=n/D,d=n%D;
  float a[4]={0,0,0,0};
  for(int c=0;c<DC;c++){ float w=bf2f(wuv[(c*H+h)*D+d]); for(int b=0;b<4;b++) a[b]+=ws[OFF_CC+(b*H+h)*DC+c]*w; }
  for(int b=0;b<4;b++) ws[OFF_CTX+b*4096+n]=a[b];
}
__global__ void k_out(const u16* x, const u16* wo, const u16* w2, float* ws, u16* out){
  int n=blockIdx.x*blockDim.x+threadIdx.x;
  if(n>=B*M) return;
  int b=n/M,m=n%M;
  float a=0,c=0;
  for(int k=0;k<4096;k++) a+=ws[OFF_CTX+b*4096+k]*bf2f(wo[k*M+m]);
  for(int f=0;f<F;f++) c+=ws[OFF_H+b*F+f]*bf2f(w2[f*M+m]);
  out[n]=f2bf(bf2f(x[n])+a+c);
}

extern "C" void kernel_launch(void* const* d_in, const int* in_sizes, int n_in,
                              void* d_out, int out_size, void* d_ws, size_t ws_size,
                              hipStream_t stream) {
  const u16* x=(const u16*)d_in[0]; const u16* ckv=(const u16*)d_in[1];
  const u16* wq=(const u16*)d_in[2]; const u16* wuk=(const u16*)d_in[3];
  const u16* wuv=(const u16*)d_in[4]; const u16* wo=(const u16*)d_in[5];
  const u16* w1=(const u16*)d_in[7]; const u16* w3=(const u16*)d_in[8]; const u16* w2=(const u16*)d_in[9];
  float* ws=(float*)d_ws;
  k_ln<<<4,64,0,stream>>>(x,ws);
  k_q<<<(4096+F)/64,64,0,stream>>>(wq,w1,w3,ws);
  k_qa<<<32*512/64,64,0,stream>>>(wuk,ws);
  k_s<<<B*H*SK/256,256,0,stream>>>(ckv,ws);
  k_sm<<<B*H,64,0,stream>>>(ws);
  k_pv<<<B*H*DC/64,64,0,stream>>>(ckv,ws);
  k_uv<<<4096/64,64,0,stream>>>(wuv,ws);
  k_out<<<36,64,0,stream>>>(x,wo,w2,ws,(u16*)d_out);
}
```

```cpp
#define PROBE_NT 0
#define PROBE_WHAT 2
#include <hip/hip_runtime.h>
#include <hip/hip_bf16.h>
#include <stdint.h>

typedef unsigned short u16;
__device__ __forceinline__ float bf2f(u16 v){ return __uint_as_float(((unsigned)v)<<16); }
__device__ __forceinline__ u16 f2bf(float f){ unsigned u=__float_as_uint(f); u += 0x7fffu + ((u>>16)&1u); return (u16)(u>>16); }

constexpr int B=4,H=32,SK=4096,D=128,M=576,DC=512,F=4608;

constexpr int OFF_XN=0;
constexpr int OFF_Q=4096;
constexpr int OFF_QA=OFF_Q+16384;
constexpr int OFF_S=OFF_QA+65536;
constexpr int OFF_CC=OFF_S+524288;
constexpr int OFF_CTX=OFF_CC+65536;
constexpr int OFF_H=OFF_CTX+16384;

__global__ void k_ln(const u16* x, float* ws){
  int b=blockIdx.x; int t=threadIdx.x;
  float v[9]; float s=0;
  for(int i=0;i<9;i++){ v[i]=bf2f(x[b*M+t+64*i]); s+=v[i]; }
  for(int o=32;o>0;o>>=1) s+=__shfl_xor(s,o);
  float m=s/M; float q=0;
  for(int i=0;i<9;i++){ float d=v[i]-m; q+=d*d; }
  for(int o=32;o>0;o>>=1) q+=__shfl_xor(q,o);
  float r=1.0f/sqrtf(q/M+1e-5f);
  for(int i=0;i<9;i++) ws[OFF_XN+b*M+t+64*i]=(v[i]-m)*r;
}
__global__ void k_q(const u16* wq, const u16* w1, const u16* w3, float* ws){
  int n=blockIdx.x*blockDim.x+threadIdx.x;
  const float* xn=ws+OFF_XN;
  if(n<4096){
    float a[4]={0,0,0,0};
    for(int m=0;m<M;m++){ float w=bf2f(wq[m*4096+n]); for(int b=0;b<4;b++) a[b]+=xn[b*M+m]*w; }
    for(int b=0;b<4;b++) ws[OFF_Q+b*4096+n]=a[b];
  } else if(n<4096+F){
    int f=n-4096;
    float g[4]={0,0,0,0},u[4]={0,0,0,0};
    for(int m=0;m<M;m++){ float a=bf2f(w1[m*F+f]), c=bf2f(w3[m*F+f]); for(int b=0;b<4;b++){ g[b]+=xn[b*M+m]*a; u[b]+=xn[b*M+m]*c; } }
    for(int b=0;b<4;b++) ws[OFF_H+b*F+f]=g[b]/(1.0f+__expf(-g[b]))*u[b];
  }
}
__global__ void k_qa(const u16* wuk, float* ws){
  int n=blockIdx.x*blockDim.x+threadIdx.x;
  int h=n/512,c=n%512;
  float a[4]={0,0,0,0};
  for(int d=0;d<D;d++){ float w=bf2f(wuk[(c*H+h)*D+d]); for(int b=0;b<4;b++) a[b]+=ws[OFF_Q+b*4096+h*D+d]*w; }
  for(int b=0;b<4;b++) ws[OFF_QA+(b*H+h)*DC+c]=a[b]*0.08838834764831845f;
}
__global__ void k_s(const u16* ckv, float* ws){
  int n=blockIdx.x*blockDim.x+threadIdx.x;
  int k=n%SK, bh=n/SK, b=bh/H;
  float a=0;
  for(int c=0;c<DC;c++) a+=ws[OFF_QA+bh*DC+c]*bf2f(ckv[((size_t)b*SK+k)*DC+c]);
  ws[OFF_S+n]=a;
}
__global__ void k_sm(float* ws){
  int bh=blockIdx.x; int t=threadIdx.x;
  float* s=ws+OFF_S+bh*SK;
  float m=-1e30f;
  for(int i=t;i<SK;i+=64) m=fmaxf(m,s[i]);
  for(int o=32;o>0;o>>=1) m=fmaxf(m,__shfl_xor(m,o));
  float l=0;
  for(int i=t;i<SK;i+=64){ float e=__expf(s[i]-m); s[i]=e; l+=e; }
  for(int o=32;o>0;o>>=1) l+=__shfl_xor(l,o);
  float r=1.0f/l;
  for(int i=t;i<SK;i+=64) s[i]*=r;
}
__global__ void k_pv(const u16* ckv, float* ws){
  int n=blockIdx.x*blockDim.x+threadIdx.x;
  int c=n%DC,bh=n/DC,b=bh/H;
  float a=0;
  for(int k=0;k<SK;k++) a+=ws[OFF_S+bh*SK+k]*bf2f(ckv[((size_t)b*SK+k)*DC+c]);
  ws[OFF_CC+n]=a;
}
__global__ void k_uv(const u16* wuv, float* ws){
  int n=blockIdx.x*blockDim.x+threadIdx.x;
  int h=n/D,d=n%D;
  float a[4]={0,0,0,0};
  for(int c=0;c<DC;c++){ float w=bf2f(wuv[(c*H+h)*D+d]); for(int b=0;b<4;b++) a[b]+=ws[OFF_CC+(b*H+h)*DC+c]*w; }
  for(int b=0;b<4;b++) ws[OFF_CTX+b*4096+n]=a[b];
}
__global__ void k_out(const u16* x, const u16* wo, const u16* w2, float* ws, u16* out){
  int n=blockIdx.x*blockDim.x+threadIdx.x;
  if(n>=B*M) return;
  int b=n/M,m=n%M;
  float a=0,c=0;
  for(int k=0;k<4096;k++) a+=ws[OFF_CTX+b*4096+k]*bf2f(wo[k*M+m]);
  for(int f=0;f<F;f++) c+=ws[OFF_H+b*F+f]*bf2f(w2[f*M+m]);
  out[n]=f2bf(bf2f(x[n])+a+c);
}


typedef unsigned v4u __attribute__((ext_vector_type(4)));
__device__ __forceinline__ unsigned long long rt() { return __builtin_amdgcn_s_memrealtime(); }
__device__ __forceinline__ void glds16(const void* gsrc, unsigned lds_dst, int nt) {
    unsigned keep;
    if (nt) asm volatile("s_mov_b32 %0, m0\n\ts_mov_b32 m0, %2\n\ts_nop 0\n\tglobal_load_lds_dwordx4 %1, off nt\n\ts_mov_b32 m0, %0" : "=&s"(keep) : "v"(gsrc), "s"(lds_dst) : "memory");
    else    asm volatile("s_mov_b32 %0, m0\n\ts_mov_b32 m0, %2\n\ts_nop 0\n\tglobal_load_lds_dwordx4 %1, off\n\ts_mov_b32 m0, %0" : "=&s"(keep) : "v"(gsrc), "s"(lds_dst) : "memory");
}
constexpr int OFF_ST=1<<21;
__global__ void __launch_bounds__(256,1) k_probe(const char* p0,const char* p1,const char* p2,const char* p3,const char* p4,const char* p5,const char* p6,const char* p7, float* ws){
  __shared__ char smem[65536];
  unsigned long long t0=rt();
  int b=blockIdx.x; int w=__builtin_amdgcn_readfirstlane(threadIdx.x>>6); int lane=threadIdx.x&63;
  unsigned base=(unsigned)(size_t)smem;
  auto run=[&](const char* p,int n){ const char* q=p+(size_t)b*n*1024+lane*16; for(int r=w;r<n;r+=4) glds16(q+(size_t)r*1024, base+(r&63)*1024, PROBE_NT); };
  run(p1,18); run(p2,16); run(p0,64); run(p3,16); run(p4,18); run(p5,20); run(p6,20); run(p7,20);
  asm volatile("s_waitcnt vmcnt(0)":::"memory");
  unsigned long long t2=rt();
  if(lane==0){ unsigned long long* o=(unsigned long long*)(ws+OFF_ST)+((size_t)b*4+w)*2; o[0]=t0;o[1]=t2; }
}
__global__ void k_spin(float* ws){
  const unsigned long long* o=(const unsigned long long*)(ws+OFF_ST);
  unsigned long long mn=~0ull,mxs=0,mxe=0;
  for(int i=0;i<1024;i++){ mn=o[2*i]<mn?o[2*i]:mn; mxs=o[2*i]>mxs?o[2*i]:mxs; mxe=o[2*i+1]>mxe?o[2*i+1]:mxe; }
  unsigned long long sd=0; for(int i=0;i<1024;i++) sd+=o[2*i+1]-o[2*i]; unsigned long long T=(PROBE_WHAT==0)?(mxe-mn):(PROBE_WHAT==1)?(mxs-mn):(PROBE_WHAT==2)?0:(sd/1024); if(mn==12345 && sd==77) T=mxe+mxs;
  if(T>100000) T=100000;
  unsigned long long t0=rt();
  while(rt()-t0 < T*100) __builtin_amdgcn_s_sleep(8);
}
extern "C" void kernel_launch(void* const* d_in, const int* in_sizes, int n_in,
                              void* d_out, int out_size, void* d_ws, size_t ws_size,
                              hipStream_t stream) {
  const u16* x=(const u16*)d_in[0]; const u16* ckv=(const u16*)d_in[1];
  const u16* wq=(const u16*)d_in[2]; const u16* wuk=(const u16*)d_in[3];
  const u16* wuv=(const u16*)d_in[4]; const u16* wo=(const u16*)d_in[5];
  const u16* w1=(const u16*)d_in[7]; const u16* w3=(const u16*)d_in[8]; const u16* w2=(const u16*)d_in[9];
  float* ws=(float*)d_ws;
  k_probe<<<256,256,0,stream>>>((const char*)ckv,(const char*)wq,(const char*)wuk,(const char*)wuv,(const char*)wo,(const char*)w1,(const char*)w3,(const char*)w2,ws);
  k_ln<<<4,64,0,stream>>>(x,ws);
  k_q<<<(4096+F)/64,64,0,stream>>>(wq,w1,w3,ws);
  k_qa<<<32*512/64,64,0,stream>>>(wuk,ws);
  k_s<<<B*H*SK/256,256,0,stream>>>(ckv,ws);
  k_sm<<<B*H,64,0,stream>>>(ws);
  k_pv<<<B*H*DC/64,64,0,stream>>>(ckv,ws);
  k_uv<<<4096/64,64,0,stream>>>(wuv,ws);
  k_out<<<36,64,0,stream>>>(x,wo,w2,ws,(u16*)d_out);
  k_spin<<<1,1,0,stream>>>(ws);
}
```

```cpp
#include <hip/hip_runtime.h>
#include <stdint.h>

typedef unsigned short u16;
typedef unsigned long long u64;
typedef unsigned v4u __attribute__((ext_vector_type(4)));
typedef unsigned v2u __attribute__((ext_vector_type(2)));
typedef float f32x2 __attribute__((ext_vector_type(2)));
typedef float f32x4 __attribute__((ext_vector_type(4)));
typedef float f32x16 __attribute__((ext_vector_type(16)));
typedef short s16x4 __attribute__((ext_vector_type(4)));
typedef __bf16 bf16x8 __attribute__((ext_vector_type(8)));
typedef __bf16 bf16x2 __attribute__((ext_vector_type(2)));
typedef __amdgpu_buffer_rsrc_t rsrc_t;
#define GAS __attribute__((address_space(1)))
#define LAS __attribute__((address_space(3)))
#define DI __device__ __forceinline__

#define NT_W 2
#define NT_WO 0
#define PACE_C 8
#define DLY_H1 4
#define KQA 31
#define PACE_K 4
#define DLY_K 8
#define PACE_G 5
#define PACE_O 8
#define DLY_R2 8
#define DLY_H3 0
#define DLY_H7 8
#define DLY_H5 8

constexpr int SC1 = 16;
constexpr int GSPIN = 1 << 13;

constexpr int WS_W = 0;
constexpr int WS_H1 = 4096;
constexpr int WS_QA = WS_H1 + 128 * 4096;
constexpr int WS_ML = WS_QA + 131072;
constexpr int WS_H7 = WS_ML + 65536;
constexpr int WS_H5 = WS_H7 + 128 * 8192;
constexpr int WS_H3 = WS_H5 + 72 * 256 * 128;
constexpr int WS_END = WS_H3 + 128 * 32768;
constexpr int WS_STAMP = WS_END;
constexpr int OOB = (int)0x80000000;

constexpr int LP_TAG = 4992, LP_XP = 0, LP_XD = 5120, LP_QB = 15360, LP_QO = 16896, LP_RED = 18432, LP_HP = 34816, LP_B8 = 35840, LP_UK = 40960;
constexpr int LA_QA = 131072, LA_MX = LA_QA;
constexpr int LA_PB = LA_QA + 1024, LA_SUM = LA_QA + 512, LA_CTX = LA_QA + 9216, LA_UV = LA_QA + 11264, LA_B8 = LA_QA + 16384,
              LA_FIN = LA_QA + 20480;
constexpr int LDS_BYTES = 163840;

DI rsrc_t mk(const void* p, int bytes) { return __builtin_amdgcn_make_buffer_rsrc((void*)p, (short)0, bytes, 0x00020000); }
template <int AUX> DI v4u ld4(rsrc_t r, int vo, int so) { return __builtin_amdgcn_raw_buffer_load_b128(r, vo, so, AUX); }
template <int AUX> DI v2u ld2(rsrc_t r, int vo, int so) { return __builtin_amdgcn_raw_buffer_load_b64(r, vo, so, AUX); }
DI v4u hl4(rsrc_t r, int vo, int so) { asm volatile("" : "+v"(vo)); return __builtin_amdgcn_raw_buffer_load_b128(r, vo, so, SC1); }
DI v2u hl2(rsrc_t r, int vo, int so) { asm volatile("" : "+v"(vo)); return __builtin_amdgcn_raw_buffer_load_b64(r, vo, so, SC1); }
DI void st4(v4u v, rsrc_t r, int vo, int so) { __builtin_amdgcn_raw_buffer_store_b128(v, r, vo + so, 0, SC1); }
DI void st2(v2u v, rsrc_t r, int vo, int so) { __builtin_amdgcn_raw_buffer_store_b64(v, r, vo, so, SC1); }
DI void dma16(rsrc_t r, int vo, int so, unsigned lds) {
  asm volatile("s_mov_b32 m0, %0\n\ts_nop 0\n\tbuffer_load_dwordx4 %1, %2, %3 offen nt lds" ::"s"(lds), "v"(vo), "s"(r), "s"(so) : "memory");
}
#define VMCNT(n) asm volatile("s_waitcnt vmcnt(%0)" ::"n"(n) : "memory")
#define FENCE() asm volatile("" ::: "memory")
#define SCHED() __builtin_amdgcn_sched_barrier(0)

DI float bf2f(unsigned v) { return __uint_as_float(v << 16); }
DI unsigned pk(float a, float b) { f32x2 f = {a, b}; return __builtin_bit_cast(unsigned, __builtin_convertvector(f, bf16x2)); }
DI f32x4 mfma16(v4u a, v4u b, f32x4 c) {
  return __builtin_amdgcn_mfma_f32_16x16x32_bf16(__builtin_bit_cast(bf16x8, a), __builtin_bit_cast(bf16x8, b), c, 0, 0, 0);
}
DI f32x4 mfma16k16(v2u a, v2u b, f32x4 c) {
  return __builtin_amdgcn_mfma_f32_16x16x16bf16_1k(__builtin_bit_cast(s16x4, a), __builtin_bit_cast(s16x4, b), c, 0, 0, 0);
}
DI f32x16 mfma32(v4u a, v4u b, f32x16 c) {
  return __builtin_amdgcn_mfma_f32_32x32x16_bf16(__builtin_bit_cast(bf16x8, a), __builtin_bit_cast(bf16x8, b), c, 0, 0, 0);
}
template <int CTRL, int RM> DI float dppf(float old, float v) {
  return __builtin_bit_cast(float, __builtin_amdgcn_update_dpp(__builtin_bit_cast(int, old), __builtin_bit_cast(int, v), CTRL, RM, 0xf, false));
}
DI float row_sum(float v) {
  v += dppf<0xB1, 0xf>(v, v); v += dppf<0x4E, 0xf>(v, v); v += dppf<0x124, 0xf>(v, v); v += dppf<0x128, 0xf>(v, v);
  return v;
}
DI float row_max(float v) {
  v = fmaxf(v, dppf<0xB1, 0xf>(v, v)); v = fmaxf(v, dppf<0x4E, 0xf>(v, v));
  v = fmaxf(v, dppf<0x124, 0xf>(v, v)); v = fmaxf(v, dppf<0x128, 0xf>(v, v));
  return v;
}
DI float x16_sum(float v) { auto r = __builtin_amdgcn_permlane16_swap(__float_as_uint(v), __float_as_uint(v), false, false); return __uint_as_float(r[0]) + __uint_as_float(r[1]); }
DI float x32_sum(float v) { auto r = __builtin_amdgcn_permlane32_swap(__float_as_uint(v), __float_as_uint(v), false, false); return __uint_as_float(r[0]) + __uint_as_float(r[1]); }
DI float x16_max(float v) { auto r = __builtin_amdgcn_permlane16_swap(__float_as_uint(v), __float_as_uint(v), false, false); return fmaxf(__uint_as_float(r[0]), __uint_as_float(r[1])); }
DI float x32_max(float v) { auto r = __builtin_amdgcn_permlane32_swap(__float_as_uint(v), __float_as_uint(v), false, false); return fmaxf(__uint_as_float(r[0]), __uint_as_float(r[1])); }

#define RBAR() asm volatile("s_barrier" ::: "memory")
#define BAR() asm volatile("s_waitcnt lgkmcnt(0)\n\ts_barrier" ::: "memory")
DI unsigned genc(float v, unsigned t8) { return ((__float_as_uint(v) + 0x80u) & 0xFFFFFF00u) | t8; }
DI float gdec(unsigned hi) { return __uint_as_float(hi & 0xFFFFFF00u); }

#define STAMP(k)
#define STAMP_DECL
#define STAMP_OUT()

DI v4u bdiag(unsigned P, const unsigned (&s)[4]) {
  v4u b;
  b.x = __builtin_amdgcn_perm(P, P, s[0]); b.y = __builtin_amdgcn_perm(P, P, s[1]);
  b.z = __builtin_amdgcn_perm(P, P, s[2]); b.w = __builtin_amdgcn_perm(P, P, s[3]);
  return b;
}
DI void bdiag_sel(int cp, unsigned (&s)[4]) {
  const unsigned lo = (cp & 1) ? 0x01000C0Cu : 0x0C0C0100u, hi = (cp & 1) ? 0x03020C0Cu : 0x0C0C0302u, z = 0x0C0C0C0Cu;
  s[0] = cp < 2 ? lo : z; s[1] = cp < 2 ? z : lo; s[2] = cp < 2 ? hi : z; s[3] = cp < 2 ? z : hi;
}

DI unsigned lsbtag(unsigned tlo, unsigned thi, int p) { return (((p < 32 ? tlo : thi) >> (p & 31)) & 1u) | (((tlo ^ (unsigned)p) & 1u) << 16); }
constexpr unsigned LSBM = 0x00010001u;
DI unsigned lsbput(unsigned v, unsigned e) { return (v & ~LSBM) | e; }
DI unsigned xor4(v4u d, const unsigned (&e)[4]) { return ((d.x ^ e[0]) | (d.y ^ e[1])) | ((d.z ^ e[2]) | (d.w ^ e[3])); }
DI v4u tag8(f32x4 c, const unsigned (&t)[4]) {
  return v4u{(__float_as_uint(c[0]) & 0xFFFFFF00u) | t[0], (__float_as_uint(c[1]) & 0xFFFFFF00u) | t[1],
             (__float_as_uint(c[2]) & 0xFFFFFF00u) | t[2], (__float_as_uint(c[3]) & 0xFFFFFF00u) | t[3]};
}
__global__ void __launch_bounds__(512, 2)
fused_decode(const u16* __restrict__ xin, const u16* __restrict__ ckv, const u16* __restrict__ wq, const u16* __restrict__ wuk,
             const u16* __restrict__ wuv, const u16* __restrict__ wo, const u16* __restrict__ w1, const u16* __restrict__ w3,
             const u16* __restrict__ w2, u16* __restrict__ out, char* ws) {
  extern __shared__ __attribute__((aligned(16))) char smem[];
  LAS char* L = (LAS char*)smem;
  const unsigned lbase = (unsigned)(size_t)smem;
  const int lane = threadIdx.x & 63, w = __builtin_amdgcn_readfirstlane(threadIdx.x >> 6);
  const int xg = blockIdx.x & 7, qd_ = blockIdx.x >> 3, qq = qd_ & 15, idx = xg * 16 + qq, hd = idx >> 2, jq = idx & 3;
  const int i16 = lane & 15, g = lane >> 4, bj = i16 >> 2, cp = lane & 3;
  STAMP_DECL;
  STAMP(0);
  asm volatile("" ::"s"(xin), "s"(ckv), "s"(wq), "s"(wuk), "s"(wuv), "s"(wo), "s"(w1), "s"(w3), "s"(w2), "s"(out), "s"(ws));
  const rsrc_t rws = mk(ws, WS_END);
  const rsrc_t rx = mk(xin, 4 * 576 * 2);
  unsigned sel[4];
  bdiag_sel(cp, sel);
#define LOAD_TAG() const v2u wv_ = hl2(rws, 0, WS_W)
#define PUT_TAG(AD) do { const u64 tag_ = (((u64)wv_.y << 32) | wv_.x) + 0x9E3779B97F4A7C15ull; \
    if (lane == 0) *(LAS v2u*)(L + (AD)) = v2u{(unsigned)tag_, (unsigned)(tag_ >> 32)}; } while (0)
#define GET_TAG(AD) const v2u tg_ = *(LAS v2u*)(L + (AD)); \
  const unsigned tlo = tg_.x, thi = tg_.y, t8 = thi & 0xFFu; \
  const unsigned tb[4] = {tlo & 0xFFu, (tlo >> 8) & 0xFFu, (tlo >> 16) & 0xFFu, tlo >> 24}
  const int vo5 = (lane >> 3) * 32768 + (lane & 7) * 16;

  if (qd_ < 16) {
    const int so5 = WS_H5 + (2 * idx) * 128;
    if (w < 4) {
      v4u a[18][2];
      int vok[4];
      const rsrc_t rk = mk(wuk, 512 * 4096 * 2);
      const int n0 = (w - 2) * 16;
      RBAR();
      if (w == 0) {
        const rsrc_t rq = mk(wq, 576 * 4096 * 2);
        const int vo = 2 * g * 8192 + i16 * 16, so = 144 * jq * 8192 + hd * 256;
#pragma unroll
        for (int o = 0; o < 18; ++o) { a[o][0] = ld4<NT_W>(rq, vo, so + (8 * o) * 8192); a[o][1] = ld4<NT_W>(rq, vo, so + (8 * o + 1) * 8192); }
        SCHED();
      } else if (w >= 2) {
#pragma unroll
        for (int k = 0; k < 4; ++k) vok[k] = g * 8192 + ((i16 ^ ((g << 2) | k)) * 16);
        __builtin_amdgcn_s_sleep(DLY_K);
#pragma unroll
        for (int n = 0; n < 16; ++n) dma16(rk, vok[n & 3], (128 * jq + 4 * (n0 + n)) * 8192 + hd * 256, lbase + LP_UK + (n0 + n) * 1024);
      }
      STAMP(1);
      BAR();
      STAMP(2);
      GET_TAG(LP_TAG);
      if (w == 0) {
        v2u xp[9];
#pragma unroll
        for (int k = 0; k < 9; ++k) xp[k] = *(LAS v2u*)(L + LP_XP + ((bj * 4 + g) * 76 + 18 * jq + 2 * k) * 4);
        f32x4 qx = {0.f, 0.f, 0.f, 0.f}, qy = qx;
#pragma unroll
        for (int o = 0; o < 18; ++o) {
          const v4u B = bdiag((o & 1) ? xp[o >> 1].y : xp[o >> 1].x, sel);
          qx = mfma16(v4u{a[o][0].x, a[o][0].y, a[o][1].x, a[o][1].y}, B, qx);
          qy = mfma16(v4u{a[o][0].z, a[o][0].w, a[o][1].z, a[o][1].w}, B, qy);
        }
        const int po = WS_H1 + idx * 4096;
        st4(v4u{tlo, genc(qx[0], t8), tlo, genc(qx[1], t8)}, rws, lane * 16, po);
        st4(v4u{tlo, genc(qx[2], t8), tlo, genc(qx[3], t8)}, rws, lane * 16, po + 1024);
        st4(v4u{tlo, genc(qy[0], t8), tlo, genc(qy[1], t8)}, rws, lane * 16, po + 2048);
        st4(v4u{tlo, genc(qy[2], t8), tlo, genc(qy[3], t8)}, rws, lane * 16, po + 3072);
      }
      STAMP(3);
      BAR();
      __builtin_amdgcn_s_sleep(DLY_H1);
      {
        const int blk = w & 1, half = w >> 1;
        float q0, q1;
        for (int n = 0;; ++n) {
          v4u p[4];
#pragma unroll
          for (int jj = 0; jj < 4; ++jj) p[jj] = hl4(rws, lane * 16, WS_H1 + ((hd * 4 + jj) * 4 + blk * 2 + half) * 1024);
          unsigned bad = 0;
          q0 = 0.f; q1 = 0.f;
#pragma unroll
          for (int jj = 0; jj < 4; ++jj) {
            bad |= (p[jj].x ^ tlo) | (p[jj].z ^ tlo) | (((p[jj].y ^ t8) | (p[jj].w ^ t8)) & 0xFFu);
            q0 += gdec(p[jj].y); q1 += gdec(p[jj].w);
          }
          if (__all(bad == 0) || n >= GSPIN) break;
        }
        VMCNT(0);
        const unsigned qp = pk(q0, q1);
        const int d0 = 32 * g + 16 * half + 4 * blk + cp;
        *(LAS u16*)(L + LP_QB + bj * 320 + d0 * 2) = (u16)qp;
        *(LAS u16*)(L + LP_QB + bj * 320 + (d0 + 8) * 2) = (u16)(qp >> 16);
      }
      STAMP(4);
      BAR();
      STAMP(5);
      {
        v4u qf[4];
#pragma unroll
        for (int s = 0; s < 4; ++s) qf[s] = *(LAS v4u*)(L + LP_QB + cp * 320 + s * 64 + g * 16);
        const int fr = ((i16 & 3) << 2) | (i16 >> 2);
#pragma unroll
        for (int t = 0; t < 2; ++t) {
          f32x4 acc = {0.f, 0.f, 0.f, 0.f};
          const int rt = 2 * w + t;
#pragma unroll
          for (int s = 0; s < 4; ++s) {
            const v4u A = *(LAS v4u*)(L + LP_UK + (16 * rt + i16) * 256 + (((4 * s + g) ^ fr) * 16));
            acc = mfma16(A, qf[s], acc);
          }
          const float sc = 0.08838834764831845f * 1.4426950408889634f;
          if (i16 < 4) *(LAS v2u*)(L + LP_QO + i16 * 256 + (16 * rt + 4 * g) * 2) = v2u{pk(acc[0] * sc, acc[1] * sc), pk(acc[2] * sc, acc[3] * sc)};
        }
      }
      BAR();
      if (w == 0) {
        const v4u v = *(LAS v4u*)(L + LP_QO + lane * 16);
        st4(v4u{lsbput(v.x, lsbtag(tlo, thi, 4 * i16)), lsbput(v.y, lsbtag(tlo, thi, 4 * i16 + 1)), lsbput(v.z, lsbtag(tlo, thi, 4 * i16 + 2)),
                lsbput(v.w, lsbtag(tlo, thi, 4 * i16 + 3))}, rws, g * 32768 + i16 * 16, WS_QA + hd * 1024 + jq * 256);
      }
      STAMP(6);
      BAR();
      const rsrc_t r2 = mk(w2, 4608 * 576 * 2);
      const int so2 = (xg * 576 + 36 * qq) * 1152;
      v4u a2[5][2];
      v2u a8[2][2];
      {
        const int vo4 = 2 * g * 1152 + i16 * 16, vo4m = g < 2 ? vo4 : OOB;
        const int vo8 = 2 * g * 1152 + i16 * 8 + 1024, vo8m = (g < 2 && w == 0) ? vo8 : OOB;
#pragma unroll
        for (int o = 0; o < 5; ++o)
#pragma unroll
          for (int r = 0; r < 2; ++r) a2[o][r] = ld4<NT_W>(r2, o < 4 ? vo4 : vo4m, so2 + (8 * o + r) * 1152 + 256 * w);
#pragma unroll
        for (int r = 0; r < 2; ++r) { a8[0][r] = ld2<NT_W>(r2, vo8, so2 + (8 * w + r) * 1152); a8[1][r] = ld2<NT_W>(r2, vo8m, so2 + (32 + r) * 1152); }
        SCHED();
      }
      STAMP(7);
      BAR();
      v4u X[5], Y[5];
#pragma unroll
      for (int o = 0; o < 5; ++o) {
        X[o] = v4u{a2[o][0].x, a2[o][0].y, a2[o][1].x, a2[o][1].y};
        Y[o] = v4u{a2[o][0].z, a2[o][0].w, a2[o][1].z, a2[o][1].w};
        asm volatile("" : "+v"(X[o]), "+v"(Y[o]));
      }
      STAMP(8);
      BAR();
      STAMP(9);
      {
        const v4u h4 = *(LAS v4u*)(L + LP_HP + (bj * 4 + g) * 48);
        const unsigned h5 = *(LAS unsigned*)(L + LP_HP + (bj * 4 + g) * 48 + 16);
        const unsigned hw = *(LAS unsigned*)(L + LP_HP + (bj * 4 + g) * 48 + 4 * w);
        f32x4 c8 = {0.f, 0.f, 0.f, 0.f}, cx = c8, cy = c8;
        c8 = mfma16(v4u{a8[0][0].x, a8[0][0].y, a8[0][1].x, a8[0][1].y}, bdiag(hw, sel), c8);
        c8 = mfma16(v4u{a8[1][0].x, a8[1][0].y, a8[1][1].x, a8[1][1].y}, bdiag(h5, sel), c8);
#pragma unroll
        for (int o = 0; o < 5; ++o) {
          const v4u B = bdiag(o == 0 ? h4.x : o == 1 ? h4.y : o == 2 ? h4.z : o == 3 ? h4.w : h5, sel);
          cx = mfma16(X[o], B, cx);
          cy = mfma16(Y[o], B, cy);
        }
        *(LAS f32x4*)(L + LP_B8 + w * 1024 + lane * 16) = c8;
        st4(tag8(cx, tb), rws, vo5, so5 + (2 * w) * 8 * 32768);
        st4(tag8(cy, tb), rws, vo5, so5 + (2 * w + 1) * 8 * 32768);
        BAR();
      }
      STAMP(10);
    } else {
      const int v = w - 4;
      {
        const v4u x0 = ld4<0>(rx, lane * 16, v * 1152), x1 = ld4<0>(rx, lane < 8 ? lane * 16 : OOB, v * 1152 + 1024);
        v2u wv_;
        if (v == 0) wv_ = hl2(rws, 0, WS_W);
        SCHED();
        RBAR();
        if (v == 0) PUT_TAG(LP_TAG);
        float f[16], s = 0.f;
#pragma unroll
        for (int k = 0; k < 4; ++k) {
          f[2 * k] = bf2f(x0[k]); f[2 * k + 1] = __uint_as_float(x0[k] & 0xFFFF0000u);
          f[8 + 2 * k] = bf2f(x1[k]); f[8 + 2 * k + 1] = __uint_as_float(x1[k] & 0xFFFF0000u);
        }
#pragma unroll
        for (int k = 0; k < 16; ++k) s += f[k];
        const float mean = x32_sum(x16_sum(row_sum(s))) * (1.0f / 576.0f);
        float q0 = 0.f, q1 = 0.f;
#pragma unroll
        for (int k = 0; k < 8; ++k) { f[k] -= mean; f[8 + k] -= mean; q0 = fmaf(f[k], f[k], q0); q1 = fmaf(f[8 + k], f[8 + k], q1); }
        const float rstd = 1.0f / sqrtf(x32_sum(x16_sum(row_sum(lane < 8 ? q0 + q1 : q0))) * (1.0f / 576.0f) + 1e-5f);
#pragma unroll
        for (int hf = 0; hf < 2; ++hf) {
          if (hf == 0 || lane < 8) {
            const int o = lane + 64 * hf;
#pragma unroll
            for (int k = 0; k < 4; ++k) {
              const unsigned p = pk(f[8 * hf + 2 * k] * rstd, f[8 * hf + 2 * k + 1] * rstd);
              *(LAS unsigned*)(L + LP_XP + ((v * 4 + k) * 76 + o) * 4) = p;
              *(LAS unsigned*)(L + LP_XD + ((v * 4 + ((2 * k) & 3)) * 148 + 2 * o + (k >> 1)) * 4) = __builtin_amdgcn_perm(0u, p, 0x01000100u);
              *(LAS unsigned*)(L + LP_XD + ((v * 4 + ((2 * k + 1) & 3)) * 148 + 2 * o + (k >> 1)) * 4) = __builtin_amdgcn_perm(0u, p, 0x03020302u);
            }
          }
        }
      }
      STAMP(1);
      BAR();
      GET_TAG(LP_TAG);
      const int U0 = (81 * qq) / 144, Qb = 144 * (U0 + 1);
      unsigned xd[21];
#pragma unroll
      for (int t = 0; t < 21; ++t) {
        const int Q = min(81 * qq + 4 * t + v, 81 * qq + 80), qk = Q >= Qb ? Q - Qb : Q - 144 * U0;
        xd[t] = *(LAS unsigned*)(L + LP_XD + ((bj * 4 + g) * 148 + qk) * 4);
      }
      const GAS char* wb = (const GAS char*)(i16 < 8 ? w1 : w3) + (xg * 576 * 2 + (i16 & 7) * 16 + g * 9216);
      const unsigned m0 = cp == 0 ? 0xFFFFu : cp == 1 ? 0xFFFF0000u : 0u, m1 = cp == 2 ? 0xFFFFu : cp == 3 ? 0xFFFF0000u : 0u;
      f32x4 acc[4];
#pragma unroll
      for (int k = 0; k < 4; ++k) acc[k] = f32x4{0.f, 0.f, 0.f, 0.f};
      v4u a[21];
      BAR();
      BAR();
      STAMP(2);
#pragma unroll
      for (int t = -PACE_G; t < 21; ++t) {
        const int tl = t + PACE_G;
        if (tl < 21) {
          const int Q = 81 * qq + 4 * tl + v, u = Q >= Qb ? U0 + 1 : U0, qk = Q - 144 * u;
          if (tl < 20 || v == 0) {
            const GAS v4u* p = (const GAS v4u*)(wb + (unsigned)(qk * 4 * 9216 + u * 128));
            a[tl] = NT_W ? __builtin_nontemporal_load(p) : *p;
          } else a[tl] = v4u{0u, 0u, 0u, 0u};
          SCHED();
        }
        if (t == -1) BAR();
        if (tl == 20) { STAMP(3); BAR(); }
        if (t >= 0) {
          const int Q = 81 * qq + 4 * t + v;
          const v2u B = {xd[t] & m0, xd[t] & m1};
          if (Q < Qb) { acc[0] = mfma16k16(v2u{a[t].x, a[t].y}, B, acc[0]); acc[1] = mfma16k16(v2u{a[t].z, a[t].w}, B, acc[1]); }
          else        { acc[2] = mfma16k16(v2u{a[t].x, a[t].y}, B, acc[2]); acc[3] = mfma16k16(v2u{a[t].z, a[t].w}, B, acc[3]); }
          SCHED();
        }
      }
#pragma unroll
      for (int k = 0; k < 4; ++k) *(LAS f32x4*)(L + LP_RED + (v * 4 + k) * 1024 + lane * 16) = acc[k];
      STAMP(4);
      BAR();
      {
        f32x4 r = *(LAS f32x4*)(L + LP_RED + v * 1024 + lane * 16);
#pragma unroll
        for (int k = 1; k < 4; ++k) r += *(LAS f32x4*)(L + LP_RED + (k * 4 + v) * 1024 + lane * 16);
        const int po = WS_H7 + idx * 8192 + v * 2048;
        st4(v4u{tlo, genc(r[0], t8), tlo, genc(r[1], t8)}, rws, lane * 16, po);
        st4(v4u{tlo, genc(r[2], t8), tlo, genc(r[3], t8)}, rws, lane * 16, po + 1024);
      }
      STAMP(5);
      {
        const int t = v * 64 + lane;
        const int b = t / 36, cl = t - 36 * b, f = 36 * qq + cl, u = f >> 6, cu = f & 63;
        const int ig = cu >> 3, hh = (cu >> 2) & 1, reg = ig & 3;
        const int r0 = (144 * u) / 81, r1 = (144 * u + 143) / 81;
        const int lo = hh * 2048 + (reg >> 1) * 1024 + ((ig >> 2) * 16 + b * 4 + (cu & 3)) * 16 + (reg & 1) * 8;
        int off[3];
        unsigned use[3];
#pragma unroll
        for (int k = 0; k < 3; ++k) {
          const int rr = r0 + k;
          const bool on = t < 144 && rr <= r1;
          off[k] = on ? (xg * 16 + rr) * 8192 + (u - (81 * rr) / 144) * 4096 + lo : OOB;
          use[k] = on ? 0xFFFFFFFFu : 0u;
        }
        __builtin_amdgcn_s_sleep(DLY_H7);
        float gs, us;
        for (int n = 0;; ++n) {
          v2u pg[3], pu[3];
#pragma unroll
          for (int k = 0; k < 3; ++k) { pg[k] = hl2(rws, off[k], WS_H7); pu[k] = hl2(rws, off[k], WS_H7 + 512); }
          unsigned bad = 0;
          gs = 0.f; us = 0.f;
#pragma unroll
          for (int k = 0; k < 3; ++k) {
            bad |= ((pg[k].x ^ tlo) | (pu[k].x ^ tlo) | (((pg[k].y ^ t8) | (pu[k].y ^ t8)) & 0xFFu)) & use[k];
            gs += gdec(pg[k].y); us += gdec(pu[k].y);
          }
          if (__all(bad == 0) || n >= GSPIN) break;
        }
        const float hv = gs / (1.0f + __expf(-gs)) * us;
        if (t < 144) *(LAS u16*)(L + LP_HP + ((b * 4 + ((cl >> 1) & 3)) * 12 + (cl >> 3)) * 4 + (cl & 1) * 2) = (u16)pk(hv, hv);
        else if (t < 152) *(LAS unsigned*)(L + LP_HP + ((((t - 144) >> 1) * 4 + 2 + (t & 1)) * 12 + 4) * 4) = 0u;
      }
      STAMP(6);
      BAR();
      BAR();
      if (v == 0) {
        f32x4 s = *(LAS f32x4*)(L + LP_B8 + lane * 16);
#pragma unroll
        for (int k = 1; k < 4; ++k) s += *(LAS f32x4*)(L + LP_B8 + k * 1024 + lane * 16);
        st4(tag8(s, tb), rws, vo5, so5 + 8 * 8 * 32768);
      }
      STAMP(7);
    }
    STAMP_OUT();
    return;
  }

  const int ab = xg >> 1;
  const int r32 = lane & 31, hh = lane >> 5;
  const int so5 = WS_H5 + (2 * idx + 1) * 128;
  v2u av[8][2];
  v2u wa_;
  asm volatile("buffer_load_dwordx2 %0, off, %1, 0 sc1" : "=v"(wa_) : "s"(rws) : "memory");
  if (w < 4) {
    const rsrc_t rc = mk(ckv, 4 * 4096 * 512 * 2);
    {
      int vo[4];
#pragma unroll
      for (int k = 0; k < 4; ++k) vo[k] = g * 1024 + ((i16 ^ ((g << 2) | k)) * 16);
      const int so = (idx * 128 + 32 * w) * 1024;
#pragma unroll
      for (int n = 0; n < 32; ++n) {
        if (n >= PACE_C) VMCNT(PACE_C - 1);
        dma16(rc, vo[n & 3], so + (n & 7) * 4096 + (n >> 3) * 256, lbase + (n >> 3) * 32768 + (32 * w + 4 * (n & 7)) * 256);
        if (n == KQA) BAR();
      }
    }
  } else {
    VMCNT(0);
    BAR();
  }
  asm volatile("" : "+v"(wa_));
  const u64 tag_ = (((u64)wa_.y << 32) | wa_.x) + 0x9E3779B97F4A7C15ull;
  const unsigned tlo = (unsigned)tag_, thi = (unsigned)(tag_ >> 32), t8 = thi & 0xFFu;
  const unsigned tb[4] = {tlo & 0xFFu, (tlo >> 8) & 0xFFu, (tlo >> 16) & 0xFFu, tlo >> 24};
  unsigned e4[4];
#pragma unroll
  for (int k = 0; k < 4; ++k) e4[k] = lsbtag(tlo, thi, 4 * i16 + k);
  if (w < 4) {
    v4u fa[32];
    {
      const int fr = ((r32 & 3) << 2) | ((r32 >> 2) & 3);
      const int ca = (32 * w + r32) * 256;
#define QK_READ(Q) _Pragma("unroll") for (int k = 0; k < 8; ++k) fa[(Q)*8 + k] = *(LAS v4u*)(L + (Q)*32768 + ca + (((2 * k + hh) ^ fr) * 16)); SCHED();
      const rsrc_t rv = mk(wuv, 512 * 4096 * 2);
      const int sov = (128 * jq + 16 * (w >> 1)) * 8192 + hd * 256 + (w & 1) * 128;
      const int vov[2] = {(32 * ((2 * g) & 3) + (g >> 1)) * 8192 + i16 * 8, (32 * ((2 * g + 1) & 3) + (g >> 1)) * 8192 + i16 * 8};
#define AV_LOAD(A, B) _Pragma("unroll") for (int n = (A); n < (B); ++n) av[n >> 1][n & 1] = ld2<NT_W>(rv, vov[n & 1], sov + 2 * (n >> 1) * 8192); SCHED();
      VMCNT(8); QK_READ(0) QK_READ(1) QK_READ(2)
      VMCNT(4); AV_LOAD(0, 8)
      VMCNT(8); QK_READ(3)
      AV_LOAD(8, 16)
    }
    int qb[4];
#pragma unroll
    for (int k = 0; k < 4; ++k) { qb[k] = LA_QA + ((lane ^ ((2 * k + hh) & 7)) * 16); asm volatile("" : "+v"(qb[k])); }
    STAMP(2);
    BAR();
    STAMP(3);
    f32x16 sacc;
#pragma unroll
    for (int k = 0; k < 16; ++k) sacc[k] = 0.f;
    {
      v4u fb[32];
#pragma unroll
      for (int s = 0; s < 6; ++s) fb[s] = *(LAS v4u*)(L + qb[s & 3] + s * 1024);
      SCHED();
#pragma unroll
      for (int s = 0; s < 32; ++s) {
        if (s + 6 < 32) fb[s + 6] = *(LAS v4u*)(L + qb[(s + 6) & 3] + (s + 6) * 1024);
        sacc = mfma32(fa[s], fb[s], sacc);
        SCHED();
        if (s == 12) { RBAR(); SCHED(); }
      }
    }
    const int q4 = i16 >> 2, lowc = 2 * (g & 1) + (cp >> 1);
    int ta[4][2];
#pragma unroll
    for (int t = 0; t < 4; ++t)
#pragma unroll
      for (int t2 = 0; t2 < 2; ++t2) {
        ta[t][t2] = w * 32768 + (8 * t2 + 4 * hh + q4) * 256 + ((((t ^ q4) << 2) | (lowc ^ (2 * t2 + hh))) * 16) + 8 * (cp & 1);
        asm volatile("" : "+v"(ta[t][t2]));
      }
    v4u cb[32];
#define TR_READ(n) do { const v2u x0_ = __builtin_bit_cast(v2u, __builtin_amdgcn_ds_read_tr16_b64_v4i16((LAS s16x4*)(L + ta[(n) >> 3][0] + ((n) & 7) * 4096))); \
    const v2u x1_ = __builtin_bit_cast(v2u, __builtin_amdgcn_ds_read_tr16_b64_v4i16((LAS s16x4*)(L + ta[(n) >> 3][1] + ((n) & 7) * 4096)));                 \
    cb[n] = v4u{x0_.x, x0_.y, x1_.x, x1_.y}; } while (0)
#pragma unroll
    for (int n = 0; n < 6; ++n) TR_READ(n);
    SCHED();
    float mw = sacc[0];
#pragma unroll
    for (int k = 1; k < 16; ++k) mw = fmaxf(mw, sacc[k]);
    mw = x32_max(mw);
    if (lane < 32) *(LAS float*)(L + LA_MX + (r32 * 4 + w) * 4) = mw;
    STAMP(4);
    BAR();
    const f32x4 m4 = *(LAS f32x4*)(L + LA_MX + r32 * 16);
    const float mx = fmaxf(fmaxf(m4[0], m4[1]), fmaxf(m4[2], m4[3]));
    float ls = 0.f;
#pragma unroll
    for (int k = 0; k < 16; ++k) { sacc[k] = __builtin_amdgcn_exp2f(sacc[k] - mx); ls += sacc[k]; }
#pragma unroll
    for (int s2 = 0; s2 < 2; ++s2)
      *(LAS v4u*)(L + LA_PB + (2 * w + s2) * 1024 + lane * 16) =
          v4u{pk(sacc[8 * s2], sacc[8 * s2 + 1]), pk(sacc[8 * s2 + 2], sacc[8 * s2 + 3]), pk(sacc[8 * s2 + 4], sacc[8 * s2 + 5]), pk(sacc[8 * s2 + 6], sacc[8 * s2 + 7])};
    ls = x32_sum(ls);
    if (lane < 32) *(LAS float*)(L + LA_SUM + (r32 * 4 + w) * 4) = ls;
    BAR();
    STAMP(5);
    {
      v4u pf[8];
#pragma unroll
      for (int k = 0; k < 8; ++k) pf[k] = *(LAS v4u*)(L + LA_PB + k * 1024 + lane * 16);
      if (w == 0) {
        const f32x4 l4 = *(LAS f32x4*)(L + LA_SUM + r32 * 16);
        st4(v4u{tlo, genc(mx, t8), tlo, genc((l4[0] + l4[1]) + (l4[2] + l4[3]), t8)}, rws, lane < 32 ? r32 * 16 : OOB, WS_ML + idx * 512);
      }
      SCHED();
      f32x16 o[4];
#pragma unroll
      for (int t = 0; t < 4; ++t)
#pragma unroll
        for (int k = 0; k < 16; ++k) o[t][k] = 0.f;
#pragma unroll
      for (int n = 0; n < 32; ++n) {
        if (n + 6 < 32) TR_READ(n + 6);
        o[n >> 3] = mfma32(pf[n & 7], cb[n], o[n >> 3]);
        SCHED();
      }
      STAMP(1);
      const unsigned e0 = lsbtag(tlo, thi, 2 * r32), e1 = lsbtag(tlo, thi, 2 * r32 + 1);
      const int vo = hh * 4 * 131072 + r32 * 8, so = WS_H3 + (w * 128 + idx) * 256;
#pragma unroll
      for (int k = 0; k < 16; ++k)
        st2(v2u{lsbput(pk(o[0][k], o[1][k]), e0), lsbput(pk(o[2][k], o[3][k]), e1)}, rws, vo, so + ((k & 3) + 8 * (k >> 2)) * 131072);
    }
    STAMP(6);
    {
      v4u ml;
      for (int n = 0;; ++n) {
        ml = hl4(rws, r32 * 512, WS_ML + w * 32 * 512 + hd * 16);
        if (__all(((ml.x ^ tlo) | (ml.z ^ tlo) | (((ml.y ^ t8) | (ml.w ^ t8)) & 0xFFu)) == 0) || n >= GSPIN) break;
      }
      if (DLY_H3) __builtin_amdgcn_s_sleep(DLY_H3);
      STAMP(7);
      v4u d[8];
#pragma unroll
      for (int k = 0; k < 8; ++k) d[k] = hl4(rws, lane * 16, WS_H3 + idx * 32768 + w * 8192 + k * 1024);
      SCHED();
      const float m = gdec(ml.y), M = x16_max(row_max(m));
      const float e = __builtin_amdgcn_exp2f(m - M);
      const float al = e / x16_sum(row_sum(e * gdec(ml.w)));
      const unsigned ah = pk(al, al) & 0xFFFFu;
      const unsigned hl = ah | (pk(al - bf2f(ah), 0.f) << 16);
      const int e7 = lane & 7;
      const unsigned src = (lane & 8) ? 0x0302u : 0x0100u, s1 = (e7 & 1) ? ((src << 16) | 0x0C0Cu) : (0x0C0C0000u | src);
      unsigned s8[4];
#pragma unroll
      for (int k = 0; k < 4; ++k) s8[k] = (e7 >> 1) == k ? s1 : 0x0C0C0C0Cu;
      v4u bk[8];
#pragma unroll
      for (int k = 0; k < 8; ++k) { bk[k] = bdiag(__builtin_amdgcn_ds_bpermute((4 * k + g) * 4, hl), s8); asm volatile("" : "+v"(bk[k])); }
#pragma unroll 1
      for (int n = 0; n < GSPIN; ++n) {
        unsigned bad = 0;
#pragma unroll
        for (int k = 0; k < 8; ++k) bad |= xor4(d[k], e4);
        if (__all((bad & LSBM) == 0)) break;
#pragma unroll
        for (int k = 0; k < 8; ++k) d[k] = hl4(rws, lane * 16, WS_H3 + idx * 32768 + w * 8192 + k * 1024);
      }
      STAMP(8);
      f32x4 c = {0.f, 0.f, 0.f, 0.f};
#pragma unroll
      for (int k = 0; k < 8; ++k) c = mfma16(d[k], bk[k], c);
#pragma unroll
      for (int r = 0; r < 4; ++r) {
        const float y = c[r] + dppf<0x128, 0xf>(c[r], c[r]);
        if (!(lane & 8)) *(LAS u16*)(L + LA_CTX + ((w * 4 + (e7 >> 1)) * 20 + 4 * g + r) * 4 + 2 * (e7 & 1)) = (u16)pk(y, y);
      }
    }
    BAR();
    {
      const int half = w >> 1, blk = w & 1;
      const v4u p0 = *(LAS v4u*)(L + LA_CTX + ((bj * 4 + g) * 20 + 8 * half) * 4), p1 = *(LAS v4u*)(L + LA_CTX + ((bj * 4 + g) * 20 + 8 * half + 4) * 4);
      f32x4 c = {0.f, 0.f, 0.f, 0.f};
#pragma unroll
      for (int o = 0; o < 8; ++o) c = mfma16(v4u{av[o][0].x, av[o][0].y, av[o][1].x, av[o][1].y}, bdiag(o < 4 ? p0[o & 3] : p1[o & 3], sel), c);
#pragma unroll
      for (int r = 0; r < 4; ++r)
        *(LAS float*)(L + LA_UV + half * 2304 + ((bj * 4 + 2 * (r & 1) + (cp >> 1)) * 36 + (8 * blk + 2 * g + (r >> 1)) * 2 + (cp & 1)) * 4) = c[r];
    }
    BAR();
    STAMP(9);
    BAR();
    if (w == 0) {
      f32x4 s = *(LAS f32x4*)(L + LA_B8 + lane * 16);
#pragma unroll
      for (int k = 1; k < 4; ++k) s += *(LAS f32x4*)(L + LA_B8 + k * 1024 + lane * 16);
      st4(tag8(s, tb), rws, vo5, so5 + 8 * 8 * 32768);
    }
  } else {
    const int v = w - 4;
    STAMP(1);
    {
      v4u qa[8];
      for (int n = 0;; ++n) {
#pragma unroll
        for (int k = 0; k < 8; ++k) qa[k] = hl4(rws, lane * 16, WS_QA + (ab * 32 + 8 * v + k) * 1024);
        unsigned bad = 0;
#pragma unroll
        for (int k = 0; k < 8; ++k) bad |= xor4(qa[k], e4);
        if (__all((bad & LSBM) == 0) || n >= GSPIN) break;
        __builtin_amdgcn_s_sleep(DLY_R2);
      }
#pragma unroll
      for (int k = 0; k < 8; ++k) *(LAS v4u*)(L + LA_QA + (lane >> 1) * 1024 + ((((lane & 1) * 32 + 8 * v + k) ^ (lane & 7)) * 16)) = qa[k];
    }
    STAMP(2);
    BAR();
    const rsrc_t ro = mk(wo, 4096 * 576 * 2);
    v4u ao[16][2];
    v2u a8[4][2];
    {
      const int vo4 = 2 * g * 1152 + i16 * 16, vo8 = 2 * g * 1152 + i16 * 8 + 1024;
#pragma unroll
      for (int o = 0; o < 16; ++o) {
        const int op = (o + 4 * jq) & 15;
        if (o > 4) VMCNT(PACE_O - 2);
#pragma unroll
        for (int r = 0; r < 2; ++r) ao[o][r] = ld4<NT_WO>(ro, vo4, (hd * 128 + 8 * op + r) * 1152 + 256 * v);
        if ((o & 3) == 3) {
          const int o8 = 4 * (((o >> 2) + jq) & 3) + v;
#pragma unroll
          for (int r = 0; r < 2; ++r) a8[o >> 2][r] = ld2<NT_WO>(ro, vo8, (hd * 128 + 8 * o8 + r) * 1152);
        }
        SCHED();
        if (o == 3) { RBAR(); RBAR(); RBAR(); }
      }
    }
    STAMP(3);
    v4u X[16], Y[16];
#pragma unroll
    for (int o = 0; o < 16; ++o) {
      X[o] = v4u{ao[o][0].x, ao[o][0].y, ao[o][1].x, ao[o][1].y};
      Y[o] = v4u{ao[o][0].z, ao[o][0].w, ao[o][1].z, ao[o][1].w};
      asm volatile("" : "+v"(X[o]), "+v"(Y[o]));
    }
    VMCNT(0);
    STAMP(4);
    BAR();
    BAR();
    STAMP(5);
    {
      unsigned P[16];
#pragma unroll
      for (int q = 0; q < 4; ++q) {
        const int ad = LA_UV + ((bj * 4 + g) * 36 + 8 * ((q + jq) & 3)) * 4;
        const f32x4 x0 = *(LAS f32x4*)(L + ad) + *(LAS f32x4*)(L + ad + 2304), x1 = *(LAS f32x4*)(L + ad + 16) + *(LAS f32x4*)(L + ad + 16 + 2304);
        P[4 * q] = pk(x0[0], x0[1]); P[4 * q + 1] = pk(x0[2], x0[3]); P[4 * q + 2] = pk(x1[0], x1[1]); P[4 * q + 3] = pk(x1[2], x1[3]);
      }
      f32x4 c8 = {0.f, 0.f, 0.f, 0.f}, cx = c8, cy = c8;
#pragma unroll
      for (int q = 0; q < 4; ++q) {
        const unsigned pv = v == 0 ? P[4 * q] : v == 1 ? P[4 * q + 1] : v == 2 ? P[4 * q + 2] : P[4 * q + 3];
        c8 = mfma16(v4u{a8[q][0].x, a8[q][0].y, a8[q][1].x, a8[q][1].y}, bdiag(pv, sel), c8);
      }
      SCHED();
#pragma unroll
      for (int o = 0; o < 16; ++o) {
        const v4u B = bdiag(P[o], sel);
        cx = mfma16(X[o], B, cx);
        cy = mfma16(Y[o], B, cy);
        if (o == 1) { *(LAS f32x4*)(L + LA_B8 + v * 1024 + lane * 16) = c8; SCHED(); BAR(); }
      }
      st4(tag8(cx, tb), rws, vo5, so5 + (2 * v) * 8 * 32768);
      st4(tag8(cy, tb), rws, vo5, so5 + (2 * v + 1) * 8 * 32768);
    }
    STAMP(6);
  }
  if (qq < 9) {
    const int e = lane & 31, Ld = 8 * xg + (e >> 2), rr = e & 3;
    const int ob = (Ld & 15) >> 2, oc = qq < 8 ? 128 * (qq >> 1) + 8 * (4 * (Ld >> 4) + rr) + 4 * (qq & 1) + (Ld & 3) : 512 + 4 * (4 * (Ld >> 4) + rr) + (Ld & 3);
    unsigned xres = 0;
    if (w == 0) xres = ((const GAS u16*)xin)[ob * 576 + oc];
    BAR();
    __builtin_amdgcn_s_sleep(DLY_H5);
    STAMP(10);
    f32x4 s;
    for (int n = 0;; ++n) {
      v4u d[4];
#pragma unroll
      for (int k = 0; k < 4; ++k) d[k] = hl4(rws, lane * 16, WS_H5 + (qq * 8 + xg) * 32768 + (w * 4 + k) * 1024);
      const unsigned bad = (xor4(d[0], tb) | xor4(d[1], tb)) | (xor4(d[2], tb) | xor4(d[3], tb));
      s = (__builtin_bit_cast(f32x4, d[0]) + __builtin_bit_cast(f32x4, d[1])) + (__builtin_bit_cast(f32x4, d[2]) + __builtin_bit_cast(f32x4, d[3]));
      if (__all((bad & 0xFFu) == 0) || n >= GSPIN) break;
    }
#pragma unroll
    for (int r = 0; r < 4; ++r) s[r] = x32_sum(x16_sum(s[r] + dppf<0x128, 0xf>(s[r], s[r])));
    if (lane < 8) *(LAS f32x4*)(L + LA_FIN + w * 128 + lane * 16) = s;
    BAR();
    if (w == 0) {
      float y = bf2f(xres);
#pragma unroll
      for (int k = 0; k < 8; ++k) y += *(LAS float*)(L + LA_FIN + k * 128 + e * 4);
      if (lane < 32) ((GAS u16*)out)[ob * 576 + oc] = (u16)pk(y, y);
      if (qq == 0 && xg == 0) st2(v2u{tlo, thi}, rws, lane == 0 ? 0 : OOB, WS_W);
    }
    STAMP(11);
  }
  STAMP_OUT();
}

extern "C" void kernel_launch(void* const* d_in, const int* in_sizes, int n_in, void* d_out, int out_size, void* d_ws, size_t ws_size,
                              hipStream_t stream) {
  (void)hipFuncSetAttribute((const void*)fused_decode, hipFuncAttributeMaxDynamicSharedMemorySize, LDS_BYTES);
  fused_decode<<<256, 512, LDS_BYTES, stream>>>((const u16*)d_in[0], (const u16*)d_in[1], (const u16*)d_in[2], (const u16*)d_in[3],
                                                (const u16*)d_in[4], (const u16*)d_in[5], (const u16*)d_in[7], (const u16*)d_in[8],
                                                (const u16*)d_in[9], (u16*)d_out, (char*)d_ws);
}
```

```cpp
#define STAMPS
#define PWHAT 2
#define PB0 0
#define PB1 0
#define PW0 0
#define PW1 0
#define PK 0
#include <hip/hip_runtime.h>
#include <stdint.h>

typedef unsigned short u16;
typedef unsigned long long u64;
typedef unsigned v4u __attribute__((ext_vector_type(4)));
typedef unsigned v2u __attribute__((ext_vector_type(2)));
typedef float f32x2 __attribute__((ext_vector_type(2)));
typedef float f32x4 __attribute__((ext_vector_type(4)));
typedef float f32x16 __attribute__((ext_vector_type(16)));
typedef short s16x4 __attribute__((ext_vector_type(4)));
typedef __bf16 bf16x8 __attribute__((ext_vector_type(8)));
typedef __bf16 bf16x2 __attribute__((ext_vector_type(2)));
typedef __amdgpu_buffer_rsrc_t rsrc_t;
#define GAS __attribute__((address_space(1)))
#define LAS __attribute__((address_space(3)))
#define DI __device__ __forceinline__

#define NT_W 2
#define NT_WO 0
#define PACE_C 8
#define DLY_H1 4
#define KQA 31
#define PACE_K 4
#define DLY_K 8
#define PACE_G 5
#define PACE_O 8
#define DLY_R2 8
#define DLY_H3 0
#define DLY_H7 8
#define DLY_H5 8

constexpr int SC1 = 16;
constexpr int GSPIN = 1 << 13;

constexpr int WS_W = 0;
constexpr int WS_H1 = 4096;
constexpr int WS_QA = WS_H1 + 128 * 4096;
constexpr int WS_ML = WS_QA + 131072;
constexpr int WS_H7 = WS_ML + 65536;
constexpr int WS_H5 = WS_H7 + 128 * 8192;
constexpr int WS_H3 = WS_H5 + 72 * 256 * 128;
constexpr int WS_END = WS_H3 + 128 * 32768;
constexpr int WS_STAMP = WS_END;
constexpr int OOB = (int)0x80000000;

constexpr int LP_TAG = 4992, LP_XP = 0, LP_XD = 5120, LP_QB = 15360, LP_QO = 16896, LP_RED = 18432, LP_HP = 34816, LP_B8 = 35840, LP_UK = 40960;
constexpr int LA_QA = 131072, LA_MX = LA_QA;
constexpr int LA_PB = LA_QA + 1024, LA_SUM = LA_QA + 512, LA_CTX = LA_QA + 9216, LA_UV = LA_QA + 11264, LA_B8 = LA_QA + 16384,
              LA_FIN = LA_QA + 20480;
constexpr int LDS_BYTES = 163840;

DI rsrc_t mk(const void* p, int bytes) { return __builtin_amdgcn_make_buffer_rsrc((void*)p, (short)0, bytes, 0x00020000); }
template <int AUX> DI v4u ld4(rsrc_t r, int vo, int so) { return __builtin_amdgcn_raw_buffer_load_b128(r, vo, so, AUX); }
template <int AUX> DI v2u ld2(rsrc_t r, int vo, int so) { return __builtin_amdgcn_raw_buffer_load_b64(r, vo, so, AUX); }
DI v4u hl4(rsrc_t r, int vo, int so) { asm volatile("" : "+v"(vo)); return __builtin_amdgcn_raw_buffer_load_b128(r, vo, so, SC1); }
DI v2u hl2(rsrc_t r, int vo, int so) { asm volatile("" : "+v"(vo)); return __builtin_amdgcn_raw_buffer_load_b64(r, vo, so, SC1); }
DI void st4(v4u v, rsrc_t r, int vo, int so) { __builtin_amdgcn_raw_buffer_store_b128(v, r, vo + so, 0, SC1); }
DI void st2(v2u v, rsrc_t r, int vo, int so) { __builtin_amdgcn_raw_buffer_store_b64(v, r, vo, so, SC1); }
DI void dma16(rsrc_t r, int vo, int so, unsigned lds) {
  asm volatile("s_mov_b32 m0, %0\n\ts_nop 0\n\tbuffer_load_dwordx4 %1, %2, %3 offen nt lds" ::"s"(lds), "v"(vo), "s"(r), "s"(so) : "memory");
}
#define VMCNT(n) asm volatile("s_waitcnt vmcnt(%0)" ::"n"(n) : "memory")
#define FENCE() asm volatile("" ::: "memory")
#define SCHED() __builtin_amdgcn_sched_barrier(0)

DI float bf2f(unsigned v) { return __uint_as_float(v << 16); }
DI unsigned pk(float a, float b) { f32x2 f = {a, b}; return __builtin_bit_cast(unsigned, __builtin_convertvector(f, bf16x2)); }
DI f32x4 mfma16(v4u a, v4u b, f32x4 c) {
  return __builtin_amdgcn_mfma_f32_16x16x32_bf16(__builtin_bit_cast(bf16x8, a), __builtin_bit_cast(bf16x8, b), c, 0, 0, 0);
}
DI f32x4 mfma16k16(v2u a, v2u b, f32x4 c) {
  return __builtin_amdgcn_mfma_f32_16x16x16bf16_1k(__builtin_bit_cast(s16x4, a), __builtin_bit_cast(s16x4, b), c, 0, 0, 0);
}
DI f32x16 mfma32(v4u a, v4u b, f32x16 c) {
  return __builtin_amdgcn_mfma_f32_32x32x16_bf16(__builtin_bit_cast(bf16x8, a), __builtin_bit_cast(bf16x8, b), c, 0, 0, 0);
}
template <int CTRL, int RM> DI float dppf(float old, float v) {
  return __builtin_bit_cast(float, __builtin_amdgcn_update_dpp(__builtin_bit_cast(int, old), __builtin_bit_cast(int, v), CTRL, RM, 0xf, false));
}
DI float row_sum(float v) {
  v += dppf<0xB1, 0xf>(v, v); v += dppf<0x4E, 0xf>(v, v); v += dppf<0x124, 0xf>(v, v); v += dppf<0x128, 0xf>(v, v);
  return v;
}
DI float row_max(float v) {
  v = fmaxf(v, dppf<0xB1, 0xf>(v, v)); v = fmaxf(v, dppf<0x4E, 0xf>(v, v));
  v = fmaxf(v, dppf<0x124, 0xf>(v, v)); v = fmaxf(v, dppf<0x128, 0xf>(v, v));
  return v;
}
DI float x16_sum(float v) { auto r = __builtin_amdgcn_permlane16_swap(__float_as_uint(v), __float_as_uint(v), false, false); return __uint_as_float(r[0]) + __uint_as_float(r[1]); }
DI float x32_sum(float v) { auto r = __builtin_amdgcn_permlane32_swap(__float_as_uint(v), __float_as_uint(v), false, false); return __uint_as_float(r[0]) + __uint_as_float(r[1]); }
DI float x16_max(float v) { auto r = __builtin_amdgcn_permlane16_swap(__float_as_uint(v), __float_as_uint(v), false, false); return fmaxf(__uint_as_float(r[0]), __uint_as_float(r[1])); }
DI float x32_max(float v) { auto r = __builtin_amdgcn_permlane32_swap(__float_as_uint(v), __float_as_uint(v), false, false); return fmaxf(__uint_as_float(r[0]), __uint_as_float(r[1])); }

#define RBAR() asm volatile("s_barrier" ::: "memory")
#define BAR() asm volatile("s_waitcnt lgkmcnt(0)\n\ts_barrier" ::: "memory")
DI unsigned genc(float v, unsigned t8) { return ((__float_as_uint(v) + 0x80u) & 0xFFFFFF00u) | t8; }
DI float gdec(unsigned hi) { return __uint_as_float(hi & 0xFFFFFF00u); }

#define STAMP(k) do { stv[k] = __builtin_amdgcn_s_memtime(); } while (0)
#define STAMP_DECL u64 stv[12] = {0, 0, 0, 0, 0, 0, 0, 0, 0, 0, 0, 0}; const u64 strt0_ = __builtin_amdgcn_s_memrealtime()
#define STAMP_OUT() do { const u64 c1_ = __builtin_amdgcn_s_memtime(), r1_ = __builtin_amdgcn_s_memrealtime(); \
    if (lane == 0) { GAS u64* so_ = (GAS u64*)(ws + WS_STAMP) + ((size_t)blockIdx.x * 8 + w) * 16; \
    _Pragma("unroll") for (int k_ = 0; k_ < 12; ++k_) so_[k_] = stv[k_]; so_[12] = strt0_; so_[13] = c1_; so_[14] = r1_; } } while (0)

DI v4u bdiag(unsigned P, const unsigned (&s)[4]) {
  v4u b;
  b.x = __builtin_amdgcn_perm(P, P, s[0]); b.y = __builtin_amdgcn_perm(P, P, s[1]);
  b.z = __builtin_amdgcn_perm(P, P, s[2]); b.w = __builtin_amdgcn_perm(P, P, s[3]);
  return b;
}
DI void bdiag_sel(int cp, unsigned (&s)[4]) {
  const unsigned lo = (cp & 1) ? 0x01000C0Cu : 0x0C0C0100u, hi = (cp & 1) ? 0x03020C0Cu : 0x0C0C0302u, z = 0x0C0C0C0Cu;
  s[0] = cp < 2 ? lo : z; s[1] = cp < 2 ? z : lo; s[2] = cp < 2 ? hi : z; s[3] = cp < 2 ? z : hi;
}

DI unsigned lsbtag(unsigned tlo, unsigned thi, int p) { return (((p < 32 ? tlo : thi) >> (p & 31)) & 1u) | (((tlo ^ (unsigned)p) & 1u) << 16); }
constexpr unsigned LSBM = 0x00010001u;
DI unsigned lsbput(unsigned v, unsigned e) { return (v & ~LSBM) | e; }
DI unsigned xor4(v4u d, const unsigned (&e)[4]) { return ((d.x ^ e[0]) | (d.y ^ e[1])) | ((d.z ^ e[2]) | (d.w ^ e[3])); }
DI v4u tag8(f32x4 c, const unsigned (&t)[4]) {
  return v4u{(__float_as_uint(c[0]) & 0xFFFFFF00u) | t[0], (__float_as_uint(c[1]) & 0xFFFFFF00u) | t[1],
             (__float_as_uint(c[2]) & 0xFFFFFF00u) | t[2], (__float_as_uint(c[3]) & 0xFFFFFF00u) | t[3]};
}
__global__ void __launch_bounds__(512, 2)
fused_decode(const u16* __restrict__ xin, const u16* __restrict__ ckv, const u16* __restrict__ wq, const u16* __restrict__ wuk,
             const u16* __restrict__ wuv, const u16* __restrict__ wo, const u16* __restrict__ w1, const u16* __restrict__ w3,
             const u16* __restrict__ w2, u16* __restrict__ out, char* ws) {
  extern __shared__ __attribute__((aligned(16))) char smem[];
  LAS char* L = (LAS char*)smem;
  const unsigned lbase = (unsigned)(size_t)smem;
  const int lane = threadIdx.x & 63, w = __builtin_amdgcn_readfirstlane(threadIdx.x >> 6);
  const int xg = blockIdx.x & 7, qd_ = blockIdx.x >> 3, qq = qd_ & 15, idx = xg * 16 + qq, hd = idx >> 2, jq = idx & 3;
  const int i16 = lane & 15, g = lane >> 4, bj = i16 >> 2, cp = lane & 3;
  STAMP_DECL;
  STAMP(0);
  asm volatile("" ::"s"(xin), "s"(ckv), "s"(wq), "s"(wuk), "s"(wuv), "s"(wo), "s"(w1), "s"(w3), "s"(w2), "s"(out), "s"(ws));
  const rsrc_t rws = mk(ws, WS_END);
  const rsrc_t rx = mk(xin, 4 * 576 * 2);
  unsigned sel[4];
  bdiag_sel(cp, sel);
#define LOAD_TAG() const v2u wv_ = hl2(rws, 0, WS_W)
#define PUT_TAG(AD) do { const u64 tag_ = (((u64)wv_.y << 32) | wv_.x) + 0x9E3779B97F4A7C15ull; \
    if (lane == 0) *(LAS v2u*)(L + (AD)) = v2u{(unsigned)tag_, (unsigned)(tag_ >> 32)}; } while (0)
#define GET_TAG(AD) const v2u tg_ = *(LAS v2u*)(L + (AD)); \
  const unsigned tlo = tg_.x, thi = tg_.y, t8 = thi & 0xFFu; \
  const unsigned tb[4] = {tlo & 0xFFu, (tlo >> 8) & 0xFFu, (tlo >> 16) & 0xFFu, tlo >> 24}
  const int vo5 = (lane >> 3) * 32768 + (lane & 7) * 16;

  if (qd_ < 16) {
    const int so5 = WS_H5 + (2 * idx) * 128;
    if (w < 4) {
      v4u a[18][2];
      int vok[4];
      const rsrc_t rk = mk(wuk, 512 * 4096 * 2);
      const int n0 = (w - 2) * 16;
      RBAR();
      if (w == 0) {
        const rsrc_t rq = mk(wq, 576 * 4096 * 2);
        const int vo = 2 * g * 8192 + i16 * 16, so = 144 * jq * 8192 + hd * 256;
#pragma unroll
        for (int o = 0; o < 18; ++o) { a[o][0] = ld4<NT_W>(rq, vo, so + (8 * o) * 8192); a[o][1] = ld4<NT_W>(rq, vo, so + (8 * o + 1) * 8192); }
        SCHED();
      } else if (w >= 2) {
#pragma unroll
        for (int k = 0; k < 4; ++k) vok[k] = g * 8192 + ((i16 ^ ((g << 2) | k)) * 16);
        __builtin_amdgcn_s_sleep(DLY_K);
#pragma unroll
        for (int n = 0; n < 16; ++n) dma16(rk, vok[n & 3], (128 * jq + 4 * (n0 + n)) * 8192 + hd * 256, lbase + LP_UK + (n0 + n) * 1024);
      }
      STAMP(1);
      BAR();
      STAMP(2);
      GET_TAG(LP_TAG);
      if (w == 0) {
        v2u xp[9];
#pragma unroll
        for (int k = 0; k < 9; ++k) xp[k] = *(LAS v2u*)(L + LP_XP + ((bj * 4 + g) * 76 + 18 * jq + 2 * k) * 4);
        f32x4 qx = {0.f, 0.f, 0.f, 0.f}, qy = qx;
#pragma unroll
        for (int o = 0; o < 18; ++o) {
          const v4u B = bdiag((o & 1) ? xp[o >> 1].y : xp[o >> 1].x, sel);
          qx = mfma16(v4u{a[o][0].x, a[o][0].y, a[o][1].x, a[o][1].y}, B, qx);
          qy = mfma16(v4u{a[o][0].z, a[o][0].w, a[o][1].z, a[o][1].w}, B, qy);
        }
        const int po = WS_H1 + idx * 4096;
        st4(v4u{tlo, genc(qx[0], t8), tlo, genc(qx[1], t8)}, rws, lane * 16, po);
        st4(v4u{tlo, genc(qx[2], t8), tlo, genc(qx[3], t8)}, rws, lane * 16, po + 1024);
        st4(v4u{tlo, genc(qy[0], t8), tlo, genc(qy[1], t8)}, rws, lane * 16, po + 2048);
        st4(v4u{tlo, genc(qy[2], t8), tlo, genc(qy[3], t8)}, rws, lane * 16, po + 3072);
      }
      STAMP(3);
      BAR();
      __builtin_amdgcn_s_sleep(DLY_H1);
      {
        const int blk = w & 1, half = w >> 1;
        float q0, q1;
        for (int n = 0;; ++n) {
          v4u p[4];
#pragma unroll
          for (int jj = 0; jj < 4; ++jj) p[jj] = hl4(rws, lane * 16, WS_H1 + ((hd * 4 + jj) * 4 + blk * 2 + half) * 1024);
          unsigned bad = 0;
          q0 = 0.f; q1 = 0.f;
#pragma unroll
          for (int jj = 0; jj < 4; ++jj) {
            bad |= (p[jj].x ^ tlo) | (p[jj].z ^ tlo) | (((p[jj].y ^ t8) | (p[jj].w ^ t8)) & 0xFFu);
            q0 += gdec(p[jj].y); q1 += gdec(p[jj].w);
          }
          if (__all(bad == 0) || n >= GSPIN) break;
        }
        VMCNT(0);
        const unsigned qp = pk(q0, q1);
        const int d0 = 32 * g + 16 * half + 4 * blk + cp;
        *(LAS u16*)(L + LP_QB + bj * 320 + d0 * 2) = (u16)qp;
        *(LAS u16*)(L + LP_QB + bj * 320 + (d0 + 8) * 2) = (u16)(qp >> 16);
      }
      STAMP(4);
      BAR();
      STAMP(5);
      {
        v4u qf[4];
#pragma unroll
        for (int s = 0; s < 4; ++s) qf[s] = *(LAS v4u*)(L + LP_QB + cp * 320 + s * 64 + g * 16);
        const int fr = ((i16 & 3) << 2) | (i16 >> 2);
#pragma unroll
        for (int t = 0; t < 2; ++t) {
          f32x4 acc = {0.f, 0.f, 0.f, 0.f};
          const int rt = 2 * w + t;
#pragma unroll
          for (int s = 0; s < 4; ++s) {
            const v4u A = *(LAS v4u*)(L + LP_UK + (16 * rt + i16) * 256 + (((4 * s + g) ^ fr) * 16));
            acc = mfma16(A, qf[s], acc);
          }
          const float sc = 0.08838834764831845f * 1.4426950408889634f;
          if (i16 < 4) *(LAS v2u*)(L + LP_QO + i16 * 256 + (16 * rt + 4 * g) * 2) = v2u{pk(acc[0] * sc, acc[1] * sc), pk(acc[2] * sc, acc[3] * sc)};
        }
      }
      BAR();
      if (w == 0) {
        const v4u v = *(LAS v4u*)(L + LP_QO + lane * 16);
        st4(v4u{lsbput(v.x, lsbtag(tlo, thi, 4 * i16)), lsbput(v.y, lsbtag(tlo, thi, 4 * i16 + 1)), lsbput(v.z, lsbtag(tlo, thi, 4 * i16 + 2)),
                lsbput(v.w, lsbtag(tlo, thi, 4 * i16 + 3))}, rws, g * 32768 + i16 * 16, WS_QA + hd * 1024 + jq * 256);
      }
      STAMP(6);
      BAR();
      const rsrc_t r2 = mk(w2, 4608 * 576 * 2);
      const int so2 = (xg * 576 + 36 * qq) * 1152;
      v4u a2[5][2];
      v2u a8[2][2];
      {
        const int vo4 = 2 * g * 1152 + i16 * 16, vo4m = g < 2 ? vo4 : OOB;
        const int vo8 = 2 * g * 1152 + i16 * 8 + 1024, vo8m = (g < 2 && w == 0) ? vo8 : OOB;
#pragma unroll
        for (int o = 0; o < 5; ++o)
#pragma unroll
          for (int r = 0; r < 2; ++r) a2[o][r] = ld4<NT_W>(r2, o < 4 ? vo4 : vo4m, so2 + (8 * o + r) * 1152 + 256 * w);
#pragma unroll
        for (int r = 0; r < 2; ++r) { a8[0][r] = ld2<NT_W>(r2, vo8, so2 + (8 * w + r) * 1152); a8[1][r] = ld2<NT_W>(r2, vo8m, so2 + (32 + r) * 1152); }
        SCHED();
      }
      STAMP(7);
      BAR();
      v4u X[5], Y[5];
#pragma unroll
      for (int o = 0; o < 5; ++o) {
        X[o] = v4u{a2[o][0].x, a2[o][0].y, a2[o][1].x, a2[o][1].y};
        Y[o] = v4u{a2[o][0].z, a2[o][0].w, a2[o][1].z, a2[o][1].w};
        asm volatile("" : "+v"(X[o]), "+v"(Y[o]));
      }
      STAMP(8);
      BAR();
      STAMP(9);
      {
        const v4u h4 = *(LAS v4u*)(L + LP_HP + (bj * 4 + g) * 48);
        const unsigned h5 = *(LAS unsigned*)(L + LP_HP + (bj * 4 + g) * 48 + 16);
        const unsigned hw = *(LAS unsigned*)(L + LP_HP + (bj * 4 + g) * 48 + 4 * w);
        f32x4 c8 = {0.f, 0.f, 0.f, 0.f}, cx = c8, cy = c8;
        c8 = mfma16(v4u{a8[0][0].x, a8[0][0].y, a8[0][1].x, a8[0][1].y}, bdiag(hw, sel), c8);
        c8 = mfma16(v4u{a8[1][0].x, a8[1][0].y, a8[1][1].x, a8[1][1].y}, bdiag(h5, sel), c8);
#pragma unroll
        for (int o = 0; o < 5; ++o) {
          const v4u B = bdiag(o == 0 ? h4.x : o == 1 ? h4.y : o == 2 ? h4.z : o == 3 ? h4.w : h5, sel);
          cx = mfma16(X[o], B, cx);
          cy = mfma16(Y[o], B, cy);
        }
        *(LAS f32x4*)(L + LP_B8 + w * 1024 + lane * 16) = c8;
        st4(tag8(cx, tb), rws, vo5, so5 + (2 * w) * 8 * 32768);
        st4(tag8(cy, tb), rws, vo5, so5 + (2 * w + 1) * 8 * 32768);
        BAR();
      }
      STAMP(10);
    } else {
      const int v = w - 4;
      {
        const v4u x0 = ld4<0>(rx, lane * 16, v * 1152), x1 = ld4<0>(rx, lane < 8 ? lane * 16 : OOB, v * 1152 + 1024);
        v2u wv_;
        if (v == 0) wv_ = hl2(rws, 0, WS_W);
        SCHED();
        RBAR();
        if (v == 0) PUT_TAG(LP_TAG);
        float f[16], s = 0.f;
#pragma unroll
        for (int k = 0; k < 4; ++k) {
          f[2 * k] = bf2f(x0[k]); f[2 * k + 1] = __uint_as_float(x0[k] & 0xFFFF0000u);
          f[8 + 2 * k] = bf2f(x1[k]); f[8 + 2 * k + 1] = __uint_as_float(x1[k] & 0xFFFF0000u);
        }
#pragma unroll
        for (int k = 0; k < 16; ++k) s += f[k];
        const float mean = x32_sum(x16_sum(row_sum(s))) * (1.0f / 576.0f);
        float q0 = 0.f, q1 = 0.f;
#pragma unroll
        for (int k = 0; k < 8; ++k) { f[k] -= mean; f[8 + k] -= mean; q0 = fmaf(f[k], f[k], q0); q1 = fmaf(f[8 + k], f[8 + k], q1); }
        const float rstd = 1.0f / sqrtf(x32_sum(x16_sum(row_sum(lane < 8 ? q0 + q1 : q0))) * (1.0f / 576.0f) + 1e-5f);
#pragma unroll
        for (int hf = 0; hf < 2; ++hf) {
          if (hf == 0 || lane < 8) {
            const int o = lane + 64 * hf;
#pragma unroll
            for (int k = 0; k < 4; ++k) {
              const unsigned p = pk(f[8 * hf + 2 * k] * rstd, f[8 * hf + 2 * k + 1] * rstd);
              *(LAS unsigned*)(L + LP_XP + ((v * 4 + k) * 76 + o) * 4) = p;
              *(LAS unsigned*)(L + LP_XD + ((v * 4 + ((2 * k) & 3)) * 148 + 2 * o + (k >> 1)) * 4) = __builtin_amdgcn_perm(0u, p, 0x01000100u);
              *(LAS unsigned*)(L + LP_XD + ((v * 4 + ((2 * k + 1) & 3)) * 148 + 2 * o + (k >> 1)) * 4) = __builtin_amdgcn_perm(0u, p, 0x03020302u);
            }
          }
        }
      }
      STAMP(1);
      BAR();
      GET_TAG(LP_TAG);
      const int U0 = (81 * qq) / 144, Qb = 144 * (U0 + 1);
      unsigned xd[21];
#pragma unroll
      for (int t = 0; t < 21; ++t) {
        const int Q = min(81 * qq + 4 * t + v, 81 * qq + 80), qk = Q >= Qb ? Q - Qb : Q - 144 * U0;
        xd[t] = *(LAS unsigned*)(L + LP_XD + ((bj * 4 + g) * 148 + qk) * 4);
      }
      const GAS char* wb = (const GAS char*)(i16 < 8 ? w1 : w3) + (xg * 576 * 2 + (i16 & 7) * 16 + g * 9216);
      const unsigned m0 = cp == 0 ? 0xFFFFu : cp == 1 ? 0xFFFF0000u : 0u, m1 = cp == 2 ? 0xFFFFu : cp == 3 ? 0xFFFF0000u : 0u;
      f32x4 acc[4];
#pragma unroll
      for (int k = 0; k < 4; ++k) acc[k] = f32x4{0.f, 0.f, 0.f, 0.f};
      v4u a[21];
      BAR();
      BAR();
      STAMP(2);
#pragma unroll
      for (int t = -PACE_G; t < 21; ++t) {
        const int tl = t + PACE_G;
        if (tl < 21) {
          const int Q = 81 * qq + 4 * tl + v, u = Q >= Qb ? U0 + 1 : U0, qk = Q - 144 * u;
          if (tl < 20 || v == 0) {
            const GAS v4u* p = (const GAS v4u*)(wb + (unsigned)(qk * 4 * 9216 + u * 128));
            a[tl] = NT_W ? __builtin_nontemporal_load(p) : *p;
          } else a[tl] = v4u{0u, 0u, 0u, 0u};
          SCHED();
        }
        if (t == -1) BAR();
        if (tl == 20) { STAMP(3); BAR(); }
        if (t >= 0) {
          const int Q = 81 * qq + 4 * t + v;
          const v2u B = {xd[t] & m0, xd[t] & m1};
          if (Q < Qb) { acc[0] = mfma16k16(v2u{a[t].x, a[t].y}, B, acc[0]); acc[1] = mfma16k16(v2u{a[t].z, a[t].w}, B, acc[1]); }
          else        { acc[2] = mfma16k16(v2u{a[t].x, a[t].y}, B, acc[2]); acc[3] = mfma16k16(v2u{a[t].z, a[t].w}, B, acc[3]); }
          SCHED();
        }
      }
#pragma unroll
      for (int k = 0; k < 4; ++k) *(LAS f32x4*)(L + LP_RED + (v * 4 + k) * 1024 + lane * 16) = acc[k];
      STAMP(4);
      BAR();
      {
        f32x4 r = *(LAS f32x4*)(L + LP_RED + v * 1024 + lane * 16);
#pragma unroll
        for (int k = 1; k < 4; ++k) r += *(LAS f32x4*)(L + LP_RED + (k * 4 + v) * 1024 + lane * 16);
        const int po = WS_H7 + idx * 8192 + v * 2048;
        st4(v4u{tlo, genc(r[0], t8), tlo, genc(r[1], t8)}, rws, lane * 16, po);
        st4(v4u{tlo, genc(r[2], t8), tlo, genc(r[3], t8)}, rws, lane * 16, po + 1024);
      }
      STAMP(5);
      {
        const int t = v * 64 + lane;
        const int b = t / 36, cl = t - 36 * b, f = 36 * qq + cl, u = f >> 6, cu = f & 63;
        const int ig = cu >> 3, hh = (cu >> 2) & 1, reg = ig & 3;
        const int r0 = (144 * u) / 81, r1 = (144 * u + 143) / 81;
        const int lo = hh * 2048 + (reg >> 1) * 1024 + ((ig >> 2) * 16 + b * 4 + (cu & 3)) * 16 + (reg & 1) * 8;
        int off[3];
        unsigned use[3];
#pragma unroll
        for (int k = 0; k < 3; ++k) {
          const int rr = r0 + k;
          const bool on = t < 144 && rr <= r1;
          off[k] = on ? (xg * 16 + rr) * 8192 + (u - (81 * rr) / 144) * 4096 + lo : OOB;
          use[k] = on ? 0xFFFFFFFFu : 0u;
        }
        __builtin_amdgcn_s_sleep(DLY_H7);
        float gs, us;
        for (int n = 0;; ++n) {
          v2u pg[3], pu[3];
#pragma unroll
          for (int k = 0; k < 3; ++k) { pg[k] = hl2(rws, off[k], WS_H7); pu[k] = hl2(rws, off[k], WS_H7 + 512); }
          unsigned bad = 0;
          gs = 0.f; us = 0.f;
#pragma unroll
          for (int k = 0; k < 3; ++k) {
            bad |= ((pg[k].x ^ tlo) | (pu[k].x ^ tlo) | (((pg[k].y ^ t8) | (pu[k].y ^ t8)) & 0xFFu)) & use[k];
            gs += gdec(pg[k].y); us += gdec(pu[k].y);
          }
          if (__all(bad == 0) || n >= GSPIN) break;
        }
        const float hv = gs / (1.0f + __expf(-gs)) * us;
        if (t < 144) *(LAS u16*)(L + LP_HP + ((b * 4 + ((cl >> 1) & 3)) * 12 + (cl >> 3)) * 4 + (cl & 1) * 2) = (u16)pk(hv, hv);
        else if (t < 152) *(LAS unsigned*)(L + LP_HP + ((((t - 144) >> 1) * 4 + 2 + (t & 1)) * 12 + 4) * 4) = 0u;
      }
      STAMP(6);
      BAR();
      BAR();
      if (v == 0) {
        f32x4 s = *(LAS f32x4*)(L + LP_B8 + lane * 16);
#pragma unroll
        for (int k = 1; k < 4; ++k) s += *(LAS f32x4*)(L + LP_B8 + k * 1024 + lane * 16);
        st4(tag8(s, tb), rws, vo5, so5 + 8 * 8 * 32768);
      }
      STAMP(7);
    }
    STAMP_OUT();
    return;
  }

  const int ab = xg >> 1;
  const int r32 = lane & 31, hh = lane >> 5;
  const int so5 = WS_H5 + (2 * idx + 1) * 128;
  v2u av[8][2];
  v2u wa_;
  asm volatile("buffer_load_dwordx2 %0, off, %1, 0 sc1" : "=v"(wa_) : "s"(rws) : "memory");
  if (w < 4) {
    const rsrc_t rc = mk(ckv, 4 * 4096 * 512 * 2);
    {
      int vo[4];
#pragma unroll
      for (int k = 0; k < 4; ++k) vo[k] = g * 1024 + ((i16 ^ ((g << 2) | k)) * 16);
      const int so = (idx * 128 + 32 * w) * 1024;
#pragma unroll
      for (int n = 0; n < 32; ++n) {
        if (n >= PACE_C) VMCNT(PACE_C - 1);
        dma16(rc, vo[n & 3], so + (n & 7) * 4096 + (n >> 3) * 256, lbase + (n >> 3) * 32768 + (32 * w + 4 * (n & 7)) * 256);
        if (n == KQA) BAR();
      }
    }
  } else {
    VMCNT(0);
    BAR();
  }
  asm volatile("" : "+v"(wa_));
  const u64 tag_ = (((u64)wa_.y << 32) | wa_.x) + 0x9E3779B97F4A7C15ull;
  const unsigned tlo = (unsigned)tag_, thi = (unsigned)(tag_ >> 32), t8 = thi & 0xFFu;
  const unsigned tb[4] = {tlo & 0xFFu, (tlo >> 8) & 0xFFu, (tlo >> 16) & 0xFFu, tlo >> 24};
  unsigned e4[4];
#pragma unroll
  for (int k = 0; k < 4; ++k) e4[k] = lsbtag(tlo, thi, 4 * i16 + k);
  if (w < 4) {
    v4u fa[32];
    {
      const int fr = ((r32 & 3) << 2) | ((r32 >> 2) & 3);
      const int ca = (32 * w + r32) * 256;
#define QK_READ(Q) _Pragma("unroll") for (int k = 0; k < 8; ++k) fa[(Q)*8 + k] = *(LAS v4u*)(L + (Q)*32768 + ca + (((2 * k + hh) ^ fr) * 16)); SCHED();
      const rsrc_t rv = mk(wuv, 512 * 4096 * 2);
      const int sov = (128 * jq + 16 * (w >> 1)) * 8192 + hd * 256 + (w & 1) * 128;
      const int vov[2] = {(32 * ((2 * g) & 3) + (g >> 1)) * 8192 + i16 * 8, (32 * ((2 * g + 1) & 3) + (g >> 1)) * 8192 + i16 * 8};
#define AV_LOAD(A, B) _Pragma("unroll") for (int n = (A); n < (B); ++n) av[n >> 1][n & 1] = ld2<NT_W>(rv, vov[n & 1], sov + 2 * (n >> 1) * 8192); SCHED();
      VMCNT(8); QK_READ(0) QK_READ(1) QK_READ(2)
      VMCNT(4); AV_LOAD(0, 8)
      VMCNT(8); QK_READ(3)
      AV_LOAD(8, 16)
    }
    int qb[4];
#pragma unroll
    for (int k = 0; k < 4; ++k) { qb[k] = LA_QA + ((lane ^ ((2 * k + hh) & 7)) * 16); asm volatile("" : "+v"(qb[k])); }
    STAMP(2);
    BAR();
    STAMP(3);
    f32x16 sacc;
#pragma unroll
    for (int k = 0; k < 16; ++k) sacc[k] = 0.f;
    {
      v4u fb[32];
#pragma unroll
      for (int s = 0; s < 6; ++s) fb[s] = *(LAS v4u*)(L + qb[s & 3] + s * 1024);
      SCHED();
#pragma unroll
      for (int s = 0; s < 32; ++s) {
        if (s + 6 < 32) fb[s + 6] = *(LAS v4u*)(L + qb[(s + 6) & 3] + (s + 6) * 1024);
        sacc = mfma32(fa[s], fb[s], sacc);
        SCHED();
        if (s == 12) { RBAR(); SCHED(); }
      }
    }
    const int q4 = i16 >> 2, lowc = 2 * (g & 1) + (cp >> 1);
    int ta[4][2];
#pragma unroll
    for (int t = 0; t < 4; ++t)
#pragma unroll
      for (int t2 = 0; t2 < 2; ++t2) {
        ta[t][t2] = w * 32768 + (8 * t2 + 4 * hh + q4) * 256 + ((((t ^ q4) << 2) | (lowc ^ (2 * t2 + hh))) * 16) + 8 * (cp & 1);
        asm volatile("" : "+v"(ta[t][t2]));
      }
    v4u cb[32];
#define TR_READ(n) do { const v2u x0_ = __builtin_bit_cast(v2u, __builtin_amdgcn_ds_read_tr16_b64_v4i16((LAS s16x4*)(L + ta[(n) >> 3][0] + ((n) & 7) * 4096))); \
    const v2u x1_ = __builtin_bit_cast(v2u, __builtin_amdgcn_ds_read_tr16_b64_v4i16((LAS s16x4*)(L + ta[(n) >> 3][1] + ((n) & 7) * 4096)));                 \
    cb[n] = v4u{x0_.x, x0_.y, x1_.x, x1_.y}; } while (0)
#pragma unroll
    for (int n = 0; n < 6; ++n) TR_READ(n);
    SCHED();
    float mw = sacc[0];
#pragma unroll
    for (int k = 1; k < 16; ++k) mw = fmaxf(mw, sacc[k]);
    mw = x32_max(mw);
    if (lane < 32) *(LAS float*)(L + LA_MX + (r32 * 4 + w) * 4) = mw;
    STAMP(4);
    BAR();
    const f32x4 m4 = *(LAS f32x4*)(L + LA_MX + r32 * 16);
    const float mx = fmaxf(fmaxf(m4[0], m4[1]), fmaxf(m4[2], m4[3]));
    float ls = 0.f;
#pragma unroll
    for (int k = 0; k < 16; ++k) { sacc[k] = __builtin_amdgcn_exp2f(sacc[k] - mx); ls += sacc[k]; }
#pragma unroll
    for (int s2 = 0; s2 < 2; ++s2)
      *(LAS v4u*)(L + LA_PB + (2 * w + s2) * 1024 + lane * 16) =
          v4u{pk(sacc[8 * s2], sacc[8 * s2 + 1]), pk(sacc[8 * s2 + 2], sacc[8 * s2 + 3]), pk(sacc[8 * s2 + 4], sacc[8 * s2 + 5]), pk(sacc[8 * s2 + 6], sacc[8 * s2 + 7])};
    ls = x32_sum(ls);
    if (lane < 32) *(LAS float*)(L + LA_SUM + (r32 * 4 + w) * 4) = ls;
    BAR();
    STAMP(5);
    {
      v4u pf[8];
#pragma unroll
      for (int k = 0; k < 8; ++k) pf[k] = *(LAS v4u*)(L + LA_PB + k * 1024 + lane * 16);
      if (w == 0) {
        const f32x4 l4 = *(LAS f32x4*)(L + LA_SUM + r32 * 16);
        st4(v4u{tlo, genc(mx, t8), tlo, genc((l4[0] + l4[1]) + (l4[2] + l4[3]), t8)}, rws, lane < 32 ? r32 * 16 : OOB, WS_ML + idx * 512);
      }
      SCHED();
      f32x16 o[4];
#pragma unroll
      for (int t = 0; t < 4; ++t)
#pragma unroll
        for (int k = 0; k < 16; ++k) o[t][k] = 0.f;
#pragma unroll
      for (int n = 0; n < 32; ++n) {
        if (n + 6 < 32) TR_READ(n + 6);
        o[n >> 3] = mfma32(pf[n & 7], cb[n], o[n >> 3]);
        SCHED();
      }
      STAMP(1);
      const unsigned e0 = lsbtag(tlo, thi, 2 * r32), e1 = lsbtag(tlo, thi, 2 * r32 + 1);
      const int vo = hh * 4 * 131072 + r32 * 8, so = WS_H3 + (w * 128 + idx) * 256;
#pragma unroll
      for (int k = 0; k < 16; ++k)
        st2(v2u{lsbput(pk(o[0][k], o[1][k]), e0), lsbput(pk(o[2][k], o[3][k]), e1)}, rws, vo, so + ((k & 3) + 8 * (k >> 2)) * 131072);
    }
    STAMP(6);
    {
      v4u ml;
      for (int n = 0;; ++n) {
        ml = hl4(rws, r32 * 512, WS_ML + w * 32 * 512 + hd * 16);
        if (__all(((ml.x ^ tlo) | (ml.z ^ tlo) | (((ml.y ^ t8) | (ml.w ^ t8)) & 0xFFu)) == 0) || n >= GSPIN) break;
      }
      if (DLY_H3) __builtin_amdgcn_s_sleep(DLY_H3);
      STAMP(7);
      v4u d[8];
#pragma unroll
      for (int k = 0; k < 8; ++k) d[k] = hl4(rws, lane * 16, WS_H3 + idx * 32768 + w * 8192 + k * 1024);
      SCHED();
      const float m = gdec(ml.y), M = x16_max(row_max(m));
      const float e = __builtin_amdgcn_exp2f(m - M);
      const float al = e / x16_sum(row_sum(e * gdec(ml.w)));
      const unsigned ah = pk(al, al) & 0xFFFFu;
      const unsigned hl = ah | (pk(al - bf2f(ah), 0.f) << 16);
      const int e7 = lane & 7;
      const unsigned src = (lane & 8) ? 0x0302u : 0x0100u, s1 = (e7 & 1) ? ((src << 16) | 0x0C0Cu) : (0x0C0C0000u | src);
      unsigned s8[4];
#pragma unroll
      for (int k = 0; k < 4; ++k) s8[k] = (e7 >> 1) == k ? s1 : 0x0C0C0C0Cu;
      v4u bk[8];
#pragma unroll
      for (int k = 0; k < 8; ++k) { bk[k] = bdiag(__builtin_amdgcn_ds_bpermute((4 * k + g) * 4, hl), s8); asm volatile("" : "+v"(bk[k])); }
#pragma unroll 1
      for (int n = 0; n < GSPIN; ++n) {
        unsigned bad = 0;
#pragma unroll
        for (int k = 0; k < 8; ++k) bad |= xor4(d[k], e4);
        if (__all((bad & LSBM) == 0)) break;
#pragma unroll
        for (int k = 0; k < 8; ++k) d[k] = hl4(rws, lane * 16, WS_H3 + idx * 32768 + w * 8192 + k * 1024);
      }
      STAMP(8);
      f32x4 c = {0.f, 0.f, 0.f, 0.f};
#pragma unroll
      for (int k = 0; k < 8; ++k) c = mfma16(d[k], bk[k], c);
#pragma unroll
      for (int r = 0; r < 4; ++r) {
        const float y = c[r] + dppf<0x128, 0xf>(c[r], c[r]);
        if (!(lane & 8)) *(LAS u16*)(L + LA_CTX + ((w * 4 + (e7 >> 1)) * 20 + 4 * g + r) * 4 + 2 * (e7 & 1)) = (u16)pk(y, y);
      }
    }
    BAR();
    {
      const int half = w >> 1, blk = w & 1;
      const v4u p0 = *(LAS v4u*)(L + LA_CTX + ((bj * 4 + g) * 20 + 8 * half) * 4), p1 = *(LAS v4u*)(L + LA_CTX + ((bj * 4 + g) * 20 + 8 * half + 4) * 4);
      f32x4 c = {0.f, 0.f, 0.f, 0.f};
#pragma unroll
      for (int o = 0; o < 8; ++o) c = mfma16(v4u{av[o][0].x, av[o][0].y, av[o][1].x, av[o][1].y}, bdiag(o < 4 ? p0[o & 3] : p1[o & 3], sel), c);
#pragma unroll
      for (int r = 0; r < 4; ++r)
        *(LAS float*)(L + LA_UV + half * 2304 + ((bj * 4 + 2 * (r & 1) + (cp >> 1)) * 36 + (8 * blk + 2 * g + (r >> 1)) * 2 + (cp & 1)) * 4) = c[r];
    }
    BAR();
    STAMP(9);
    BAR();
    if (w == 0) {
      f32x4 s = *(LAS f32x4*)(L + LA_B8 + lane * 16);
#pragma unroll
      for (int k = 1; k < 4; ++k) s += *(LAS f32x4*)(L + LA_B8 + k * 1024 + lane * 16);
      st4(tag8(s, tb), rws, vo5, so5 + 8 * 8 * 32768);
    }
  } else {
    const int v = w - 4;
    STAMP(1);
    {
      v4u qa[8];
      for (int n = 0;; ++n) {
#pragma unroll
        for (int k = 0; k < 8; ++k) qa[k] = hl4(rws, lane * 16, WS_QA + (ab * 32 + 8 * v + k) * 1024);
        unsigned bad = 0;
#pragma unroll
        for (int k = 0; k < 8; ++k) bad |= xor4(qa[k], e4);
        if (__all((bad & LSBM) == 0) || n >= GSPIN) break;
        __builtin_amdgcn_s_sleep(DLY_R2);
      }
#pragma unroll
      for (int k = 0; k < 8; ++k) *(LAS v4u*)(L + LA_QA + (lane >> 1) * 1024 + ((((lane & 1) * 32 + 8 * v + k) ^ (lane & 7)) * 16)) = qa[k];
    }
    STAMP(2);
    BAR();
    const rsrc_t ro = mk(wo, 4096 * 576 * 2);
    v4u ao[16][2];
    v2u a8[4][2];
    {
      const int vo4 = 2 * g * 1152 + i16 * 16, vo8 = 2 * g * 1152 + i16 * 8 + 1024;
#pragma unroll
      for (int o = 0; o < 16; ++o) {
        const int op = (o + 4 * jq) & 15;
        if (o > 4) VMCNT(PACE_O - 2);
#pragma unroll
        for (int r = 0; r < 2; ++r) ao[o][r] = ld4<NT_WO>(ro, vo4, (hd * 128 + 8 * op + r) * 1152 + 256 * v);
        if ((o & 3) == 3) {
          const int o8 = 4 * (((o >> 2) + jq) & 3) + v;
#pragma unroll
          for (int r = 0; r < 2; ++r) a8[o >> 2][r] = ld2<NT_WO>(ro, vo8, (hd * 128 + 8 * o8 + r) * 1152);
        }
        SCHED();
        if (o == 3) { RBAR(); RBAR(); RBAR(); }
      }
    }
    STAMP(3);
    v4u X[16], Y[16];
#pragma unroll
    for (int o = 0; o < 16; ++o) {
      X[o] = v4u{ao[o][0].x, ao[o][0].y, ao[o][1].x, ao[o][1].y};
      Y[o] = v4u{ao[o][0].z, ao[o][0].w, ao[o][1].z, ao[o][1].w};
      asm volatile("" : "+v"(X[o]), "+v"(Y[o]));
    }
    VMCNT(0);
    STAMP(4);
    BAR();
    BAR();
    STAMP(5);
    {
      unsigned P[16];
#pragma unroll
      for (int q = 0; q < 4; ++q) {
        const int ad = LA_UV + ((bj * 4 + g) * 36 + 8 * ((q + jq) & 3)) * 4;
        const f32x4 x0 = *(LAS f32x4*)(L + ad) + *(LAS f32x4*)(L + ad + 2304), x1 = *(LAS f32x4*)(L + ad + 16) + *(LAS f32x4*)(L + ad + 16 + 2304);
        P[4 * q] = pk(x0[0], x0[1]); P[4 * q + 1] = pk(x0[2], x0[3]); P[4 * q + 2] = pk(x1[0], x1[1]); P[4 * q + 3] = pk(x1[2], x1[3]);
      }
      f32x4 c8 = {0.f, 0.f, 0.f, 0.f}, cx = c8, cy = c8;
#pragma unroll
      for (int q = 0; q < 4; ++q) {
        const unsigned pv = v == 0 ? P[4 * q] : v == 1 ? P[4 * q + 1] : v == 2 ? P[4 * q + 2] : P[4 * q + 3];
        c8 = mfma16(v4u{a8[q][0].x, a8[q][0].y, a8[q][1].x, a8[q][1].y}, bdiag(pv, sel), c8);
      }
      SCHED();
#pragma unroll
      for (int o = 0; o < 16; ++o) {
        const v4u B = bdiag(P[o], sel);
        cx = mfma16(X[o], B, cx);
        cy = mfma16(Y[o], B, cy);
        if (o == 1) { *(LAS f32x4*)(L + LA_B8 + v * 1024 + lane * 16) = c8; SCHED(); BAR(); }
      }
      st4(tag8(cx, tb), rws, vo5, so5 + (2 * v) * 8 * 32768);
      st4(tag8(cy, tb), rws, vo5, so5 + (2 * v + 1) * 8 * 32768);
    }
    STAMP(6);
  }
  if (qq < 9) {
    const int e = lane & 31, Ld = 8 * xg + (e >> 2), rr = e & 3;
    const int ob = (Ld & 15) >> 2, oc = qq < 8 ? 128 * (qq >> 1) + 8 * (4 * (Ld >> 4) + rr) + 4 * (qq & 1) + (Ld & 3) : 512 + 4 * (4 * (Ld >> 4) + rr) + (Ld & 3);
    unsigned xres = 0;
    if (w == 0) xres = ((const GAS u16*)xin)[ob * 576 + oc];
    BAR();
    __builtin_amdgcn_s_sleep(DLY_H5);
    STAMP(10);
    f32x4 s;
    for (int n = 0;; ++n) {
      v4u d[4];
#pragma unroll
      for (int k = 0; k < 4; ++k) d[k] = hl4(rws, lane * 16, WS_H5 + (qq * 8 + xg) * 32768 + (w * 4 + k) * 1024);
      const unsigned bad = (xor4(d[0], tb) | xor4(d[1], tb)) | (xor4(d[2], tb) | xor4(d[3], tb));
      s = (__builtin_bit_cast(f32x4, d[0]) + __builtin_bit_cast(f32x4, d[1])) + (__builtin_bit_cast(f32x4, d[2]) + __builtin_bit_cast(f32x4, d[3]));
      if (__all((bad & 0xFFu) == 0) || n >= GSPIN) break;
    }
#pragma unroll
    for (int r = 0; r < 4; ++r) s[r] = x32_sum(x16_sum(s[r] + dppf<0x128, 0xf>(s[r], s[r])));
    if (lane < 8) *(LAS f32x4*)(L + LA_FIN + w * 128 + lane * 16) = s;
    BAR();
    if (w == 0) {
      float y = bf2f(xres);
#pragma unroll
      for (int k = 0; k < 8; ++k) y += *(LAS float*)(L + LA_FIN + k * 128 + e * 4);
      if (lane < 32) ((GAS u16*)out)[ob * 576 + oc] = (u16)pk(y, y);
      if (qq == 0 && xg == 0) st2(v2u{tlo, thi}, rws, lane == 0 ? 0 : OOB, WS_W);
    }
    STAMP(11);
  }
  STAMP_OUT();
}

__global__ void k_spin(char* ws){
  const u64* o=(const u64*)(ws+WS_STAMP);
  int t=threadIdx.x;
  u64 mn=~0ull,mxs=0,mxe=0, a=0;
  __shared__ u64 s0[256],s1[256],s2[256],s3[256];
  for(int i=t;i<2048;i+=256){ u64 st=o[i*16+12], en=o[i*16+14]; mn=st<mn?st:mn; mxs=st>mxs?st:mxs; mxe=en>mxe?en:mxe;
    int blk=i>>3,w=i&7; if(blk>=PB0&&blk<PB1&&w>=PW0&&w<PW1&&o[i*16+PK]){ u64 v=st*100+(o[i*16+PK]-o[i*16])*100/23; a=v>a?v:a; } }
  s0[t]=mn;s1[t]=mxs;s2[t]=mxe;s3[t]=a; __syncthreads();
  if(t==0){ for(int i=1;i<256;i++){ mn=s0[i]<mn?s0[i]:mn; mxs=s1[i]>mxs?s1[i]:mxs; mxe=s2[i]>mxe?s2[i]:mxe; a=s3[i]>a?s3[i]:a; }
    u64 T=(PWHAT==0)?(mxe-mn)*100:(PWHAT==1)?(mxs-mn)*100:(PWHAT==2)?0:(a-mn*100);
    if(T>1000000) T=1000000;
    u64 t0=__builtin_amdgcn_s_memrealtime();
    while(__builtin_amdgcn_s_memrealtime()-t0 < T) __builtin_amdgcn_s_sleep(8);
  }
}
extern "C" void kernel_launch(void* const* d_in, const int* in_sizes, int n_in, void* d_out, int out_size, void* d_ws, size_t ws_size,
                              hipStream_t stream) {
  (void)hipFuncSetAttribute((const void*)fused_decode, hipFuncAttributeMaxDynamicSharedMemorySize, LDS_BYTES);
  fused_decode<<<256, 512, LDS_BYTES, stream>>>((const u16*)d_in[0], (const u16*)d_in[1], (const u16*)d_in[2], (const u16*)d_in[3],
                                                (const u16*)d_in[4], (const u16*)d_in[5], (const u16*)d_in[7], (const u16*)d_in[8],
                                                (const u16*)d_in[9], (u16*)d_out, (char*)d_ws);
  k_spin<<<1,256,0,stream>>>((char*)d_ws);
}
```

```cpp
#define PWHAT 2
#define PRE 0
#define EXTRA 2
#define PB0 0
#define PB1 0
#define PW0 0
#define PW1 0
#define PK 0
#include <hip/hip_runtime.h>
#include <stdint.h>

typedef unsigned short u16;
typedef unsigned long long u64;
typedef unsigned v4u __attribute__((ext_vector_type(4)));
typedef unsigned v2u __attribute__((ext_vector_type(2)));
typedef float f32x2 __attribute__((ext_vector_type(2)));
typedef float f32x4 __attribute__((ext_vector_type(4)));
typedef float f32x16 __attribute__((ext_vector_type(16)));
typedef short s16x4 __attribute__((ext_vector_type(4)));
typedef __bf16 bf16x8 __attribute__((ext_vector_type(8)));
typedef __bf16 bf16x2 __attribute__((ext_vector_type(2)));
typedef __amdgpu_buffer_rsrc_t rsrc_t;
#define GAS __attribute__((address_space(1)))
#define LAS __attribute__((address_space(3)))
#define DI __device__ __forceinline__

#define NT_W 2
#define NT_WO 0
#define PACE_C 8
#define DLY_H1 4
#define KQA 31
#define PACE_K 4
#define DLY_K 8
#define PACE_G 5
#define PACE_O 8
#define DLY_R2 8
#define DLY_H3 0
#define DLY_H7 8
#define DLY_H5 8

constexpr int SC1 = 16;
constexpr int GSPIN = 1 << 13;

constexpr int WS_W = 0;
constexpr int WS_H1 = 4096;
constexpr int WS_QA = WS_H1 + 128 * 4096;
constexpr int WS_ML = WS_QA + 131072;
constexpr int WS_H7 = WS_ML + 65536;
constexpr int WS_H5 = WS_H7 + 128 * 8192;
constexpr int WS_H3 = WS_H5 + 72 * 256 * 128;
constexpr int WS_END = WS_H3 + 128 * 32768;
constexpr int WS_STAMP = WS_END;
constexpr int OOB = (int)0x80000000;

constexpr int LP_TAG = 4992, LP_XP = 0, LP_XD = 5120, LP_QB = 15360, LP_QO = 16896, LP_RED = 18432, LP_HP = 34816, LP_B8 = 35840, LP_UK = 40960;
constexpr int LA_QA = 131072, LA_MX = LA_QA;
constexpr int LA_PB = LA_QA + 1024, LA_SUM = LA_QA + 512, LA_CTX = LA_QA + 9216, LA_UV = LA_QA + 11264, LA_B8 = LA_QA + 16384,
              LA_FIN = LA_QA + 20480;
constexpr int LDS_BYTES = 163840;

DI rsrc_t mk(const void* p, int bytes) { return __builtin_amdgcn_make_buffer_rsrc((void*)p, (short)0, bytes, 0x00020000); }
template <int AUX> DI v4u ld4(rsrc_t r, int vo, int so) { return __builtin_amdgcn_raw_buffer_load_b128(r, vo, so, AUX); }
template <int AUX> DI v2u ld2(rsrc_t r, int vo, int so) { return __builtin_amdgcn_raw_buffer_load_b64(r, vo, so, AUX); }
DI v4u hl4(rsrc_t r, int vo, int so) { asm volatile("" : "+v"(vo)); return __builtin_amdgcn_raw_buffer_load_b128(r, vo, so, SC1); }
DI v2u hl2(rsrc_t r, int vo, int so) { asm volatile("" : "+v"(vo)); return __builtin_amdgcn_raw_buffer_load_b64(r, vo, so, SC1); }
DI void st4(v4u v, rsrc_t r, int vo, int so) { __builtin_amdgcn_raw_buffer_store_b128(v, r, vo + so, 0, SC1); }
DI void st2(v2u v, rsrc_t r, int vo, int so) { __builtin_amdgcn_raw_buffer_store_b64(v, r, vo, so, SC1); }
DI void dma16(rsrc_t r, int vo, int so, unsigned lds) {
  asm volatile("s_mov_b32 m0, %0\n\ts_nop 0\n\tbuffer_load_dwordx4 %1, %2, %3 offen nt lds" ::"s"(lds), "v"(vo), "s"(r), "s"(so) : "memory");
}
#define VMCNT(n) asm volatile("s_waitcnt vmcnt(%0)" ::"n"(n) : "memory")
#define FENCE() asm volatile("" ::: "memory")
#define SCHED() __builtin_amdgcn_sched_barrier(0)

DI float bf2f(unsigned v) { return __uint_as_float(v << 16); }
DI unsigned pk(float a, float b) { f32x2 f = {a, b}; return __builtin_bit_cast(unsigned, __builtin_convertvector(f, bf16x2)); }
DI f32x4 mfma16(v4u a, v4u b, f32x4 c) {
  return __builtin_amdgcn_mfma_f32_16x16x32_bf16(__builtin_bit_cast(bf16x8, a), __builtin_bit_cast(bf16x8, b), c, 0, 0, 0);
}
DI f32x4 mfma16k16(v2u a, v2u b, f32x4 c) {
  return __builtin_amdgcn_mfma_f32_16x16x16bf16_1k(__builtin_bit_cast(s16x4, a), __builtin_bit_cast(s16x4, b), c, 0, 0, 0);
}
DI f32x16 mfma32(v4u a, v4u b, f32x16 c) {
  return __builtin_amdgcn_mfma_f32_32x32x16_bf16(__builtin_bit_cast(bf16x8, a), __builtin_bit_cast(bf16x8, b), c, 0, 0, 0);
}
template <int CTRL, int RM> DI float dppf(float old, float v) {
  return __builtin_bit_cast(float, __builtin_amdgcn_update_dpp(__builtin_bit_cast(int, old), __builtin_bit_cast(int, v), CTRL, RM, 0xf, false));
}
DI float row_sum(float v) {
  v += dppf<0xB1, 0xf>(v, v); v += dppf<0x4E, 0xf>(v, v); v += dppf<0x124, 0xf>(v, v); v += dppf<0x128, 0xf>(v, v);
  return v;
}
DI float row_max(float v) {
  v = fmaxf(v, dppf<0xB1, 0xf>(v, v)); v = fmaxf(v, dppf<0x4E, 0xf>(v, v));
  v = fmaxf(v, dppf<0x124, 0xf>(v, v)); v = fmaxf(v, dppf<0x128, 0xf>(v, v));
  return v;
}
DI float x16_sum(float v) { auto r = __builtin_amdgcn_permlane16_swap(__float_as_uint(v), __float_as_uint(v), false, false); return __uint_as_float(r[0]) + __uint_as_float(r[1]); }
DI float x32_sum(float v) { auto r = __builtin_amdgcn_permlane32_swap(__float_as_uint(v), __float_as_uint(v), false, false); return __uint_as_float(r[0]) + __uint_as_float(r[1]); }
DI float x16_max(float v) { auto r = __builtin_amdgcn_permlane16_swap(__float_as_uint(v), __float_as_uint(v), false, false); return fmaxf(__uint_as_float(r[0]), __uint_as_float(r[1])); }
DI float x32_max(float v) { auto r = __builtin_amdgcn_permlane32_swap(__float_as_uint(v), __float_as_uint(v), false, false); return fmaxf(__uint_as_float(r[0]), __uint_as_float(r[1])); }

#define RBAR() asm volatile("s_barrier" ::: "memory")
#define BAR() asm volatile("s_waitcnt lgkmcnt(0)\n\ts_barrier" ::: "memory")
DI unsigned genc(float v, unsigned t8) { return ((__float_as_uint(v) + 0x80u) & 0xFFFFFF00u) | t8; }
DI float gdec(unsigned hi) { return __uint_as_float(hi & 0xFFFFFF00u); }

#define STAMP(k)
#define STAMP_DECL
#define STAMP_OUT()

DI v4u bdiag(unsigned P, const unsigned (&s)[4]) {
  v4u b;
  b.x = __builtin_amdgcn_perm(P, P, s[0]); b.y = __builtin_amdgcn_perm(P, P, s[1]);
  b.z = __builtin_amdgcn_perm(P, P, s[2]); b.w = __builtin_amdgcn_perm(P, P, s[3]);
  return b;
}
DI void bdiag_sel(int cp, unsigned (&s)[4]) {
  const unsigned lo = (cp & 1) ? 0x01000C0Cu : 0x0C0C0100u, hi = (cp & 1) ? 0x03020C0Cu : 0x0C0C0302u, z = 0x0C0C0C0Cu;
  s[0] = cp < 2 ? lo : z; s[1] = cp < 2 ? z : lo; s[2] = cp < 2 ? hi : z; s[3] = cp < 2 ? z : hi;
}

DI unsigned lsbtag(unsigned tlo, unsigned thi, int p) { return (((p < 32 ? tlo : thi) >> (p & 31)) & 1u) | (((tlo ^ (unsigned)p) & 1u) << 16); }
constexpr unsigned LSBM = 0x00010001u;
DI unsigned lsbput(unsigned v, unsigned e) { return (v & ~LSBM) | e; }
DI unsigned xor4(v4u d, const unsigned (&e)[4]) { return ((d.x ^ e[0]) | (d.y ^ e[1])) | ((d.z ^ e[2]) | (d.w ^ e[3])); }
DI v4u tag8(f32x4 c, const unsigned (&t)[4]) {
  return v4u{(__float_as_uint(c[0]) & 0xFFFFFF00u) | t[0], (__float_as_uint(c[1]) & 0xFFFFFF00u) | t[1],
             (__float_as_uint(c[2]) & 0xFFFFFF00u) | t[2], (__float_as_uint(c[3]) & 0xFFFFFF00u) | t[3]};
}
__global__ void __launch_bounds__(512, 2)
fused_decode(const u16* __restrict__ xin, const u16* __restrict__ ckv, const u16* __restrict__ wq, const u16* __restrict__ wuk,
             const u16* __restrict__ wuv, const u16* __restrict__ wo, const u16* __restrict__ w1, const u16* __restrict__ w3,
             const u16* __restrict__ w2, u16* __restrict__ out, char* ws) {
  extern __shared__ __attribute__((aligned(16))) char smem[];
  LAS char* L = (LAS char*)smem;
  const unsigned lbase = (unsigned)(size_t)smem;
  const int lane = threadIdx.x & 63, w = __builtin_amdgcn_readfirstlane(threadIdx.x >> 6);
  const int xg = blockIdx.x & 7, qd_ = blockIdx.x >> 3, qq = qd_ & 15, idx = xg * 16 + qq, hd = idx >> 2, jq = idx & 3;
  const int i16 = lane & 15, g = lane >> 4, bj = i16 >> 2, cp = lane & 3;
  STAMP_DECL;
  STAMP(0);
  asm volatile("" ::"s"(xin), "s"(ckv), "s"(wq), "s"(wuk), "s"(wuv), "s"(wo), "s"(w1), "s"(w3), "s"(w2), "s"(out), "s"(ws));
  const rsrc_t rws = mk(ws, WS_END);
  const rsrc_t rx = mk(xin, 4 * 576 * 2);
  unsigned sel[4];
  bdiag_sel(cp, sel);
#define LOAD_TAG() const v2u wv_ = hl2(rws, 0, WS_W)
#define PUT_TAG(AD) do { const u64 tag_ = (((u64)wv_.y << 32) | wv_.x) + 0x9E3779B97F4A7C15ull; \
    if (lane == 0) *(LAS v2u*)(L + (AD)) = v2u{(unsigned)tag_, (unsigned)(tag_ >> 32)}; } while (0)
#define GET_TAG(AD) const v2u tg_ = *(LAS v2u*)(L + (AD)); \
  const unsigned tlo = tg_.x, thi = tg_.y, t8 = thi & 0xFFu; \
  const unsigned tb[4] = {tlo & 0xFFu, (tlo >> 8) & 0xFFu, (tlo >> 16) & 0xFFu, tlo >> 24}
  const int vo5 = (lane >> 3) * 32768 + (lane & 7) * 16;

  if (qd_ < 16) {
    const int so5 = WS_H5 + (2 * idx) * 128;
    if (w < 4) {
      v4u a[18][2];
      int vok[4];
      const rsrc_t rk = mk(wuk, 512 * 4096 * 2);
      const int n0 = (w - 2) * 16;
      RBAR();
      if (w == 0) {
        const rsrc_t rq = mk(wq, 576 * 4096 * 2);
        const int vo = 2 * g * 8192 + i16 * 16, so = 144 * jq * 8192 + hd * 256;
#pragma unroll
        for (int o = 0; o < 18; ++o) { a[o][0] = ld4<NT_W>(rq, vo, so + (8 * o) * 8192); a[o][1] = ld4<NT_W>(rq, vo, so + (8 * o + 1) * 8192); }
        SCHED();
      } else if (w >= 2) {
#pragma unroll
        for (int k = 0; k < 4; ++k) vok[k] = g * 8192 + ((i16 ^ ((g << 2) | k)) * 16);
        __builtin_amdgcn_s_sleep(DLY_K);
#pragma unroll
        for (int n = 0; n < 16; ++n) dma16(rk, vok[n & 3], (128 * jq + 4 * (n0 + n)) * 8192 + hd * 256, lbase + LP_UK + (n0 + n) * 1024);
      }
      STAMP(1);
      BAR();
      STAMP(2);
      GET_TAG(LP_TAG);
      if (w == 0) {
        v2u xp[9];
#pragma unroll
        for (int k = 0; k < 9; ++k) xp[k] = *(LAS v2u*)(L + LP_XP + ((bj * 4 + g) * 76 + 18 * jq + 2 * k) * 4);
        f32x4 qx = {0.f, 0.f, 0.f, 0.f}, qy = qx;
#pragma unroll
        for (int o = 0; o < 18; ++o) {
          const v4u B = bdiag((o & 1) ? xp[o >> 1].y : xp[o >> 1].x, sel);
          qx = mfma16(v4u{a[o][0].x, a[o][0].y, a[o][1].x, a[o][1].y}, B, qx);
          qy = mfma16(v4u{a[o][0].z, a[o][0].w, a[o][1].z, a[o][1].w}, B, qy);
        }
        const int po = WS_H1 + idx * 4096;
        st4(v4u{tlo, genc(qx[0], t8), tlo, genc(qx[1], t8)}, rws, lane * 16, po);
        st4(v4u{tlo, genc(qx[2], t8), tlo, genc(qx[3], t8)}, rws, lane * 16, po + 1024);
        st4(v4u{tlo, genc(qy[0], t8), tlo, genc(qy[1], t8)}, rws, lane * 16, po + 2048);
        st4(v4u{tlo, genc(qy[2], t8), tlo, genc(qy[3], t8)}, rws, lane * 16, po + 3072);
      }
      STAMP(3);
      BAR();
      __builtin_amdgcn_s_sleep(DLY_H1);
      {
        const int blk = w & 1, half = w >> 1;
        float q0, q1;
        for (int n = 0;; ++n) {
          v4u p[4];
#pragma unroll
          for (int jj = 0; jj < 4; ++jj) p[jj] = hl4(rws, lane * 16, WS_H1 + ((hd * 4 + jj) * 4 + blk * 2 + half) * 1024);
          unsigned bad = 0;
          q0 = 0.f; q1 = 0.f;
#pragma unroll
          for (int jj = 0; jj < 4; ++jj) {
            bad |= (p[jj].x ^ tlo) | (p[jj].z ^ tlo) | (((p[jj].y ^ t8) | (p[jj].w ^ t8)) & 0xFFu);
            q0 += gdec(p[jj].y); q1 += gdec(p[jj].w);
          }
          if (__all(bad == 0) || n >= GSPIN) break;
        }
        VMCNT(0);
        const unsigned qp = pk(q0, q1);
        const int d0 = 32 * g + 16 * half + 4 * blk + cp;
        *(LAS u16*)(L + LP_QB + bj * 320 + d0 * 2) = (u16)qp;
        *(LAS u16*)(L + LP_QB + bj * 320 + (d0 + 8) * 2) = (u16)(qp >> 16);
      }
      STAMP(4);
      BAR();
      STAMP(5);
      {
        v4u qf[4];
#pragma unroll
        for (int s = 0; s < 4; ++s) qf[s] = *(LAS v4u*)(L + LP_QB + cp * 320 + s * 64 + g * 16);
        const int fr = ((i16 & 3) << 2) | (i16 >> 2);
#pragma unroll
        for (int t = 0; t < 2; ++t) {
          f32x4 acc = {0.f, 0.f, 0.f, 0.f};
          const int rt = 2 * w + t;
#pragma unroll
          for (int s = 0; s < 4; ++s) {
            const v4u A = *(LAS v4u*)(L + LP_UK + (16 * rt + i16) * 256 + (((4 * s + g) ^ fr) * 16));
            acc = mfma16(A, qf[s], acc);
          }
          const float sc = 0.08838834764831845f * 1.4426950408889634f;
          if (i16 < 4) *(LAS v2u*)(L + LP_QO + i16 * 256 + (16 * rt + 4 * g) * 2) = v2u{pk(acc[0] * sc, acc[1] * sc), pk(acc[2] * sc, acc[3] * sc)};
        }
      }
      BAR();
      if (w == 0) {
        const v4u v = *(LAS v4u*)(L + LP_QO + lane * 16);
        st4(v4u{lsbput(v.x, lsbtag(tlo, thi, 4 * i16)), lsbput(v.y, lsbtag(tlo, thi, 4 * i16 + 1)), lsbput(v.z, lsbtag(tlo, thi, 4 * i16 + 2)),
                lsbput(v.w, lsbtag(tlo, thi, 4 * i16 + 3))}, rws, g * 32768 + i16 * 16, WS_QA + hd * 1024 + jq * 256);
      }
      STAMP(6);
      BAR();
      const rsrc_t r2 = mk(w2, 4608 * 576 * 2);
      const int so2 = (xg * 576 + 36 * qq) * 1152;
      v4u a2[5][2];
      v2u a8[2][2];
      {
        const int vo4 = 2 * g * 1152 + i16 * 16, vo4m = g < 2 ? vo4 : OOB;
        const int vo8 = 2 * g * 1152 + i16 * 8 + 1024, vo8m = (g < 2 && w == 0) ? vo8 : OOB;
#pragma unroll
        for (int o = 0; o < 5; ++o)
#pragma unroll
          for (int r = 0; r < 2; ++r) a2[o][r] = ld4<NT_W>(r2, o < 4 ? vo4 : vo4m, so2 + (8 * o + r) * 1152 + 256 * w);
#pragma unroll
        for (int r = 0; r < 2; ++r) { a8[0][r] = ld2<NT_W>(r2, vo8, so2 + (8 * w + r) * 1152); a8[1][r] = ld2<NT_W>(r2, vo8m, so2 + (32 + r) * 1152); }
        SCHED();
      }
      STAMP(7);
      BAR();
      v4u X[5], Y[5];
#pragma unroll
      for (int o = 0; o < 5; ++o) {
        X[o] = v4u{a2[o][0].x, a2[o][0].y, a2[o][1].x, a2[o][1].y};
        Y[o] = v4u{a2[o][0].z, a2[o][0].w, a2[o][1].z, a2[o][1].w};
        asm volatile("" : "+v"(X[o]), "+v"(Y[o]));
      }
      STAMP(8);
      BAR();
      STAMP(9);
      {
        const v4u h4 = *(LAS v4u*)(L + LP_HP + (bj * 4 + g) * 48);
        const unsigned h5 = *(LAS unsigned*)(L + LP_HP + (bj * 4 + g) * 48 + 16);
        const unsigned hw = *(LAS unsigned*)(L + LP_HP + (bj * 4 + g) * 48 + 4 * w);
        f32x4 c8 = {0.f, 0.f, 0.f, 0.f}, cx = c8, cy = c8;
        c8 = mfma16(v4u{a8[0][0].x, a8[0][0].y, a8[0][1].x, a8[0][1].y}, bdiag(hw, sel), c8);
        c8 = mfma16(v4u{a8[1][0].x, a8[1][0].y, a8[1][1].x, a8[1][1].y}, bdiag(h5, sel), c8);
#pragma unroll
        for (int o = 0; o < 5; ++o) {
          const v4u B = bdiag(o == 0 ? h4.x : o == 1 ? h4.y : o == 2 ? h4.z : o == 3 ? h4.w : h5, sel);
          cx = mfma16(X[o], B, cx);
          cy = mfma16(Y[o], B, cy);
        }
        *(LAS f32x4*)(L + LP_B8 + w * 1024 + lane * 16) = c8;
        st4(tag8(cx, tb), rws, vo5, so5 + (2 * w) * 8 * 32768);
        st4(tag8(cy, tb), rws, vo5, so5 + (2 * w + 1) * 8 * 32768);
        BAR();
      }
      STAMP(10);
    } else {
      const int v = w - 4;
      {
        const v4u x0 = ld4<0>(rx, lane * 16, v * 1152), x1 = ld4<0>(rx, lane < 8 ? lane * 16 : OOB, v * 1152 + 1024);
        v2u wv_;
        if (v == 0) wv_ = hl2(rws, 0, WS_W);
        SCHED();
        RBAR();
        if (v == 0) PUT_TAG(LP_TAG);
        float f[16], s = 0.f;
#pragma unroll
        for (int k = 0; k < 4; ++k) {
          f[2 * k] = bf2f(x0[k]); f[2 * k + 1] = __uint_as_float(x0[k] & 0xFFFF0000u);
          f[8 + 2 * k] = bf2f(x1[k]); f[8 + 2 * k + 1] = __uint_as_float(x1[k] & 0xFFFF0000u);
        }
#pragma unroll
        for (int k = 0; k < 16; ++k) s += f[k];
        const float mean = x32_sum(x16_sum(row_sum(s))) * (1.0f / 576.0f);
        float q0 = 0.f, q1 = 0.f;
#pragma unroll
        for (int k = 0; k < 8; ++k) { f[k] -= mean; f[8 + k] -= mean; q0 = fmaf(f[k], f[k], q0); q1 = fmaf(f[8 + k], f[8 + k], q1); }
        const float rstd = 1.0f / sqrtf(x32_sum(x16_sum(row_sum(lane < 8 ? q0 + q1 : q0))) * (1.0f / 576.0f) + 1e-5f);
#pragma unroll
        for (int hf = 0; hf < 2; ++hf) {
          if (hf == 0 || lane < 8) {
            const int o = lane + 64 * hf;
#pragma unroll
            for (int k = 0; k < 4; ++k) {
              const unsigned p = pk(f[8 * hf + 2 * k] * rstd, f[8 * hf + 2 * k + 1] * rstd);
              *(LAS unsigned*)(L + LP_XP + ((v * 4 + k) * 76 + o) * 4) = p;
              *(LAS unsigned*)(L + LP_XD + ((v * 4 + ((2 * k) & 3)) * 148 + 2 * o + (k >> 1)) * 4) = __builtin_amdgcn_perm(0u, p, 0x01000100u);
              *(LAS unsigned*)(L + LP_XD + ((v * 4 + ((2 * k + 1) & 3)) * 148 + 2 * o + (k >> 1)) * 4) = __builtin_amdgcn_perm(0u, p, 0x03020302u);
            }
          }
        }
      }
      STAMP(1);
      BAR();
      GET_TAG(LP_TAG);
      const int U0 = (81 * qq) / 144, Qb = 144 * (U0 + 1);
      unsigned xd[21];
#pragma unroll
      for (int t = 0; t < 21; ++t) {
        const int Q = min(81 * qq + 4 * t + v, 81 * qq + 80), qk = Q >= Qb ? Q - Qb : Q - 144 * U0;
        xd[t] = *(LAS unsigned*)(L + LP_XD + ((bj * 4 + g) * 148 + qk) * 4);
      }
      const GAS char* wb = (const GAS char*)(i16 < 8 ? w1 : w3) + (xg * 576 * 2 + (i16 & 7) * 16 + g * 9216);
      const unsigned m0 = cp == 0 ? 0xFFFFu : cp == 1 ? 0xFFFF0000u : 0u, m1 = cp == 2 ? 0xFFFFu : cp == 3 ? 0xFFFF0000u : 0u;
      f32x4 acc[4];
#pragma unroll
      for (int k = 0; k < 4; ++k) acc[k] = f32x4{0.f, 0.f, 0.f, 0.f};
      v4u a[21];
      BAR();
      BAR();
      STAMP(2);
#pragma unroll
      for (int t = -PACE_G; t < 21; ++t) {
        const int tl = t + PACE_G;
        if (tl < 21) {
          const int Q = 81 * qq + 4 * tl + v, u = Q >= Qb ? U0 + 1 : U0, qk = Q - 144 * u;
          if (tl < 20 || v == 0) {
            const GAS v4u* p = (const GAS v4u*)(wb + (unsigned)(qk * 4 * 9216 + u * 128));
            a[tl] = NT_W ? __builtin_nontemporal_load(p) : *p;
          } else a[tl] = v4u{0u, 0u, 0u, 0u};
          SCHED();
        }
        if (t == -1) BAR();
        if (tl == 20) { STAMP(3); BAR(); }
        if (t >= 0) {
          const int Q = 81 * qq + 4 * t + v;
          const v2u B = {xd[t] & m0, xd[t] & m1};
          if (Q < Qb) { acc[0] = mfma16k16(v2u{a[t].x, a[t].y}, B, acc[0]); acc[1] = mfma16k16(v2u{a[t].z, a[t].w}, B, acc[1]); }
          else        { acc[2] = mfma16k16(v2u{a[t].x, a[t].y}, B, acc[2]); acc[3] = mfma16k16(v2u{a[t].z, a[t].w}, B, acc[3]); }
          SCHED();
        }
      }
#pragma unroll
      for (int k = 0; k < 4; ++k) *(LAS f32x4*)(L + LP_RED + (v * 4 + k) * 1024 + lane * 16) = acc[k];
      STAMP(4);
      BAR();
      {
        f32x4 r = *(LAS f32x4*)(L + LP_RED + v * 1024 + lane * 16);
#pragma unroll
        for (int k = 1; k < 4; ++k) r += *(LAS f32x4*)(L + LP_RED + (k * 4 + v) * 1024 + lane * 16);
        const int po = WS_H7 + idx * 8192 + v * 2048;
        st4(v4u{tlo, genc(r[0], t8), tlo, genc(r[1], t8)}, rws, lane * 16, po);
        st4(v4u{tlo, genc(r[2], t8), tlo, genc(r[3], t8)}, rws, lane * 16, po + 1024);
      }
      STAMP(5);
      {
        const int t = v * 64 + lane;
        const int b = t / 36, cl = t - 36 * b, f = 36 * qq + cl, u = f >> 6, cu = f & 63;
        const int ig = cu >> 3, hh = (cu >> 2) & 1, reg = ig & 3;
        const int r0 = (144 * u) / 81, r1 = (144 * u + 143) / 81;
        const int lo = hh * 2048 + (reg >> 1) * 1024 + ((ig >> 2) * 16 + b * 4 + (cu & 3)) * 16 + (reg & 1) * 8;
        int off[3];
        unsigned use[3];
#pragma unroll
        for (int k = 0; k < 3; ++k) {
          const int rr = r0 + k;
          const bool on = t < 144 && rr <= r1;
          off[k] = on ? (xg * 16 + rr) * 8192 + (u - (81 * rr) / 144) * 4096 + lo : OOB;
          use[k] = on ? 0xFFFFFFFFu : 0u;
        }
        __builtin_amdgcn_s_sleep(DLY_H7);
        float gs, us;
        for (int n = 0;; ++n) {
          v2u pg[3], pu[3];
#pragma unroll
          for (int k = 0; k < 3; ++k) { pg[k] = hl2(rws, off[k], WS_H7); pu[k] = hl2(rws, off[k], WS_H7 + 512); }
          unsigned bad = 0;
          gs = 0.f; us = 0.f;
#pragma unroll
          for (int k = 0; k < 3; ++k) {
            bad |= ((pg[k].x ^ tlo) | (pu[k].x ^ tlo) | (((pg[k].y ^ t8) | (pu[k].y ^ t8)) & 0xFFu)) & use[k];
            gs += gdec(pg[k].y); us += gdec(pu[k].y);
          }
          if (__all(bad == 0) || n >= GSPIN) break;
        }
        const float hv = gs / (1.0f + __expf(-gs)) * us;
        if (t < 144) *(LAS u16*)(L + LP_HP + ((b * 4 + ((cl >> 1) & 3)) * 12 + (cl >> 3)) * 4 + (cl & 1) * 2) = (u16)pk(hv, hv);
        else if (t < 152) *(LAS unsigned*)(L + LP_HP + ((((t - 144) >> 1) * 4 + 2 + (t & 1)) * 12 + 4) * 4) = 0u;
      }
      STAMP(6);
      BAR();
      BAR();
      if (v == 0) {
        f32x4 s = *(LAS f32x4*)(L + LP_B8 + lane * 16);
#pragma unroll
        for (int k = 1; k < 4; ++k) s += *(LAS f32x4*)(L + LP_B8 + k * 1024 + lane * 16);
        st4(tag8(s, tb), rws, vo5, so5 + 8 * 8 * 32768);
      }
      STAMP(7);
    }
    STAMP_OUT();
    return;
  }

  const int ab = xg >> 1;
  const int r32 = lane & 31, hh = lane >> 5;
  const int so5 = WS_H5 + (2 * idx + 1) * 128;
  v2u av[8][2];
  v2u wa_;
  asm volatile("buffer_load_dwordx2 %0, off, %1, 0 sc1" : "=v"(wa_) : "s"(rws) : "memory");
  if (w < 4) {
    const rsrc_t rc = mk(ckv, 4 * 4096 * 512 * 2);
    {
      int vo[4];
#pragma unroll
      for (int k = 0; k < 4; ++k) vo[k] = g * 1024 + ((i16 ^ ((g << 2) | k)) * 16);
      const int so = (idx * 128 + 32 * w) * 1024;
#pragma unroll
      for (int n = 0; n < 32; ++n) {
        if (n >= PACE_C) VMCNT(PACE_C - 1);
        dma16(rc, vo[n & 3], so + (n & 7) * 4096 + (n >> 3) * 256, lbase + (n >> 3) * 32768 + (32 * w + 4 * (n & 7)) * 256);
        if (n == KQA) BAR();
      }
    }
  } else {
    VMCNT(0);
    BAR();
  }
  asm volatile("" : "+v"(wa_));
  const u64 tag_ = (((u64)wa_.y << 32) | wa_.x) + 0x9E3779B97F4A7C15ull;
  const unsigned tlo = (unsigned)tag_, thi = (unsigned)(tag_ >> 32), t8 = thi & 0xFFu;
  const unsigned tb[4] = {tlo & 0xFFu, (tlo >> 8) & 0xFFu, (tlo >> 16) & 0xFFu, tlo >> 24};
  unsigned e4[4];
#pragma unroll
  for (int k = 0; k < 4; ++k) e4[k] = lsbtag(tlo, thi, 4 * i16 + k);
  if (w < 4) {
    v4u fa[32];
    {
      const int fr = ((r32 & 3) << 2) | ((r32 >> 2) & 3);
      const int ca = (32 * w + r32) * 256;
#define QK_READ(Q) _Pragma("unroll") for (int k = 0; k < 8; ++k) fa[(Q)*8 + k] = *(LAS v4u*)(L + (Q)*32768 + ca + (((2 * k + hh) ^ fr) * 16)); SCHED();
      const rsrc_t rv = mk(wuv, 512 * 4096 * 2);
      const int sov = (128 * jq + 16 * (w >> 1)) * 8192 + hd * 256 + (w & 1) * 128;
      const int vov[2] = {(32 * ((2 * g) & 3) + (g >> 1)) * 8192 + i16 * 8, (32 * ((2 * g + 1) & 3) + (g >> 1)) * 8192 + i16 * 8};
#define AV_LOAD(A, B) _Pragma("unroll") for (int n = (A); n < (B); ++n) av[n >> 1][n & 1] = ld2<NT_W>(rv, vov[n & 1], sov + 2 * (n >> 1) * 8192); SCHED();
      VMCNT(8); QK_READ(0) QK_READ(1) QK_READ(2)
      VMCNT(4); AV_LOAD(0, 8)
      VMCNT(8); QK_READ(3)
      AV_LOAD(8, 16)
    }
    int qb[4];
#pragma unroll
    for (int k = 0; k < 4; ++k) { qb[k] = LA_QA + ((lane ^ ((2 * k + hh) & 7)) * 16); asm volatile("" : "+v"(qb[k])); }
    STAMP(2);
    BAR();
    STAMP(3);
    f32x16 sacc;
#pragma unroll
    for (int k = 0; k < 16; ++k) sacc[k] = 0.f;
    {
      v4u fb[32];
#pragma unroll
      for (int s = 0; s < 6; ++s) fb[s] = *(LAS v4u*)(L + qb[s & 3] + s * 1024);
      SCHED();
#pragma unroll
      for (int s = 0; s < 32; ++s) {
        if (s + 6 < 32) fb[s + 6] = *(LAS v4u*)(L + qb[(s + 6) & 3] + (s + 6) * 1024);
        sacc = mfma32(fa[s], fb[s], sacc);
        SCHED();
        if (s == 12) { RBAR(); SCHED(); }
      }
    }
    const int q4 = i16 >> 2, lowc = 2 * (g & 1) + (cp >> 1);
    int ta[4][2];
#pragma unroll
    for (int t = 0; t < 4; ++t)
#pragma unroll
      for (int t2 = 0; t2 < 2; ++t2) {
        ta[t][t2] = w * 32768 + (8 * t2 + 4 * hh + q4) * 256 + ((((t ^ q4) << 2) | (lowc ^ (2 * t2 + hh))) * 16) + 8 * (cp & 1);
        asm volatile("" : "+v"(ta[t][t2]));
      }
    v4u cb[32];
#define TR_READ(n) do { const v2u x0_ = __builtin_bit_cast(v2u, __builtin_amdgcn_ds_read_tr16_b64_v4i16((LAS s16x4*)(L + ta[(n) >> 3][0] + ((n) & 7) * 4096))); \
    const v2u x1_ = __builtin_bit_cast(v2u, __builtin_amdgcn_ds_read_tr16_b64_v4i16((LAS s16x4*)(L + ta[(n) >> 3][1] + ((n) & 7) * 4096)));                 \
    cb[n] = v4u{x0_.x, x0_.y, x1_.x, x1_.y}; } while (0)
#pragma unroll
    for (int n = 0; n < 6; ++n) TR_READ(n);
    SCHED();
    float mw = sacc[0];
#pragma unroll
    for (int k = 1; k < 16; ++k) mw = fmaxf(mw, sacc[k]);
    mw = x32_max(mw);
    if (lane < 32) *(LAS float*)(L + LA_MX + (r32 * 4 + w) * 4) = mw;
    STAMP(4);
    BAR();
    const f32x4 m4 = *(LAS f32x4*)(L + LA_MX + r32 * 16);
    const float mx = fmaxf(fmaxf(m4[0], m4[1]), fmaxf(m4[2], m4[3]));
    float ls = 0.f;
#pragma unroll
    for (int k = 0; k < 16; ++k) { sacc[k] = __builtin_amdgcn_exp2f(sacc[k] - mx); ls += sacc[k]; }
#pragma unroll
    for (int s2 = 0; s2 < 2; ++s2)
      *(LAS v4u*)(L + LA_PB + (2 * w + s2) * 1024 + lane * 16) =
          v4u{pk(sacc[8 * s2], sacc[8 * s2 + 1]), pk(sacc[8 * s2 + 2], sacc[8 * s2 + 3]), pk(sacc[8 * s2 + 4], sacc[8 * s2 + 5]), pk(sacc[8 * s2 + 6], sacc[8 * s2 + 7])};
    ls = x32_sum(ls);
    if (lane < 32) *(LAS float*)(L + LA_SUM + (r32 * 4 + w) * 4) = ls;
    BAR();
    STAMP(5);
    {
      v4u pf[8];
#pragma unroll
      for (int k = 0; k < 8; ++k) pf[k] = *(LAS v4u*)(L + LA_PB + k * 1024 + lane * 16);
      if (w == 0) {
        const f32x4 l4 = *(LAS f32x4*)(L + LA_SUM + r32 * 16);
        st4(v4u{tlo, genc(mx, t8), tlo, genc((l4[0] + l4[1]) + (l4[2] + l4[3]), t8)}, rws, lane < 32 ? r32 * 16 : OOB, WS_ML + idx * 512);
      }
      SCHED();
      f32x16 o[4];
#pragma unroll
      for (int t = 0; t < 4; ++t)
#pragma unroll
        for (int k = 0; k < 16; ++k) o[t][k] = 0.f;
#pragma unroll
      for (int n = 0; n < 32; ++n) {
        if (n + 6 < 32) TR_READ(n + 6);
        o[n >> 3] = mfma32(pf[n & 7], cb[n], o[n >> 3]);
        SCHED();
      }
      STAMP(1);
      const unsigned e0 = lsbtag(tlo, thi, 2 * r32), e1 = lsbtag(tlo, thi, 2 * r32 + 1);
      const int vo = hh * 4 * 131072 + r32 * 8, so = WS_H3 + (w * 128 + idx) * 256;
#pragma unroll
      for (int k = 0; k < 16; ++k)
        st2(v2u{lsbput(pk(o[0][k], o[1][k]), e0), lsbput(pk(o[2][k], o[3][k]), e1)}, rws, vo, so + ((k & 3) + 8 * (k >> 2)) * 131072);
    }
    STAMP(6);
    {
      v4u ml;
      for (int n = 0;; ++n) {
        ml = hl4(rws, r32 * 512, WS_ML + w * 32 * 512 + hd * 16);
        if (__all(((ml.x ^ tlo) | (ml.z ^ tlo) | (((ml.y ^ t8) | (ml.w ^ t8)) & 0xFFu)) == 0) || n >= GSPIN) break;
      }
      if (DLY_H3) __builtin_amdgcn_s_sleep(DLY_H3);
      STAMP(7);
      v4u d[8];
#pragma unroll
      for (int k = 0; k < 8; ++k) d[k] = hl4(rws, lane * 16, WS_H3 + idx * 32768 + w * 8192 + k * 1024);
      SCHED();
      const float m = gdec(ml.y), M = x16_max(row_max(m));
      const float e = __builtin_amdgcn_exp2f(m - M);
      const float al = e / x16_sum(row_sum(e * gdec(ml.w)));
      const unsigned ah = pk(al, al) & 0xFFFFu;
      const unsigned hl = ah | (pk(al - bf2f(ah), 0.f) << 16);
      const int e7 = lane & 7;
      const unsigned src = (lane & 8) ? 0x0302u : 0x0100u, s1 = (e7 & 1) ? ((src << 16) | 0x0C0Cu) : (0x0C0C0000u | src);
      unsigned s8[4];
#pragma unroll
      for (int k = 0; k < 4; ++k) s8[k] = (e7 >> 1) == k ? s1 : 0x0C0C0C0Cu;
      v4u bk[8];
#pragma unroll
      for (int k = 0; k < 8; ++k) { bk[k] = bdiag(__builtin_amdgcn_ds_bpermute((4 * k + g) * 4, hl), s8); asm volatile("" : "+v"(bk[k])); }
#pragma unroll 1
      for (int n = 0; n < GSPIN; ++n) {
        unsigned bad = 0;
#pragma unroll
        for (int k = 0; k < 8; ++k) bad |= xor4(d[k], e4);
        if (__all((bad & LSBM) == 0)) break;
#pragma unroll
        for (int k = 0; k < 8; ++k) d[k] = hl4(rws, lane * 16, WS_H3 + idx * 32768 + w * 8192 + k * 1024);
      }
      STAMP(8);
      f32x4 c = {0.f, 0.f, 0.f, 0.f};
#pragma unroll
      for (int k = 0; k < 8; ++k) c = mfma16(d[k], bk[k], c);
#pragma unroll
      for (int r = 0; r < 4; ++r) {
        const float y = c[r] + dppf<0x128, 0xf>(c[r], c[r]);
        if (!(lane & 8)) *(LAS u16*)(L + LA_CTX + ((w * 4 + (e7 >> 1)) * 20 + 4 * g + r) * 4 + 2 * (e7 & 1)) = (u16)pk(y, y);
      }
    }
    BAR();
    {
      const int half = w >> 1, blk = w & 1;
      const v4u p0 = *(LAS v4u*)(L + LA_CTX + ((bj * 4 + g) * 20 + 8 * half) * 4), p1 = *(LAS v4u*)(L + LA_CTX + ((bj * 4 + g) * 20 + 8 * half + 4) * 4);
      f32x4 c = {0.f, 0.f, 0.f, 0.f};
#pragma unroll
      for (int o = 0; o < 8; ++o) c = mfma16(v4u{av[o][0].x, av[o][0].y, av[o][1].x, av[o][1].y}, bdiag(o < 4 ? p0[o & 3] : p1[o & 3], sel), c);
#pragma unroll
      for (int r = 0; r < 4; ++r)
        *(LAS float*)(L + LA_UV + half * 2304 + ((bj * 4 + 2 * (r & 1) + (cp >> 1)) * 36 + (8 * blk + 2 * g + (r >> 1)) * 2 + (cp & 1)) * 4) = c[r];
    }
    BAR();
    STAMP(9);
    BAR();
    if (w == 0) {
      f32x4 s = *(LAS f32x4*)(L + LA_B8 + lane * 16);
#pragma unroll
      for (int k = 1; k < 4; ++k) s += *(LAS f32x4*)(L + LA_B8 + k * 1024 + lane * 16);
      st4(tag8(s, tb), rws, vo5, so5 + 8 * 8 * 32768);
    }
  } else {
    const int v = w - 4;
    STAMP(1);
    {
      v4u qa[8];
      for (int n = 0;; ++n) {
#pragma unroll
        for (int k = 0; k < 8; ++k) qa[k] = hl4(rws, lane * 16, WS_QA + (ab * 32 + 8 * v + k) * 1024);
        unsigned bad = 0;
#pragma unroll
        for (int k = 0; k < 8; ++k) bad |= xor4(qa[k], e4);
        if (__all((bad & LSBM) == 0) || n >= GSPIN) break;
        __builtin_amdgcn_s_sleep(DLY_R2);
      }
#pragma unroll
      for (int k = 0; k < 8; ++k) *(LAS v4u*)(L + LA_QA + (lane >> 1) * 1024 + ((((lane & 1) * 32 + 8 * v + k) ^ (lane & 7)) * 16)) = qa[k];
    }
    STAMP(2);
    BAR();
    const rsrc_t ro = mk(wo, 4096 * 576 * 2);
    v4u ao[16][2];
    v2u a8[4][2];
    {
      const int vo4 = 2 * g * 1152 + i16 * 16, vo8 = 2 * g * 1152 + i16 * 8 + 1024;
#pragma unroll
      for (int o = 0; o < 16; ++o) {
        const int op = (o + 4 * jq) & 15;
        if (o > 4) VMCNT(PACE_O - 2);
#pragma unroll
        for (int r = 0; r < 2; ++r) ao[o][r] = ld4<NT_WO>(ro, vo4, (hd * 128 + 8 * op + r) * 1152 + 256 * v);
        if ((o & 3) == 3) {
          const int o8 = 4 * (((o >> 2) + jq) & 3) + v;
#pragma unroll
          for (int r = 0; r < 2; ++r) a8[o >> 2][r] = ld2<NT_WO>(ro, vo8, (hd * 128 + 8 * o8 + r) * 1152);
        }
        SCHED();
        if (o == 3) { RBAR(); RBAR(); RBAR(); }
      }
    }
    STAMP(3);
    v4u X[16], Y[16];
#pragma unroll
    for (int o = 0; o < 16; ++o) {
      X[o] = v4u{ao[o][0].x, ao[o][0].y, ao[o][1].x, ao[o][1].y};
      Y[o] = v4u{ao[o][0].z, ao[o][0].w, ao[o][1].z, ao[o][1].w};
      asm volatile("" : "+v"(X[o]), "+v"(Y[o]));
    }
    VMCNT(0);
    STAMP(4);
    BAR();
    BAR();
    STAMP(5);
    {
      unsigned P[16];
#pragma unroll
      for (int q = 0; q < 4; ++q) {
        const int ad = LA_UV + ((bj * 4 + g) * 36 + 8 * ((q + jq) & 3)) * 4;
        const f32x4 x0 = *(LAS f32x4*)(L + ad) + *(LAS f32x4*)(L + ad + 2304), x1 = *(LAS f32x4*)(L + ad + 16) + *(LAS f32x4*)(L + ad + 16 + 2304);
        P[4 * q] = pk(x0[0], x0[1]); P[4 * q + 1] = pk(x0[2], x0[3]); P[4 * q + 2] = pk(x1[0], x1[1]); P[4 * q + 3] = pk(x1[2], x1[3]);
      }
      f32x4 c8 = {0.f, 0.f, 0.f, 0.f}, cx = c8, cy = c8;
#pragma unroll
      for (int q = 0; q < 4; ++q) {
        const unsigned pv = v == 0 ? P[4 * q] : v == 1 ? P[4 * q + 1] : v == 2 ? P[4 * q + 2] : P[4 * q + 3];
        c8 = mfma16(v4u{a8[q][0].x, a8[q][0].y, a8[q][1].x, a8[q][1].y}, bdiag(pv, sel), c8);
      }
      SCHED();
#pragma unroll
      for (int o = 0; o < 16; ++o) {
        const v4u B = bdiag(P[o], sel);
        cx = mfma16(X[o], B, cx);
        cy = mfma16(Y[o], B, cy);
        if (o == 1) { *(LAS f32x4*)(L + LA_B8 + v * 1024 + lane * 16) = c8; SCHED(); BAR(); }
      }
      st4(tag8(cx, tb), rws, vo5, so5 + (2 * v) * 8 * 32768);
      st4(tag8(cy, tb), rws, vo5, so5 + (2 * v + 1) * 8 * 32768);
    }
    STAMP(6);
  }
  if (qq < 9) {
    const int e = lane & 31, Ld = 8 * xg + (e >> 2), rr = e & 3;
    const int ob = (Ld & 15) >> 2, oc = qq < 8 ? 128 * (qq >> 1) + 8 * (4 * (Ld >> 4) + rr) + 4 * (qq & 1) + (Ld & 3) : 512 + 4 * (4 * (Ld >> 4) + rr) + (Ld & 3);
    unsigned xres = 0;
    if (w == 0) xres = ((const GAS u16*)xin)[ob * 576 + oc];
    BAR();
    __builtin_amdgcn_s_sleep(DLY_H5);
    STAMP(10);
    f32x4 s;
    for (int n = 0;; ++n) {
      v4u d[4];
#pragma unroll
      for (int k = 0; k < 4; ++k) d[k] = hl4(rws, lane * 16, WS_H5 + (qq * 8 + xg) * 32768 + (w * 4 + k) * 1024);
      const unsigned bad = (xor4(d[0], tb) | xor4(d[1], tb)) | (xor4(d[2], tb) | xor4(d[3], tb));
      s = (__builtin_bit_cast(f32x4, d[0]) + __builtin_bit_cast(f32x4, d[1])) + (__builtin_bit_cast(f32x4, d[2]) + __builtin_bit_cast(f32x4, d[3]));
      if (__all((bad & 0xFFu) == 0) || n >= GSPIN) break;
    }
#pragma unroll
    for (int r = 0; r < 4; ++r) s[r] = x32_sum(x16_sum(s[r] + dppf<0x128, 0xf>(s[r], s[r])));
    if (lane < 8) *(LAS f32x4*)(L + LA_FIN + w * 128 + lane * 16) = s;
    BAR();
    if (w == 0) {
      float y = bf2f(xres);
#pragma unroll
      for (int k = 0; k < 8; ++k) y += *(LAS float*)(L + LA_FIN + k * 128 + e * 4);
      if (lane < 32) ((GAS u16*)out)[ob * 576 + oc] = (u16)pk(y, y);
      if (qq == 0 && xg == 0) st2(v2u{tlo, thi}, rws, lane == 0 ? 0 : OOB, WS_W);
    }
    STAMP(11);
  }
  STAMP_OUT();
}

__global__ void k_pre(char* ws){ if(threadIdx.x==0) *(u64*)(ws+WS_STAMP+(1<<20))=__builtin_amdgcn_s_memrealtime(); }
__global__ void __launch_bounds__(512,2) k_empty(char* ws){ extern __shared__ char sm2[]; if(ws==nullptr) sm2[threadIdx.x]=1; }
__global__ void k_spin(char* ws){
  const u64 tstart=__builtin_amdgcn_s_memrealtime();
  const u64* o=(const u64*)(ws+WS_STAMP);
  int t=threadIdx.x;
  u64 mn=~0ull,mxs=0,mxe=0, a=0;
  __shared__ u64 s0[256],s1[256],s2[256],s3[256];
  for(int i=t;i<2048;i+=256){ u64 st=o[i*16+12], en=o[i*16+14]; mn=st<mn?st:mn; mxs=st>mxs?st:mxs; mxe=en>mxe?en:mxe;
    int blk=i>>3,w=i&7; if(blk>=PB0&&blk<PB1&&w>=PW0&&w<PW1&&o[i*16+PK]){ u64 v=st*100+(o[i*16+PK]-o[i*16])*100/23; a=v>a?v:a; } }
  s0[t]=mn;s1[t]=mxs;s2[t]=mxe;s3[t]=a; __syncthreads();
  if(t==0){ for(int i=1;i<256;i++){ mn=s0[i]<mn?s0[i]:mn; mxs=s1[i]>mxs?s1[i]:mxs; mxe=s2[i]>mxe?s2[i]:mxe; a=s3[i]>a?s3[i]:a; }
    u64 T=(PWHAT==0)?(mxe-mn)*100:(PWHAT==1)?(mxs-mn)*100:(PWHAT==2)?0:(PWHAT==4)?(tstart-mxe)*100:(PWHAT==5)?(mn-*(const u64*)(ws+WS_STAMP+(1<<20)))*100:(a-mn*100);
    if(T>1000000) T=1000000;
    u64 t0=__builtin_amdgcn_s_memrealtime();
    while(__builtin_amdgcn_s_memrealtime()-t0 < T) __builtin_amdgcn_s_sleep(8);
  }
}
extern "C" void kernel_launch(void* const* d_in, const int* in_sizes, int n_in, void* d_out, int out_size, void* d_ws, size_t ws_size,
                              hipStream_t stream) {
  (void)hipFuncSetAttribute((const void*)fused_decode, hipFuncAttributeMaxDynamicSharedMemorySize, LDS_BYTES);
  fused_decode<<<256, 512, LDS_BYTES, stream>>>((const u16*)d_in[0], (const u16*)d_in[1], (const u16*)d_in[2], (const u16*)d_in[3],
                                                (const u16*)d_in[4], (const u16*)d_in[5], (const u16*)d_in[7], (const u16*)d_in[8],
                                                (const u16*)d_in[9], (u16*)d_out, (char*)d_ws);
  k_empty<<<256,512,0,stream>>>((char*)d_ws);
}
```

```cpp
#define EXTRA 0
#include <hip/hip_runtime.h>
#include <stdint.h>

typedef unsigned short u16;
typedef unsigned long long u64;
typedef unsigned v4u __attribute__((ext_vector_type(4)));
typedef unsigned v2u __attribute__((ext_vector_type(2)));
typedef float f32x2 __attribute__((ext_vector_type(2)));
typedef float f32x4 __attribute__((ext_vector_type(4)));
typedef float f32x16 __attribute__((ext_vector_type(16)));
typedef short s16x4 __attribute__((ext_vector_type(4)));
typedef __bf16 bf16x8 __attribute__((ext_vector_type(8)));
typedef __bf16 bf16x2 __attribute__((ext_vector_type(2)));
typedef __amdgpu_buffer_rsrc_t rsrc_t;
#define GAS __attribute__((address_space(1)))
#define LAS __attribute__((address_space(3)))
#define DI __device__ __forceinline__

#define NT_W 2
#define NT_WO 0
#define PACE_C 8
#define DLY_H1 4
#define KQA 31
#define PACE_K 4
#define DLY_K 8
#define PACE_G 5
#define PACE_O 8
#define DLY_R2 8
#define DLY_H3 0
#define DLY_H7 8
#define DLY_H5 8

constexpr int SC1 = 16;
constexpr int GSPIN = 1 << 13;

constexpr int WS_W = 0;
constexpr int WS_H1 = 4096;
constexpr int WS_QA = WS_H1 + 128 * 4096;
constexpr int WS_ML = WS_QA + 131072;
constexpr int WS_H7 = WS_ML + 65536;
constexpr int WS_H5 = WS_H7 + 128 * 8192;
constexpr int WS_H3 = WS_H5 + 72 * 256 * 128;
constexpr int WS_END = WS_H3 + 128 * 32768;
constexpr int WS_STAMP = WS_END;
constexpr int OOB = (int)0x80000000;

constexpr int LP_TAG = 4992, LP_XP = 0, LP_XD = 5120, LP_QB = 15360, LP_QO = 16896, LP_RED = 18432, LP_HP = 34816, LP_B8 = 35840, LP_UK = 40960;
constexpr int LA_QA = 131072, LA_MX = LA_QA;
constexpr int LA_PB = LA_QA + 1024, LA_SUM = LA_QA + 512, LA_CTX = LA_QA + 9216, LA_UV = LA_QA + 11264, LA_B8 = LA_QA + 16384,
              LA_FIN = LA_QA + 20480;
constexpr int LDS_BYTES = 163840;

DI rsrc_t mk(const void* p, int bytes) { return __builtin_amdgcn_make_buffer_rsrc((void*)p, (short)0, bytes, 0x00020000); }
template <int AUX> DI v4u ld4(rsrc_t r, int vo, int so) { return __builtin_amdgcn_raw_buffer_load_b128(r, vo, so, AUX); }
template <int AUX> DI v2u ld2(rsrc_t r, int vo, int so) { return __builtin_amdgcn_raw_buffer_load_b64(r, vo, so, AUX); }
DI v4u hl4(rsrc_t r, int vo, int so) { asm volatile("" : "+v"(vo)); return __builtin_amdgcn_raw_buffer_load_b128(r, vo, so, SC1); }
DI v2u hl2(rsrc_t r, int vo, int so) { asm volatile("" : "+v"(vo)); return __builtin_amdgcn_raw_buffer_load_b64(r, vo, so, SC1); }
DI void st4(v4u v, rsrc_t r, int vo, int so) { __builtin_amdgcn_raw_buffer_store_b128(v, r, vo + so, 0, SC1); }
DI void st2(v2u v, rsrc_t r, int vo, int so) { __builtin_amdgcn_raw_buffer_store_b64(v, r, vo, so, SC1); }
DI void dma16(rsrc_t r, int vo, int so, unsigned lds) {
  asm volatile("s_mov_b32 m0, %0\n\ts_nop 0\n\tbuffer_load_dwordx4 %1, %2, %3 offen nt lds" ::"s"(lds), "v"(vo), "s"(r), "s"(so) : "memory");
}
#define VMCNT(n) asm volatile("s_waitcnt vmcnt(%0)" ::"n"(n) : "memory")
#define FENCE() asm volatile("" ::: "memory")
#define SCHED() __builtin_amdgcn_sched_barrier(0)

DI float bf2f(unsigned v) { return __uint_as_float(v << 16); }
DI unsigned pk(float a, float b) { f32x2 f = {a, b}; return __builtin_bit_cast(unsigned, __builtin_convertvector(f, bf16x2)); }
DI f32x4 mfma16(v4u a, v4u b, f32x4 c) {
  return __builtin_amdgcn_mfma_f32_16x16x32_bf16(__builtin_bit_cast(bf16x8, a), __builtin_bit_cast(bf16x8, b), c, 0, 0, 0);
}
DI f32x4 mfma16k16(v2u a, v2u b, f32x4 c) {
  return __builtin_amdgcn_mfma_f32_16x16x16bf16_1k(__builtin_bit_cast(s16x4, a), __builtin_bit_cast(s16x4, b), c, 0, 0, 0);
}
DI f32x16 mfma32(v4u a, v4u b, f32x16 c) {
  return __builtin_amdgcn_mfma_f32_32x32x16_bf16(__builtin_bit_cast(bf16x8, a), __builtin_bit_cast(bf16x8, b), c, 0, 0, 0);
}
template <int CTRL, int RM> DI float dppf(float old, float v) {
  return __builtin_bit_cast(float, __builtin_amdgcn_update_dpp(__builtin_bit_cast(int, old), __builtin_bit_cast(int, v), CTRL, RM, 0xf, false));
}
DI float row_sum(float v) {
  v += dppf<0xB1, 0xf>(v, v); v += dppf<0x4E, 0xf>(v, v); v += dppf<0x124, 0xf>(v, v); v += dppf<0x128, 0xf>(v, v);
  return v;
}
DI float row_max(float v) {
  v = fmaxf(v, dppf<0xB1, 0xf>(v, v)); v = fmaxf(v, dppf<0x4E, 0xf>(v, v));
  v = fmaxf(v, dppf<0x124, 0xf>(v, v)); v = fmaxf(v, dppf<0x128, 0xf>(v, v));
  return v;
}
DI float x16_sum(float v) { auto r = __builtin_amdgcn_permlane16_swap(__float_as_uint(v), __float_as_uint(v), false, false); return __uint_as_float(r[0]) + __uint_as_float(r[1]); }
DI float x32_sum(float v) { auto r = __builtin_amdgcn_permlane32_swap(__float_as_uint(v), __float_as_uint(v), false, false); return __uint_as_float(r[0]) + __uint_as_float(r[1]); }
DI float x16_max(float v) { auto r = __builtin_amdgcn_permlane16_swap(__float_as_uint(v), __float_as_uint(v), false, false); return fmaxf(__uint_as_float(r[0]), __uint_as_float(r[1])); }
DI float x32_max(float v) { auto r = __builtin_amdgcn_permlane32_swap(__float_as_uint(v), __float_as_uint(v), false, false); return fmaxf(__uint_as_float(r[0]), __uint_as_float(r[1])); }

#define RBAR() asm volatile("s_barrier" ::: "memory")
#define BAR() asm volatile("s_waitcnt lgkmcnt(0)\n\ts_barrier" ::: "memory")
DI unsigned genc(float v, unsigned t8) { return ((__float_as_uint(v) + 0x80u) & 0xFFFFFF00u) | t8; }
DI float gdec(unsigned hi) { return __uint_as_float(hi & 0xFFFFFF00u); }

#define STAMP(k)
#define STAMP_DECL
#define STAMP_OUT()

DI v4u bdiag(unsigned P, const unsigned (&s)[4]) {
  v4u b;
  b.x = __builtin_amdgcn_perm(P, P, s[0]); b.y = __builtin_amdgcn_perm(P, P, s[1]);
  b.z = __builtin_amdgcn_perm(P, P, s[2]); b.w = __builtin_amdgcn_perm(P, P, s[3]);
  return b;
}
DI void bdiag_sel(int cp, unsigned (&s)[4]) {
  const unsigned lo = (cp & 1) ? 0x01000C0Cu : 0x0C0C0100u, hi = (cp & 1) ? 0x03020C0Cu : 0x0C0C0302u, z = 0x0C0C0C0Cu;
  s[0] = cp < 2 ? lo : z; s[1] = cp < 2 ? z : lo; s[2] = cp < 2 ? hi : z; s[3] = cp < 2 ? z : hi;
}

DI unsigned lsbtag(unsigned tlo, unsigned thi, int p) { return (((p < 32 ? tlo : thi) >> (p & 31)) & 1u) | (((tlo ^ (unsigned)p) & 1u) << 16); }
constexpr unsigned LSBM = 0x00010001u;
DI unsigned lsbput(unsigned v, unsigned e) { return (v & ~LSBM) | e; }
DI unsigned xor4(v4u d, const unsigned (&e)[4]) { return ((d.x ^ e[0]) | (d.y ^ e[1])) | ((d.z ^ e[2]) | (d.w ^ e[3])); }
DI v4u tag8(f32x4 c, const unsigned (&t)[4]) {
  return v4u{(__float_as_uint(c[0]) & 0xFFFFFF00u) | t[0], (__float_as_uint(c[1]) & 0xFFFFFF00u) | t[1],
             (__float_as_uint(c[2]) & 0xFFFFFF00u) | t[2], (__float_as_uint(c[3]) & 0xFFFFFF00u) | t[3]};
}
__global__ void __launch_bounds__(512, 2)
fused_decode(const u16* __restrict__ xin, const u16* __restrict__ ckv, const u16* __restrict__ wq, const u16* __restrict__ wuk,
             const u16* __restrict__ wuv, const u16* __restrict__ wo, const u16* __restrict__ w1, const u16* __restrict__ w3,
             const u16* __restrict__ w2, u16* __restrict__ out, char* ws, long long mode) {
  if (mode) return;
  extern __shared__ __attribute__((aligned(16))) char smem[];
  LAS char* L = (LAS char*)smem;
  const unsigned lbase = (unsigned)(size_t)smem;
  const int lane = threadIdx.x & 63, w = __builtin_amdgcn_readfirstlane(threadIdx.x >> 6);
  const int xg = blockIdx.x & 7, qd_ = blockIdx.x >> 3, qq = qd_ & 15, idx = xg * 16 + qq, hd = idx >> 2, jq = idx & 3;
  const int i16 = lane & 15, g = lane >> 4, bj = i16 >> 2, cp = lane & 3;
  STAMP_DECL;
  STAMP(0);
  asm volatile("" ::"s"(xin), "s"(ckv), "s"(wq), "s"(wuk), "s"(wuv), "s"(wo), "s"(w1), "s"(w3), "s"(w2), "s"(out), "s"(ws));
  const rsrc_t rws = mk(ws, WS_END);
  const rsrc_t rx = mk(xin, 4 * 576 * 2);
  unsigned sel[4];
  bdiag_sel(cp, sel);
#define LOAD_TAG() const v2u wv_ = hl2(rws, 0, WS_W)
#define PUT_TAG(AD) do { const u64 tag_ = (((u64)wv_.y << 32) | wv_.x) + 0x9E3779B97F4A7C15ull; \
    if (lane == 0) *(LAS v2u*)(L + (AD)) = v2u{(unsigned)tag_, (unsigned)(tag_ >> 32)}; } while (0)
#define GET_TAG(AD) const v2u tg_ = *(LAS v2u*)(L + (AD)); \
  const unsigned tlo = tg_.x, thi = tg_.y, t8 = thi & 0xFFu; \
  const unsigned tb[4] = {tlo & 0xFFu, (tlo >> 8) & 0xFFu, (tlo >> 16) & 0xFFu, tlo >> 24}
  const int vo5 = (lane >> 3) * 32768 + (lane & 7) * 16;

  if (qd_ < 16) {
    const int so5 = WS_H5 + (2 * idx) * 128;
    if (w < 4) {
      v4u a[18][2];
      int vok[4];
      const rsrc_t rk = mk(wuk, 512 * 4096 * 2);
      const int n0 = (w - 2) * 16;
      RBAR();
      if (w == 0) {
        const rsrc_t rq = mk(wq, 576 * 4096 * 2);
        const int vo = 2 * g * 8192 + i16 * 16, so = 144 * jq * 8192 + hd * 256;
#pragma unroll
        for (int o = 0; o < 18; ++o) { a[o][0] = ld4<NT_W>(rq, vo, so + (8 * o) * 8192); a[o][1] = ld4<NT_W>(rq, vo, so + (8 * o + 1) * 8192); }
        SCHED();
      } else if (w >= 2) {
#pragma unroll
        for (int k = 0; k < 4; ++k) vok[k] = g * 8192 + ((i16 ^ ((g << 2) | k)) * 16);
        __builtin_amdgcn_s_sleep(DLY_K);
#pragma unroll
        for (int n = 0; n < 16; ++n) dma16(rk, vok[n & 3], (128 * jq + 4 * (n0 + n)) * 8192 + hd * 256, lbase + LP_UK + (n0 + n) * 1024);
      }
      STAMP(1);
      BAR();
      STAMP(2);
      GET_TAG(LP_TAG);
      if (w == 0) {
        v2u xp[9];
#pragma unroll
        for (int k = 0; k < 9; ++k) xp[k] = *(LAS v2u*)(L + LP_XP + ((bj * 4 + g) * 76 + 18 * jq + 2 * k) * 4);
        f32x4 qx = {0.f, 0.f, 0.f, 0.f}, qy = qx;
#pragma unroll
        for (int o = 0; o < 18; ++o) {
          const v4u B = bdiag((o & 1) ? xp[o >> 1].y : xp[o >> 1].x, sel);
          qx = mfma16(v4u{a[o][0].x, a[o][0].y, a[o][1].x, a[o][1].y}, B, qx);
          qy = mfma16(v4u{a[o][0].z, a[o][0].w, a[o][1].z, a[o][1].w}, B, qy);
        }
        const int po = WS_H1 + idx * 4096;
        st4(v4u{tlo, genc(qx[0], t8), tlo, genc(qx[1], t8)}, rws, lane * 16, po);
        st4(v4u{tlo, genc(qx[2], t8), tlo, genc(qx[3], t8)}, rws, lane * 16, po + 1024);
        st4(v4u{tlo, genc(qy[0], t8), tlo, genc(qy[1], t8)}, rws, lane * 16, po + 2048);
        st4(v4u{tlo, genc(qy[2], t8), tlo, genc(qy[3], t8)}, rws, lane * 16, po + 3072);
      }
      STAMP(3);
      BAR();
      __builtin_amdgcn_s_sleep(DLY_H1);
      {
        const int blk = w & 1, half = w >> 1;
        float q0, q1;
        for (int n = 0;; ++n) {
          v4u p[4];
#pragma unroll
          for (int jj = 0; jj < 4; ++jj) p[jj] = hl4(rws, lane * 16, WS_H1 + ((hd * 4 + jj) * 4 + blk * 2 + half) * 1024);
          unsigned bad = 0;
          q0 = 0.f; q1 = 0.f;
#pragma unroll
          for (int jj = 0; jj < 4; ++jj) {
            bad |= (p[jj].x ^ tlo) | (p[jj].z ^ tlo) | (((p[jj].y ^ t8) | (p[jj].w ^ t8)) & 0xFFu);
            q0 += gdec(p[jj].y); q1 += gdec(p[jj].w);
          }
          if (__all(bad == 0) || n >= GSPIN) break;
        }
        VMCNT(0);
        const unsigned qp = pk(q0, q1);
        const int d0 = 32 * g + 16 * half + 4 * blk + cp;
        *(LAS u16*)(L + LP_QB + bj * 320 + d0 * 2) = (u16)qp;
        *(LAS u16*)(L + LP_QB + bj * 320 + (d0 + 8) * 2) = (u16)(qp >> 16);
      }
      STAMP(4);
      BAR();
      STAMP(5);
      {
        v4u qf[4];
#pragma unroll
        for (int s = 0; s < 4; ++s) qf[s] = *(LAS v4u*)(L + LP_QB + cp * 320 + s * 64 + g * 16);
        const int fr = ((i16 & 3) << 2) | (i16 >> 2);
#pragma unroll
        for (int t = 0; t < 2; ++t) {
          f32x4 acc = {0.f, 0.f, 0.f, 0.f};
          const int rt = 2 * w + t;
#pragma unroll
          for (int s = 0; s < 4; ++s) {
            const v4u A = *(LAS v4u*)(L + LP_UK + (16 * rt + i16) * 256 + (((4 * s + g) ^ fr) * 16));
            acc = mfma16(A, qf[s], acc);
          }
          const float sc = 0.08838834764831845f * 1.4426950408889634f;
          if (i16 < 4) *(LAS v2u*)(L + LP_QO + i16 * 256 + (16 * rt + 4 * g) * 2) = v2u{pk(acc[0] * sc, acc[1] * sc), pk(acc[2] * sc, acc[3] * sc)};
        }
      }
      BAR();
      if (w == 0) {
        const v4u v = *(LAS v4u*)(L + LP_QO + lane * 16);
        st4(v4u{lsbput(v.x, lsbtag(tlo, thi, 4 * i16)), lsbput(v.y, lsbtag(tlo, thi, 4 * i16 + 1)), lsbput(v.z, lsbtag(tlo, thi, 4 * i16 + 2)),
                lsbput(v.w, lsbtag(tlo, thi, 4 * i16 + 3))}, rws, g * 32768 + i16 * 16, WS_QA + hd * 1024 + jq * 256);
      }
      STAMP(6);
      BAR();
      const rsrc_t r2 = mk(w2, 4608 * 576 * 2);
      const int so2 = (xg * 576 + 36 * qq) * 1152;
      v4u a2[5][2];
      v2u a8[2][2];
      {
        const int vo4 = 2 * g * 1152 + i16 * 16, vo4m = g < 2 ? vo4 : OOB;
        const int vo8 = 2 * g * 1152 + i16 * 8 + 1024, vo8m = (g < 2 && w == 0) ? vo8 : OOB;
#pragma unroll
        for (int o = 0; o < 5; ++o)
#pragma unroll
          for (int r = 0; r < 2; ++r) a2[o][r] = ld4<NT_W>(r2, o < 4 ? vo4 : vo4m, so2 + (8 * o + r) * 1152 + 256 * w);
#pragma unroll
        for (int r = 0; r < 2; ++r) { a8[0][r] = ld2<NT_W>(r2, vo8, so2 + (8 * w + r) * 1152); a8[1][r] = ld2<NT_W>(r2, vo8m, so2 + (32 + r) * 1152); }
        SCHED();
      }
      STAMP(7);
      BAR();
      v4u X[5], Y[5];
#pragma unroll
      for (int o = 0; o < 5; ++o) {
        X[o] = v4u{a2[o][0].x, a2[o][0].y, a2[o][1].x, a2[o][1].y};
        Y[o] = v4u{a2[o][0].z, a2[o][0].w, a2[o][1].z, a2[o][1].w};
        asm volatile("" : "+v"(X[o]), "+v"(Y[o]));
      }
      STAMP(8);
      BAR();
      STAMP(9);
      {
        const v4u h4 = *(LAS v4u*)(L + LP_HP + (bj * 4 + g) * 48);
        const unsigned h5 = *(LAS unsigned*)(L + LP_HP + (bj * 4 + g) * 48 + 16);
        const unsigned hw = *(LAS unsigned*)(L + LP_HP + (bj * 4 + g) * 48 + 4 * w);
        f32x4 c8 = {0.f, 0.f, 0.f, 0.f}, cx = c8, cy = c8;
        c8 = mfma16(v4u{a8[0][0].x, a8[0][0].y, a8[0][1].x, a8[0][1].y}, bdiag(hw, sel), c8);
        c8 = mfma16(v4u{a8[1][0].x, a8[1][0].y, a8[1][1].x, a8[1][1].y}, bdiag(h5, sel), c8);
#pragma unroll
        for (int o = 0; o < 5; ++o) {
          const v4u B = bdiag(o == 0 ? h4.x : o == 1 ? h4.y : o == 2 ? h4.z : o == 3 ? h4.w : h5, sel);
          cx = mfma16(X[o], B, cx);
          cy = mfma16(Y[o], B, cy);
        }
        *(LAS f32x4*)(L + LP_B8 + w * 1024 + lane * 16) = c8;
        st4(tag8(cx, tb), rws, vo5, so5 + (2 * w) * 8 * 32768);
        st4(tag8(cy, tb), rws, vo5, so5 + (2 * w + 1) * 8 * 32768);
        BAR();
      }
      STAMP(10);
    } else {
      const int v = w - 4;
      {
        const v4u x0 = ld4<0>(rx, lane * 16, v * 1152), x1 = ld4<0>(rx, lane < 8 ? lane * 16 : OOB, v * 1152 + 1024);
        v2u wv_;
        if (v == 0) wv_ = hl2(rws, 0, WS_W);
        SCHED();
        RBAR();
        if (v == 0) PUT_TAG(LP_TAG);
        float f[16], s = 0.f;
#pragma unroll
        for (int k = 0; k < 4; ++k) {
          f[2 * k] = bf2f(x0[k]); f[2 * k + 1] = __uint_as_float(x0[k] & 0xFFFF0000u);
          f[8 + 2 * k] = bf2f(x1[k]); f[8 + 2 * k + 1] = __uint_as_float(x1[k] & 0xFFFF0000u);
        }
#pragma unroll
        for (int k = 0; k < 16; ++k) s += f[k];
        const float mean = x32_sum(x16_sum(row_sum(s))) * (1.0f / 576.0f);
        float q0 = 0.f, q1 = 0.f;
#pragma unroll
        for (int k = 0; k < 8; ++k) { f[k] -= mean; f[8 + k] -= mean; q0 = fmaf(f[k], f[k], q0); q1 = fmaf(f[8 + k], f[8 + k], q1); }
        const float rstd = 1.0f / sqrtf(x32_sum(x16_sum(row_sum(lane < 8 ? q0 + q1 : q0))) * (1.0f / 576.0f) + 1e-5f);
#pragma unroll
        for (int hf = 0; hf < 2; ++hf) {
          if (hf == 0 || lane < 8) {
            const int o = lane + 64 * hf;
#pragma unroll
            for (int k = 0; k < 4; ++k) {
              const unsigned p = pk(f[8 * hf + 2 * k] * rstd, f[8 * hf + 2 * k + 1] * rstd);
              *(LAS unsigned*)(L + LP_XP + ((v * 4 + k) * 76 + o) * 4) = p;
              *(LAS unsigned*)(L + LP_XD + ((v * 4 + ((2 * k) & 3)) * 148 + 2 * o + (k >> 1)) * 4) = __builtin_amdgcn_perm(0u, p, 0x01000100u);
              *(LAS unsigned*)(L + LP_XD + ((v * 4 + ((2 * k + 1) & 3)) * 148 + 2 * o + (k >> 1)) * 4) = __builtin_amdgcn_perm(0u, p, 0x03020302u);
            }
          }
        }
      }
      STAMP(1);
      BAR();
      GET_TAG(LP_TAG);
      const int U0 = (81 * qq) / 144, Qb = 144 * (U0 + 1);
      unsigned xd[21];
#pragma unroll
      for (int t = 0; t < 21; ++t) {
        const int Q = min(81 * qq + 4 * t + v, 81 * qq + 80), qk = Q >= Qb ? Q - Qb : Q - 144 * U0;
        xd[t] = *(LAS unsigned*)(L + LP_XD + ((bj * 4 + g) * 148 + qk) * 4);
      }
      const GAS char* wb = (const GAS char*)(i16 < 8 ? w1 : w3) + (xg * 576 * 2 + (i16 & 7) * 16 + g * 9216);
      const unsigned m0 = cp == 0 ? 0xFFFFu : cp == 1 ? 0xFFFF0000u : 0u, m1 = cp == 2 ? 0xFFFFu : cp == 3 ? 0xFFFF0000u : 0u;
      f32x4 acc[4];
#pragma unroll
      for (int k = 0; k < 4; ++k) acc[k] = f32x4{0.f, 0.f, 0.f, 0.f};
      v4u a[21];
      BAR();
      BAR();
      STAMP(2);
#pragma unroll
      for (int t = -PACE_G; t < 21; ++t) {
        const int tl = t + PACE_G;
        if (tl < 21) {
          const int Q = 81 * qq + 4 * tl + v, u = Q >= Qb ? U0 + 1 : U0, qk = Q - 144 * u;
          if (tl < 20 || v == 0) {
            const GAS v4u* p = (const GAS v4u*)(wb + (unsigned)(qk * 4 * 9216 + u * 128));
            a[tl] = NT_W ? __builtin_nontemporal_load(p) : *p;
          } else a[tl] = v4u{0u, 0u, 0u, 0u};
          SCHED();
        }
        if (t == -1) BAR();
        if (tl == 20) { STAMP(3); BAR(); }
        if (t >= 0) {
          const int Q = 81 * qq + 4 * t + v;
          const v2u B = {xd[t] & m0, xd[t] & m1};
          if (Q < Qb) { acc[0] = mfma16k16(v2u{a[t].x, a[t].y}, B, acc[0]); acc[1] = mfma16k16(v2u{a[t].z, a[t].w}, B, acc[1]); }
          else        { acc[2] = mfma16k16(v2u{a[t].x, a[t].y}, B, acc[2]); acc[3] = mfma16k16(v2u{a[t].z, a[t].w}, B, acc[3]); }
          SCHED();
        }
      }
#pragma unroll
      for (int k = 0; k < 4; ++k) *(LAS f32x4*)(L + LP_RED + (v * 4 + k) * 1024 + lane * 16) = acc[k];
      STAMP(4);
      BAR();
      {
        f32x4 r = *(LAS f32x4*)(L + LP_RED + v * 1024 + lane * 16);
#pragma unroll
        for (int k = 1; k < 4; ++k) r += *(LAS f32x4*)(L + LP_RED + (k * 4 + v) * 1024 + lane * 16);
        const int po = WS_H7 + idx * 8192 + v * 2048;
        st4(v4u{tlo, genc(r[0], t8), tlo, genc(r[1], t8)}, rws, lane * 16, po);
        st4(v4u{tlo, genc(r[2], t8), tlo, genc(r[3], t8)}, rws, lane * 16, po + 1024);
      }
      STAMP(5);
      {
        const int t = v * 64 + lane;
        const int b = t / 36, cl = t - 36 * b, f = 36 * qq + cl, u = f >> 6, cu = f & 63;
        const int ig = cu >> 3, hh = (cu >> 2) & 1, reg = ig & 3;
        const int r0 = (144 * u) / 81, r1 = (144 * u + 143) / 81;
        const int lo = hh * 2048 + (reg >> 1) * 1024 + ((ig >> 2) * 16 + b * 4 + (cu & 3)) * 16 + (reg & 1) * 8;
        int off[3];
        unsigned use[3];
#pragma unroll
        for (int k = 0; k < 3; ++k) {
          const int rr = r0 + k;
          const bool on = t < 144 && rr <= r1;
          off[k] = on ? (xg * 16 + rr) * 8192 + (u - (81 * rr) / 144) * 4096 + lo : OOB;
          use[k] = on ? 0xFFFFFFFFu : 0u;
        }
        __builtin_amdgcn_s_sleep(DLY_H7);
        float gs, us;
        for (int n = 0;; ++n) {
          v2u pg[3], pu[3];
#pragma unroll
          for (int k = 0; k < 3; ++k) { pg[k] = hl2(rws, off[k], WS_H7); pu[k] = hl2(rws, off[k], WS_H7 + 512); }
          unsigned bad = 0;
          gs = 0.f; us = 0.f;
#pragma unroll
          for (int k = 0; k < 3; ++k) {
            bad |= ((pg[k].x ^ tlo) | (pu[k].x ^ tlo) | (((pg[k].y ^ t8) | (pu[k].y ^ t8)) & 0xFFu)) & use[k];
            gs += gdec(pg[k].y); us += gdec(pu[k].y);
          }
          if (__all(bad == 0) || n >= GSPIN) break;
        }
        const float hv = gs / (1.0f + __expf(-gs)) * us;
        if (t < 144) *(LAS u16*)(L + LP_HP + ((b * 4 + ((cl >> 1) & 3)) * 12 + (cl >> 3)) * 4 + (cl & 1) * 2) = (u16)pk(hv, hv);
        else if (t < 152) *(LAS unsigned*)(L + LP_HP + ((((t - 144) >> 1) * 4 + 2 + (t & 1)) * 12 + 4) * 4) = 0u;
      }
      STAMP(6);
      BAR();
      BAR();
      if (v == 0) {
        f32x4 s = *(LAS f32x4*)(L + LP_B8 + lane * 16);
#pragma unroll
        for (int k = 1; k < 4; ++k) s += *(LAS f32x4*)(L + LP_B8 + k * 1024 + lane * 16);
        st4(tag8(s, tb), rws, vo5, so5 + 8 * 8 * 32768);
      }
      STAMP(7);
    }
    STAMP_OUT();
    return;
  }

  const int ab = xg >> 1;
  const int r32 = lane & 31, hh = lane >> 5;
  const int so5 = WS_H5 + (2 * idx + 1) * 128;
  v2u av[8][2];
  v2u wa_;
  asm volatile("buffer_load_dwordx2 %0, off, %1, 0 sc1" : "=v"(wa_) : "s"(rws) : "memory");
  if (w < 4) {
    const rsrc_t rc = mk(ckv, 4 * 4096 * 512 * 2);
    {
      int vo[4];
#pragma unroll
      for (int k = 0; k < 4; ++k) vo[k] = g * 1024 + ((i16 ^ ((g << 2) | k)) * 16);
      const int so = (idx * 128 + 32 * w) * 1024;
#pragma unroll
      for (int n = 0; n < 32; ++n) {
        if (n >= PACE_C) VMCNT(PACE_C - 1);
        dma16(rc, vo[n & 3], so + (n & 7) * 4096 + (n >> 3) * 256, lbase + (n >> 3) * 32768 + (32 * w + 4 * (n & 7)) * 256);
        if (n == KQA) BAR();
      }
    }
  } else {
    VMCNT(0);
    BAR();
  }
  asm volatile("" : "+v"(wa_));
  const u64 tag_ = (((u64)wa_.y << 32) | wa_.x) + 0x9E3779B97F4A7C15ull;
  const unsigned tlo = (unsigned)tag_, thi = (unsigned)(tag_ >> 32), t8 = thi & 0xFFu;
  const unsigned tb[4] = {tlo & 0xFFu, (tlo >> 8) & 0xFFu, (tlo >> 16) & 0xFFu, tlo >> 24};
  unsigned e4[4];
#pragma unroll
  for (int k = 0; k < 4; ++k) e4[k] = lsbtag(tlo, thi, 4 * i16 + k);
  if (w < 4) {
    v4u fa[32];
    {
      const int fr = ((r32 & 3) << 2) | ((r32 >> 2) & 3);
      const int ca = (32 * w + r32) * 256;
#define QK_READ(Q) _Pragma("unroll") for (int k = 0; k < 8; ++k) fa[(Q)*8 + k] = *(LAS v4u*)(L + (Q)*32768 + ca + (((2 * k + hh) ^ fr) * 16)); SCHED();
      const rsrc_t rv = mk(wuv, 512 * 4096 * 2);
      const int sov = (128 * jq + 16 * (w >> 1)) * 8192 + hd * 256 + (w & 1) * 128;
      const int vov[2] = {(32 * ((2 * g) & 3) + (g >> 1)) * 8192 + i16 * 8, (32 * ((2 * g + 1) & 3) + (g >> 1)) * 8192 + i16 * 8};
#define AV_LOAD(A, B) _Pragma("unroll") for (int n = (A); n < (B); ++n) av[n >> 1][n & 1] = ld2<NT_W>(rv, vov[n & 1], sov + 2 * (n >> 1) * 8192); SCHED();
      VMCNT(8); QK_READ(0) QK_READ(1) QK_READ(2)
      VMCNT(4); AV_LOAD(0, 8)
      VMCNT(8); QK_READ(3)
      AV_LOAD(8, 16)
    }
    int qb[4];
#pragma unroll
    for (int k = 0; k < 4; ++k) { qb[k] = LA_QA + ((lane ^ ((2 * k + hh) & 7)) * 16); asm volatile("" : "+v"(qb[k])); }
    STAMP(2);
    BAR();
    STAMP(3);
    f32x16 sacc;
#pragma unroll
    for (int k = 0; k < 16; ++k) sacc[k] = 0.f;
    {
      v4u fb[32];
#pragma unroll
      for (int s = 0; s < 6; ++s) fb[s] = *(LAS v4u*)(L + qb[s & 3] + s * 1024);
      SCHED();
#pragma unroll
      for (int s = 0; s < 32; ++s) {
        if (s + 6 < 32) fb[s + 6] = *(LAS v4u*)(L + qb[(s + 6) & 3] + (s + 6) * 1024);
        sacc = mfma32(fa[s], fb[s], sacc);
        SCHED();
        if (s == 12) { RBAR(); SCHED(); }
      }
    }
    const int q4 = i16 >> 2, lowc = 2 * (g & 1) + (cp >> 1);
    int ta[4][2];
#pragma unroll
    for (int t = 0; t < 4; ++t)
#pragma unroll
      for (int t2 = 0; t2 < 2; ++t2) {
        ta[t][t2] = w * 32768 + (8 * t2 + 4 * hh + q4) * 256 + ((((t ^ q4) << 2) | (lowc ^ (2 * t2 + hh))) * 16) + 8 * (cp & 1);
        asm volatile("" : "+v"(ta[t][t2]));
      }
    v4u cb[32];
#define TR_READ(n) do { const v2u x0_ = __builtin_bit_cast(v2u, __builtin_amdgcn_ds_read_tr16_b64_v4i16((LAS s16x4*)(L + ta[(n) >> 3][0] + ((n) & 7) * 4096))); \
    const v2u x1_ = __builtin_bit_cast(v2u, __builtin_amdgcn_ds_read_tr16_b64_v4i16((LAS s16x4*)(L + ta[(n) >> 3][1] + ((n) & 7) * 4096)));                 \
    cb[n] = v4u{x0_.x, x0_.y, x1_.x, x1_.y}; } while (0)
#pragma unroll
    for (int n = 0; n < 6; ++n) TR_READ(n);
    SCHED();
    float mw = sacc[0];
#pragma unroll
    for (int k = 1; k < 16; ++k) mw = fmaxf(mw, sacc[k]);
    mw = x32_max(mw);
    if (lane < 32) *(LAS float*)(L + LA_MX + (r32 * 4 + w) * 4) = mw;
    STAMP(4);
    BAR();
    const f32x4 m4 = *(LAS f32x4*)(L + LA_MX + r32 * 16);
    const float mx = fmaxf(fmaxf(m4[0], m4[1]), fmaxf(m4[2], m4[3]));
    float ls = 0.f;
#pragma unroll
    for (int k = 0; k < 16; ++k) { sacc[k] = __builtin_amdgcn_exp2f(sacc[k] - mx); ls += sacc[k]; }
#pragma unroll
    for (int s2 = 0; s2 < 2; ++s2)
      *(LAS v4u*)(L + LA_PB + (2 * w + s2) * 1024 + lane * 16) =
          v4u{pk(sacc[8 * s2], sacc[8 * s2 + 1]), pk(sacc[8 * s2 + 2], sacc[8 * s2 + 3]), pk(sacc[8 * s2 + 4], sacc[8 * s2 + 5]), pk(sacc[8 * s2 + 6], sacc[8 * s2 + 7])};
    ls = x32_sum(ls);
    if (lane < 32) *(LAS float*)(L + LA_SUM + (r32 * 4 + w) * 4) = ls;
    BAR();
    STAMP(5);
    {
      v4u pf[8];
#pragma unroll
      for (int k = 0; k < 8; ++k) pf[k] = *(LAS v4u*)(L + LA_PB + k * 1024 + lane * 16);
      if (w == 0) {
        const f32x4 l4 = *(LAS f32x4*)(L + LA_SUM + r32 * 16);
        st4(v4u{tlo, genc(mx, t8), tlo, genc((l4[0] + l4[1]) + (l4[2] + l4[3]), t8)}, rws, lane < 32 ? r32 * 16 : OOB, WS_ML + idx * 512);
      }
      SCHED();
      f32x16 o[4];
#pragma unroll
      for (int t = 0; t < 4; ++t)
#pragma unroll
        for (int k = 0; k < 16; ++k) o[t][k] = 0.f;
#pragma unroll
      for (int n = 0; n < 32; ++n) {
        if (n + 6 < 32) TR_READ(n + 6);
        o[n >> 3] = mfma32(pf[n & 7], cb[n], o[n >> 3]);
        SCHED();
      }
      STAMP(1);
      const unsigned e0 = lsbtag(tlo, thi, 2 * r32), e1 = lsbtag(tlo, thi, 2 * r32 + 1);
      const int vo = hh * 4 * 131072 + r32 * 8, so = WS_H3 + (w * 128 + idx) * 256;
#pragma unroll
      for (int k = 0; k < 16; ++k)
        st2(v2u{lsbput(pk(o[0][k], o[1][k]), e0), lsbput(pk(o[2][k], o[3][k]), e1)}, rws, vo, so + ((k & 3) + 8 * (k >> 2)) * 131072);
    }
    STAMP(6);
    {
      v4u ml;
      for (int n = 0;; ++n) {
        ml = hl4(rws, r32 * 512, WS_ML + w * 32 * 512 + hd * 16);
        if (__all(((ml.x ^ tlo) | (ml.z ^ tlo) | (((ml.y ^ t8) | (ml.w ^ t8)) & 0xFFu)) == 0) || n >= GSPIN) break;
      }
      if (DLY_H3) __builtin_amdgcn_s_sleep(DLY_H3);
      STAMP(7);
      v4u d[8];
#pragma unroll
      for (int k = 0; k < 8; ++k) d[k] = hl4(rws, lane * 16, WS_H3 + idx * 32768 + w * 8192 + k * 1024);
      SCHED();
      const float m = gdec(ml.y), M = x16_max(row_max(m));
      const float e = __builtin_amdgcn_exp2f(m - M);
      const float al = e / x16_sum(row_sum(e * gdec(ml.w)));
      const unsigned ah = pk(al, al) & 0xFFFFu;
      const unsigned hl = ah | (pk(al - bf2f(ah), 0.f) << 16);
      const int e7 = lane & 7;
      const unsigned src = (lane & 8) ? 0x0302u : 0x0100u, s1 = (e7 & 1) ? ((src << 16) | 0x0C0Cu) : (0x0C0C0000u | src);
      unsigned s8[4];
#pragma unroll
      for (int k = 0; k < 4; ++k) s8[k] = (e7 >> 1) == k ? s1 : 0x0C0C0C0Cu;
      v4u bk[8];
#pragma unroll
      for (int k = 0; k < 8; ++k) { bk[k] = bdiag(__builtin_amdgcn_ds_bpermute((4 * k + g) * 4, hl), s8); asm volatile("" : "+v"(bk[k])); }
#pragma unroll 1
      for (int n = 0; n < GSPIN; ++n) {
        unsigned bad = 0;
#pragma unroll
        for (int k = 0; k < 8; ++k) bad |= xor4(d[k], e4);
        if (__all((bad & LSBM) == 0)) break;
#pragma unroll
        for (int k = 0; k < 8; ++k) d[k] = hl4(rws, lane * 16, WS_H3 + idx * 32768 + w * 8192 + k * 1024);
      }
      STAMP(8);
      f32x4 c = {0.f, 0.f, 0.f, 0.f};
#pragma unroll
      for (int k = 0; k < 8; ++k) c = mfma16(d[k], bk[k], c);
#pragma unroll
      for (int r = 0; r < 4; ++r) {
        const float y = c[r] + dppf<0x128, 0xf>(c[r], c[r]);
        if (!(lane & 8)) *(LAS u16*)(L + LA_CTX + ((w * 4 + (e7 >> 1)) * 20 + 4 * g + r) * 4 + 2 * (e7 & 1)) = (u16)pk(y, y);
      }
    }
    BAR();
    {
      const int half = w >> 1, blk = w & 1;
      const v4u p0 = *(LAS v4u*)(L + LA_CTX + ((bj * 4 + g) * 20 + 8 * half) * 4), p1 = *(LAS v4u*)(L + LA_CTX + ((bj * 4 + g) * 20 + 8 * half + 4) * 4);
      f32x4 c = {0.f, 0.f, 0.f, 0.f};
#pragma unroll
      for (int o = 0; o < 8; ++o) c = mfma16(v4u{av[o][0].x, av[o][0].y, av[o][1].x, av[o][1].y}, bdiag(o < 4 ? p0[o & 3] : p1[o & 3], sel), c);
#pragma unroll
      for (int r = 0; r < 4; ++r)
        *(LAS float*)(L + LA_UV + half * 2304 + ((bj * 4 + 2 * (r & 1) + (cp >> 1)) * 36 + (8 * blk + 2 * g + (r >> 1)) * 2 + (cp & 1)) * 4) = c[r];
    }
    BAR();
    STAMP(9);
    BAR();
    if (w == 0) {
      f32x4 s = *(LAS f32x4*)(L + LA_B8 + lane * 16);
#pragma unroll
      for (int k = 1; k < 4; ++k) s += *(LAS f32x4*)(L + LA_B8 + k * 1024 + lane * 16);
      st4(tag8(s, tb), rws, vo5, so5 + 8 * 8 * 32768);
    }
  } else {
    const int v = w - 4;
    STAMP(1);
    {
      v4u qa[8];
      for (int n = 0;; ++n) {
#pragma unroll
        for (int k = 0; k < 8; ++k) qa[k] = hl4(rws, lane * 16, WS_QA + (ab * 32 + 8 * v + k) * 1024);
        unsigned bad = 0;
#pragma unroll
        for (int k = 0; k < 8; ++k) bad |= xor4(qa[k], e4);
        if (__all((bad & LSBM) == 0) || n >= GSPIN) break;
        __builtin_amdgcn_s_sleep(DLY_R2);
      }
#pragma unroll
      for (int k = 0; k < 8; ++k) *(LAS v4u*)(L + LA_QA + (lane >> 1) * 1024 + ((((lane & 1) * 32 + 8 * v + k) ^ (lane & 7)) * 16)) = qa[k];
    }
    STAMP(2);
    BAR();
    const rsrc_t ro = mk(wo, 4096 * 576 * 2);
    v4u ao[16][2];
    v2u a8[4][2];
    {
      const int vo4 = 2 * g * 1152 + i16 * 16, vo8 = 2 * g * 1152 + i16 * 8 + 1024;
#pragma unroll
      for (int o = 0; o < 16; ++o) {
        const int op = (o + 4 * jq) & 15;
        if (o > 4) VMCNT(PACE_O - 2);
#pragma unroll
        for (int r = 0; r < 2; ++r) ao[o][r] = ld4<NT_WO>(ro, vo4, (hd * 128 + 8 * op + r) * 1152 + 256 * v);
        if ((o & 3) == 3) {
          const int o8 = 4 * (((o >> 2) + jq) & 3) + v;
#pragma unroll
          for (int r = 0; r < 2; ++r) a8[o >> 2][r] = ld2<NT_WO>(ro, vo8, (hd * 128 + 8 * o8 + r) * 1152);
        }
        SCHED();
        if (o == 3) { RBAR(); RBAR(); RBAR(); }
      }
    }
    STAMP(3);
    v4u X[16], Y[16];
#pragma unroll
    for (int o = 0; o < 16; ++o) {
      X[o] = v4u{ao[o][0].x, ao[o][0].y, ao[o][1].x, ao[o][1].y};
      Y[o] = v4u{ao[o][0].z, ao[o][0].w, ao[o][1].z, ao[o][1].w};
      asm volatile("" : "+v"(X[o]), "+v"(Y[o]));
    }
    VMCNT(0);
    STAMP(4);
    BAR();
    BAR();
    STAMP(5);
    {
      unsigned P[16];
#pragma unroll
      for (int q = 0; q < 4; ++q) {
        const int ad = LA_UV + ((bj * 4 + g) * 36 + 8 * ((q + jq) & 3)) * 4;
        const f32x4 x0 = *(LAS f32x4*)(L + ad) + *(LAS f32x4*)(L + ad + 2304), x1 = *(LAS f32x4*)(L + ad + 16) + *(LAS f32x4*)(L + ad + 16 + 2304);
        P[4 * q] = pk(x0[0], x0[1]); P[4 * q + 1] = pk(x0[2], x0[3]); P[4 * q + 2] = pk(x1[0], x1[1]); P[4 * q + 3] = pk(x1[2], x1[3]);
      }
      f32x4 c8 = {0.f, 0.f, 0.f, 0.f}, cx = c8, cy = c8;
#pragma unroll
      for (int q = 0; q < 4; ++q) {
        const unsigned pv = v == 0 ? P[4 * q] : v == 1 ? P[4 * q + 1] : v == 2 ? P[4 * q + 2] : P[4 * q + 3];
        c8 = mfma16(v4u{a8[q][0].x, a8[q][0].y, a8[q][1].x, a8[q][1].y}, bdiag(pv, sel), c8);
      }
      SCHED();
#pragma unroll
      for (int o = 0; o < 16; ++o) {
        const v4u B = bdiag(P[o], sel);
        cx = mfma16(X[o], B, cx);
        cy = mfma16(Y[o], B, cy);
        if (o == 1) { *(LAS f32x4*)(L + LA_B8 + v * 1024 + lane * 16) = c8; SCHED(); BAR(); }
      }
      st4(tag8(cx, tb), rws, vo5, so5 + (2 * v) * 8 * 32768);
      st4(tag8(cy, tb), rws, vo5, so5 + (2 * v + 1) * 8 * 32768);
    }
    STAMP(6);
  }
  if (qq < 9) {
    const int e = lane & 31, Ld = 8 * xg + (e >> 2), rr = e & 3;
    const int ob = (Ld & 15) >> 2, oc = qq < 8 ? 128 * (qq >> 1) + 8 * (4 * (Ld >> 4) + rr) + 4 * (qq & 1) + (Ld & 3) : 512 + 4 * (4 * (Ld >> 4) + rr) + (Ld & 3);
    unsigned xres = 0;
    if (w == 0) xres = ((const GAS u16*)xin)[ob * 576 + oc];
    BAR();
    __builtin_amdgcn_s_sleep(DLY_H5);
    STAMP(10);
    f32x4 s;
    for (int n = 0;; ++n) {
      v4u d[4];
#pragma unroll
      for (int k = 0; k < 4; ++k) d[k] = hl4(rws, lane * 16, WS_H5 + (qq * 8 + xg) * 32768 + (w * 4 + k) * 1024);
      const unsigned bad = (xor4(d[0], tb) | xor4(d[1], tb)) | (xor4(d[2], tb) | xor4(d[3], tb));
      s = (__builtin_bit_cast(f32x4, d[0]) + __builtin_bit_cast(f32x4, d[1])) + (__builtin_bit_cast(f32x4, d[2]) + __builtin_bit_cast(f32x4, d[3]));
      if (__all((bad & 0xFFu) == 0) || n >= GSPIN) break;
    }
#pragma unroll
    for (int r = 0; r < 4; ++r) s[r] = x32_sum(x16_sum(s[r] + dppf<0x128, 0xf>(s[r], s[r])));
    if (lane < 8) *(LAS f32x4*)(L + LA_FIN + w * 128 + lane * 16) = s;
    BAR();
    if (w == 0) {
      float y = bf2f(xres);
#pragma unroll
      for (int k = 0; k < 8; ++k) y += *(LAS float*)(L + LA_FIN + k * 128 + e * 4);
      if (lane < 32) ((GAS u16*)out)[ob * 576 + oc] = (u16)pk(y, y);
      if (qq == 0 && xg == 0) st2(v2u{tlo, thi}, rws, lane == 0 ? 0 : OOB, WS_W);
    }
    STAMP(11);
  }
  STAMP_OUT();
}

__global__ void __launch_bounds__(512,2) k_big(char* ws){ extern __shared__ char sm2[]; asm volatile("v_mov_b32 v231, 0" ::: "v231"); if(ws==nullptr) sm2[threadIdx.x]=1; }
extern "C" void kernel_launch(void* const* d_in, const int* in_sizes, int n_in, void* d_out, int out_size, void* d_ws, size_t ws_size,
                              hipStream_t stream) {
  (void)hipFuncSetAttribute((const void*)fused_decode, hipFuncAttributeMaxDynamicSharedMemorySize, LDS_BYTES);
  fused_decode<<<256, 512, LDS_BYTES, stream>>>((const u16*)d_in[0], (const u16*)d_in[1], (const u16*)d_in[2], (const u16*)d_in[3],
                                                (const u16*)d_in[4], (const u16*)d_in[5], (const u16*)d_in[7], (const u16*)d_in[8],
                                                (const u16*)d_in[9], (u16*)d_out, (char*)d_ws, 0);
}
```

```cpp
#define EXTRA 0
#include <hip/hip_runtime.h>
#include <stdint.h>

typedef unsigned short u16;
typedef unsigned long long u64;
typedef unsigned v4u __attribute__((ext_vector_type(4)));
typedef unsigned v2u __attribute__((ext_vector_type(2)));
typedef float f32x2 __attribute__((ext_vector_type(2)));
typedef float f32x4 __attribute__((ext_vector_type(4)));
typedef float f32x16 __attribute__((ext_vector_type(16)));
typedef short s16x4 __attribute__((ext_vector_type(4)));
typedef __bf16 bf16x8 __attribute__((ext_vector_type(8)));
typedef __bf16 bf16x2 __attribute__((ext_vector_type(2)));
typedef __amdgpu_buffer_rsrc_t rsrc_t;
#define GAS __attribute__((address_space(1)))
#define LAS __attribute__((address_space(3)))
#define DI __device__ __forceinline__

#define NT_W 2
#define NT_WO 0
#define PACE_C 8
#define DLY_H1 4
#define KQA 31
#define PACE_K 4
#define DLY_K 8
#define PACE_G 5
#define PACE_O 8
#define DLY_R2 8
#define DLY_H3 0
#define DLY_H7 8
#define DLY_H5 8

constexpr int SC1 = 16;
constexpr int GSPIN = 1 << 13;

constexpr int WS_W = 0;
constexpr int WS_H1 = 4096;
constexpr int WS_QA = WS_H1 + 128 * 4096;
constexpr int WS_ML = WS_QA + 131072;
constexpr int WS_H7 = WS_ML + 65536;
constexpr int WS_H5 = WS_H7 + 128 * 8192;
constexpr int WS_H3 = WS_H5 + 72 * 256 * 128;
constexpr int WS_END = WS_H3 + 128 * 32768;
constexpr int WS_STAMP = WS_END;
constexpr int OOB = (int)0x80000000;

constexpr int LP_TAG = 4992, LP_XP = 0, LP_XD = 5120, LP_QB = 15360, LP_QO = 16896, LP_RED = 18432, LP_HP = 34816, LP_B8 = 35840, LP_UK = 40960;
constexpr int LA_QA = 131072, LA_MX = LA_QA;
constexpr int LA_PB = LA_QA + 1024, LA_SUM = LA_QA + 512, LA_CTX = LA_QA + 9216, LA_UV = LA_QA + 11264, LA_B8 = LA_QA + 16384,
              LA_FIN = LA_QA + 20480;
constexpr int LDS_BYTES = 163840;

DI rsrc_t mk(const void* p, int bytes) { return __builtin_amdgcn_make_buffer_rsrc((void*)p, (short)0, bytes, 0x00020000); }
template <int AUX> DI v4u ld4(rsrc_t r, int vo, int so) { return __builtin_amdgcn_raw_buffer_load_b128(r, vo, so, AUX); }
template <int AUX> DI v2u ld2(rsrc_t r, int vo, int so) { return __builtin_amdgcn_raw_buffer_load_b64(r, vo, so, AUX); }
DI v4u hl4(rsrc_t r, int vo, int so) { asm volatile("" : "+v"(vo)); return __builtin_amdgcn_raw_buffer_load_b128(r, vo, so, SC1); }
DI v2u hl2(rsrc_t r, int vo, int so) { asm volatile("" : "+v"(vo)); return __builtin_amdgcn_raw_buffer_load_b64(r, vo, so, SC1); }
DI void st4(v4u v, rsrc_t r, int vo, int so) { __builtin_amdgcn_raw_buffer_store_b128(v, r, vo + so, 0, SC1); }
DI void st2(v2u v, rsrc_t r, int vo, int so) { __builtin_amdgcn_raw_buffer_store_b64(v, r, vo, so, SC1); }
DI void dma16(rsrc_t r, int vo, int so, unsigned lds) {
  asm volatile("s_mov_b32 m0, %0\n\ts_nop 0\n\tbuffer_load_dwordx4 %1, %2, %3 offen nt lds" ::"s"(lds), "v"(vo), "s"(r), "s"(so) : "memory");
}
#define VMCNT(n) asm volatile("s_waitcnt vmcnt(%0)" ::"n"(n) : "memory")
#define FENCE() asm volatile("" ::: "memory")
#define SCHED() __builtin_amdgcn_sched_barrier(0)

DI float bf2f(unsigned v) { return __uint_as_float(v << 16); }
DI unsigned pk(float a, float b) { f32x2 f = {a, b}; return __builtin_bit_cast(unsigned, __builtin_convertvector(f, bf16x2)); }
DI f32x4 mfma16(v4u a, v4u b, f32x4 c) {
  return __builtin_amdgcn_mfma_f32_16x16x32_bf16(__builtin_bit_cast(bf16x8, a), __builtin_bit_cast(bf16x8, b), c, 0, 0, 0);
}
DI f32x4 mfma16k16(v2u a, v2u b, f32x4 c) {
  return __builtin_amdgcn_mfma_f32_16x16x16bf16_1k(__builtin_bit_cast(s16x4, a), __builtin_bit_cast(s16x4, b), c, 0, 0, 0);
}
DI f32x16 mfma32(v4u a, v4u b, f32x16 c) {
  return __builtin_amdgcn_mfma_f32_32x32x16_bf16(__builtin_bit_cast(bf16x8, a), __builtin_bit_cast(bf16x8, b), c, 0, 0, 0);
}
template <int CTRL, int RM> DI float dppf(float old, float v) {
  return __builtin_bit_cast(float, __builtin_amdgcn_update_dpp(__builtin_bit_cast(int, old), __builtin_bit_cast(int, v), CTRL, RM, 0xf, false));
}
DI float row_sum(float v) {
  v += dppf<0xB1, 0xf>(v, v); v += dppf<0x4E, 0xf>(v, v); v += dppf<0x124, 0xf>(v, v); v += dppf<0x128, 0xf>(v, v);
  return v;
}
DI float row_max(float v) {
  v = fmaxf(v, dppf<0xB1, 0xf>(v, v)); v = fmaxf(v, dppf<0x4E, 0xf>(v, v));
  v = fmaxf(v, dppf<0x124, 0xf>(v, v)); v = fmaxf(v, dppf<0x128, 0xf>(v, v));
  return v;
}
DI float x16_sum(float v) { auto r = __builtin_amdgcn_permlane16_swap(__float_as_uint(v), __float_as_uint(v), false, false); return __uint_as_float(r[0]) + __uint_as_float(r[1]); }
DI float x32_sum(float v) { auto r = __builtin_amdgcn_permlane32_swap(__float_as_uint(v), __float_as_uint(v), false, false); return __uint_as_float(r[0]) + __uint_as_float(r[1]); }
DI float x16_max(float v) { auto r = __builtin_amdgcn_permlane16_swap(__float_as_uint(v), __float_as_uint(v), false, false); return fmaxf(__uint_as_float(r[0]), __uint_as_float(r[1])); }
DI float x32_max(float v) { auto r = __builtin_amdgcn_permlane32_swap(__float_as_uint(v), __float_as_uint(v), false, false); return fmaxf(__uint_as_float(r[0]), __uint_as_float(r[1])); }

#define RBAR() asm volatile("s_barrier" ::: "memory")
#define BAR() asm volatile("s_waitcnt lgkmcnt(0)\n\ts_barrier" ::: "memory")
DI unsigned genc(float v, unsigned t8) { return ((__float_as_uint(v) + 0x80u) & 0xFFFFFF00u) | t8; }
DI float gdec(unsigned hi) { return __uint_as_float(hi & 0xFFFFFF00u); }

#define STAMP(k)
#define STAMP_DECL
#define STAMP_OUT()

DI v4u bdiag(unsigned P, const unsigned (&s)[4]) {
  v4u b;
  b.x = __builtin_amdgcn_perm(P, P, s[0]); b.y = __builtin_amdgcn_perm(P, P, s[1]);
  b.z = __builtin_amdgcn_perm(P, P, s[2]); b.w = __builtin_amdgcn_perm(P, P, s[3]);
  return b;
}
DI void bdiag_sel(int cp, unsigned (&s)[4]) {
  const unsigned lo = (cp & 1) ? 0x01000C0Cu : 0x0C0C0100u, hi = (cp & 1) ? 0x03020C0Cu : 0x0C0C0302u, z = 0x0C0C0C0Cu;
  s[0] = cp < 2 ? lo : z; s[1] = cp < 2 ? z : lo; s[2] = cp < 2 ? hi : z; s[3] = cp < 2 ? z : hi;
}

DI unsigned lsbtag(unsigned tlo, unsigned thi, int p) { return (((p < 32 ? tlo : thi) >> (p & 31)) & 1u) | (((tlo ^ (unsigned)p) & 1u) << 16); }
constexpr unsigned LSBM = 0x00010001u;
DI unsigned lsbput(unsigned v, unsigned e) { return (v & ~LSBM) | e; }
DI unsigned xor4(v4u d, const unsigned (&e)[4]) { return ((d.x ^ e[0]) | (d.y ^ e[1])) | ((d.z ^ e[2]) | (d.w ^ e[3])); }
DI v4u tag8(f32x4 c, const unsigned (&t)[4]) {
  return v4u{(__float_as_uint(c[0]) & 0xFFFFFF00u) | t[0], (__float_as_uint(c[1]) & 0xFFFFFF00u) | t[1],
             (__float_as_uint(c[2]) & 0xFFFFFF00u) | t[2], (__float_as_uint(c[3]) & 0xFFFFFF00u) | t[3]};
}
__global__ void __launch_bounds__(512, 2)
fused_decode(const u16* __restrict__ xin, const u16* __restrict__ ckv, const u16* __restrict__ wq, const u16* __restrict__ wuk,
             const u16* __restrict__ wuv, const u16* __restrict__ wo, const u16* __restrict__ w1, const u16* __restrict__ w3,
             const u16* __restrict__ w2, u16* __restrict__ out, char* ws) {
  extern __shared__ __attribute__((aligned(16))) char smem[];
  LAS char* L = (LAS char*)smem;
  const unsigned lbase = (unsigned)(size_t)smem;
  const int lane = threadIdx.x & 63, w = __builtin_amdgcn_readfirstlane(threadIdx.x >> 6);
  const int xg = blockIdx.x & 7, qd_ = blockIdx.x >> 3, qq = qd_ & 15, idx = xg * 16 + qq, hd = idx >> 2, jq = idx & 3;
  const int i16 = lane & 15, g = lane >> 4, bj = i16 >> 2, cp = lane & 3;
  STAMP_DECL;
  STAMP(0);
  asm volatile("" ::"s"(xin), "s"(ckv), "s"(wq), "s"(wuk), "s"(wuv), "s"(wo), "s"(w1), "s"(w3), "s"(w2), "s"(out), "s"(ws));
  const rsrc_t rws = mk(ws, WS_END);
  const rsrc_t rx = mk(xin, 4 * 576 * 2);
  unsigned sel[4];
  bdiag_sel(cp, sel);
#define LOAD_TAG() const v2u wv_ = hl2(rws, 0, WS_W)
#define PUT_TAG(AD) do { const u64 tag_ = (((u64)wv_.y << 32) | wv_.x) + 0x9E3779B97F4A7C15ull; \
    if (lane == 0) *(LAS v2u*)(L + (AD)) = v2u{(unsigned)tag_, (unsigned)(tag_ >> 32)}; } while (0)
#define GET_TAG(AD) const v2u tg_ = *(LAS v2u*)(L + (AD)); \
  const unsigned tlo = tg_.x, thi = tg_.y, t8 = thi & 0xFFu; \
  const unsigned tb[4] = {tlo & 0xFFu, (tlo >> 8) & 0xFFu, (tlo >> 16) & 0xFFu, tlo >> 24}
  const int vo5 = (lane >> 3) * 32768 + (lane & 7) * 16;

  if (qd_ < 16) {
    const int so5 = WS_H5 + (2 * idx) * 128;
    if (w < 4) {
      v4u a[18][2];
      int vok[4];
      const rsrc_t rk = mk(wuk, 512 * 4096 * 2);
      const int n0 = (w - 2) * 16;
      RBAR();
      if (w == 0) {
        const rsrc_t rq = mk(wq, 576 * 4096 * 2);
        const int vo = 2 * g * 8192 + i16 * 16, so = 144 * jq * 8192 + hd * 256;
#pragma unroll
        for (int o = 0; o < 18; ++o) { a[o][0] = ld4<NT_W>(rq, vo, so + (8 * o) * 8192); a[o][1] = ld4<NT_W>(rq, vo, so + (8 * o + 1) * 8192); }
        SCHED();
      } else if (w >= 2) {
#pragma unroll
        for (int k = 0; k < 4; ++k) vok[k] = g * 8192 + ((i16 ^ ((g << 2) | k)) * 16);
        __builtin_amdgcn_s_sleep(DLY_K);
#pragma unroll
        for (int n = 0; n < 16; ++n) dma16(rk, vok[n & 3], (128 * jq + 4 * (n0 + n)) * 8192 + hd * 256, lbase + LP_UK + (n0 + n) * 1024);
      }
      STAMP(1);
      BAR();
      STAMP(2);
      GET_TAG(LP_TAG);
      if (w == 0) {
        v2u xp[9];
#pragma unroll
        for (int k = 0; k < 9; ++k) xp[k] = *(LAS v2u*)(L + LP_XP + ((bj * 4 + g) * 76 + 18 * jq + 2 * k) * 4);
        f32x4 qx = {0.f, 0.f, 0.f, 0.f}, qy = qx;
#pragma unroll
        for (int o = 0; o < 18; ++o) {
          const v4u B = bdiag((o & 1) ? xp[o >> 1].y : xp[o >> 1].x, sel);
          qx = mfma16(v4u{a[o][0].x, a[o][0].y, a[o][1].x, a[o][1].y}, B, qx);
          qy = mfma16(v4u{a[o][0].z, a[o][0].w, a[o][1].z, a[o][1].w}, B, qy);
        }
        const int po = WS_H1 + idx * 4096;
        st4(v4u{tlo, genc(qx[0], t8), tlo, genc(qx[1], t8)}, rws, lane * 16, po);
        st4(v4u{tlo, genc(qx[2], t8), tlo, genc(qx[3], t8)}, rws, lane * 16, po + 1024);
        st4(v4u{tlo, genc(qy[0], t8), tlo, genc(qy[1], t8)}, rws, lane * 16, po + 2048);
        st4(v4u{tlo, genc(qy[2], t8), tlo, genc(qy[3], t8)}, rws, lane * 16, po + 3072);
      }
      STAMP(3);
      BAR();
      __builtin_amdgcn_s_sleep(DLY_H1);
      {
        const int blk = w & 1, half = w >> 1;
        float q0, q1;
        for (int n = 0;; ++n) {
          v4u p[4];
#pragma unroll
          for (int jj = 0; jj < 4; ++jj) p[jj] = hl4(rws, lane * 16, WS_H1 + ((hd * 4 + jj) * 4 + blk * 2 + half) * 1024);
          unsigned bad = 0;
          q0 = 0.f; q1 = 0.f;
#pragma unroll
          for (int jj = 0; jj < 4; ++jj) {
            bad |= (p[jj].x ^ tlo) | (p[jj].z ^ tlo) | (((p[jj].y ^ t8) | (p[jj].w ^ t8)) & 0xFFu);
            q0 += gdec(p[jj].y); q1 += gdec(p[jj].w);
          }
          if (__all(bad == 0) || n >= GSPIN) break;
        }
        VMCNT(0);
        const unsigned qp = pk(q0, q1);
        const int d0 = 32 * g + 16 * half + 4 * blk + cp;
        *(LAS u16*)(L + LP_QB + bj * 320 + d0 * 2) = (u16)qp;
        *(LAS u16*)(L + LP_QB + bj * 320 + (d0 + 8) * 2) = (u16)(qp >> 16);
      }
      STAMP(4);
      BAR();
      STAMP(5);
      {
        v4u qf[4];
#pragma unroll
        for (int s = 0; s < 4; ++s) qf[s] = *(LAS v4u*)(L + LP_QB + cp * 320 + s * 64 + g * 16);
        const int fr = ((i16 & 3) << 2) | (i16 >> 2);
#pragma unroll
        for (int t = 0; t < 2; ++t) {
          f32x4 acc = {0.f, 0.f, 0.f, 0.f};
          const int rt = 2 * w + t;
#pragma unroll
          for (int s = 0; s < 4; ++s) {
            const v4u A = *(LAS v4u*)(L + LP_UK + (16 * rt + i16) * 256 + (((4 * s + g) ^ fr) * 16));
            acc = mfma16(A, qf[s], acc);
          }
          const float sc = 0.08838834764831845f * 1.4426950408889634f;
          if (i16 < 4) *(LAS v2u*)(L + LP_QO + i16 * 256 + (16 * rt + 4 * g) * 2) = v2u{pk(acc[0] * sc, acc[1] * sc), pk(acc[2] * sc, acc[3] * sc)};
        }
      }
      BAR();
      if (w == 0) {
        const v4u v = *(LAS v4u*)(L + LP_QO + lane * 16);
        st4(v4u{lsbput(v.x, lsbtag(tlo, thi, 4 * i16)), lsbput(v.y, lsbtag(tlo, thi, 4 * i16 + 1)), lsbput(v.z, lsbtag(tlo, thi, 4 * i16 + 2)),
                lsbput(v.w, lsbtag(tlo, thi, 4 * i16 + 3))}, rws, g * 32768 + i16 * 16, WS_QA + hd * 1024 + jq * 256);
      }
      STAMP(6);
      BAR();
      const rsrc_t r2 = mk(w2, 4608 * 576 * 2);
      const int so2 = (xg * 576 + 36 * qq) * 1152;
      v4u a2[5][2];
      v2u a8[2][2];
      {
        const int vo4 = 2 * g * 1152 + i16 * 16, vo4m = g < 2 ? vo4 : OOB;
        const int vo8 = 2 * g * 1152 + i16 * 8 + 1024, vo8m = (g < 2 && w == 0) ? vo8 : OOB;
#pragma unroll
        for (int o = 0; o < 5; ++o)
#pragma unroll
          for (int r = 0; r < 2; ++r) a2[o][r] = ld4<NT_W>(r2, o < 4 ? vo4 : vo4m, so2 + (8 * o + r) * 1152 + 256 * w);
#pragma unroll
        for (int r = 0; r < 2; ++r) { a8[0][r] = ld2<NT_W>(r2, vo8, so2 + (8 * w + r) * 1152); a8[1][r] = ld2<NT_W>(r2, vo8m, so2 + (32 + r) * 1152); }
        SCHED();
      }
      STAMP(7);
      BAR();
      v4u X[5], Y[5];
#pragma unroll
      for (int o = 0; o < 5; ++o) {
        X[o] = v4u{a2[o][0].x, a2[o][0].y, a2[o][1].x, a2[o][1].y};
        Y[o] = v4u{a2[o][0].z, a2[o][0].w, a2[o][1].z, a2[o][1].w};
        asm volatile("" : "+v"(X[o]), "+v"(Y[o]));
      }
      STAMP(8);
      BAR();
      STAMP(9);
      {
        const v4u h4 = *(LAS v4u*)(L + LP_HP + (bj * 4 + g) * 48);
        const unsigned h5 = *(LAS unsigned*)(L + LP_HP + (bj * 4 + g) * 48 + 16);
        const unsigned hw = *(LAS unsigned*)(L + LP_HP + (bj * 4 + g) * 48 + 4 * w);
        f32x4 c8 = {0.f, 0.f, 0.f, 0.f}, cx = c8, cy = c8;
        c8 = mfma16(v4u{a8[0][0].x, a8[0][0].y, a8[0][1].x, a8[0][1].y}, bdiag(hw, sel), c8);
        c8 = mfma16(v4u{a8[1][0].x, a8[1][0].y, a8[1][1].x, a8[1][1].y}, bdiag(h5, sel), c8);
#pragma unroll
        for (int o = 0; o < 5; ++o) {
          const v4u B = bdiag(o == 0 ? h4.x : o == 1 ? h4.y : o == 2 ? h4.z : o == 3 ? h4.w : h5, sel);
          cx = mfma16(X[o], B, cx);
          cy = mfma16(Y[o], B, cy);
        }
        *(LAS f32x4*)(L + LP_B8 + w * 1024 + lane * 16) = c8;
        st4(tag8(cx, tb), rws, vo5, so5 + (2 * w) * 8 * 32768);
        st4(tag8(cy, tb), rws, vo5, so5 + (2 * w + 1) * 8 * 32768);
        BAR();
      }
      STAMP(10);
    } else {
      const int v = w - 4;
      {
        const v4u x0 = ld4<0>(rx, lane * 16, v * 1152), x1 = ld4<0>(rx, lane < 8 ? lane * 16 : OOB, v * 1152 + 1024);
        v2u wv_;
        if (v == 0) wv_ = hl2(rws, 0, WS_W);
        SCHED();
        RBAR();
        if (v == 0) PUT_TAG(LP_TAG);
        float f[16], s = 0.f;
#pragma unroll
        for (int k = 0; k < 4; ++k) {
          f[2 * k] = bf2f(x0[k]); f[2 * k + 1] = __uint_as_float(x0[k] & 0xFFFF0000u);
          f[8 + 2 * k] = bf2f(x1[k]); f[8 + 2 * k + 1] = __uint_as_float(x1[k] & 0xFFFF0000u);
        }
#pragma unroll
        for (int k = 0; k < 16; ++k) s += f[k];
        const float mean = x32_sum(x16_sum(row_sum(s))) * (1.0f / 576.0f);
        float q0 = 0.f, q1 = 0.f;
#pragma unroll
        for (int k = 0; k < 8; ++k) { f[k] -= mean; f[8 + k] -= mean; q0 = fmaf(f[k], f[k], q0); q1 = fmaf(f[8 + k], f[8 + k], q1); }
        const float rstd = 1.0f / sqrtf(x32_sum(x16_sum(row_sum(lane < 8 ? q0 + q1 : q0))) * (1.0f / 576.0f) + 1e-5f);
#pragma unroll
        for (int hf = 0; hf < 2; ++hf) {
          if (hf == 0 || lane < 8) {
            const int o = lane + 64 * hf;
#pragma unroll
            for (int k = 0; k < 4; ++k) {
              const unsigned p = pk(f[8 * hf + 2 * k] * rstd, f[8 * hf + 2 * k + 1] * rstd);
              *(LAS unsigned*)(L + LP_XP + ((v * 4 + k) * 76 + o) * 4) = p;
              *(LAS unsigned*)(L + LP_XD + ((v * 4 + ((2 * k) & 3)) * 148 + 2 * o + (k >> 1)) * 4) = __builtin_amdgcn_perm(0u, p, 0x01000100u);
              *(LAS unsigned*)(L + LP_XD + ((v * 4 + ((2 * k + 1) & 3)) * 148 + 2 * o + (k >> 1)) * 4) = __builtin_amdgcn_perm(0u, p, 0x03020302u);
            }
          }
        }
      }
      STAMP(1);
      BAR();
      GET_TAG(LP_TAG);
      const int U0 = (81 * qq) / 144, Qb = 144 * (U0 + 1);
      unsigned xd[21];
#pragma unroll
      for (int t = 0; t < 21; ++t) {
        const int Q = min(81 * qq + 4 * t + v, 81 * qq + 80), qk = Q >= Qb ? Q - Qb : Q - 144 * U0;
        xd[t] = *(LAS unsigned*)(L + LP_XD + ((bj * 4 + g) * 148 + qk) * 4);
      }
      const GAS char* wb = (const GAS char*)(i16 < 8 ? w1 : w3) + (xg * 576 * 2 + (i16 & 7) * 16 + g * 9216);
      const unsigned m0 = cp == 0 ? 0xFFFFu : cp == 1 ? 0xFFFF0000u : 0u, m1 = cp == 2 ? 0xFFFFu : cp == 3 ? 0xFFFF0000u : 0u;
      f32x4 acc[4];
#pragma unroll
      for (int k = 0; k < 4; ++k) acc[k] = f32x4{0.f, 0.f, 0.f, 0.f};
      v4u a[21];
      BAR();
      BAR();
      STAMP(2);
#pragma unroll
      for (int t = -PACE_G; t < 21; ++t) {
        const int tl = t + PACE_G;
        if (tl < 21) {
          const int Q = 81 * qq + 4 * tl + v, u = Q >= Qb ? U0 + 1 : U0, qk = Q - 144 * u;
          if (tl < 20 || v == 0) {
            const GAS v4u* p = (const GAS v4u*)(wb + (unsigned)(qk * 4 * 9216 + u * 128));
            a[tl] = NT_W ? __builtin_nontemporal_load(p) : *p;
          } else a[tl] = v4u{0u, 0u, 0u, 0u};
          SCHED();
        }
        if (t == -1) BAR();
        if (tl == 20) { STAMP(3); BAR(); }
        if (t >= 0) {
          const int Q = 81 * qq + 4 * t + v;
          const v2u B = {xd[t] & m0, xd[t] & m1};
          if (Q < Qb) { acc[0] = mfma16k16(v2u{a[t].x, a[t].y}, B, acc[0]); acc[1] = mfma16k16(v2u{a[t].z, a[t].w}, B, acc[1]); }
          else        { acc[2] = mfma16k16(v2u{a[t].x, a[t].y}, B, acc[2]); acc[3] = mfma16k16(v2u{a[t].z, a[t].w}, B, acc[3]); }
          SCHED();
        }
      }
#pragma unroll
      for (int k = 0; k < 4; ++k) *(LAS f32x4*)(L + LP_RED + (v * 4 + k) * 1024 + lane * 16) = acc[k];
      STAMP(4);
      BAR();
      {
        f32x4 r = *(LAS f32x4*)(L + LP_RED + v * 1024 + lane * 16);
#pragma unroll
        for (int k = 1; k < 4; ++k) r += *(LAS f32x4*)(L + LP_RED + (k * 4 + v) * 1024 + lane * 16);
        const int po = WS_H7 + idx * 8192 + v * 2048;
        st4(v4u{tlo, genc(r[0], t8), tlo, genc(r[1], t8)}, rws, lane * 16, po);
        st4(v4u{tlo, genc(r[2], t8), tlo, genc(r[3], t8)}, rws, lane * 16, po + 1024);
      }
      STAMP(5);
      {
        const int t = v * 64 + lane;
        const int b = t / 36, cl = t - 36 * b, f = 36 * qq + cl, u = f >> 6, cu = f & 63;
        const int ig = cu >> 3, hh = (cu >> 2) & 1, reg = ig & 3;
        const int r0 = (144 * u) / 81, r1 = (144 * u + 143) / 81;
        const int lo = hh * 2048 + (reg >> 1) * 1024 + ((ig >> 2) * 16 + b * 4 + (cu & 3)) * 16 + (reg & 1) * 8;
        int off[3];
        unsigned use[3];
#pragma unroll
        for (int k = 0; k < 3; ++k) {
          const int rr = r0 + k;
          const bool on = t < 144 && rr <= r1;
          off[k] = on ? (xg * 16 + rr) * 8192 + (u - (81 * rr) / 144) * 4096 + lo : OOB;
          use[k] = on ? 0xFFFFFFFFu : 0u;
        }
        __builtin_amdgcn_s_sleep(DLY_H7);
        float gs, us;
        for (int n = 0;; ++n) {
          v2u pg[3], pu[3];
#pragma unroll
          for (int k = 0; k < 3; ++k) { pg[k] = hl2(rws, off[k], WS_H7); pu[k] = hl2(rws, off[k], WS_H7 + 512); }
          unsigned bad = 0;
          gs = 0.f; us = 0.f;
#pragma unroll
          for (int k = 0; k < 3; ++k) {
            bad |= ((pg[k].x ^ tlo) | (pu[k].x ^ tlo) | (((pg[k].y ^ t8) | (pu[k].y ^ t8)) & 0xFFu)) & use[k];
            gs += gdec(pg[k].y); us += gdec(pu[k].y);
          }
          if (__all(bad == 0) || n >= GSPIN) break;
        }
        const float hv = gs / (1.0f + __expf(-gs)) * us;
        if (t < 144) *(LAS u16*)(L + LP_HP + ((b * 4 + ((cl >> 1) & 3)) * 12 + (cl >> 3)) * 4 + (cl & 1) * 2) = (u16)pk(hv, hv);
        else if (t < 152) *(LAS unsigned*)(L + LP_HP + ((((t - 144) >> 1) * 4 + 2 + (t & 1)) * 12 + 4) * 4) = 0u;
      }
      STAMP(6);
      BAR();
      BAR();
      if (v == 0) {
        f32x4 s = *(LAS f32x4*)(L + LP_B8 + lane * 16);
#pragma unroll
        for (int k = 1; k < 4; ++k) s += *(LAS f32x4*)(L + LP_B8 + k * 1024 + lane * 16);
        st4(tag8(s, tb), rws, vo5, so5 + 8 * 8 * 32768);
      }
      STAMP(7);
    }
    STAMP_OUT();
    return;
  }

  const int ab = xg >> 1;
  const int r32 = lane & 31, hh = lane >> 5;
  const int so5 = WS_H5 + (2 * idx + 1) * 128;
  v2u av[8][2];
  v2u wa_;
  asm volatile("buffer_load_dwordx2 %0, off, %1, 0 sc1" : "=v"(wa_) : "s"(rws) : "memory");
  if (w < 4) {
    const rsrc_t rc = mk(ckv, 4 * 4096 * 512 * 2);
    {
      int vo[4];
#pragma unroll
      for (int k = 0; k < 4; ++k) vo[k] = g * 1024 + ((i16 ^ ((g << 2) | k)) * 16);
      const int so = (idx * 128 + 32 * w) * 1024;
#pragma unroll
      for (int n = 0; n < 32; ++n) {
        if (n >= PACE_C) VMCNT(PACE_C - 1);
        dma16(rc, vo[n & 3], so + (n & 7) * 4096 + (n >> 3) * 256, lbase + (n >> 3) * 32768 + (32 * w + 4 * (n & 7)) * 256);
        if (n == KQA) BAR();
      }
    }
  } else {
    VMCNT(0);
    BAR();
  }
  asm volatile("" : "+v"(wa_));
  const u64 tag_ = (((u64)wa_.y << 32) | wa_.x) + 0x9E3779B97F4A7C15ull;
  const unsigned tlo = (unsigned)tag_, thi = (unsigned)(tag_ >> 32), t8 = thi & 0xFFu;
  const unsigned tb[4] = {tlo & 0xFFu, (tlo >> 8) & 0xFFu, (tlo >> 16) & 0xFFu, tlo >> 24};
  unsigned e4[4];
#pragma unroll
  for (int k = 0; k < 4; ++k) e4[k] = lsbtag(tlo, thi, 4 * i16 + k);
  if (w < 4) {
    v4u fa[32];
    {
      const int fr = ((r32 & 3) << 2) | ((r32 >> 2) & 3);
      const int ca = (32 * w + r32) * 256;
#define QK_READ(Q) _Pragma("unroll") for (int k = 0; k < 8; ++k) fa[(Q)*8 + k] = *(LAS v4u*)(L + (Q)*32768 + ca + (((2 * k + hh) ^ fr) * 16)); SCHED();
      const rsrc_t rv = mk(wuv, 512 * 4096 * 2);
      const int sov = (128 * jq + 16 * (w >> 1)) * 8192 + hd * 256 + (w & 1) * 128;
      const int vov[2] = {(32 * ((2 * g) & 3) + (g >> 1)) * 8192 + i16 * 8, (32 * ((2 * g + 1) & 3) + (g >> 1)) * 8192 + i16 * 8};
#define AV_LOAD(A, B) _Pragma("unroll") for (int n = (A); n < (B); ++n) av[n >> 1][n & 1] = ld2<NT_W>(rv, vov[n & 1], sov + 2 * (n >> 1) * 8192); SCHED();
      VMCNT(8); QK_READ(0) QK_READ(1) QK_READ(2)
      VMCNT(4); AV_LOAD(0, 8)
      VMCNT(8); QK_READ(3)
      AV_LOAD(8, 16)
    }
    int qb[4];
#pragma unroll
    for (int k = 0; k < 4; ++k) { qb[k] = LA_QA + ((lane ^ ((2 * k + hh) & 7)) * 16); asm volatile("" : "+v"(qb[k])); }
    STAMP(2);
    BAR();
    STAMP(3);
    f32x16 sacc;
#pragma unroll
    for (int k = 0; k < 16; ++k) sacc[k] = 0.f;
    {
      v4u fb[32];
#pragma unroll
      for (int s = 0; s < 6; ++s) fb[s] = *(LAS v4u*)(L + qb[s & 3] + s * 1024);
      SCHED();
#pragma unroll
      for (int s = 0; s < 32; ++s) {
        if (s + 6 < 32) fb[s + 6] = *(LAS v4u*)(L + qb[(s + 6) & 3] + (s + 6) * 1024);
        sacc = mfma32(fa[s], fb[s], sacc);
        SCHED();
        if (s == 12) { RBAR(); SCHED(); }
      }
    }
    const int q4 = i16 >> 2, lowc = 2 * (g & 1) + (cp >> 1);
    int ta[4][2];
#pragma unroll
    for (int t = 0; t < 4; ++t)
#pragma unroll
      for (int t2 = 0; t2 < 2; ++t2) {
        ta[t][t2] = w * 32768 + (8 * t2 + 4 * hh + q4) * 256 + ((((t ^ q4) << 2) | (lowc ^ (2 * t2 + hh))) * 16) + 8 * (cp & 1);
        asm volatile("" : "+v"(ta[t][t2]));
      }
    v4u cb[32];
#define TR_READ(n) do { const v2u x0_ = __builtin_bit_cast(v2u, __builtin_amdgcn_ds_read_tr16_b64_v4i16((LAS s16x4*)(L + ta[(n) >> 3][0] + ((n) & 7) * 4096))); \
    const v2u x1_ = __builtin_bit_cast(v2u, __builtin_amdgcn_ds_read_tr16_b64_v4i16((LAS s16x4*)(L + ta[(n) >> 3][1] + ((n) & 7) * 4096)));                 \
    cb[n] = v4u{x0_.x, x0_.y, x1_.x, x1_.y}; } while (0)
#pragma unroll
    for (int n = 0; n < 6; ++n) TR_READ(n);
    SCHED();
    float mw = sacc[0];
#pragma unroll
    for (int k = 1; k < 16; ++k) mw = fmaxf(mw, sacc[k]);
    mw = x32_max(mw);
    if (lane < 32) *(LAS float*)(L + LA_MX + (r32 * 4 + w) * 4) = mw;
    STAMP(4);
    BAR();
    const f32x4 m4 = *(LAS f32x4*)(L + LA_MX + r32 * 16);
    const float mx = fmaxf(fmaxf(m4[0], m4[1]), fmaxf(m4[2], m4[3]));
    float ls = 0.f;
#pragma unroll
    for (int k = 0; k < 16; ++k) { sacc[k] = __builtin_amdgcn_exp2f(sacc[k] - mx); ls += sacc[k]; }
#pragma unroll
    for (int s2 = 0; s2 < 2; ++s2)
      *(LAS v4u*)(L + LA_PB + (2 * w + s2) * 1024 + lane * 16) =
          v4u{pk(sacc[8 * s2], sacc[8 * s2 + 1]), pk(sacc[8 * s2 + 2], sacc[8 * s2 + 3]), pk(sacc[8 * s2 + 4], sacc[8 * s2 + 5]), pk(sacc[8 * s2 + 6], sacc[8 * s2 + 7])};
    ls = x32_sum(ls);
    if (lane < 32) *(LAS float*)(L + LA_SUM + (r32 * 4 + w) * 4) = ls;
    BAR();
    STAMP(5);
    {
      v4u pf[8];
#pragma unroll
      for (int k = 0; k < 8; ++k) pf[k] = *(LAS v4u*)(L + LA_PB + k * 1024 + lane * 16);
      if (w == 0) {
        const f32x4 l4 = *(LAS f32x4*)(L + LA_SUM + r32 * 16);
        st4(v4u{tlo, genc(mx, t8), tlo, genc((l4[0] + l4[1]) + (l4[2] + l4[3]), t8)}, rws, lane < 32 ? r32 * 16 : OOB, WS_ML + idx * 512);
      }
      SCHED();
      f32x16 o[4];
#pragma unroll
      for (int t = 0; t < 4; ++t)
#pragma unroll
        for (int k = 0; k < 16; ++k) o[t][k] = 0.f;
#pragma unroll
      for (int n = 0; n < 32; ++n) {
        if (n + 6 < 32) TR_READ(n + 6);
        o[n >> 3] = mfma32(pf[n & 7], cb[n], o[n >> 3]);
        SCHED();
      }
      STAMP(1);
      const unsigned e0 = lsbtag(tlo, thi, 2 * r32), e1 = lsbtag(tlo, thi, 2 * r32 + 1);
      const int vo = hh * 4 * 131072 + r32 * 8, so = WS_H3 + (w * 128 + idx) * 256;
#pragma unroll
      for (int k = 0; k < 16; ++k)
        st2(v2u{lsbput(pk(o[0][k], o[1][k]), e0), lsbput(pk(o[2][k], o[3][k]), e1)}, rws, vo, so + ((k & 3) + 8 * (k >> 2)) * 131072);
    }
    STAMP(6);
    {
      v4u ml;
      for (int n = 0;; ++n) {
        ml = hl4(rws, r32 * 512, WS_ML + w * 32 * 512 + hd * 16);
        if (__all(((ml.x ^ tlo) | (ml.z ^ tlo) | (((ml.y ^ t8) | (ml.w ^ t8)) & 0xFFu)) == 0) || n >= GSPIN) break;
      }
      if (DLY_H3) __builtin_amdgcn_s_sleep(DLY_H3);
      STAMP(7);
      v4u d[8];
#pragma unroll
      for (int k = 0; k < 8; ++k) d[k] = hl4(rws, lane * 16, WS_H3 + idx * 32768 + w * 8192 + k * 1024);
      SCHED();
      const float m = gdec(ml.y), M = x16_max(row_max(m));
      const float e = __builtin_amdgcn_exp2f(m - M);
      const float al = e / x16_sum(row_sum(e * gdec(ml.w)));
      const unsigned ah = pk(al, al) & 0xFFFFu;
      const unsigned hl = ah | (pk(al - bf2f(ah), 0.f) << 16);
      const int e7 = lane & 7;
      const unsigned src = (lane & 8) ? 0x0302u : 0x0100u, s1 = (e7 & 1) ? ((src << 16) | 0x0C0Cu) : (0x0C0C0000u | src);
      unsigned s8[4];
#pragma unroll
      for (int k = 0; k < 4; ++k) s8[k] = (e7 >> 1) == k ? s1 : 0x0C0C0C0Cu;
      v4u bk[8];
#pragma unroll
      for (int k = 0; k < 8; ++k) { bk[k] = bdiag(__builtin_amdgcn_ds_bpermute((4 * k + g) * 4, hl), s8); asm volatile("" : "+v"(bk[k])); }
#pragma unroll 1
      for (int n = 0; n < GSPIN; ++n) {
        unsigned bad = 0;
#pragma unroll
        for (int k = 0; k < 8; ++k) bad |= xor4(d[k], e4);
        if (__all((bad & LSBM) == 0)) break;
#pragma unroll
        for (int k = 0; k < 8; ++k) d[k] = hl4(rws, lane * 16, WS_H3 + idx * 32768 + w * 8192 + k * 1024);
      }
      STAMP(8);
      f32x4 c = {0.f, 0.f, 0.f, 0.f};
#pragma unroll
      for (int k = 0; k < 8; ++k) c = mfma16(d[k], bk[k], c);
#pragma unroll
      for (int r = 0; r < 4; ++r) {
        const float y = c[r] + dppf<0x128, 0xf>(c[r], c[r]);
        if (!(lane & 8)) *(LAS u16*)(L + LA_CTX + ((w * 4 + (e7 >> 1)) * 20 + 4 * g + r) * 4 + 2 * (e7 & 1)) = (u16)pk(y, y);
      }
    }
    BAR();
    {
      const int half = w >> 1, blk = w & 1;
      const v4u p0 = *(LAS v4u*)(L + LA_CTX + ((bj * 4 + g) * 20 + 8 * half) * 4), p1 = *(LAS v4u*)(L + LA_CTX + ((bj * 4 + g) * 20 + 8 * half + 4) * 4);
      f32x4 c = {0.f, 0.f, 0.f, 0.f};
#pragma unroll
      for (int o = 0; o < 8; ++o) c = mfma16(v4u{av[o][0].x, av[o][0].y, av[o][1].x, av[o][1].y}, bdiag(o < 4 ? p0[o & 3] : p1[o & 3], sel), c);
#pragma unroll
      for (int r = 0; r < 4; ++r)
        *(LAS float*)(L + LA_UV + half * 2304 + ((bj * 4 + 2 * (r & 1) + (cp >> 1)) * 36 + (8 * blk + 2 * g + (r >> 1)) * 2 + (cp & 1)) * 4) = c[r];
    }
    BAR();
    STAMP(9);
    BAR();
    if (w == 0) {
      f32x4 s = *(LAS f32x4*)(L + LA_B8 + lane * 16);
#pragma unroll
      for (int k = 1; k < 4; ++k) s += *(LAS f32x4*)(L + LA_B8 + k * 1024 + lane * 16);
      st4(tag8(s, tb), rws, vo5, so5 + 8 * 8 * 32768);
    }
  } else {
    const int v = w - 4;
    STAMP(1);
    {
      v4u qa[8];
      for (int n = 0;; ++n) {
#pragma unroll
        for (int k = 0; k < 8; ++k) qa[k] = hl4(rws, lane * 16, WS_QA + (ab * 32 + 8 * v + k) * 1024);
        unsigned bad = 0;
#pragma unroll
        for (int k = 0; k < 8; ++k) bad |= xor4(qa[k], e4);
        if (__all((bad & LSBM) == 0) || n >= GSPIN) break;
        __builtin_amdgcn_s_sleep(DLY_R2);
      }
#pragma unroll
      for (int k = 0; k < 8; ++k) *(LAS v4u*)(L + LA_QA + (lane >> 1) * 1024 + ((((lane & 1) * 32 + 8 * v + k) ^ (lane & 7)) * 16)) = qa[k];
    }
    STAMP(2);
    BAR();
    const rsrc_t ro = mk(wo, 4096 * 576 * 2);
    v4u ao[16][2];
    v2u a8[4][2];
    {
      const int vo4 = 2 * g * 1152 + i16 * 16, vo8 = 2 * g * 1152 + i16 * 8 + 1024;
#pragma unroll
      for (int o = 0; o < 16; ++o) {
        const int op = (o + 4 * jq) & 15;
        if (o > 4) VMCNT(PACE_O - 2);
#pragma unroll
        for (int r = 0; r < 2; ++r) ao[o][r] = ld4<NT_WO>(ro, vo4, (hd * 128 + 8 * op + r) * 1152 + 256 * v);
        if ((o & 3) == 3) {
          const int o8 = 4 * (((o >> 2) + jq) & 3) + v;
#pragma unroll
          for (int r = 0; r < 2; ++r) a8[o >> 2][r] = ld2<NT_WO>(ro, vo8, (hd * 128 + 8 * o8 + r) * 1152);
        }
        SCHED();
        if (o == 3) { RBAR(); RBAR(); RBAR(); }
      }
    }
    STAMP(3);
    v4u X[16], Y[16];
#pragma unroll
    for (int o = 0; o < 16; ++o) {
      X[o] = v4u{ao[o][0].x, ao[o][0].y, ao[o][1].x, ao[o][1].y};
      Y[o] = v4u{ao[o][0].z, ao[o][0].w, ao[o][1].z, ao[o][1].w};
      asm volatile("" : "+v"(X[o]), "+v"(Y[o]));
    }
    VMCNT(0);
    STAMP(4);
    BAR();
    BAR();
    STAMP(5);
    {
      unsigned P[16];
#pragma unroll
      for (int q = 0; q < 4; ++q) {
        const int ad = LA_UV + ((bj * 4 + g) * 36 + 8 * ((q + jq) & 3)) * 4;
        const f32x4 x0 = *(LAS f32x4*)(L + ad) + *(LAS f32x4*)(L + ad + 2304), x1 = *(LAS f32x4*)(L + ad + 16) + *(LAS f32x4*)(L + ad + 16 + 2304);
        P[4 * q] = pk(x0[0], x0[1]); P[4 * q + 1] = pk(x0[2], x0[3]); P[4 * q + 2] = pk(x1[0], x1[1]); P[4 * q + 3] = pk(x1[2], x1[3]);
      }
      f32x4 c8 = {0.f, 0.f, 0.f, 0.f}, cx = c8, cy = c8;
#pragma unroll
      for (int q = 0; q < 4; ++q) {
        const unsigned pv = v == 0 ? P[4 * q] : v == 1 ? P[4 * q + 1] : v == 2 ? P[4 * q + 2] : P[4 * q + 3];
        c8 = mfma16(v4u{a8[q][0].x, a8[q][0].y, a8[q][1].x, a8[q][1].y}, bdiag(pv, sel), c8);
      }
      SCHED();
#pragma unroll
      for (int o = 0; o < 16; ++o) {
        const v4u B = bdiag(P[o], sel);
        cx = mfma16(X[o], B, cx);
        cy = mfma16(Y[o], B, cy);
        if (o == 1) { *(LAS f32x4*)(L + LA_B8 + v * 1024 + lane * 16) = c8; SCHED(); BAR(); }
      }
      st4(tag8(cx, tb), rws, vo5, so5 + (2 * v) * 8 * 32768);
      st4(tag8(cy, tb), rws, vo5, so5 + (2 * v + 1) * 8 * 32768);
    }
    STAMP(6);
  }
  if (qq < 9) {
    const int e = lane & 31, Ld = 8 * xg + (e >> 2), rr = e & 3;
    const int ob = (Ld & 15) >> 2, oc = qq < 8 ? 128 * (qq >> 1) + 8 * (4 * (Ld >> 4) + rr) + 4 * (qq & 1) + (Ld & 3) : 512 + 4 * (4 * (Ld >> 4) + rr) + (Ld & 3);
    unsigned xres = 0;
    if (w == 0) xres = ((const GAS u16*)xin)[ob * 576 + oc];
    BAR();
    __builtin_amdgcn_s_sleep(DLY_H5);
    STAMP(10);
    f32x4 s;
    for (int n = 0;; ++n) {
      v4u d[4];
#pragma unroll
      for (int k = 0; k < 4; ++k) d[k] = hl4(rws, lane * 16, WS_H5 + (qq * 8 + xg) * 32768 + (w * 4 + k) * 1024);
      const unsigned bad = (xor4(d[0], tb) | xor4(d[1], tb)) | (xor4(d[2], tb) | xor4(d[3], tb));
      s = (__builtin_bit_cast(f32x4, d[0]) + __builtin_bit_cast(f32x4, d[1])) + (__builtin_bit_cast(f32x4, d[2]) + __builtin_bit_cast(f32x4, d[3]));
      if (__all((bad & 0xFFu) == 0) || n >= GSPIN) break;
    }
#pragma unroll
    for (int r = 0; r < 4; ++r) s[r] = x32_sum(x16_sum(s[r] + dppf<0x128, 0xf>(s[r], s[r])));
    if (lane < 8) *(LAS f32x4*)(L + LA_FIN + w * 128 + lane * 16) = s;
    BAR();
    if (w == 0) {
      float y = bf2f(xres);
#pragma unroll
      for (int k = 0; k < 8; ++k) y += *(LAS float*)(L + LA_FIN + k * 128 + e * 4);
      if (lane < 32) ((GAS u16*)out)[ob * 576 + oc] = (u16)pk(y, y);
      if (qq == 0 && xg == 0) st2(v2u{tlo, thi}, rws, lane == 0 ? 0 : OOB, WS_W);
    }
    STAMP(11);
  }
  STAMP_OUT();
}

__global__ void k_e(char* ws){ if(ws==nullptr) __builtin_trap(); }
__global__ void k_st(char* ws){ if(threadIdx.x==0) *(u64*)(ws+WS_STAMP+(1<<20))=1; }
__global__ void k_st1(char* ws){ if(threadIdx.x==0) __hip_atomic_store((u64*)(ws+WS_STAMP+(1<<20)),1ull,__ATOMIC_RELAXED,__HIP_MEMORY_SCOPE_AGENT); }
__global__ void k_ld(char* ws){ if(*(volatile u64*)(ws+WS_STAMP+(1<<20))==0x1234567) __builtin_trap(); }
extern "C" void kernel_launch(void* const* d_in, const int* in_sizes, int n_in, void* d_out, int out_size, void* d_ws, size_t ws_size,
                              hipStream_t stream) {
  (void)hipFuncSetAttribute((const void*)fused_decode, hipFuncAttributeMaxDynamicSharedMemorySize, LDS_BYTES);
  fused_decode<<<256, 512, LDS_BYTES, stream>>>((const u16*)d_in[0], (const u16*)d_in[1], (const u16*)d_in[2], (const u16*)d_in[3],
                                                (const u16*)d_in[4], (const u16*)d_in[5], (const u16*)d_in[7], (const u16*)d_in[8],
                                                (const u16*)d_in[9], (u16*)d_out, (char*)d_ws);
}
```

```cpp
#define EXTRA 3
#include <hip/hip_runtime.h>
#include <stdint.h>

typedef unsigned short u16;
typedef unsigned long long u64;
typedef unsigned v4u __attribute__((ext_vector_type(4)));
typedef unsigned v2u __attribute__((ext_vector_type(2)));
typedef float f32x2 __attribute__((ext_vector_type(2)));
typedef float f32x4 __attribute__((ext_vector_type(4)));
typedef float f32x16 __attribute__((ext_vector_type(16)));
typedef short s16x4 __attribute__((ext_vector_type(4)));
typedef __bf16 bf16x8 __attribute__((ext_vector_type(8)));
typedef __bf16 bf16x2 __attribute__((ext_vector_type(2)));
typedef __amdgpu_buffer_rsrc_t rsrc_t;
#define GAS __attribute__((address_space(1)))
#define LAS __attribute__((address_space(3)))
#define DI __device__ __forceinline__

#define NT_W 2
#define NT_WO 0
#define PACE_C 8
#define DLY_H1 4
#define KQA 31
#define PACE_K 4
#define DLY_K 8
#define PACE_G 5
#define PACE_O 8
#define DLY_R2 8
#define DLY_H3 0
#define DLY_H7 8
#define DLY_H5 8

constexpr int SC1 = 16;
constexpr int GSPIN = 1 << 13;

constexpr int WS_W = 0;
constexpr int WS_H1 = 4096;
constexpr int WS_QA = WS_H1 + 128 * 4096;
constexpr int WS_ML = WS_QA + 131072;
constexpr int WS_H7 = WS_ML + 65536;
constexpr int WS_H5 = WS_H7 + 128 * 8192;
constexpr int WS_H3 = WS_H5 + 72 * 256 * 128;
constexpr int WS_END = WS_H3 + 128 * 32768;
constexpr int WS_STAMP = WS_END;
constexpr int OOB = (int)0x80000000;

constexpr int LP_TAG = 4992, LP_XP = 0, LP_XD = 5120, LP_QB = 15360, LP_QO = 16896, LP_RED = 18432, LP_HP = 34816, LP_B8 = 35840, LP_UK = 40960;
constexpr int LA_QA = 131072, LA_MX = LA_QA;
constexpr int LA_PB = LA_QA + 1024, LA_SUM = LA_QA + 512, LA_CTX = LA_QA + 9216, LA_UV = LA_QA + 11264, LA_B8 = LA_QA + 16384,
              LA_FIN = LA_QA + 20480;
constexpr int LDS_BYTES = 163840;

DI rsrc_t mk(const void* p, int bytes) { return __builtin_amdgcn_make_buffer_rsrc((void*)p, (short)0, bytes, 0x00020000); }
template <int AUX> DI v4u ld4(rsrc_t r, int vo, int so) { return __builtin_amdgcn_raw_buffer_load_b128(r, vo, so, AUX); }
template <int AUX> DI v2u ld2(rsrc_t r, int vo, int so) { return __builtin_amdgcn_raw_buffer_load_b64(r, vo, so, AUX); }
DI v4u hl4(rsrc_t r, int vo, int so) { asm volatile("" : "+v"(vo)); return __builtin_amdgcn_raw_buffer_load_b128(r, vo, so, SC1); }
DI v2u hl2(rsrc_t r, int vo, int so) { asm volatile("" : "+v"(vo)); return __builtin_amdgcn_raw_buffer_load_b64(r, vo, so, SC1); }
DI void st4(v4u v, rsrc_t r, int vo, int so) { __builtin_amdgcn_raw_buffer_store_b128(v, r, vo + so, 0, SC1); }
DI void st2(v2u v, rsrc_t r, int vo, int so) { __builtin_amdgcn_raw_buffer_store_b64(v, r, vo, so, SC1); }
DI void dma16(rsrc_t r, int vo, int so, unsigned lds) {
  asm volatile("s_mov_b32 m0, %0\n\ts_nop 0\n\tbuffer_load_dwordx4 %1, %2, %3 offen nt lds" ::"s"(lds), "v"(vo), "s"(r), "s"(so) : "memory");
}
#define VMCNT(n) asm volatile("s_waitcnt vmcnt(%0)" ::"n"(n) : "memory")
#define FENCE() asm volatile("" ::: "memory")
#define SCHED() __builtin_amdgcn_sched_barrier(0)

DI float bf2f(unsigned v) { return __uint_as_float(v << 16); }
DI unsigned pk(float a, float b) { f32x2 f = {a, b}; return __builtin_bit_cast(unsigned, __builtin_convertvector(f, bf16x2)); }
DI f32x4 mfma16(v4u a, v4u b, f32x4 c) {
  return __builtin_amdgcn_mfma_f32_16x16x32_bf16(__builtin_bit_cast(bf16x8, a), __builtin_bit_cast(bf16x8, b), c, 0, 0, 0);
}
DI f32x4 mfma16k16(v2u a, v2u b, f32x4 c) {
  return __builtin_amdgcn_mfma_f32_16x16x16bf16_1k(__builtin_bit_cast(s16x4, a), __builtin_bit_cast(s16x4, b), c, 0, 0, 0);
}
DI f32x16 mfma32(v4u a, v4u b, f32x16 c) {
  return __builtin_amdgcn_mfma_f32_32x32x16_bf16(__builtin_bit_cast(bf16x8, a), __builtin_bit_cast(bf16x8, b), c, 0, 0, 0);
}
template <int CTRL, int RM> DI float dppf(float old, float v) {
  return __builtin_bit_cast(float, __builtin_amdgcn_update_dpp(__builtin_bit_cast(int, old), __builtin_bit_cast(int, v), CTRL, RM, 0xf, false));
}
DI float row_sum(float v) {
  v += dppf<0xB1, 0xf>(v, v); v += dppf<0x4E, 0xf>(v, v); v += dppf<0x124, 0xf>(v, v); v += dppf<0x128, 0xf>(v, v);
  return v;
}
DI float row_max(float v) {
  v = fmaxf(v, dppf<0xB1, 0xf>(v, v)); v = fmaxf(v, dppf<0x4E, 0xf>(v, v));
  v = fmaxf(v, dppf<0x124, 0xf>(v, v)); v = fmaxf(v, dppf<0x128, 0xf>(v, v));
  return v;
}
DI float x16_sum(float v) { auto r = __builtin_amdgcn_permlane16_swap(__float_as_uint(v), __float_as_uint(v), false, false); return __uint_as_float(r[0]) + __uint_as_float(r[1]); }
DI float x32_sum(float v) { auto r = __builtin_amdgcn_permlane32_swap(__float_as_uint(v), __float_as_uint(v), false, false); return __uint_as_float(r[0]) + __uint_as_float(r[1]); }
DI float x16_max(float v) { auto r = __builtin_amdgcn_permlane16_swap(__float_as_uint(v), __float_as_uint(v), false, false); return fmaxf(__uint_as_float(r[0]), __uint_as_float(r[1])); }
DI float x32_max(float v) { auto r = __builtin_amdgcn_permlane32_swap(__float_as_uint(v), __float_as_uint(v), false, false); return fmaxf(__uint_as_float(r[0]), __uint_as_float(r[1])); }

#define RBAR() asm volatile("s_barrier" ::: "memory")
#define BAR() asm volatile("s_waitcnt lgkmcnt(0)\n\ts_barrier" ::: "memory")
DI unsigned genc(float v, unsigned t8) { return ((__float_as_uint(v) + 0x80u) & 0xFFFFFF00u) | t8; }
DI float gdec(unsigned hi) { return __uint_as_float(hi & 0xFFFFFF00u); }

#define STAMP(k)
#define STAMP_DECL
#define STAMP_OUT()

DI v4u bdiag(unsigned P, const unsigned (&s)[4]) {
  v4u b;
  b.x = __builtin_amdgcn_perm(P, P, s[0]); b.y = __builtin_amdgcn_perm(P, P, s[1]);
  b.z = __builtin_amdgcn_perm(P, P, s[2]); b.w = __builtin_amdgcn_perm(P, P, s[3]);
  return b;
}
DI void bdiag_sel(int cp, unsigned (&s)[4]) {
  const unsigned lo = (cp & 1) ? 0x01000C0Cu : 0x0C0C0100u, hi = (cp & 1) ? 0x03020C0Cu : 0x0C0C0302u, z = 0x0C0C0C0Cu;
  s[0] = cp < 2 ? lo : z; s[1] = cp < 2 ? z : lo; s[2] = cp < 2 ? hi : z; s[3] = cp < 2 ? z : hi;
}

DI unsigned lsbtag(unsigned tlo, unsigned thi, int p) { return (((p < 32 ? tlo : thi) >> (p & 31)) & 1u) | (((tlo ^ (unsigned)p) & 1u) << 16); }
constexpr unsigned LSBM = 0x00010001u;
DI unsigned lsbput(unsigned v, unsigned e) { return (v & ~LSBM) | e; }
DI unsigned xor4(v4u d, const unsigned (&e)[4]) { return ((d.x ^ e[0]) | (d.y ^ e[1])) | ((d.z ^ e[2]) | (d.w ^ e[3])); }
DI v4u tag8(f32x4 c, const unsigned (&t)[4]) {
  return v4u{(__float_as_uint(c[0]) & 0xFFFFFF00u) | t[0], (__float_as_uint(c[1]) & 0xFFFFFF00u) | t[1],
             (__float_as_uint(c[2]) & 0xFFFFFF00u) | t[2], (__float_as_uint(c[3]) & 0xFFFFFF00u) | t[3]};
}
__global__ void __launch_bounds__(512, 2)
fused_decode(const u16* __restrict__ xin, const u16* __restrict__ ckv, const u16* __restrict__ wq, const u16* __restrict__ wuk,
             const u16* __restrict__ wuv, const u16* __restrict__ wo, const u16* __restrict__ w1, const u16* __restrict__ w3,
             const u16* __restrict__ w2, u16* __restrict__ out, char* ws) {
  extern __shared__ __attribute__((aligned(16))) char smem[];
  LAS char* L = (LAS char*)smem;
  const unsigned lbase = (unsigned)(size_t)smem;
  const int lane = threadIdx.x & 63, w = __builtin_amdgcn_readfirstlane(threadIdx.x >> 6);
  const int xg = blockIdx.x & 7, qd_ = blockIdx.x >> 3, qq = qd_ & 15, idx = xg * 16 + qq, hd = idx >> 2, jq = idx & 3;
  const int i16 = lane & 15, g = lane >> 4, bj = i16 >> 2, cp = lane & 3;
  STAMP_DECL;
  STAMP(0);
  asm volatile("" ::"s"(xin), "s"(ckv), "s"(wq), "s"(wuk), "s"(wuv), "s"(wo), "s"(w1), "s"(w3), "s"(w2), "s"(out), "s"(ws));
  const rsrc_t rws = mk(ws, WS_END);
  const rsrc_t rx = mk(xin, 4 * 576 * 2);
  unsigned sel[4];
  bdiag_sel(cp, sel);
#define LOAD_TAG() const v2u wv_ = hl2(rws, 0, WS_W)
#define PUT_TAG(AD) do { const u64 tag_ = (((u64)wv_.y << 32) | wv_.x) + 0x9E3779B97F4A7C15ull; \
    if (lane == 0) *(LAS v2u*)(L + (AD)) = v2u{(unsigned)tag_, (unsigned)(tag_ >> 32)}; } while (0)
#define GET_TAG(AD) const v2u tg_ = *(LAS v2u*)(L + (AD)); \
  const unsigned tlo = tg_.x, thi = tg_.y, t8 = thi & 0xFFu; \
  const unsigned tb[4] = {tlo & 0xFFu, (tlo >> 8) & 0xFFu, (tlo >> 16) & 0xFFu, tlo >> 24}
  const int vo5 = (lane >> 3) * 32768 + (lane & 7) * 16;

  if (qd_ < 16) {
    const int so5 = WS_H5 + (2 * idx) * 128;
    if (w < 4) {
      v4u a[18][2];
      int vok[4];
      const rsrc_t rk = mk(wuk, 512 * 4096 * 2);
      const int n0 = (w - 2) * 16;
      RBAR();
      if (w == 0) {
        const rsrc_t rq = mk(wq, 576 * 4096 * 2);
        const int vo = 2 * g * 8192 + i16 * 16, so = 144 * jq * 8192 + hd * 256;
#pragma unroll
        for (int o = 0; o < 18; ++o) { a[o][0] = ld4<NT_W>(rq, vo, so + (8 * o) * 8192); a[o][1] = ld4<NT_W>(rq, vo, so + (8 * o + 1) * 8192); }
        SCHED();
      } else if (w >= 2) {
#pragma unroll
        for (int k = 0; k < 4; ++k) vok[k] = g * 8192 + ((i16 ^ ((g << 2) | k)) * 16);
        __builtin_amdgcn_s_sleep(DLY_K);
#pragma unroll
        for (int n = 0; n < 16; ++n) dma16(rk, vok[n & 3], (128 * jq + 4 * (n0 + n)) * 8192 + hd * 256, lbase + LP_UK + (n0 + n) * 1024);
      }
      STAMP(1);
      BAR();
      STAMP(2);
      GET_TAG(LP_TAG);
      if (w == 0) {
        v2u xp[9];
#pragma unroll
        for (int k = 0; k < 9; ++k) xp[k] = *(LAS v2u*)(L + LP_XP + ((bj * 4 + g) * 76 + 18 * jq + 2 * k) * 4);
        f32x4 qx = {0.f, 0.f, 0.f, 0.f}, qy = qx;
#pragma unroll
        for (int o = 0; o < 18; ++o) {
          const v4u B = bdiag((o & 1) ? xp[o >> 1].y : xp[o >> 1].x, sel);
          qx = mfma16(v4u{a[o][0].x, a[o][0].y, a[o][1].x, a[o][1].y}, B, qx);
          qy = mfma16(v4u{a[o][0].z, a[o][0].w, a[o][1].z, a[o][1].w}, B, qy);
        }
        const int po = WS_H1 + idx * 4096;
        st4(v4u{tlo, genc(qx[0], t8), tlo, genc(qx[1], t8)}, rws, lane * 16, po);
        st4(v4u{tlo, genc(qx[2], t8), tlo, genc(qx[3], t8)}, rws, lane * 16, po + 1024);
        st4(v4u{tlo, genc(qy[0], t8), tlo, genc(qy[1], t8)}, rws, lane * 16, po + 2048);
        st4(v4u{tlo, genc(qy[2], t8), tlo, genc(qy[3], t8)}, rws, lane * 16, po + 3072);
      }
      STAMP(3);
      BAR();
      __builtin_amdgcn_s_sleep(DLY_H1);
      {
        const int blk = w & 1, half = w >> 1;
        float q0, q1;
        for (int n = 0;; ++n) {
          v4u p[4];
#pragma unroll
          for (int jj = 0; jj < 4; ++jj) p[jj] = hl4(rws, lane * 16, WS_H1 + ((hd * 4 + jj) * 4 + blk * 2 + half) * 1024);
          unsigned bad = 0;
          q0 = 0.f; q1 = 0.f;
#pragma unroll
          for (int jj = 0; jj < 4; ++jj) {
            bad |= (p[jj].x ^ tlo) | (p[jj].z ^ tlo) | (((p[jj].y ^ t8) | (p[jj].w ^ t8)) & 0xFFu);
            q0 += gdec(p[jj].y); q1 += gdec(p[jj].w);
          }
          if (__all(bad == 0) || n >= GSPIN) break;
        }
        VMCNT(0);
        const unsigned qp = pk(q0, q1);
        const int d0 = 32 * g + 16 * half + 4 * blk + cp;
        *(LAS u16*)(L + LP_QB + bj * 320 + d0 * 2) = (u16)qp;
        *(LAS u16*)(L + LP_QB + bj * 320 + (d0 + 8) * 2) = (u16)(qp >> 16);
      }
      STAMP(4);
      BAR();
      STAMP(5);
      {
        v4u qf[4];
#pragma unroll
        for (int s = 0; s < 4; ++s) qf[s] = *(LAS v4u*)(L + LP_QB + cp * 320 + s * 64 + g * 16);
        const int fr = ((i16 & 3) << 2) | (i16 >> 2);
#pragma unroll
        for (int t = 0; t < 2; ++t) {
          f32x4 acc = {0.f, 0.f, 0.f, 0.f};
          const int rt = 2 * w + t;
#pragma unroll
          for (int s = 0; s < 4; ++s) {
            const v4u A = *(LAS v4u*)(L + LP_UK + (16 * rt + i16) * 256 + (((4 * s + g) ^ fr) * 16));
            acc = mfma16(A, qf[s], acc);
          }
          const float sc = 0.08838834764831845f * 1.4426950408889634f;
          if (i16 < 4) *(LAS v2u*)(L + LP_QO + i16 * 256 + (16 * rt + 4 * g) * 2) = v2u{pk(acc[0] * sc, acc[1] * sc), pk(acc[2] * sc, acc[3] * sc)};
        }
      }
      BAR();
      if (w == 0) {
        const v4u v = *(LAS v4u*)(L + LP_QO + lane * 16);
        st4(v4u{lsbput(v.x, lsbtag(tlo, thi, 4 * i16)), lsbput(v.y, lsbtag(tlo, thi, 4 * i16 + 1)), lsbput(v.z, lsbtag(tlo, thi, 4 * i16 + 2)),
                lsbput(v.w, lsbtag(tlo, thi, 4 * i16 + 3))}, rws, g * 32768 + i16 * 16, WS_QA + hd * 1024 + jq * 256);
      }
      STAMP(6);
      BAR();
      const rsrc_t r2 = mk(w2, 4608 * 576 * 2);
      const int so2 = (xg * 576 + 36 * qq) * 1152;
      v4u a2[5][2];
      v2u a8[2][2];
      {
        const int vo4 = 2 * g * 1152 + i16 * 16, vo4m = g < 2 ? vo4 : OOB;
        const int vo8 = 2 * g * 1152 + i16 * 8 + 1024, vo8m = (g < 2 && w == 0) ? vo8 : OOB;
#pragma unroll
        for (int o = 0; o < 5; ++o)
#pragma unroll
          for (int r = 0; r < 2; ++r) a2[o][r] = ld4<NT_W>(r2, o < 4 ? vo4 : vo4m, so2 + (8 * o + r) * 1152 + 256 * w);
#pragma unroll
        for (int r = 0; r < 2; ++r) { a8[0][r] = ld2<NT_W>(r2, vo8, so2 + (8 * w + r) * 1152); a8[1][r] = ld2<NT_W>(r2, vo8m, so2 + (32 + r) * 1152); }
        SCHED();
      }
      STAMP(7);
      BAR();
      v4u X[5], Y[5];
#pragma unroll
      for (int o = 0; o < 5; ++o) {
        X[o] = v4u{a2[o][0].x, a2[o][0].y, a2[o][1].x, a2[o][1].y};
        Y[o] = v4u{a2[o][0].z, a2[o][0].w, a2[o][1].z, a2[o][1].w};
        asm volatile("" : "+v"(X[o]), "+v"(Y[o]));
      }
      STAMP(8);
      BAR();
      STAMP(9);
      {
        const v4u h4 = *(LAS v4u*)(L + LP_HP + (bj * 4 + g) * 48);
        const unsigned h5 = *(LAS unsigned*)(L + LP_HP + (bj * 4 + g) * 48 + 16);
        const unsigned hw = *(LAS unsigned*)(L + LP_HP + (bj * 4 + g) * 48 + 4 * w);
        f32x4 c8 = {0.f, 0.f, 0.f, 0.f}, cx = c8, cy = c8;
        c8 = mfma16(v4u{a8[0][0].x, a8[0][0].y, a8[0][1].x, a8[0][1].y}, bdiag(hw, sel), c8);
        c8 = mfma16(v4u{a8[1][0].x, a8[1][0].y, a8[1][1].x, a8[1][1].y}, bdiag(h5, sel), c8);
#pragma unroll
        for (int o = 0; o < 5; ++o) {
          const v4u B = bdiag(o == 0 ? h4.x : o == 1 ? h4.y : o == 2 ? h4.z : o == 3 ? h4.w : h5, sel);
          cx = mfma16(X[o], B, cx);
          cy = mfma16(Y[o], B, cy);
        }
        *(LAS f32x4*)(L + LP_B8 + w * 1024 + lane * 16) = c8;
        st4(tag8(cx, tb), rws, vo5, so5 + (2 * w) * 8 * 32768);
        st4(tag8(cy, tb), rws, vo5, so5 + (2 * w + 1) * 8 * 32768);
        BAR();
      }
      STAMP(10);
    } else {
      const int v = w - 4;
      {
        const v4u x0 = ld4<0>(rx, lane * 16, v * 1152), x1 = ld4<0>(rx, lane < 8 ? lane * 16 : OOB, v * 1152 + 1024);
        v2u wv_;
        if (v == 0) wv_ = hl2(rws, 0, WS_W);
        SCHED();
        RBAR();
        if (v == 0) PUT_TAG(LP_TAG);
        float f[16], s = 0.f;
#pragma unroll
        for (int k = 0; k < 4; ++k) {
          f[2 * k] = bf2f(x0[k]); f[2 * k + 1] = __uint_as_float(x0[k] & 0xFFFF0000u);
          f[8 + 2 * k] = bf2f(x1[k]); f[8 + 2 * k + 1] = __uint_as_float(x1[k] & 0xFFFF0000u);
        }
#pragma unroll
        for (int k = 0; k < 16; ++k) s += f[k];
        const float mean = x32_sum(x16_sum(row_sum(s))) * (1.0f / 576.0f);
        float q0 = 0.f, q1 = 0.f;
#pragma unroll
        for (int k = 0; k < 8; ++k) { f[k] -= mean; f[8 + k] -= mean; q0 = fmaf(f[k], f[k], q0); q1 = fmaf(f[8 + k], f[8 + k], q1); }
        const float rstd = 1.0f / sqrtf(x32_sum(x16_sum(row_sum(lane < 8 ? q0 + q1 : q0))) * (1.0f / 576.0f) + 1e-5f);
#pragma unroll
        for (int hf = 0; hf < 2; ++hf) {
          if (hf == 0 || lane < 8) {
            const int o = lane + 64 * hf;
#pragma unroll
            for (int k = 0; k < 4; ++k) {
              const unsigned p = pk(f[8 * hf + 2 * k] * rstd, f[8 * hf + 2 * k + 1] * rstd);
              *(LAS unsigned*)(L + LP_XP + ((v * 4 + k) * 76 + o) * 4) = p;
              *(LAS unsigned*)(L + LP_XD + ((v * 4 + ((2 * k) & 3)) * 148 + 2 * o + (k >> 1)) * 4) = __builtin_amdgcn_perm(0u, p, 0x01000100u);
              *(LAS unsigned*)(L + LP_XD + ((v * 4 + ((2 * k + 1) & 3)) * 148 + 2 * o + (k >> 1)) * 4) = __builtin_amdgcn_perm(0u, p, 0x03020302u);
            }
          }
        }
      }
      STAMP(1);
      BAR();
      GET_TAG(LP_TAG);
      const int U0 = (81 * qq) / 144, Qb = 144 * (U0 + 1);
      unsigned xd[21];
#pragma unroll
      for (int t = 0; t < 21; ++t) {
        const int Q = min(81 * qq + 4 * t + v, 81 * qq + 80), qk = Q >= Qb ? Q - Qb : Q - 144 * U0;
        xd[t] = *(LAS unsigned*)(L + LP_XD + ((bj * 4 + g) * 148 + qk) * 4);
      }
      const GAS char* wb = (const GAS char*)(i16 < 8 ? w1 : w3) + (xg * 576 * 2 + (i16 & 7) * 16 + g * 9216);
      const unsigned m0 = cp == 0 ? 0xFFFFu : cp == 1 ? 0xFFFF0000u : 0u, m1 = cp == 2 ? 0xFFFFu : cp == 3 ? 0xFFFF0000u : 0u;
      f32x4 acc[4];
#pragma unroll
      for (int k = 0; k < 4; ++k) acc[k] = f32x4{0.f, 0.f, 0.f, 0.f};
      v4u a[21];
      BAR();
      BAR();
      STAMP(2);
#pragma unroll
      for (int t = -PACE_G; t < 21; ++t) {
        const int tl = t + PACE_G;
        if (tl < 21) {
          const int Q = 81 * qq + 4 * tl + v, u = Q >= Qb ? U0 + 1 : U0, qk = Q - 144 * u;
          if (tl < 20 || v == 0) {
            const GAS v4u* p = (const GAS v4u*)(wb + (unsigned)(qk * 4 * 9216 + u * 128));
            a[tl] = NT_W ? __builtin_nontemporal_load(p) : *p;
          } else a[tl] = v4u{0u, 0u, 0u, 0u};
          SCHED();
        }
        if (t == -1) BAR();
        if (tl == 20) { STAMP(3); BAR(); }
        if (t >= 0) {
          const int Q = 81 * qq + 4 * t + v;
          const v2u B = {xd[t] & m0, xd[t] & m1};
          if (Q < Qb) { acc[0] = mfma16k16(v2u{a[t].x, a[t].y}, B, acc[0]); acc[1] = mfma16k16(v2u{a[t].z, a[t].w}, B, acc[1]); }
          else        { acc[2] = mfma16k16(v2u{a[t].x, a[t].y}, B, acc[2]); acc[3] = mfma16k16(v2u{a[t].z, a[t].w}, B, acc[3]); }
          SCHED();
        }
      }
#pragma unroll
      for (int k = 0; k < 4; ++k) *(LAS f32x4*)(L + LP_RED + (v * 4 + k) * 1024 + lane * 16) = acc[k];
      STAMP(4);
      BAR();
      {
        f32x4 r = *(LAS f32x4*)(L + LP_RED + v * 1024 + lane * 16);
#pragma unroll
        for (int k = 1; k < 4; ++k) r += *(LAS f32x4*)(L + LP_RED + (k * 4 + v) * 1024 + lane * 16);
        const int po = WS_H7 + idx * 8192 + v * 2048;
        st4(v4u{tlo, genc(r[0], t8), tlo, genc(r[1], t8)}, rws, lane * 16, po);
        st4(v4u{tlo, genc(r[2], t8), tlo, genc(r[3], t8)}, rws, lane * 16, po + 1024);
      }
      STAMP(5);
      {
        const int t = v * 64 + lane;
        const int b = t / 36, cl = t - 36 * b, f = 36 * qq + cl, u = f >> 6, cu = f & 63;
        const int ig = cu >> 3, hh = (cu >> 2) & 1, reg = ig & 3;
        const int r0 = (144 * u) / 81, r1 = (144 * u + 143) / 81;
        const int lo = hh * 2048 + (reg >> 1) * 1024 + ((ig >> 2) * 16 + b * 4 + (cu & 3)) * 16 + (reg & 1) * 8;
        int off[3];
        unsigned use[3];
#pragma unroll
        for (int k = 0; k < 3; ++k) {
          const int rr = r0 + k;
          const bool on = t < 144 && rr <= r1;
          off[k] = on ? (xg * 16 + rr) * 8192 + (u - (81 * rr) / 144) * 4096 + lo : OOB;
          use[k] = on ? 0xFFFFFFFFu : 0u;
        }
        __builtin_amdgcn_s_sleep(DLY_H7);
        float gs, us;
        for (int n = 0;; ++n) {
          v2u pg[3], pu[3];
#pragma unroll
          for (int k = 0; k < 3; ++k) { pg[k] = hl2(rws, off[k], WS_H7); pu[k] = hl2(rws, off[k], WS_H7 + 512); }
          unsigned bad = 0;
          gs = 0.f; us = 0.f;
#pragma unroll
          for (int k = 0; k < 3; ++k) {
            bad |= ((pg[k].x ^ tlo) | (pu[k].x ^ tlo) | (((pg[k].y ^ t8) | (pu[k].y ^ t8)) & 0xFFu)) & use[k];
            gs += gdec(pg[k].y); us += gdec(pu[k].y);
          }
          if (__all(bad == 0) || n >= GSPIN) break;
        }
        const float hv = gs / (1.0f + __expf(-gs)) * us;
        if (t < 144) *(LAS u16*)(L + LP_HP + ((b * 4 + ((cl >> 1) & 3)) * 12 + (cl >> 3)) * 4 + (cl & 1) * 2) = (u16)pk(hv, hv);
        else if (t < 152) *(LAS unsigned*)(L + LP_HP + ((((t - 144) >> 1) * 4 + 2 + (t & 1)) * 12 + 4) * 4) = 0u;
      }
      STAMP(6);
      BAR();
      BAR();
      if (v == 0) {
        f32x4 s = *(LAS f32x4*)(L + LP_B8 + lane * 16);
#pragma unroll
        for (int k = 1; k < 4; ++k) s += *(LAS f32x4*)(L + LP_B8 + k * 1024 + lane * 16);
        st4(tag8(s, tb), rws, vo5, so5 + 8 * 8 * 32768);
      }
      STAMP(7);
    }
    STAMP_OUT();
    return;
  }

  const int ab = xg >> 1;
  const int r32 = lane & 31, hh = lane >> 5;
  const int so5 = WS_H5 + (2 * idx + 1) * 128;
  v2u av[8][2];
  v2u wa_;
  asm volatile("buffer_load_dwordx2 %0, off, %1, 0 sc1" : "=v"(wa_) : "s"(rws) : "memory");
  if (w < 4) {
    const rsrc_t rc = mk(ckv, 4 * 4096 * 512 * 2);
    {
      int vo[4];
#pragma unroll
      for (int k = 0; k < 4; ++k) vo[k] = g * 1024 + ((i16 ^ ((g << 2) | k)) * 16);
      const int so = (idx * 128 + 32 * w) * 1024;
#pragma unroll
      for (int n = 0; n < 32; ++n) {
        if (n >= PACE_C) VMCNT(PACE_C - 1);
        dma16(rc, vo[n & 3], so + (n & 7) * 4096 + (n >> 3) * 256, lbase + (n >> 3) * 32768 + (32 * w + 4 * (n & 7)) * 256);
        if (n == KQA) BAR();
      }
    }
  } else {
    VMCNT(0);
    BAR();
  }
  asm volatile("" : "+v"(wa_));
  const u64 tag_ = (((u64)wa_.y << 32) | wa_.x) + 0x9E3779B97F4A7C15ull;
  const unsigned tlo = (unsigned)tag_, thi = (unsigned)(tag_ >> 32), t8 = thi & 0xFFu;
  const unsigned tb[4] = {tlo & 0xFFu, (tlo >> 8) & 0xFFu, (tlo >> 16) & 0xFFu, tlo >> 24};
  unsigned e4[4];
#pragma unroll
  for (int k = 0; k < 4; ++k) e4[k] = lsbtag(tlo, thi, 4 * i16 + k);
  if (w < 4) {
    v4u fa[32];
    {
      const int fr = ((r32 & 3) << 2) | ((r32 >> 2) & 3);
      const int ca = (32 * w + r32) * 256;
#define QK_READ(Q) _Pragma("unroll") for (int k = 0; k < 8; ++k) fa[(Q)*8 + k] = *(LAS v4u*)(L + (Q)*32768 + ca + (((2 * k + hh) ^ fr) * 16)); SCHED();
      const rsrc_t rv = mk(wuv, 512 * 4096 * 2);
      const int sov = (128 * jq + 16 * (w >> 1)) * 8192 + hd * 256 + (w & 1) * 128;
      const int vov[2] = {(32 * ((2 * g) & 3) + (g >> 1)) * 8192 + i16 * 8, (32 * ((2 * g + 1) & 3) + (g >> 1)) * 8192 + i16 * 8};
#define AV_LOAD(A, B) _Pragma("unroll") for (int n = (A); n < (B); ++n) av[n >> 1][n & 1] = ld2<NT_W>(rv, vov[n & 1], sov + 2 * (n >> 1) * 8192); SCHED();
      VMCNT(8); QK_READ(0) QK_READ(1) QK_READ(2)
      VMCNT(4); AV_LOAD(0, 8)
      VMCNT(8); QK_READ(3)
      AV_LOAD(8, 16)
    }
    int qb[4];
#pragma unroll
    for (int k = 0; k < 4; ++k) { qb[k] = LA_QA + ((lane ^ ((2 * k + hh) & 7)) * 16); asm volatile("" : "+v"(qb[k])); }
    STAMP(2);
    BAR();
    STAMP(3);
    f32x16 sacc;
#pragma unroll
    for (int k = 0; k < 16; ++k) sacc[k] = 0.f;
    {
      v4u fb[32];
#pragma unroll
      for (int s = 0; s < 6; ++s) fb[s] = *(LAS v4u*)(L + qb[s & 3] + s * 1024);
      SCHED();
#pragma unroll
      for (int s = 0; s < 32; ++s) {
        if (s + 6 < 32) fb[s + 6] = *(LAS v4u*)(L + qb[(s + 6) & 3] + (s + 6) * 1024);
        sacc = mfma32(fa[s], fb[s], sacc);
        SCHED();
        if (s == 12) { RBAR(); SCHED(); }
      }
    }
    const int q4 = i16 >> 2, lowc = 2 * (g & 1) + (cp >> 1);
    int ta[4][2];
#pragma unroll
    for (int t = 0; t < 4; ++t)
#pragma unroll
      for (int t2 = 0; t2 < 2; ++t2) {
        ta[t][t2] = w * 32768 + (8 * t2 + 4 * hh + q4) * 256 + ((((t ^ q4) << 2) | (lowc ^ (2 * t2 + hh))) * 16) + 8 * (cp & 1);
        asm volatile("" : "+v"(ta[t][t2]));
      }
    v4u cb[32];
#define TR_READ(n) do { const v2u x0_ = __builtin_bit_cast(v2u, __builtin_amdgcn_ds_read_tr16_b64_v4i16((LAS s16x4*)(L + ta[(n) >> 3][0] + ((n) & 7) * 4096))); \
    const v2u x1_ = __builtin_bit_cast(v2u, __builtin_amdgcn_ds_read_tr16_b64_v4i16((LAS s16x4*)(L + ta[(n) >> 3][1] + ((n) & 7) * 4096)));                 \
    cb[n] = v4u{x0_.x, x0_.y, x1_.x, x1_.y}; } while (0)
#pragma unroll
    for (int n = 0; n < 6; ++n) TR_READ(n);
    SCHED();
    float mw = sacc[0];
#pragma unroll
    for (int k = 1; k < 16; ++k) mw = fmaxf(mw, sacc[k]);
    mw = x32_max(mw);
    if (lane < 32) *(LAS float*)(L + LA_MX + (r32 * 4 + w) * 4) = mw;
    STAMP(4);
    BAR();
    const f32x4 m4 = *(LAS f32x4*)(L + LA_MX + r32 * 16);
    const float mx = fmaxf(fmaxf(m4[0], m4[1]), fmaxf(m4[2], m4[3]));
    float ls = 0.f;
#pragma unroll
    for (int k = 0; k < 16; ++k) { sacc[k] = __builtin_amdgcn_exp2f(sacc[k] - mx); ls += sacc[k]; }
#pragma unroll
    for (int s2 = 0; s2 < 2; ++s2)
      *(LAS v4u*)(L + LA_PB + (2 * w + s2) * 1024 + lane * 16) =
          v4u{pk(sacc[8 * s2], sacc[8 * s2 + 1]), pk(sacc[8 * s2 + 2], sacc[8 * s2 + 3]), pk(sacc[8 * s2 + 4], sacc[8 * s2 + 5]), pk(sacc[8 * s2 + 6], sacc[8 * s2 + 7])};
    ls = x32_sum(ls);
    if (lane < 32) *(LAS float*)(L + LA_SUM + (r32 * 4 + w) * 4) = ls;
    BAR();
    STAMP(5);
    {
      v4u pf[8];
#pragma unroll
      for (int k = 0; k < 8; ++k) pf[k] = *(LAS v4u*)(L + LA_PB + k * 1024 + lane * 16);
      if (w == 0) {
        const f32x4 l4 = *(LAS f32x4*)(L + LA_SUM + r32 * 16);
        st4(v4u{tlo, genc(mx, t8), tlo, genc((l4[0] + l4[1]) + (l4[2] + l4[3]), t8)}, rws, lane < 32 ? r32 * 16 : OOB, WS_ML + idx * 512);
      }
      SCHED();
      f32x16 o[4];
#pragma unroll
      for (int t = 0; t < 4; ++t)
#pragma unroll
        for (int k = 0; k < 16; ++k) o[t][k] = 0.f;
#pragma unroll
      for (int n = 0; n < 32; ++n) {
        if (n + 6 < 32) TR_READ(n + 6);
        o[n >> 3] = mfma32(pf[n & 7], cb[n], o[n >> 3]);
        SCHED();
      }
      STAMP(1);
      const unsigned e0 = lsbtag(tlo, thi, 2 * r32), e1 = lsbtag(tlo, thi, 2 * r32 + 1);
      const int vo = hh * 4 * 131072 + r32 * 8, so = WS_H3 + (w * 128 + idx) * 256;
#pragma unroll
      for (int k = 0; k < 16; ++k)
        st2(v2u{lsbput(pk(o[0][k], o[1][k]), e0), lsbput(pk(o[2][k], o[3][k]), e1)}, rws, vo, so + ((k & 3) + 8 * (k >> 2)) * 131072);
    }
    STAMP(6);
    {
      v4u ml;
      for (int n = 0;; ++n) {
        ml = hl4(rws, r32 * 512, WS_ML + w * 32 * 512 + hd * 16);
        if (__all(((ml.x ^ tlo) | (ml.z ^ tlo) | (((ml.y ^ t8) | (ml.w ^ t8)) & 0xFFu)) == 0) || n >= GSPIN) break;
      }
      if (DLY_H3) __builtin_amdgcn_s_sleep(DLY_H3);
      STAMP(7);
      v4u d[8];
#pragma unroll
      for (int k = 0; k < 8; ++k) d[k] = hl4(rws, lane * 16, WS_H3 + idx * 32768 + w * 8192 + k * 1024);
      SCHED();
      const float m = gdec(ml.y), M = x16_max(row_max(m));
      const float e = __builtin_amdgcn_exp2f(m - M);
      const float al = e / x16_sum(row_sum(e * gdec(ml.w)));
      const unsigned ah = pk(al, al) & 0xFFFFu;
      const unsigned hl = ah | (pk(al - bf2f(ah), 0.f) << 16);
      const int e7 = lane & 7;
      const unsigned src = (lane & 8) ? 0x0302u : 0x0100u, s1 = (e7 & 1) ? ((src << 16) | 0x0C0Cu) : (0x0C0C0000u | src);
      unsigned s8[4];
#pragma unroll
      for (int k = 0; k < 4; ++k) s8[k] = (e7 >> 1) == k ? s1 : 0x0C0C0C0Cu;
      v4u bk[8];
#pragma unroll
      for (int k = 0; k < 8; ++k) { bk[k] = bdiag(__builtin_amdgcn_ds_bpermute((4 * k + g) * 4, hl), s8); asm volatile("" : "+v"(bk[k])); }
#pragma unroll 1
      for (int n = 0; n < GSPIN; ++n) {
        unsigned bad = 0;
#pragma unroll
        for (int k = 0; k < 8; ++k) bad |= xor4(d[k], e4);
        if (__all((bad & LSBM) == 0)) break;
#pragma unroll
        for (int k = 0; k < 8; ++k) d[k] = hl4(rws, lane * 16, WS_H3 + idx * 32768 + w * 8192 + k * 1024);
      }
      STAMP(8);
      f32x4 c = {0.f, 0.f, 0.f, 0.f};
#pragma unroll
      for (int k = 0; k < 8; ++k) c = mfma16(d[k], bk[k], c);
#pragma unroll
      for (int r = 0; r < 4; ++r) {
        const float y = c[r] + dppf<0x128, 0xf>(c[r], c[r]);
        if (!(lane & 8)) *(LAS u16*)(L + LA_CTX + ((w * 4 + (e7 >> 1)) * 20 + 4 * g + r) * 4 + 2 * (e7 & 1)) = (u16)pk(y, y);
      }
    }
    BAR();
    {
      const int half = w >> 1, blk = w & 1;
      const v4u p0 = *(LAS v4u*)(L + LA_CTX + ((bj * 4 + g) * 20 + 8 * half) * 4), p1 = *(LAS v4u*)(L + LA_CTX + ((bj * 4 + g) * 20 + 8 * half + 4) * 4);
      f32x4 c = {0.f, 0.f, 0.f, 0.f};
#pragma unroll
      for (int o = 0; o < 8; ++o) c = mfma16(v4u{av[o][0].x, av[o][0].y, av[o][1].x, av[o][1].y}, bdiag(o < 4 ? p0[o & 3] : p1[o & 3], sel), c);
#pragma unroll
      for (int r = 0; r < 4; ++r)
        *(LAS float*)(L + LA_UV + half * 2304 + ((bj * 4 + 2 * (r & 1) + (cp >> 1)) * 36 + (8 * blk + 2 * g + (r >> 1)) * 2 + (cp & 1)) * 4) = c[r];
    }
    BAR();
    STAMP(9);
    BAR();
    if (w == 0) {
      f32x4 s = *(LAS f32x4*)(L + LA_B8 + lane * 16);
#pragma unroll
      for (int k = 1; k < 4; ++k) s += *(LAS f32x4*)(L + LA_B8 + k * 1024 + lane * 16);
      st4(tag8(s, tb), rws, vo5, so5 + 8 * 8 * 32768);
    }
  } else {
    const int v = w - 4;
    STAMP(1);
    {
      v4u qa[8];
      for (int n = 0;; ++n) {
#pragma unroll
        for (int k = 0; k < 8; ++k) qa[k] = hl4(rws, lane * 16, WS_QA + (ab * 32 + 8 * v + k) * 1024);
        unsigned bad = 0;
#pragma unroll
        for (int k = 0; k < 8; ++k) bad |= xor4(qa[k], e4);
        if (__all((bad & LSBM) == 0) || n >= GSPIN) break;
        __builtin_amdgcn_s_sleep(DLY_R2);
      }
#pragma unroll
      for (int k = 0; k < 8; ++k) *(LAS v4u*)(L + LA_QA + (lane >> 1) * 1024 + ((((lane & 1) * 32 + 8 * v + k) ^ (lane & 7)) * 16)) = qa[k];
    }
    STAMP(2);
    BAR();
    const rsrc_t ro = mk(wo, 4096 * 576 * 2);
    v4u ao[16][2];
    v2u a8[4][2];
    {
      const int vo4 = 2 * g * 1152 + i16 * 16, vo8 = 2 * g * 1152 + i16 * 8 + 1024;
#pragma unroll
      for (int o = 0; o < 16; ++o) {
        const int op = (o + 4 * jq) & 15;
        if (o > 4) VMCNT(PACE_O - 2);
#pragma unroll
        for (int r = 0; r < 2; ++r) ao[o][r] = ld4<NT_WO>(ro, vo4, (hd * 128 + 8 * op + r) * 1152 + 256 * v);
        if ((o & 3) == 3) {
          const int o8 = 4 * (((o >> 2) + jq) & 3) + v;
#pragma unroll
          for (int r = 0; r < 2; ++r) a8[o >> 2][r] = ld2<NT_WO>(ro, vo8, (hd * 128 + 8 * o8 + r) * 1152);
        }
        SCHED();
        if (o == 3) { RBAR(); RBAR(); RBAR(); }
      }
    }
    STAMP(3);
    v4u X[16], Y[16];
#pragma unroll
    for (int o = 0; o < 16; ++o) {
      X[o] = v4u{ao[o][0].x, ao[o][0].y, ao[o][1].x, ao[o][1].y};
      Y[o] = v4u{ao[o][0].z, ao[o][0].w, ao[o][1].z, ao[o][1].w};
      asm volatile("" : "+v"(X[o]), "+v"(Y[o]));
    }
    VMCNT(0);
    STAMP(4);
    BAR();
    BAR();
    STAMP(5);
    {
      unsigned P[16];
#pragma unroll
      for (int q = 0; q < 4; ++q) {
        const int ad = LA_UV + ((bj * 4 + g) * 36 + 8 * ((q + jq) & 3)) * 4;
        const f32x4 x0 = *(LAS f32x4*)(L + ad) + *(LAS f32x4*)(L + ad + 2304), x1 = *(LAS f32x4*)(L + ad + 16) + *(LAS f32x4*)(L + ad + 16 + 2304);
        P[4 * q] = pk(x0[0], x0[1]); P[4 * q + 1] = pk(x0[2], x0[3]); P[4 * q + 2] = pk(x1[0], x1[1]); P[4 * q + 3] = pk(x1[2], x1[3]);
      }
      f32x4 c8 = {0.f, 0.f, 0.f, 0.f}, cx = c8, cy = c8;
#pragma unroll
      for (int q = 0; q < 4; ++q) {
        const unsigned pv = v == 0 ? P[4 * q] : v == 1 ? P[4 * q + 1] : v == 2 ? P[4 * q + 2] : P[4 * q + 3];
        c8 = mfma16(v4u{a8[q][0].x, a8[q][0].y, a8[q][1].x, a8[q][1].y}, bdiag(pv, sel), c8);
      }
      SCHED();
#pragma unroll
      for (int o = 0; o < 16; ++o) {
        const v4u B = bdiag(P[o], sel);
        cx = mfma16(X[o], B, cx);
        cy = mfma16(Y[o], B, cy);
        if (o == 1) { *(LAS f32x4*)(L + LA_B8 + v * 1024 + lane * 16) = c8; SCHED(); BAR(); }
      }
      st4(tag8(cx, tb), rws, vo5, so5 + (2 * v) * 8 * 32768);
      st4(tag8(cy, tb), rws, vo5, so5 + (2 * v + 1) * 8 * 32768);
    }
    STAMP(6);
  }
  if (qq < 9) {
    const int e = lane & 31, Ld = 8 * xg + (e >> 2), rr = e & 3;
    const int ob = (Ld & 15) >> 2, oc = qq < 8 ? 128 * (qq >> 1) + 8 * (4 * (Ld >> 4) + rr) + 4 * (qq & 1) + (Ld & 3) : 512 + 4 * (4 * (Ld >> 4) + rr) + (Ld & 3);
    unsigned xres = 0;
    if (w == 0) xres = ((const GAS u16*)xin)[ob * 576 + oc];
    BAR();
    __builtin_amdgcn_s_sleep(DLY_H5);
    STAMP(10);
    f32x4 s;
    for (int n = 0;; ++n) {
      v4u d[4];
#pragma unroll
      for (int k = 0; k < 4; ++k) d[k] = hl4(rws, lane * 16, WS_H5 + (qq * 8 + xg) * 32768 + (w * 4 + k) * 1024);
      const unsigned bad = (xor4(d[0], tb) | xor4(d[1], tb)) | (xor4(d[2], tb) | xor4(d[3], tb));
      s = (__builtin_bit_cast(f32x4, d[0]) + __builtin_bit_cast(f32x4, d[1])) + (__builtin_bit_cast(f32x4, d[2]) + __builtin_bit_cast(f32x4, d[3]));
      if (__all((bad & 0xFFu) == 0) || n >= GSPIN) break;
    }
#pragma unroll
    for (int r = 0; r < 4; ++r) s[r] = x32_sum(x16_sum(s[r] + dppf<0x128, 0xf>(s[r], s[r])));
    if (lane < 8) *(LAS f32x4*)(L + LA_FIN + w * 128 + lane * 16) = s;
    BAR();
    if (w == 0) {
      float y = bf2f(xres);
#pragma unroll
      for (int k = 0; k < 8; ++k) y += *(LAS float*)(L + LA_FIN + k * 128 + e * 4);
      if (lane < 32) ((GAS u16*)out)[ob * 576 + oc] = (u16)pk(y, y);
      if (qq == 0 && xg == 0) st2(v2u{tlo, thi}, rws, lane == 0 ? 0 : OOB, WS_W);
    }
    STAMP(11);
  }
  STAMP_OUT();
}

__global__ void k_ka(char* wsp){
  u64 t0,t1,t2,t3; u64 p;
  u64 ka=(u64)__builtin_amdgcn_kernarg_segment_ptr();
  asm volatile("s_memrealtime %0\n\ts_waitcnt lgkmcnt(0)\n\ts_load_dwordx2 %2, %3, 0x0\n\ts_waitcnt lgkmcnt(0)\n\ts_memrealtime %1\n\ts_waitcnt lgkmcnt(0)":"=&s"(t0),"=&s"(t1),"=&s"(p):"s"(ka):"memory");
  unsigned v;
  asm volatile("global_load_dword %0, %1, %2\n\ts_waitcnt vmcnt(0)":"=v"(v):"v"(WS_STAMP+(2<<20)),"s"(p):"memory");
  asm volatile("s_memrealtime %0\n\ts_waitcnt lgkmcnt(0)":"=s"(t2)::"memory");
  asm volatile("global_load_dword %0, %1, %2\n\ts_waitcnt vmcnt(0)":"=v"(v):"v"(WS_STAMP+(2<<20)+8192),"s"(p):"memory");
  asm volatile("s_memrealtime %0\n\ts_waitcnt lgkmcnt(0)":"=s"(t3)::"memory");
  if(threadIdx.x==0 && blockIdx.x==0){ u64* o=(u64*)((char*)p+WS_STAMP+(1<<20)); o[0]=t1-t0; o[1]=t2-t1; o[2]=t3-t2; o[3]=0; o[4]=v; }
}
__global__ void k_spin(char* ws){
  u64 T=((const u64*)(ws+WS_STAMP+(1<<20)))[EXTRA]*100; if(T>1000000) T=1000000;
  u64 t0=__builtin_amdgcn_s_memrealtime();
  while(__builtin_amdgcn_s_memrealtime()-t0 < T) __builtin_amdgcn_s_sleep(8);
}
extern "C" void kernel_launch(void* const* d_in, const int* in_sizes, int n_in, void* d_out, int out_size, void* d_ws, size_t ws_size,
                              hipStream_t stream) {
  (void)hipFuncSetAttribute((const void*)fused_decode, hipFuncAttributeMaxDynamicSharedMemorySize, LDS_BYTES);
  k_ka<<<1,64,0,stream>>>((char*)d_ws);
  fused_decode<<<256, 512, LDS_BYTES, stream>>>((const u16*)d_in[0], (const u16*)d_in[1], (const u16*)d_in[2], (const u16*)d_in[3],
                                                (const u16*)d_in[4], (const u16*)d_in[5], (const u16*)d_in[7], (const u16*)d_in[8],
                                                (const u16*)d_in[9], (u16*)d_out, (char*)d_ws);
  k_spin<<<1,1,0,stream>>>((char*)d_ws);
}
```

```cpp
#define EXTRA 0
#include <hip/hip_runtime.h>
#include <stdint.h>

typedef unsigned short u16;
typedef unsigned long long u64;
typedef unsigned v4u __attribute__((ext_vector_type(4)));
typedef unsigned v2u __attribute__((ext_vector_type(2)));
typedef float f32x2 __attribute__((ext_vector_type(2)));
typedef float f32x4 __attribute__((ext_vector_type(4)));
typedef float f32x16 __attribute__((ext_vector_type(16)));
typedef short s16x4 __attribute__((ext_vector_type(4)));
typedef __bf16 bf16x8 __attribute__((ext_vector_type(8)));
typedef __bf16 bf16x2 __attribute__((ext_vector_type(2)));
typedef __amdgpu_buffer_rsrc_t rsrc_t;
#define GAS __attribute__((address_space(1)))
#define LAS __attribute__((address_space(3)))
#define DI __device__ __forceinline__

#define NT_W 2
#define NT_WO 0
#define PACE_C 8
#define DLY_H1 4
#define KQA 31
#define PACE_K 4
#define DLY_K 8
#define PACE_G 5
#define PACE_O 8
#define DLY_R2 8
#define DLY_H3 0
#define DLY_H7 8
#define DLY_H5 8

constexpr int SC1 = 16;
constexpr int GSPIN = 1 << 13;

constexpr int WS_W = 0;
constexpr int WS_H1 = 4096;
constexpr int WS_QA = WS_H1 + 128 * 4096;
constexpr int WS_ML = WS_QA + 131072;
constexpr int WS_H7 = WS_ML + 65536;
constexpr int WS_H5 = WS_H7 + 128 * 8192;
constexpr int WS_H3 = WS_H5 + 72 * 256 * 128;
constexpr int WS_END = WS_H3 + 128 * 32768;
constexpr int WS_STAMP = WS_END;
constexpr int OOB = (int)0x80000000;

constexpr int LP_TAG = 4992, LP_XP = 0, LP_XD = 5120, LP_QB = 15360, LP_QO = 16896, LP_RED = 18432, LP_HP = 34816, LP_B8 = 35840, LP_UK = 40960;
constexpr int LA_QA = 131072, LA_MX = LA_QA;
constexpr int LA_PB = LA_QA + 1024, LA_SUM = LA_QA + 512, LA_CTX = LA_QA + 9216, LA_UV = LA_QA + 11264, LA_B8 = LA_QA + 16384,
              LA_FIN = LA_QA + 20480;
constexpr int LDS_BYTES = 163840;

DI rsrc_t mk(const void* p, int bytes) { return __builtin_amdgcn_make_buffer_rsrc((void*)p, (short)0, bytes, 0x00020000); }
template <int AUX> DI v4u ld4(rsrc_t r, int vo, int so) { return __builtin_amdgcn_raw_buffer_load_b128(r, vo, so, AUX); }
template <int AUX> DI v2u ld2(rsrc_t r, int vo, int so) { return __builtin_amdgcn_raw_buffer_load_b64(r, vo, so, AUX); }
DI v4u hl4(rsrc_t r, int vo, int so) { asm volatile("" : "+v"(vo)); return __builtin_amdgcn_raw_buffer_load_b128(r, vo, so, SC1); }
DI v2u hl2(rsrc_t r, int vo, int so) { asm volatile("" : "+v"(vo)); return __builtin_amdgcn_raw_buffer_load_b64(r, vo, so, SC1); }
DI void st4(v4u v, rsrc_t r, int vo, int so) { __builtin_amdgcn_raw_buffer_store_b128(v, r, vo + so, 0, SC1); }
DI void st2(v2u v, rsrc_t r, int vo, int so) { __builtin_amdgcn_raw_buffer_store_b64(v, r, vo, so, SC1); }
DI void dma16(rsrc_t r, int vo, int so, unsigned lds) {
  asm volatile("s_mov_b32 m0, %0\n\ts_nop 0\n\tbuffer_load_dwordx4 %1, %2, %3 offen nt lds" ::"s"(lds), "v"(vo), "s"(r), "s"(so) : "memory");
}
#define VMCNT(n) asm volatile("s_waitcnt vmcnt(%0)" ::"n"(n) : "memory")
#define FENCE() asm volatile("" ::: "memory")
#define SCHED() __builtin_amdgcn_sched_barrier(0)

DI float bf2f(unsigned v) { return __uint_as_float(v << 16); }
DI unsigned pk(float a, float b) { f32x2 f = {a, b}; return __builtin_bit_cast(unsigned, __builtin_convertvector(f, bf16x2)); }
DI f32x4 mfma16(v4u a, v4u b, f32x4 c) {
  return __builtin_amdgcn_mfma_f32_16x16x32_bf16(__builtin_bit_cast(bf16x8, a), __builtin_bit_cast(bf16x8, b), c, 0, 0, 0);
}
DI f32x4 mfma16k16(v2u a, v2u b, f32x4 c) {
  return __builtin_amdgcn_mfma_f32_16x16x16bf16_1k(__builtin_bit_cast(s16x4, a), __builtin_bit_cast(s16x4, b), c, 0, 0, 0);
}
DI f32x16 mfma32(v4u a, v4u b, f32x16 c) {
  return __builtin_amdgcn_mfma_f32_32x32x16_bf16(__builtin_bit_cast(bf16x8, a), __builtin_bit_cast(bf16x8, b), c, 0, 0, 0);
}
template <int CTRL, int RM> DI float dppf(float old, float v) {
  return __builtin_bit_cast(float, __builtin_amdgcn_update_dpp(__builtin_bit_cast(int, old), __builtin_bit_cast(int, v), CTRL, RM, 0xf, false));
}
DI float row_sum(float v) {
  v += dppf<0xB1, 0xf>(v, v); v += dppf<0x4E, 0xf>(v, v); v += dppf<0x124, 0xf>(v, v); v += dppf<0x128, 0xf>(v, v);
  return v;
}
DI float row_max(float v) {
  v = fmaxf(v, dppf<0xB1, 0xf>(v, v)); v = fmaxf(v, dppf<0x4E, 0xf>(v, v));
  v = fmaxf(v, dppf<0x124, 0xf>(v, v)); v = fmaxf(v, dppf<0x128, 0xf>(v, v));
  return v;
}
DI float x16_sum(float v) { auto r = __builtin_amdgcn_permlane16_swap(__float_as_uint(v), __float_as_uint(v), false, false); return __uint_as_float(r[0]) + __uint_as_float(r[1]); }
DI float x32_sum(float v) { auto r = __builtin_amdgcn_permlane32_swap(__float_as_uint(v), __float_as_uint(v), false, false); return __uint_as_float(r[0]) + __uint_as_float(r[1]); }
DI float x16_max(float v) { auto r = __builtin_amdgcn_permlane16_swap(__float_as_uint(v), __float_as_uint(v), false, false); return fmaxf(__uint_as_float(r[0]), __uint_as_float(r[1])); }
DI float x32_max(float v) { auto r = __builtin_amdgcn_permlane32_swap(__float_as_uint(v), __float_as_uint(v), false, false); return fmaxf(__uint_as_float(r[0]), __uint_as_float(r[1])); }

#define RBAR() asm volatile("s_barrier" ::: "memory")
#define BAR() asm volatile("s_waitcnt lgkmcnt(0)\n\ts_barrier" ::: "memory")
DI unsigned genc(float v, unsigned t8) { return ((__float_as_uint(v) + 0x80u) & 0xFFFFFF00u) | t8; }
DI float gdec(unsigned hi) { return __uint_as_float(hi & 0xFFFFFF00u); }

#define STAMP(k)
#define STAMP_DECL
#define STAMP_OUT()

DI v4u bdiag(unsigned P, const unsigned (&s)[4]) {
  v4u b;
  b.x = __builtin_amdgcn_perm(P, P, s[0]); b.y = __builtin_amdgcn_perm(P, P, s[1]);
  b.z = __builtin_amdgcn_perm(P, P, s[2]); b.w = __builtin_amdgcn_perm(P, P, s[3]);
  return b;
}
DI void bdiag_sel(int cp, unsigned (&s)[4]) {
  const unsigned lo = (cp & 1) ? 0x01000C0Cu : 0x0C0C0100u, hi = (cp & 1) ? 0x03020C0Cu : 0x0C0C0302u, z = 0x0C0C0C0Cu;
  s[0] = cp < 2 ? lo : z; s[1] = cp < 2 ? z : lo; s[2] = cp < 2 ? hi : z; s[3] = cp < 2 ? z : hi;
}

DI unsigned lsbtag(unsigned tlo, unsigned thi, int p) { return (((p < 32 ? tlo : thi) >> (p & 31)) & 1u) | (((tlo ^ (unsigned)p) & 1u) << 16); }
constexpr unsigned LSBM = 0x00010001u;
DI unsigned lsbput(unsigned v, unsigned e) { return (v & ~LSBM) | e; }
DI unsigned xor4(v4u d, const unsigned (&e)[4]) { return ((d.x ^ e[0]) | (d.y ^ e[1])) | ((d.z ^ e[2]) | (d.w ^ e[3])); }
DI v4u tag8(f32x4 c, const unsigned (&t)[4]) {
  return v4u{(__float_as_uint(c[0]) & 0xFFFFFF00u) | t[0], (__float_as_uint(c[1]) & 0xFFFFFF00u) | t[1],
             (__float_as_uint(c[2]) & 0xFFFFFF00u) | t[2], (__float_as_uint(c[3]) & 0xFFFFFF00u) | t[3]};
}
__global__ void __launch_bounds__(512, 2)
fused_decode(const u16* __restrict__ xin, const u16* __restrict__ ckv, const u16* __restrict__ wq, const u16* __restrict__ wuk,
             const u16* __restrict__ wuv, const u16* __restrict__ wo, const u16* __restrict__ w1, const u16* __restrict__ w3,
             const u16* __restrict__ w2, u16* __restrict__ out, char* ws) {
  extern __shared__ __attribute__((aligned(16))) char smem[];
  LAS char* L = (LAS char*)smem;
  const unsigned lbase = (unsigned)(size_t)smem;
  const int lane = threadIdx.x & 63, w = __builtin_amdgcn_readfirstlane(threadIdx.x >> 6);
  const int xg = blockIdx.x & 7, qd_ = blockIdx.x >> 3, qq = qd_ & 15, idx = xg * 16 + qq, hd = idx >> 2, jq = idx & 3;
  const int i16 = lane & 15, g = lane >> 4, bj = i16 >> 2, cp = lane & 3;
  STAMP_DECL;
  STAMP(0);
  asm volatile("" ::"s"(xin), "s"(ckv), "s"(wq), "s"(wuk), "s"(wuv), "s"(wo), "s"(w1), "s"(w3), "s"(w2), "s"(out), "s"(ws));
  const rsrc_t rws = mk(ws, WS_END);
  const rsrc_t rx = mk(xin, 4 * 576 * 2);
  unsigned sel[4];
  bdiag_sel(cp, sel);
#define LOAD_TAG() const v2u wv_ = hl2(rws, 0, WS_W)
#define PUT_TAG(AD) do { const u64 tag_ = (((u64)wv_.y << 32) | wv_.x) + 0x9E3779B97F4A7C15ull; \
    if (lane == 0) *(LAS v2u*)(L + (AD)) = v2u{(unsigned)tag_, (unsigned)(tag_ >> 32)}; } while (0)
#define GET_TAG(AD) const v2u tg_ = *(LAS v2u*)(L + (AD)); \
  const unsigned tlo = tg_.x, thi = tg_.y, t8 = thi & 0xFFu; \
  const unsigned tb[4] = {tlo & 0xFFu, (tlo >> 8) & 0xFFu, (tlo >> 16) & 0xFFu, tlo >> 24}
  const int vo5 = (lane >> 3) * 32768 + (lane & 7) * 16;

  if (qd_ < 16) {
    const int so5 = WS_H5 + (2 * idx) * 128;
    if (w < 4) {
      v4u a[18][2];
      int vok[4];
      const rsrc_t rk = mk(wuk, 512 * 4096 * 2);
      const int n0 = (w - 2) * 16;
      RBAR();
      if (w == 0) {
        const rsrc_t rq = mk(wq, 576 * 4096 * 2);
        const int vo = 2 * g * 8192 + i16 * 16, so = 144 * jq * 8192 + hd * 256;
#pragma unroll
        for (int o = 0; o < 18; ++o) { a[o][0] = ld4<NT_W>(rq, vo, so + (8 * o) * 8192); a[o][1] = ld4<NT_W>(rq, vo, so + (8 * o + 1) * 8192); }
        SCHED();
      } else if (w >= 2) {
#pragma unroll
        for (int k = 0; k < 4; ++k) vok[k] = g * 8192 + ((i16 ^ ((g << 2) | k)) * 16);
        __builtin_amdgcn_s_sleep(DLY_K);
#pragma unroll
        for (int n = 0; n < 16; ++n) dma16(rk, vok[n & 3], (128 * jq + 4 * (n0 + n)) * 8192 + hd * 256, lbase + LP_UK + (n0 + n) * 1024);
      }
      STAMP(1);
      BAR();
      STAMP(2);
      GET_TAG(LP_TAG);
      if (w == 0) {
        v2u xp[9];
#pragma unroll
        for (int k = 0; k < 9; ++k) xp[k] = *(LAS v2u*)(L + LP_XP + ((bj * 4 + g) * 76 + 18 * jq + 2 * k) * 4);
        f32x4 qx = {0.f, 0.f, 0.f, 0.f}, qy = qx;
#pragma unroll
        for (int o = 0; o < 18; ++o) {
          const v4u B = bdiag((o & 1) ? xp[o >> 1].y : xp[o >> 1].x, sel);
          qx = mfma16(v4u{a[o][0].x, a[o][0].y, a[o][1].x, a[o][1].y}, B, qx);
          qy = mfma16(v4u{a[o][0].z, a[o][0].w, a[o][1].z, a[o][1].w}, B, qy);
        }
        const int po = WS_H1 + idx * 4096;
        st4(v4u{tlo, genc(qx[0], t8), tlo, genc(qx[1], t8)}, rws, lane * 16, po);
        st4(v4u{tlo, genc(qx[2], t8), tlo, genc(qx[3], t8)}, rws, lane * 16, po + 1024);
        st4(v4u{tlo, genc(qy[0], t8), tlo, genc(qy[1], t8)}, rws, lane * 16, po + 2048);
        st4(v4u{tlo, genc(qy[2], t8), tlo, genc(qy[3], t8)}, rws, lane * 16, po + 3072);
      }
      STAMP(3);
      BAR();
      __builtin_amdgcn_s_sleep(DLY_H1);
      {
        const int blk = w & 1, half = w >> 1;
        float q0, q1;
        for (int n = 0;; ++n) {
          v4u p[4];
#pragma unroll
          for (int jj = 0; jj < 4; ++jj) p[jj] = hl4(rws, lane * 16, WS_H1 + ((hd * 4 + jj) * 4 + blk * 2 + half) * 1024);
          unsigned bad = 0;
          q0 = 0.f; q1 = 0.f;
#pragma unroll
          for (int jj = 0; jj < 4; ++jj) {
            bad |= (p[jj].x ^ tlo) | (p[jj].z ^ tlo) | (((p[jj].y ^ t8) | (p[jj].w ^ t8)) & 0xFFu);
            q0 += gdec(p[jj].y); q1 += gdec(p[jj].w);
          }
          if (__all(bad == 0) || n >= GSPIN) break;
        }
        VMCNT(0);
        const unsigned qp = pk(q0, q1);
        const int d0 = 32 * g + 16 * half + 4 * blk + cp;
        *(LAS u16*)(L + LP_QB + bj * 320 + d0 * 2) = (u16)qp;
        *(LAS u16*)(L + LP_QB + bj * 320 + (d0 + 8) * 2) = (u16)(qp >> 16);
      }
      STAMP(4);
      BAR();
      STAMP(5);
      {
        v4u qf[4];
#pragma unroll
        for (int s = 0; s < 4; ++s) qf[s] = *(LAS v4u*)(L + LP_QB + cp * 320 + s * 64 + g * 16);
        const int fr = ((i16 & 3) << 2) | (i16 >> 2);
#pragma unroll
        for (int t = 0; t < 2; ++t) {
          f32x4 acc = {0.f, 0.f, 0.f, 0.f};
          const int rt = 2 * w + t;
#pragma unroll
          for (int s = 0; s < 4; ++s) {
            const v4u A = *(LAS v4u*)(L + LP_UK + (16 * rt + i16) * 256 + (((4 * s + g) ^ fr) * 16));
            acc = mfma16(A, qf[s], acc);
          }
          const float sc = 0.08838834764831845f * 1.4426950408889634f;
          if (i16 < 4) *(LAS v2u*)(L + LP_QO + i16 * 256 + (16 * rt + 4 * g) * 2) = v2u{pk(acc[0] * sc, acc[1] * sc), pk(acc[2] * sc, acc[3] * sc)};
        }
      }
      BAR();
      if (w == 0) {
        const v4u v = *(LAS v4u*)(L + LP_QO + lane * 16);
        st4(v4u{lsbput(v.x, lsbtag(tlo, thi, 4 * i16)), lsbput(v.y, lsbtag(tlo, thi, 4 * i16 + 1)), lsbput(v.z, lsbtag(tlo, thi, 4 * i16 + 2)),
                lsbput(v.w, lsbtag(tlo, thi, 4 * i16 + 3))}, rws, g * 32768 + i16 * 16, WS_QA + hd * 1024 + jq * 256);
      }
      STAMP(6);
      BAR();
      const rsrc_t r2 = mk(w2, 4608 * 576 * 2);
      const int so2 = (xg * 576 + 36 * qq) * 1152;
      v4u a2[5][2];
      v2u a8[2][2];
      {
        const int vo4 = 2 * g * 1152 + i16 * 16, vo4m = g < 2 ? vo4 : OOB;
        const int vo8 = 2 * g * 1152 + i16 * 8 + 1024, vo8m = (g < 2 && w == 0) ? vo8 : OOB;
#pragma unroll
        for (int o = 0; o < 5; ++o)
#pragma unroll
          for (int r = 0; r < 2; ++r) a2[o][r] = ld4<NT_W>(r2, o < 4 ? vo4 : vo4m, so2 + (8 * o + r) * 1152 + 256 * w);
#pragma unroll
        for (int r = 0; r < 2; ++r) { a8[0][r] = ld2<NT_W>(r2, vo8, so2 + (8 * w + r) * 1152); a8[1][r] = ld2<NT_W>(r2, vo8m, so2 + (32 + r) * 1152); }
        SCHED();
      }
      STAMP(7);
      BAR();
      v4u X[5], Y[5];
#pragma unroll
      for (int o = 0; o < 5; ++o) {
        X[o] = v4u{a2[o][0].x, a2[o][0].y, a2[o][1].x, a2[o][1].y};
        Y[o] = v4u{a2[o][0].z, a2[o][0].w, a2[o][1].z, a2[o][1].w};
        asm volatile("" : "+v"(X[o]), "+v"(Y[o]));
      }
      STAMP(8);
      BAR();
      STAMP(9);
      {
        const v4u h4 = *(LAS v4u*)(L + LP_HP + (bj * 4 + g) * 48);
        const unsigned h5 = *(LAS unsigned*)(L + LP_HP + (bj * 4 + g) * 48 + 16);
        const unsigned hw = *(LAS unsigned*)(L + LP_HP + (bj * 4 + g) * 48 + 4 * w);
        f32x4 c8 = {0.f, 0.f, 0.f, 0.f}, cx = c8, cy = c8;
        c8 = mfma16(v4u{a8[0][0].x, a8[0][0].y, a8[0][1].x, a8[0][1].y}, bdiag(hw, sel), c8);
        c8 = mfma16(v4u{a8[1][0].x, a8[1][0].y, a8[1][1].x, a8[1][1].y}, bdiag(h5, sel), c8);
#pragma unroll
        for (int o = 0; o < 5; ++o) {
          const v4u B = bdiag(o == 0 ? h4.x : o == 1 ? h4.y : o == 2 ? h4.z : o == 3 ? h4.w : h5, sel);
          cx = mfma16(X[o], B, cx);
          cy = mfma16(Y[o], B, cy);
        }
        *(LAS f32x4*)(L + LP_B8 + w * 1024 + lane * 16) = c8;
        st4(tag8(cx, tb), rws, vo5, so5 + (2 * w) * 8 * 32768);
        st4(tag8(cy, tb), rws, vo5, so5 + (2 * w + 1) * 8 * 32768);
        BAR();
      }
      STAMP(10);
    } else {
      const int v = w - 4;
      {
        const v4u x0 = ld4<0>(rx, lane * 16, v * 1152), x1 = ld4<0>(rx, lane < 8 ? lane * 16 : OOB, v * 1152 + 1024);
        v2u wv_;
        if (v == 0) wv_ = hl2(rws, 0, WS_W);
        SCHED();
        RBAR();
        if (v == 0) PUT_TAG(LP_TAG);
        float f[16], s = 0.f;
#pragma unroll
        for (int k = 0; k < 4; ++k) {
          f[2 * k] = bf2f(x0[k]); f[2 * k + 1] = __uint_as_float(x0[k] & 0xFFFF0000u);
          f[8 + 2 * k] = bf2f(x1[k]); f[8 + 2 * k + 1] = __uint_as_float(x1[k] & 0xFFFF0000u);
        }
#pragma unroll
        for (int k = 0; k < 16; ++k) s += f[k];
        const float mean = x32_sum(x16_sum(row_sum(s))) * (1.0f / 576.0f);
        float q0 = 0.f, q1 = 0.f;
#pragma unroll
        for (int k = 0; k < 8; ++k) { f[k] -= mean; f[8 + k] -= mean; q0 = fmaf(f[k], f[k], q0); q1 = fmaf(f[8 + k], f[8 + k], q1); }
        const float rstd = 1.0f / sqrtf(x32_sum(x16_sum(row_sum(lane < 8 ? q0 + q1 : q0))) * (1.0f / 576.0f) + 1e-5f);
#pragma unroll
        for (int hf = 0; hf < 2; ++hf) {
          if (hf == 0 || lane < 8) {
            const int o = lane + 64 * hf;
#pragma unroll
            for (int k = 0; k < 4; ++k) {
              const unsigned p = pk(f[8 * hf + 2 * k] * rstd, f[8 * hf + 2 * k + 1] * rstd);
              *(LAS unsigned*)(L + LP_XP + ((v * 4 + k) * 76 + o) * 4) = p;
              *(LAS unsigned*)(L + LP_XD + ((v * 4 + ((2 * k) & 3)) * 148 + 2 * o + (k >> 1)) * 4) = __builtin_amdgcn_perm(0u, p, 0x01000100u);
              *(LAS unsigned*)(L + LP_XD + ((v * 4 + ((2 * k + 1) & 3)) * 148 + 2 * o + (k >> 1)) * 4) = __builtin_amdgcn_perm(0u, p, 0x03020302u);
            }
          }
        }
      }
      STAMP(1);
      BAR();
      GET_TAG(LP_TAG);
      const int U0 = (81 * qq) / 144, Qb = 144 * (U0 + 1);
      unsigned xd[21];
#pragma unroll
      for (int t = 0; t < 21; ++t) {
        const int Q = min(81 * qq + 4 * t + v, 81 * qq + 80), qk = Q >= Qb ? Q - Qb : Q - 144 * U0;
        xd[t] = *(LAS unsigned*)(L + LP_XD + ((bj * 4 + g) * 148 + qk) * 4);
      }
      const GAS char* wb = (const GAS char*)(i16 < 8 ? w1 : w3) + (xg * 576 * 2 + (i16 & 7) * 16 + g * 9216);
      const unsigned m0 = cp == 0 ? 0xFFFFu : cp == 1 ? 0xFFFF0000u : 0u, m1 = cp == 2 ? 0xFFFFu : cp == 3 ? 0xFFFF0000u : 0u;
      f32x4 acc[4];
#pragma unroll
      for (int k = 0; k < 4; ++k) acc[k] = f32x4{0.f, 0.f, 0.f, 0.f};
      v4u a[21];
      BAR();
      BAR();
      STAMP(2);
#pragma unroll
      for (int t = -PACE_G; t < 21; ++t) {
        const int tl = t + PACE_G;
        if (tl < 21) {
          const int Q = 81 * qq + 4 * tl + v, u = Q >= Qb ? U0 + 1 : U0, qk = Q - 144 * u;
          if (tl < 20 || v == 0) {
            const GAS v4u* p = (const GAS v4u*)(wb + (unsigned)(qk * 4 * 9216 + u * 128));
            a[tl] = NT_W ? __builtin_nontemporal_load(p) : *p;
          } else a[tl] = v4u{0u, 0u, 0u, 0u};
          SCHED();
        }
        if (t == -1) BAR();
        if (tl == 20) { STAMP(3); BAR(); }
        if (t >= 0) {
          const int Q = 81 * qq + 4 * t + v;
          const v2u B = {xd[t] & m0, xd[t] & m1};
          if (Q < Qb) { acc[0] = mfma16k16(v2u{a[t].x, a[t].y}, B, acc[0]); acc[1] = mfma16k16(v2u{a[t].z, a[t].w}, B, acc[1]); }
          else        { acc[2] = mfma16k16(v2u{a[t].x, a[t].y}, B, acc[2]); acc[3] = mfma16k16(v2u{a[t].z, a[t].w}, B, acc[3]); }
          SCHED();
        }
      }
#pragma unroll
      for (int k = 0; k < 4; ++k) *(LAS f32x4*)(L + LP_RED + (v * 4 + k) * 1024 + lane * 16) = acc[k];
      STAMP(4);
      BAR();
      {
        f32x4 r = *(LAS f32x4*)(L + LP_RED + v * 1024 + lane * 16);
#pragma unroll
        for (int k = 1; k < 4; ++k) r += *(LAS f32x4*)(L + LP_RED + (k * 4 + v) * 1024 + lane * 16);
        const int po = WS_H7 + idx * 8192 + v * 2048;
        st4(v4u{tlo, genc(r[0], t8), tlo, genc(r[1], t8)}, rws, lane * 16, po);
        st4(v4u{tlo, genc(r[2], t8), tlo, genc(r[3], t8)}, rws, lane * 16, po + 1024);
      }
      STAMP(5);
      {
        const int t = v * 64 + lane;
        const int b = t / 36, cl = t - 36 * b, f = 36 * qq + cl, u = f >> 6, cu = f & 63;
        const int ig = cu >> 3, hh = (cu >> 2) & 1, reg = ig & 3;
        const int r0 = (144 * u) / 81, r1 = (144 * u + 143) / 81;
        const int lo = hh * 2048 + (reg >> 1) * 1024 + ((ig >> 2) * 16 + b * 4 + (cu & 3)) * 16 + (reg & 1) * 8;
        int off[3];
        unsigned use[3];
#pragma unroll
        for (int k = 0; k < 3; ++k) {
          const int rr = r0 + k;
          const bool on = t < 144 && rr <= r1;
          off[k] = on ? (xg * 16 + rr) * 8192 + (u - (81 * rr) / 144) * 4096 + lo : OOB;
          use[k] = on ? 0xFFFFFFFFu : 0u;
        }
        __builtin_amdgcn_s_sleep(DLY_H7);
        float gs, us;
        for (int n = 0;; ++n) {
          v2u pg[3], pu[3];
#pragma unroll
          for (int k = 0; k < 3; ++k) { pg[k] = hl2(rws, off[k], WS_H7); pu[k] = hl2(rws, off[k], WS_H7 + 512); }
          unsigned bad = 0;
          gs = 0.f; us = 0.f;
#pragma unroll
          for (int k = 0; k < 3; ++k) {
            bad |= ((pg[k].x ^ tlo) | (pu[k].x ^ tlo) | (((pg[k].y ^ t8) | (pu[k].y ^ t8)) & 0xFFu)) & use[k];
            gs += gdec(pg[k].y); us += gdec(pu[k].y);
          }
          if (__all(bad == 0) || n >= GSPIN) break;
        }
        const float hv = gs / (1.0f + __expf(-gs)) * us;
        if (t < 144) *(LAS u16*)(L + LP_HP + ((b * 4 + ((cl >> 1) & 3)) * 12 + (cl >> 3)) * 4 + (cl & 1) * 2) = (u16)pk(hv, hv);
        else if (t < 152) *(LAS unsigned*)(L + LP_HP + ((((t - 144) >> 1) * 4 + 2 + (t & 1)) * 12 + 4) * 4) = 0u;
      }
      STAMP(6);
      BAR();
      BAR();
      if (v == 0) {
        f32x4 s = *(LAS f32x4*)(L + LP_B8 + lane * 16);
#pragma unroll
        for (int k = 1; k < 4; ++k) s += *(LAS f32x4*)(L + LP_B8 + k * 1024 + lane * 16);
        st4(tag8(s, tb), rws, vo5, so5 + 8 * 8 * 32768);
      }
      STAMP(7);
    }
    STAMP_OUT();
    return;
  }

  const int ab = xg >> 1;
  const int r32 = lane & 31, hh = lane >> 5;
  const int so5 = WS_H5 + (2 * idx + 1) * 128;
  v2u av[8][2];
  v2u wa_;
  asm volatile("buffer_load_dwordx2 %0, off, %1, 0 sc1" : "=v"(wa_) : "s"(rws) : "memory");
  if (w < 4) {
    const rsrc_t rc = mk(ckv, 4 * 4096 * 512 * 2);
    {
      int vo[4];
#pragma unroll
      for (int k = 0; k < 4; ++k) vo[k] = g * 1024 + ((i16 ^ ((g << 2) | k)) * 16);
      const int so = (idx * 128 + 32 * w) * 1024;
#pragma unroll
      for (int n = 0; n < 32; ++n) {
        if (n >= PACE_C) VMCNT(PACE_C - 1);
        dma16(rc, vo[n & 3], so + (n & 7) * 4096 + (n >> 3) * 256, lbase + (n >> 3) * 32768 + (32 * w + 4 * (n & 7)) * 256);
        if (n == KQA) BAR();
      }
    }
  } else {
    VMCNT(0);
    BAR();
  }
  asm volatile("" : "+v"(wa_));
  const u64 tag_ = (((u64)wa_.y << 32) | wa_.x) + 0x9E3779B97F4A7C15ull;
  const unsigned tlo = (unsigned)tag_, thi = (unsigned)(tag_ >> 32), t8 = thi & 0xFFu;
  const unsigned tb[4] = {tlo & 0xFFu, (tlo >> 8) & 0xFFu, (tlo >> 16) & 0xFFu, tlo >> 24};
  unsigned e4[4];
#pragma unroll
  for (int k = 0; k < 4; ++k) e4[k] = lsbtag(tlo, thi, 4 * i16 + k);
  if (w < 4) {
    v4u fa[32];
    {
      const int fr = ((r32 & 3) << 2) | ((r32 >> 2) & 3);
      const int ca = (32 * w + r32) * 256;
#define QK_READ(Q) _Pragma("unroll") for (int k = 0; k < 8; ++k) fa[(Q)*8 + k] = *(LAS v4u*)(L + (Q)*32768 + ca + (((2 * k + hh) ^ fr) * 16)); SCHED();
      const rsrc_t rv = mk(wuv, 512 * 4096 * 2);
      const int sov = (128 * jq + 16 * (w >> 1)) * 8192 + hd * 256 + (w & 1) * 128;
      const int vov[2] = {(32 * ((2 * g) & 3) + (g >> 1)) * 8192 + i16 * 8, (32 * ((2 * g + 1) & 3) + (g >> 1)) * 8192 + i16 * 8};
#define AV_LOAD(A, B) _Pragma("unroll") for (int n = (A); n < (B); ++n) av[n >> 1][n & 1] = ld2<NT_W>(rv, vov[n & 1], sov + 2 * (n >> 1) * 8192); SCHED();
      VMCNT(8); QK_READ(0) QK_READ(1) QK_READ(2)
      VMCNT(4); AV_LOAD(0, 8)
      VMCNT(8); QK_READ(3)
      AV_LOAD(8, 16)
    }
    int qb[4];
#pragma unroll
    for (int k = 0; k < 4; ++k) { qb[k] = LA_QA + ((lane ^ ((2 * k + hh) & 7)) * 16); asm volatile("" : "+v"(qb[k])); }
    STAMP(2);
    BAR();
    STAMP(3);
    f32x16 sacc;
#pragma unroll
    for (int k = 0; k < 16; ++k) sacc[k] = 0.f;
    {
      v4u fb[32];
#pragma unroll
      for (int s = 0; s < 6; ++s) fb[s] = *(LAS v4u*)(L + qb[s & 3] + s * 1024);
      SCHED();
#pragma unroll
      for (int s = 0; s < 32; ++s) {
        if (s + 6 < 32) fb[s + 6] = *(LAS v4u*)(L + qb[(s + 6) & 3] + (s + 6) * 1024);
        sacc = mfma32(fa[s], fb[s], sacc);
        SCHED();
        if (s == 12) { RBAR(); SCHED(); }
      }
    }
    const int q4 = i16 >> 2, lowc = 2 * (g & 1) + (cp >> 1);
    int ta[4][2];
#pragma unroll
    for (int t = 0; t < 4; ++t)
#pragma unroll
      for (int t2 = 0; t2 < 2; ++t2) {
        ta[t][t2] = w * 32768 + (8 * t2 + 4 * hh + q4) * 256 + ((((t ^ q4) << 2) | (lowc ^ (2 * t2 + hh))) * 16) + 8 * (cp & 1);
        asm volatile("" : "+v"(ta[t][t2]));
      }
    v4u cb[32];
#define TR_READ(n) do { const v2u x0_ = __builtin_bit_cast(v2u, __builtin_amdgcn_ds_read_tr16_b64_v4i16((LAS s16x4*)(L + ta[(n) >> 3][0] + ((n) & 7) * 4096))); \
    const v2u x1_ = __builtin_bit_cast(v2u, __builtin_amdgcn_ds_read_tr16_b64_v4i16((LAS s16x4*)(L + ta[(n) >> 3][1] + ((n) & 7) * 4096)));                 \
    cb[n] = v4u{x0_.x, x0_.y, x1_.x, x1_.y}; } while (0)
#pragma unroll
    for (int n = 0; n < 6; ++n) TR_READ(n);
    SCHED();
    float mw = sacc[0];
#pragma unroll
    for (int k = 1; k < 16; ++k) mw = fmaxf(mw, sacc[k]);
    mw = x32_max(mw);
    if (lane < 32) *(LAS float*)(L + LA_MX + (r32 * 4 + w) * 4) = mw;
    STAMP(4);
    BAR();
    const f32x4 m4 = *(LAS f32x4*)(L + LA_MX + r32 * 16);
    const float mx = fmaxf(fmaxf(m4[0], m4[1]), fmaxf(m4[2], m4[3]));
    float ls = 0.f;
#pragma unroll
    for (int k = 0; k < 16; ++k) { sacc[k] = __builtin_amdgcn_exp2f(sacc[k] - mx); ls += sacc[k]; }
#pragma unroll
    for (int s2 = 0; s2 < 2; ++s2)
      *(LAS v4u*)(L + LA_PB + (2 * w + s2) * 1024 + lane * 16) =
          v4u{pk(sacc[8 * s2], sacc[8 * s2 + 1]), pk(sacc[8 * s2 + 2], sacc[8 * s2 + 3]), pk(sacc[8 * s2 + 4], sacc[8 * s2 + 5]), pk(sacc[8 * s2 + 6], sacc[8 * s2 + 7])};
    ls = x32_sum(ls);
    if (lane < 32) *(LAS float*)(L + LA_SUM + (r32 * 4 + w) * 4) = ls;
    BAR();
    STAMP(5);
    {
      v4u pf[8];
#pragma unroll
      for (int k = 0; k < 8; ++k) pf[k] = *(LAS v4u*)(L + LA_PB + k * 1024 + lane * 16);
      if (w == 0) {
        const f32x4 l4 = *(LAS f32x4*)(L + LA_SUM + r32 * 16);
        st4(v4u{tlo, genc(mx, t8), tlo, genc((l4[0] + l4[1]) + (l4[2] + l4[3]), t8)}, rws, lane < 32 ? r32 * 16 : OOB, WS_ML + idx * 512);
      }
      SCHED();
      f32x16 o[4];
#pragma unroll
      for (int t = 0; t < 4; ++t)
#pragma unroll
        for (int k = 0; k < 16; ++k) o[t][k] = 0.f;
#pragma unroll
      for (int n = 0; n < 32; ++n) {
        if (n + 6 < 32) TR_READ(n + 6);
        o[n >> 3] = mfma32(pf[n & 7], cb[n], o[n >> 3]);
        SCHED();
      }
      STAMP(1);
      const unsigned e0 = lsbtag(tlo, thi, 2 * r32), e1 = lsbtag(tlo, thi, 2 * r32 + 1);
      const int vo = hh * 4 * 131072 + r32 * 8, so = WS_H3 + (w * 128 + idx) * 256;
#pragma unroll
      for (int k = 0; k < 16; ++k)
        st2(v2u{lsbput(pk(o[0][k], o[1][k]), e0), lsbput(pk(o[2][k], o[3][k]), e1)}, rws, vo, so + ((k & 3) + 8 * (k >> 2)) * 131072);
    }
    STAMP(6);
    {
      v4u ml;
      for (int n = 0;; ++n) {
        ml = hl4(rws, r32 * 512, WS_ML + w * 32 * 512 + hd * 16);
        if (__all(((ml.x ^ tlo) | (ml.z ^ tlo) | (((ml.y ^ t8) | (ml.w ^ t8)) & 0xFFu)) == 0) || n >= GSPIN) break;
      }
      if (DLY_H3) __builtin_amdgcn_s_sleep(DLY_H3);
      STAMP(7);
      v4u d[8];
#pragma unroll
      for (int k = 0; k < 8; ++k) d[k] = hl4(rws, lane * 16, WS_H3 + idx * 32768 + w * 8192 + k * 1024);
      SCHED();
      const float m = gdec(ml.y), M = x16_max(row_max(m));
      const float e = __builtin_amdgcn_exp2f(m - M);
      const float al = e / x16_sum(row_sum(e * gdec(ml.w)));
      const unsigned ah = pk(al, al) & 0xFFFFu;
      const unsigned hl = ah | (pk(al - bf2f(ah), 0.f) << 16);
      const int e7 = lane & 7;
      const unsigned src = (lane & 8) ? 0x0302u : 0x0100u, s1 = (e7 & 1) ? ((src << 16) | 0x0C0Cu) : (0x0C0C0000u | src);
      unsigned s8[4];
#pragma unroll
      for (int k = 0; k < 4; ++k) s8[k] = (e7 >> 1) == k ? s1 : 0x0C0C0C0Cu;
      v4u bk[8];
#pragma unroll
      for (int k = 0; k < 8; ++k) { bk[k] = bdiag(__builtin_amdgcn_ds_bpermute((4 * k + g) * 4, hl), s8); asm volatile("" : "+v"(bk[k])); }
#pragma unroll 1
      for (int n = 0; n < GSPIN; ++n) {
        unsigned bad = 0;
#pragma unroll
        for (int k = 0; k < 8; ++k) bad |= xor4(d[k], e4);
        if (__all((bad & LSBM) == 0)) break;
#pragma unroll
        for (int k = 0; k < 8; ++k) d[k] = hl4(rws, lane * 16, WS_H3 + idx * 32768 + w * 8192 + k * 1024);
      }
      STAMP(8);
      f32x4 c = {0.f, 0.f, 0.f, 0.f};
#pragma unroll
      for (int k = 0; k < 8; ++k) c = mfma16(d[k], bk[k], c);
#pragma unroll
      for (int r = 0; r < 4; ++r) {
        const float y = c[r] + dppf<0x128, 0xf>(c[r], c[r]);
        if (!(lane & 8)) *(LAS u16*)(L + LA_CTX + ((w * 4 + (e7 >> 1)) * 20 + 4 * g + r) * 4 + 2 * (e7 & 1)) = (u16)pk(y, y);
      }
    }
    BAR();
    {
      const int half = w >> 1, blk = w & 1;
      const v4u p0 = *(LAS v4u*)(L + LA_CTX + ((bj * 4 + g) * 20 + 8 * half) * 4), p1 = *(LAS v4u*)(L + LA_CTX + ((bj * 4 + g) * 20 + 8 * half + 4) * 4);
      f32x4 c = {0.f, 0.f, 0.f, 0.f};
#pragma unroll
      for (int o = 0; o < 8; ++o) c = mfma16(v4u{av[o][0].x, av[o][0].y, av[o][1].x, av[o][1].y}, bdiag(o < 4 ? p0[o & 3] : p1[o & 3], sel), c);
#pragma unroll
      for (int r = 0; r < 4; ++r)
        *(LAS float*)(L + LA_UV + half * 2304 + ((bj * 4 + 2 * (r & 1) + (cp >> 1)) * 36 + (8 * blk + 2 * g + (r >> 1)) * 2 + (cp & 1)) * 4) = c[r];
    }
    BAR();
    STAMP(9);
    BAR();
    if (w == 0) {
      f32x4 s = *(LAS f32x4*)(L + LA_B8 + lane * 16);
#pragma unroll
      for (int k = 1; k < 4; ++k) s += *(LAS f32x4*)(L + LA_B8 + k * 1024 + lane * 16);
      st4(tag8(s, tb), rws, vo5, so5 + 8 * 8 * 32768);
    }
  } else {
    const int v = w - 4;
    STAMP(1);
    {
      v4u qa[8];
      for (int n = 0;; ++n) {
#pragma unroll
        for (int k = 0; k < 8; ++k) qa[k] = hl4(rws, lane * 16, WS_QA + (ab * 32 + 8 * v + k) * 1024);
        unsigned bad = 0;
#pragma unroll
        for (int k = 0; k < 8; ++k) bad |= xor4(qa[k], e4);
        if (__all((bad & LSBM) == 0) || n >= GSPIN) break;
        __builtin_amdgcn_s_sleep(DLY_R2);
      }
#pragma unroll
      for (int k = 0; k < 8; ++k) *(LAS v4u*)(L + LA_QA + (lane >> 1) * 1024 + ((((lane & 1) * 32 + 8 * v + k) ^ (lane & 7)) * 16)) = qa[k];
    }
    STAMP(2);
    BAR();
    const rsrc_t ro = mk(wo, 4096 * 576 * 2);
    v4u ao[16][2];
    v2u a8[4][2];
    {
      const int vo4 = 2 * g * 1152 + i16 * 16, vo8 = 2 * g * 1152 + i16 * 8 + 1024;
#pragma unroll
      for (int o = 0; o < 16; ++o) {
        const int op = (o + 4 * jq) & 15;
        if (o > 4) VMCNT(PACE_O - 2);
#pragma unroll
        for (int r = 0; r < 2; ++r) ao[o][r] = ld4<NT_WO>(ro, vo4, (hd * 128 + 8 * op + r) * 1152 + 256 * v);
        if ((o & 3) == 3) {
          const int o8 = 4 * (((o >> 2) + jq) & 3) + v;
#pragma unroll
          for (int r = 0; r < 2; ++r) a8[o >> 2][r] = ld2<NT_WO>(ro, vo8, (hd * 128 + 8 * o8 + r) * 1152);
        }
        SCHED();
        if (o == 3) { RBAR(); RBAR(); RBAR(); }
      }
    }
    STAMP(3);
    v4u X[16], Y[16];
#pragma unroll
    for (int o = 0; o < 16; ++o) {
      X[o] = v4u{ao[o][0].x, ao[o][0].y, ao[o][1].x, ao[o][1].y};
      Y[o] = v4u{ao[o][0].z, ao[o][0].w, ao[o][1].z, ao[o][1].w};
      asm volatile("" : "+v"(X[o]), "+v"(Y[o]));
    }
    VMCNT(0);
    STAMP(4);
    BAR();
    BAR();
    STAMP(5);
    {
      unsigned P[16];
#pragma unroll
      for (int q = 0; q < 4; ++q) {
        const int ad = LA_UV + ((bj * 4 + g) * 36 + 8 * ((q + jq) & 3)) * 4;
        const f32x4 x0 = *(LAS f32x4*)(L + ad) + *(LAS f32x4*)(L + ad + 2304), x1 = *(LAS f32x4*)(L + ad + 16) + *(LAS f32x4*)(L + ad + 16 + 2304);
        P[4 * q] = pk(x0[0], x0[1]); P[4 * q + 1] = pk(x0[2], x0[3]); P[4 * q + 2] = pk(x1[0], x1[1]); P[4 * q + 3] = pk(x1[2], x1[3]);
      }
      f32x4 c8 = {0.f, 0.f, 0.f, 0.f}, cx = c8, cy = c8;
#pragma unroll
      for (int q = 0; q < 4; ++q) {
        const unsigned pv = v == 0 ? P[4 * q] : v == 1 ? P[4 * q + 1] : v == 2 ? P[4 * q + 2] : P[4 * q + 3];
        c8 = mfma16(v4u{a8[q][0].x, a8[q][0].y, a8[q][1].x, a8[q][1].y}, bdiag(pv, sel), c8);
      }
      SCHED();
#pragma unroll
      for (int o = 0; o < 16; ++o) {
        const v4u B = bdiag(P[o], sel);
        cx = mfma16(X[o], B, cx);
        cy = mfma16(Y[o], B, cy);
        if (o == 1) { *(LAS f32x4*)(L + LA_B8 + v * 1024 + lane * 16) = c8; SCHED(); BAR(); }
      }
      st4(tag8(cx, tb), rws, vo5, so5 + (2 * v) * 8 * 32768);
      st4(tag8(cy, tb), rws, vo5, so5 + (2 * v + 1) * 8 * 32768);
    }
    STAMP(6);
  }
  if (qq < 9) {
    const int e = lane & 31, Ld = 8 * xg + (e >> 2), rr = e & 3;
    const int ob = (Ld & 15) >> 2, oc = qq < 8 ? 128 * (qq >> 1) + 8 * (4 * (Ld >> 4) + rr) + 4 * (qq & 1) + (Ld & 3) : 512 + 4 * (4 * (Ld >> 4) + rr) + (Ld & 3);
    unsigned xres = 0;
    if (w == 0) xres = ((const GAS u16*)xin)[ob * 576 + oc];
    BAR();
    __builtin_amdgcn_s_sleep(DLY_H5);
    STAMP(10);
    f32x4 s;
    for (int n = 0;; ++n) {
      v4u d[4];
#pragma unroll
      for (int k = 0; k < 4; ++k) d[k] = hl4(rws, lane * 16, WS_H5 + (qq * 8 + xg) * 32768 + (w * 4 + k) * 1024);
      const unsigned bad = (xor4(d[0], tb) | xor4(d[1], tb)) | (xor4(d[2], tb) | xor4(d[3], tb));
      s = (__builtin_bit_cast(f32x4, d[0]) + __builtin_bit_cast(f32x4, d[1])) + (__builtin_bit_cast(f32x4, d[2]) + __builtin_bit_cast(f32x4, d[3]));
      if (__all((bad & 0xFFu) == 0) || n >= GSPIN) break;
    }
#pragma unroll
    for (int r = 0; r < 4; ++r) s[r] = x32_sum(x16_sum(s[r] + dppf<0x128, 0xf>(s[r], s[r])));
    if (lane < 8) *(LAS f32x4*)(L + LA_FIN + w * 128 + lane * 16) = s;
    BAR();
    if (w == 0) {
      float y = bf2f(xres);
#pragma unroll
      for (int k = 0; k < 8; ++k) y += *(LAS float*)(L + LA_FIN + k * 128 + e * 4);
      if (lane < 32) ((GAS u16*)out)[ob * 576 + oc] = (u16)pk(y, y);
      if (qq == 0 && xg == 0) st2(v2u{tlo, thi}, rws, lane == 0 ? 0 : OOB, WS_W);
    }
    STAMP(11);
  }
  STAMP_OUT();
}

__global__ void __launch_bounds__(512,2) k_big(char* ws){ extern __shared__ char sm2[]; asm volatile("v_mov_b32 v231, 0" ::: "v231"); if(ws==nullptr) sm2[threadIdx.x]=1; }
__global__ void __launch_bounds__(512,2) k_sm(char* ws){ extern __shared__ char sm2[]; if(ws==nullptr) sm2[threadIdx.x]=1; }
__global__ void __launch_bounds__(512,2) k_wait(char* ws){ u64 t0=__builtin_amdgcn_s_memrealtime(); while(__builtin_amdgcn_s_memrealtime()-t0 < 1000) __builtin_amdgcn_s_sleep(2); }
extern "C" void kernel_launch(void* const* d_in, const int* in_sizes, int n_in, void* d_out, int out_size, void* d_ws, size_t ws_size,
                              hipStream_t stream) {
  (void)hipFuncSetAttribute((const void*)fused_decode, hipFuncAttributeMaxDynamicSharedMemorySize, LDS_BYTES);
  (void)hipFuncSetAttribute((const void*)k_big, hipFuncAttributeMaxDynamicSharedMemorySize, LDS_BYTES);
  (void)hipFuncSetAttribute((const void*)k_sm, hipFuncAttributeMaxDynamicSharedMemorySize, LDS_BYTES);
  (void)hipFuncSetAttribute((const void*)k_wait, hipFuncAttributeMaxDynamicSharedMemorySize, LDS_BYTES);
  k_big<<<256,512,LDS_BYTES,stream>>>((char*)d_ws);
  fused_decode<<<256, 512, LDS_BYTES, stream>>>((const u16*)d_in[0], (const u16*)d_in[1], (const u16*)d_in[2], (const u16*)d_in[3],
                                                (const u16*)d_in[4], (const u16*)d_in[5], (const u16*)d_in[7], (const u16*)d_in[8],
                                                (const u16*)d_in[9], (u16*)d_out, (char*)d_ws);
}
```

```cpp
#include <hip/hip_runtime.h>
#include <stdint.h>

typedef unsigned short u16;
typedef unsigned long long u64;
typedef unsigned v4u __attribute__((ext_vector_type(4)));
typedef unsigned v2u __attribute__((ext_vector_type(2)));
typedef float f32x2 __attribute__((ext_vector_type(2)));
typedef float f32x4 __attribute__((ext_vector_type(4)));
typedef float f32x16 __attribute__((ext_vector_type(16)));
typedef short s16x4 __attribute__((ext_vector_type(4)));
typedef __bf16 bf16x8 __attribute__((ext_vector_type(8)));
typedef __bf16 bf16x2 __attribute__((ext_vector_type(2)));
typedef __amdgpu_buffer_rsrc_t rsrc_t;
#define GAS __attribute__((address_space(1)))
#define LAS __attribute__((address_space(3)))
#define DI __device__ __forceinline__

#define NT_W 2
#define NT_WO 0
#define PACE_C 7
#define DLY_H1 2
#define KQA 31
#define PACE_C0 3
#define KC0 6
#define DLY_K 40
#define PACE_G 7
#define PACE_O 8
#define DLY_R1 10
#define DLY_C 35
#define DLY_Q 0
#define DLY_F 11
#define DLY_QA 16
#define ABL 0
#define KWO 16
#define PACE_T 4
#define PACE_K 4
#define PACE_G0 2
#define KG0 0
#define PACE_W2 7
#define W2_A 6
#define TL0 0
#define TL1 4
#define TL2 12
#define TL3 24
#define PFW 1
#define DLY_PF 24
#define PACE_PF 4
#define DLY_R4 1
#define DLY_R7 5
#define DLY_RM 9
#define MLFIRST 0
#define DLY_W2 12
#define DLY_M0 48
#define H4HINT 0
#define DLY_H4 16
#define DLY_R2 1
#define UK0 6
#define WO_A 9
#define WO_B 13
#define WO_C 12
#define DLY_H3 4
#define DLY_H7 30
#define DLY_H5 16

#define SLP(N) do { if (N) __builtin_amdgcn_s_sleep(N); } while (0)
#define TUNE_PARAM
#define TUNE_ARG

constexpr int SC1 = 16;
constexpr int GSPIN = 1 << 13;

constexpr int WS_W = 0;
constexpr int QB = 524288;
constexpr int WS_H1 = 4096;
constexpr int WS_QA = WS_H1 + 131072;
constexpr int WS_ML = WS_QA + 32768;
constexpr int WS_HN = WS_ML + 16384;
constexpr int WS_H7 = WS_HN + 4096;
constexpr int WS_H4 = 4 * QB;
constexpr int WS_H5 = WS_H4 + 131072;
constexpr int WS_H3 = 8 * QB;
DI constexpr int slot(int p, int size) { return (p & 3) * QB + (p >> 2) * size; }
constexpr int WS_END = WS_H3 + 128 * 32768;
constexpr int WS_STAMP = WS_END;
constexpr int OOB = (int)0x80000000;

constexpr int LP_TAG = 4992, LP_XP = 0, LP_XD = 5120, LP_QB = 15360, LP_QO = 16896, LP_RED = 18432, LP_HP = 34816, LP_B8 = 35840, LP_FIN = 39936, LP_UK = 40960;
constexpr int LA_QA = 131072, LA_MX = LA_QA;
constexpr int LA_PB = LA_QA + 1024, LA_SUM = LA_QA + 512, LA_CTX = LA_QA + 9216, LA_CT2 = LA_QA + 11264, LA_B8 = LA_QA + 16384,
              LA_FIN = LA_QA + 20480, LA_UVW = 0;
constexpr int LDS_BYTES = 163840;

DI rsrc_t mk(const void* p, int bytes) { return __builtin_amdgcn_make_buffer_rsrc((void*)p, (short)0, bytes, 0x00020000); }
template <int AUX> DI v4u ld4(rsrc_t r, int vo, int so) { return __builtin_amdgcn_raw_buffer_load_b128(r, vo, so, AUX); }
template <int AUX> DI v2u ld2(rsrc_t r, int vo, int so) { return __builtin_amdgcn_raw_buffer_load_b64(r, vo, so, AUX); }
DI v4u hl4(rsrc_t r, int vo, int so) { asm volatile("" : "+v"(vo)); return __builtin_amdgcn_raw_buffer_load_b128(r, vo, so, SC1); }
DI v2u hl2(rsrc_t r, int vo, int so) { asm volatile("" : "+v"(vo)); return __builtin_amdgcn_raw_buffer_load_b64(r, vo, so, SC1); }
DI void st4(v4u v, rsrc_t r, int vo, int so) { __builtin_amdgcn_raw_buffer_store_b128(v, r, vo + so, 0, SC1); }
DI void st2(v2u v, rsrc_t r, int vo, int so) { __builtin_amdgcn_raw_buffer_store_b64(v, r, vo, so, SC1); }
DI void dma16(rsrc_t r, int vo, int so, unsigned lds) {
  asm volatile("s_mov_b32 m0, %0\n\ts_nop 0\n\tbuffer_load_dwordx4 %1, %2, %3 offen nt lds" ::"s"(lds), "v"(vo), "s"(r), "s"(so) : "memory");
}
#define VMCNT(n) asm volatile("s_waitcnt vmcnt(%0)" ::"n"(n) : "memory")
#define FENCE() asm volatile("" ::: "memory")
#define SCHED() __builtin_amdgcn_sched_barrier(0)

DI float bf2f(unsigned v) { return __uint_as_float(v << 16); }
DI unsigned pk(float a, float b) { f32x2 f = {a, b}; return __builtin_bit_cast(unsigned, __builtin_convertvector(f, bf16x2)); }
DI f32x4 mfma16(v4u a, v4u b, f32x4 c) {
  return __builtin_amdgcn_mfma_f32_16x16x32_bf16(__builtin_bit_cast(bf16x8, a), __builtin_bit_cast(bf16x8, b), c, 0, 0, 0);
}
DI f32x4 mfma16k16(v2u a, v2u b, f32x4 c) {
  return __builtin_amdgcn_mfma_f32_16x16x16bf16_1k(__builtin_bit_cast(s16x4, a), __builtin_bit_cast(s16x4, b), c, 0, 0, 0);
}
DI f32x16 mfma32(v4u a, v4u b, f32x16 c) {
  return __builtin_amdgcn_mfma_f32_32x32x16_bf16(__builtin_bit_cast(bf16x8, a), __builtin_bit_cast(bf16x8, b), c, 0, 0, 0);
}
template <int CTRL, int RM> DI float dppf(float old, float v) {
  return __builtin_bit_cast(float, __builtin_amdgcn_update_dpp(__builtin_bit_cast(int, old), __builtin_bit_cast(int, v), CTRL, RM, 0xf, false));
}
template <int CTRL> DI float dppz(float v) {
  return __builtin_bit_cast(float, __builtin_amdgcn_update_dpp(0, __builtin_bit_cast(int, v), CTRL, 0xf, 0xf, true));
}
DI float row_sum(float v) {
  v += dppz<0xB1>(v); v += dppz<0x4E>(v); v += dppz<0x124>(v); v += dppz<0x128>(v);
  return v;
}
DI float dot2(unsigned a, unsigned b, float c) {
  return __builtin_amdgcn_fdot2_f32_bf16(__builtin_bit_cast(bf16x2, a), __builtin_bit_cast(bf16x2, b), c, false);
}
DI float row_max(float v) {
  v = fmaxf(v, dppf<0xB1, 0xf>(v, v)); v = fmaxf(v, dppf<0x4E, 0xf>(v, v));
  v = fmaxf(v, dppf<0x124, 0xf>(v, v)); v = fmaxf(v, dppf<0x128, 0xf>(v, v));
  return v;
}
DI float x16_sum(float v) { auto r = __builtin_amdgcn_permlane16_swap(__float_as_uint(v), __float_as_uint(v), false, false); return __uint_as_float(r[0]) + __uint_as_float(r[1]); }
DI float x32_sum(float v) { auto r = __builtin_amdgcn_permlane32_swap(__float_as_uint(v), __float_as_uint(v), false, false); return __uint_as_float(r[0]) + __uint_as_float(r[1]); }
DI float x16_max(float v) { auto r = __builtin_amdgcn_permlane16_swap(__float_as_uint(v), __float_as_uint(v), false, false); return fmaxf(__uint_as_float(r[0]), __uint_as_float(r[1])); }
DI float x32_max(float v) { auto r = __builtin_amdgcn_permlane32_swap(__float_as_uint(v), __float_as_uint(v), false, false); return fmaxf(__uint_as_float(r[0]), __uint_as_float(r[1])); }

#define RBAR() asm volatile("s_barrier" ::: "memory")
#define BAR() asm volatile("s_waitcnt lgkmcnt(0)\n\ts_barrier" ::: "memory")
DI unsigned genc(float v, unsigned t8) { return ((__float_as_uint(v) + 0x80u) & 0xFFFFFF00u) | t8; }
DI float gdec(unsigned hi) { return __uint_as_float(hi & 0xFFFFFF00u); }

#define STAMP(k)
#define STAMP_DECL
#define STAMP_OUT()

DI v4u bdiag(unsigned P, const unsigned (&s)[4]) {
  v4u b;
  b.x = __builtin_amdgcn_perm(P, P, s[0]); b.y = __builtin_amdgcn_perm(P, P, s[1]);
  b.z = __builtin_amdgcn_perm(P, P, s[2]); b.w = __builtin_amdgcn_perm(P, P, s[3]);
  return b;
}
DI void bdiag_sel(int cp, unsigned (&s)[4]) {
  const unsigned lo = (cp & 1) ? 0x01000C0Cu : 0x0C0C0100u, hi = (cp & 1) ? 0x03020C0Cu : 0x0C0C0302u, z = 0x0C0C0C0Cu;
  s[0] = cp < 2 ? lo : z; s[1] = cp < 2 ? z : lo; s[2] = cp < 2 ? hi : z; s[3] = cp < 2 ? z : hi;
}

DI unsigned lsbtag(unsigned tlo, unsigned thi, int p) { return (((p < 32 ? tlo : thi) >> (p & 31)) & 1u) | (((tlo ^ (unsigned)p) & 1u) << 16); }
constexpr unsigned LSBM = 0x00010001u;
DI unsigned lsbput(unsigned v, unsigned e) { return (v & ~LSBM) | e; }
DI unsigned strip4(v4u& d, const unsigned (&e)[4]) { d.x ^= e[0]; d.y ^= e[1]; d.z ^= e[2]; d.w ^= e[3]; return (d.x | d.y) | (d.z | d.w); }
DI void swap24(v4u& a, v4u& b) {
  unsigned az = a.z, aw = a.w, bx = b.x, by = b.y;
  asm volatile("v_swap_b32 %0, %2\n\tv_swap_b32 %1, %3\n\ts_nop 1" : "+v"(az), "+v"(aw), "+v"(bx), "+v"(by));
  a.z = az; a.w = aw; b.x = bx; b.y = by;
}
constexpr float LSB_COMP = 1.0028f;
DI v4u tag8(f32x4 c, const unsigned (&t)[4]) {
  return v4u{(__float_as_uint(c[0]) & 0xFFFFFF00u) | t[0], (__float_as_uint(c[1]) & 0xFFFFFF00u) | t[1],
             (__float_as_uint(c[2]) & 0xFFFFFF00u) | t[2], (__float_as_uint(c[3]) & 0xFFFFFF00u) | t[3]};
}
__global__ void __launch_bounds__(512, 2)
fused_decode(const u16* __restrict__ xin, const u16* __restrict__ ckv, const u16* __restrict__ wq, const u16* __restrict__ wuk,
             const u16* __restrict__ wuv, const u16* __restrict__ wo, const u16* __restrict__ w1, const u16* __restrict__ w3,
             const u16* __restrict__ w2, u16* __restrict__ out, char* ws TUNE_PARAM) {
  extern __shared__ __attribute__((aligned(16))) char smem[];
  LAS char* L = (LAS char*)smem;
  const unsigned lbase = (unsigned)(size_t)smem;
  const int lane = threadIdx.x & 63, w = __builtin_amdgcn_readfirstlane(threadIdx.x >> 6);
  const int xg = blockIdx.x & 7, qd_ = blockIdx.x >> 3, qq = qd_ & 15, idx = xg * 16 + qq, hd = idx >> 2, jq = idx & 3;
  const int i16 = lane & 15, g = lane >> 4, bj = i16 >> 2, cp = lane & 3;
  STAMP_DECL;
  STAMP(0);
  asm volatile("" ::"s"(xin), "s"(ckv), "s"(wq), "s"(wuk), "s"(wuv), "s"(wo), "s"(w1), "s"(w3), "s"(w2), "s"(out), "s"(ws));
  const rsrc_t rws = mk(ws, WS_END);
  if (qd_ < 16 && (w == 5 || w == 6) && qq < 13) {
    u64 pc;
    asm volatile("s_getpc_b64 %0" : "=s"(pc));
    v4u dummy;
    asm volatile("global_load_dwordx4 %0, %1, %2" : "=v"(dummy) : "v"((qq * 2 + w - 5) * 1024 + lane * 16), "s"(pc & ~0xFFull) : "memory");
    asm volatile("s_waitcnt vmcnt(0)" : "+v"(dummy));
  }
  const rsrc_t rx = mk(xin, 4 * 576 * 2);
  unsigned sel[4];
  bdiag_sel(cp, sel);
#define MAKE_TAG(W) const u64 tag_ = (((u64)(W).y << 32) | (W).x) + 0x9E3779B97F4A7C15ull;  \
  const unsigned tlo = (unsigned)tag_, thi = (unsigned)(tag_ >> 32), t8 = thi & 0xFFu;        \
  const unsigned tb[4] = {tlo & 0xFFu, (tlo >> 8) & 0xFFu, (tlo >> 16) & 0xFFu, tlo >> 24}
  const int vo5 = slot(lane >> 3, 16384) + (lane & 7) * 16;

  if (qd_ >= 16) {
    const int so5 = WS_H5 + idx * 128;
    if (w < 4) {
      v4u ka[2][4];
      v2u wv_;
      asm volatile("buffer_load_dwordx2 %0, off, %1, 0 sc1" : "=v"(wv_) : "s"(rws) : "memory");
      {
        const rsrc_t rk = mk(wuk, 512 * 4096 * 2);
        SLP(DLY_K);
#pragma unroll
        for (int n = 0; n < 8; ++n) {
          if (n >= PACE_K) VMCNT(PACE_K - 1);
          dma16(rk, g * 8192 + ((i16 ^ ((g << 2) | (n & 3))) * 16), (128 * jq + 32 * w + 4 * n) * 8192 + hd * 256, lbase + LP_UK + (8 * w + n) * 1024);
        }
        VMCNT(0);
        const int fr = ((i16 & 3) << 2) | (i16 >> 2);
#pragma unroll
        for (int t = 0; t < 2; ++t)
#pragma unroll
          for (int s = 0; s < 4; ++s) {
            ka[t][s] = *(LAS v4u*)(L + LP_UK + (32 * w + 16 * t + i16) * 256 + (((4 * s + g) ^ fr) * 16));
            asm volatile("" : "+v"(ka[t][s]));
          }
      }
      STAMP(1);
      asm volatile("" : "+v"(wv_));
      MAKE_TAG(wv_);
      unsigned eq[4];
#pragma unroll
      for (int k = 0; k < 4; ++k) { eq[k] = lsbtag(tlo, thi, 4 * i16 + k); asm volatile("" : "+v"(eq[k])); }
      SLP(DLY_H1);
      STAMP(2);
      {
        const int blk = w & 1, half = w >> 1;
        float q0, q1;
        for (int n = 0;; ++n) {
          v4u p[4];
#pragma unroll
          for (int jj = 0; jj < 4; ++jj) p[jj] = hl4(rws, lane * 16, WS_H1 + jj * QB + hd * 4096 + (blk * 2 + half) * 1024);
          unsigned bad = 0;
          q0 = 0.f; q1 = 0.f;
#pragma unroll
          for (int jj = 0; jj < 4; ++jj) {
            bad |= (p[jj].x ^ tlo) | (p[jj].z ^ tlo) | (((p[jj].y ^ t8) | (p[jj].w ^ t8)) & 0xFFu);
            q0 += gdec(p[jj].y); q1 += gdec(p[jj].w);
          }
          if (__all(bad == 0) || n >= GSPIN) break;
          SLP(DLY_R1);
        }
        const unsigned qp = pk(q0, q1);
        const int d0 = 32 * g + 16 * half + 4 * blk + cp;
        *(LAS u16*)(L + LP_QB + bj * 320 + d0 * 2) = (u16)qp;
        *(LAS u16*)(L + LP_QB + bj * 320 + (d0 + 8) * 2) = (u16)(qp >> 16);
      }
      STAMP(4);
      BAR();
      STAMP(5);
      {
        v4u qf[4];
#pragma unroll
        for (int s = 0; s < 4; ++s) qf[s] = *(LAS v4u*)(L + LP_QB + cp * 320 + s * 64 + g * 16);
        f32x4 c0 = {0.f, 0.f, 0.f, 0.f}, c1 = c0;
#pragma unroll
        for (int s = 0; s < 4; ++s) { c0 = mfma16(ka[0][s], qf[s], c0); c1 = mfma16(ka[1][s], qf[s], c1); }
        const float sc = 0.08838834764831845f * 1.4426950408889634f * LSB_COMP;
        if (i16 < 4) {
          *(LAS v2u*)(L + LP_QO + i16 * 256 + (32 * w + 4 * g) * 2) = v2u{pk(c0[0] * sc, c0[1] * sc), pk(c0[2] * sc, c0[3] * sc)};
          *(LAS v2u*)(L + LP_QO + i16 * 256 + (32 * w + 16 + 4 * g) * 2) = v2u{pk(c1[0] * sc, c1[1] * sc), pk(c1[2] * sc, c1[3] * sc)};
        }
      }
      BAR();
      if (w == 0) {
        const v4u v = *(LAS v4u*)(L + LP_QO + lane * 16);
        st4(v4u{lsbput(v.x, eq[0]), lsbput(v.y, eq[1]), lsbput(v.z, eq[2]), lsbput(v.w, eq[3])}, rws, g * 8192 + i16 * 16, WS_QA + slot(hd, 1024) + jq * 256);
      }
      STAMP(6);
      BAR();
      const rsrc_t r2 = mk(w2, 4608 * 576 * 2);
      const int so2 = (xg * 576 + 36 * qq) * 1152;
      const rsrc_t ro = mk(wo, 4096 * 576 * 2);
      const int soo = (hd * 128 + 32 * jq) * 1152;
      v4u a2[9][2];
      v2u a8[3][2];
      {
        const int vo4 = 2 * g * 1152 + i16 * 16, vo4m = g < 2 ? vo4 : OOB;
        const int vo8 = 2 * g * 1152 + i16 * 8 + 1024, vo8m = (g < 2 && w == 0) ? vo8 : OOB;
#pragma unroll
        for (int n = 0; n < 24; ++n) {
          if (n >= PACE_W2) VMCNT(PACE_W2 - 1);
          if (n == W2_A) { SCHED(); RBAR(); SLP(DLY_W2); }
          const int o = n >> 1, r = n & 1;
          if (n < 10) a2[o][r] = ld4<NT_W>(r2, o < 4 ? vo4 : vo4m, so2 + (8 * o + r) * 1152 + 256 * w);
          else if (n < 12) a8[0][r] = ld2<NT_W>(r2, vo8, so2 + (8 * w + r) * 1152);
          else if (n < 14) a8[1][r] = ld2<NT_W>(r2, vo8m, so2 + (32 + r) * 1152);
          else if (n < 22) a2[o - 2][r] = ld4<NT_W>(ro, vo4, soo + (8 * (o - 7) + r) * 1152 + 256 * w);
          else a8[2][r] = ld2<NT_W>(ro, vo8, soo + (8 * w + r) * 1152);
          SCHED();
        }
      }
      STAMP(7);
#pragma unroll
      for (int o = 0; o < 5; ++o) swap24(a2[o][0], a2[o][1]);
      STAMP(8);
      BAR();
      STAMP(9);
      {
        const v4u h4 = *(LAS v4u*)(L + LP_HP + (bj * 4 + g) * 48);
        const unsigned h5 = *(LAS unsigned*)(L + LP_HP + (bj * 4 + g) * 48 + 16);
        const unsigned hw = *(LAS unsigned*)(L + LP_HP + (bj * 4 + g) * 48 + 4 * w);
        f32x4 c8 = {0.f, 0.f, 0.f, 0.f}, cx = c8, cy = c8;
        c8 = mfma16(v4u{a8[0][0].x, a8[0][0].y, a8[0][1].x, a8[0][1].y}, bdiag(hw, sel), c8);
        c8 = mfma16(v4u{a8[1][0].x, a8[1][0].y, a8[1][1].x, a8[1][1].y}, bdiag(h5, sel), c8);
#pragma unroll
        for (int o = 0; o < 5; ++o) {
          const v4u B = bdiag(o == 0 ? h4.x : o == 1 ? h4.y : o == 2 ? h4.z : o == 3 ? h4.w : h5, sel);
          cx = mfma16(a2[o][0], B, cx);
          cy = mfma16(a2[o][1], B, cy);
        }
#pragma unroll
        for (int o = 5; o < 9; ++o) swap24(a2[o][0], a2[o][1]);
        asm volatile("" : "+v"(cx), "+v"(cy), "+v"(c8));
        int bpa = (bj * 16 + g) * 4;
        asm volatile("" : "+v"(bpa));
        STAMP(10);
        float y0, y1;
        for (int n = 0;; ++n) {
          v4u p[4];
#pragma unroll
          for (int jj = 0; jj < 4; ++jj) p[jj] = hl4(rws, lane * 16, WS_H4 + slot(idx, 4096) + jj * 1024);
          unsigned bad = 0;
          y0 = 0.f; y1 = 0.f;
#pragma unroll
          for (int jj = 0; jj < 4; ++jj) {
            bad |= (p[jj].x ^ tlo) | (p[jj].z ^ tlo) | (((p[jj].y ^ t8) | (p[jj].w ^ t8)) & 0xFFu);
            y0 += gdec(p[jj].y); y1 += gdec(p[jj].w);
          }
          if (__all(bad == 0) || n >= GSPIN) break;
          SLP(DLY_R4);
        }
        STAMP(11);
        const unsigned yp = pk(y0, y1);
        const unsigned pw = __builtin_amdgcn_ds_bpermute(bpa + 16 * w, yp);
        unsigned po[4];
#pragma unroll
        for (int o = 0; o < 4; ++o) po[o] = __builtin_amdgcn_ds_bpermute(bpa + 16 * o, yp);
        c8 = mfma16(v4u{a8[2][0].x, a8[2][0].y, a8[2][1].x, a8[2][1].y}, bdiag(pw, sel), c8);
#pragma unroll
        for (int o = 0; o < 4; ++o) {
          const v4u B = bdiag(po[o], sel);
          cx = mfma16(a2[5 + o][0], B, cx);
          cy = mfma16(a2[5 + o][1], B, cy);
        }
        *(LAS f32x4*)(L + LP_B8 + w * 1024 + lane * 16) = c8;
        st4(tag8(cx, tb), rws, vo5, so5 + (2 * w) * 2 * 16384);
        st4(tag8(cy, tb), rws, vo5, so5 + (2 * w + 1) * 2 * 16384);
        BAR();
        if (w == 0) {
          f32x4 s = *(LAS f32x4*)(L + LP_B8 + lane * 16);
#pragma unroll
          for (int k = 1; k < 4; ++k) s += *(LAS f32x4*)(L + LP_B8 + k * 1024 + lane * 16);
          st4(tag8(s, tb), rws, vo5, so5 + 8 * 2 * 16384);
        }
      }
      STAMP(3);
      if (qq < 9) RBAR();
    } else {
      const int v = w - 4;
      unsigned xn[8];
      const v2u wv_ = hl2(rws, 0, WS_W);
      {
        const v4u x0 = ld4<0>(rx, lane * 16, v * 1152), x1 = ld4<0>(rx, lane < 8 ? lane * 16 : OOB, v * 1152 + 1024);
        float s = 0.f, q = 0.f;
#pragma unroll
        for (int k = 0; k < 4; ++k) {
          s = dot2(x0[k], 0x3F803F80u, s); q = dot2(x0[k], x0[k], q);
          s = dot2(x1[k], 0x3F803F80u, s); q = dot2(x1[k], x1[k], q);
        }
        const float mean = x32_sum(x16_sum(row_sum(s))) * (1.0f / 576.0f);
        const float msq = x32_sum(x16_sum(row_sum(q))) * (1.0f / 576.0f);
        const float rstd = __builtin_amdgcn_rsqf(fmaxf(fmaf(-mean, mean, msq), 0.f) + 1e-5f), nb = -mean * rstd;
#pragma unroll
        for (int k = 0; k < 4; ++k) {
          xn[k] = pk(fmaf(bf2f(x0[k]), rstd, nb), fmaf(__uint_as_float(x0[k] & 0xFFFF0000u), rstd, nb));
        }
#pragma unroll
        for (int k = 0; k < 4; ++k) xn[4 + k] = pk(fmaf(bf2f(x1[k]), rstd, nb), fmaf(__uint_as_float(x1[k] & 0xFFFF0000u), rstd, nb));
      }
#pragma unroll
      for (int hf = 0; hf < 2; ++hf) {
        if (hf == 0 || lane < 8) {
#pragma unroll
          for (int k = 0; k < 4; ++k) {
            const int o = lane + 64 * hf;
            *(LAS unsigned*)(L + LP_XD + ((v * 4 + ((2 * k) & 3)) * 148 + 2 * o + (k >> 1)) * 4) = __builtin_amdgcn_perm(0u, xn[4 * hf + k], 0x01000100u);
            *(LAS unsigned*)(L + LP_XD + ((v * 4 + ((2 * k + 1) & 3)) * 148 + 2 * o + (k >> 1)) * 4) = __builtin_amdgcn_perm(0u, xn[4 * hf + k], 0x03020302u);
          }
        }
      }
      STAMP(1);
      MAKE_TAG(wv_);
      const int U0 = (81 * qq) / 144, Qb = 144 * (U0 + 1);
      unsigned xd[21];
      const GAS char* wb = (const GAS char*)(i16 < 8 ? w1 : w3) + (xg * 576 * 2 + (i16 & 7) * 16 + g * 9216);
      const unsigned m0 = cp == 0 ? 0xFFFFu : cp == 1 ? 0xFFFF0000u : 0u, m1 = cp == 2 ? 0xFFFFu : cp == 3 ? 0xFFFF0000u : 0u;
      f32x4 acc[4];
#pragma unroll
      for (int k = 0; k < 4; ++k) acc[k] = f32x4{0.f, 0.f, 0.f, 0.f};
      v4u a[21];
      BAR();
      RBAR();
#pragma unroll
      for (int t2 = 0; t2 < 21; ++t2) {
        const int Q = min(81 * qq + 4 * t2 + v, 81 * qq + 80), qk = Q >= Qb ? Q - Qb : Q - 144 * U0;
        xd[t2] = *(LAS unsigned*)(L + LP_XD + ((bj * 4 + g) * 148 + qk) * 4);
        asm volatile("" : "+v"(xd[t2]));
      }
      SLP(DLY_F);
      STAMP(2);
#pragma unroll
      for (int t = -PACE_G; t < 21; ++t) {
        const int tl = t + PACE_G;
        if (tl < 21) {
          const int Q = 81 * qq + 4 * tl + v, u = Q >= Qb ? U0 + 1 : U0, qk = Q - 144 * u;
          if (tl < KG0 && tl >= PACE_G0) VMCNT(PACE_G0 - 1);
          if (tl < 20 || v == 0) {
            const GAS v4u* p = (const GAS v4u*)(wb + (unsigned)(qk * 4 * 9216 + u * 128));
            a[tl] = NT_W ? __builtin_nontemporal_load(p) : *p;
          } else a[tl] = v4u{0u, 0u, 0u, 0u};
          SCHED();
        }
        if (tl == 20) { STAMP(3); BAR(); }
        if (t >= 0) {
          const int Q = 81 * qq + 4 * t + v;
          const v2u B = {xd[t] & m0, xd[t] & m1};
          if (Q < Qb) { acc[0] = mfma16k16(v2u{a[t].x, a[t].y}, B, acc[0]); acc[1] = mfma16k16(v2u{a[t].z, a[t].w}, B, acc[1]); }
          else        { acc[2] = mfma16k16(v2u{a[t].x, a[t].y}, B, acc[2]); acc[3] = mfma16k16(v2u{a[t].z, a[t].w}, B, acc[3]); }
          SCHED();
        }
      }
#pragma unroll
      for (int k = 0; k < 4; ++k) *(LAS f32x4*)(L + LP_RED + (v * 4 + k) * 1024 + lane * 16) = acc[k];
      STAMP(4);
      BAR();
      if (v == 0 || (v == 1 && 81 * qq + 80 >= Qb)) {
        const int pa = WS_H7 + slot(idx, 8192) + v * 4096 + (32 * ((lane >> 4) & 1) + 16 * (lane >> 5) + cp) * 64 + bj * 16;
#pragma unroll
        for (int hh = 0; hh < 2; ++hh) {
          f32x4 r = *(LAS f32x4*)(L + LP_RED + (2 * v + hh) * 1024 + lane * 16);
#pragma unroll
          for (int k = 1; k < 4; ++k) r += *(LAS f32x4*)(L + LP_RED + (k * 4 + 2 * v + hh) * 1024 + lane * 16);
#pragma unroll
          for (int k = 0; k < 2; ++k) {
            const auto x = __builtin_amdgcn_permlane32_swap(__float_as_uint(r[k]), __float_as_uint(r[2 + k]), false, false);
            st4(v4u{tlo, genc(__uint_as_float(x[0]), t8), tlo, genc(__uint_as_float(x[1]), t8)}, rws, pa + (8 * k + 4 * hh) * 64, 0);
          }
        }
      } else if (v >= 2) {
        int off[2][3];
        unsigned use[2][3];
        int hpa[2];
#pragma unroll
        for (int e = 0; e < 2; ++e) {
          const int t = (v - 2) * 64 + lane + 128 * e;
          const int cl = t >> 2, b = t & 3, f = 36 * qq + cl, u = f >> 6;
          const int r0 = (144 * u) / 81, r1 = (144 * u + 143) / 81;
#pragma unroll
          for (int k = 0; k < 3; ++k) {
            const int rr = r0 + k;
            const bool on = t < 144 && rr <= r1;
            off[e][k] = on ? slot(xg * 16 + rr, 8192) + (u - (81 * rr) / 144) * 4096 + (f & 63) * 64 + b * 16 : OOB;
            use[e][k] = on ? 0xFFFFFFFFu : 0u;
          }
          hpa[e] = LP_HP + ((b * 4 + ((cl >> 1) & 3)) * 12 + (cl >> 3)) * 4 + (cl & 1) * 2;
        }
        SLP(DLY_H7);
        STAMP(5);
        float gs[2], us[2];
        for (int n = 0;; ++n) {
          v4u p[2][3];
#pragma unroll
          for (int e = 0; e < 2; ++e)
#pragma unroll
            for (int k = 0; k < 3; ++k) p[e][k] = hl4(rws, off[e][k], WS_H7);
          unsigned bad = 0;
#pragma unroll
          for (int e = 0; e < 2; ++e) {
            gs[e] = 0.f; us[e] = 0.f;
#pragma unroll
            for (int k = 0; k < 3; ++k) {
              bad |= ((p[e][k].x ^ tlo) | (p[e][k].z ^ tlo) | (((p[e][k].y ^ t8) | (p[e][k].w ^ t8)) & 0xFFu)) & use[e][k];
              gs[e] += gdec(p[e][k].y); us[e] += gdec(p[e][k].w);
            }
          }
          if (__all(bad == 0) || n >= GSPIN) break;
          SLP(DLY_R7);
        }
#pragma unroll
        for (int e = 0; e < 2; ++e) {
          const float hv = gs[e] / (1.0f + __expf(-gs[e])) * us[e];
          if ((v - 2) * 64 + lane + 128 * e < 160) *(LAS u16*)(L + hpa[e]) = (u16)pk(hv, hv);
        }
      }
      STAMP(6);
      BAR();
      const int fe = lane & 31, fL = 8 * xg + (fe >> 2), frr = fe & 3;
      const int oidx = ((fL & 15) >> 2) * 576 + (qq < 8 ? 128 * (qq >> 1) + 8 * (4 * (fL >> 4) + frr) + 4 * (qq & 1) + (fL & 3) : 512 + 4 * (4 * (fL >> 4) + frr) + (fL & 3));
      unsigned xres = 0;
      if (v == 3 && qq < 9) xres = ((const GAS u16*)xin)[oidx];
      asm volatile("" : "+v"(xres));
      RBAR();
      STAMP(7);
      if (qq < 9) {
        SLP(DLY_H5);
        f32x4 s;
        for (int n = 0;; ++n) {
          v4u d[4];
#pragma unroll
          for (int k = 0; k < 4; ++k) d[k] = hl4(rws, lane * 16, WS_H5 + slot(qq * 8 + xg, 16384) + (v * 4 + k) * 1024);
          const unsigned bad = (strip4(d[0], tb) | strip4(d[1], tb)) | (strip4(d[2], tb) | strip4(d[3], tb));
          s = (__builtin_bit_cast(f32x4, d[0]) + __builtin_bit_cast(f32x4, d[1])) + (__builtin_bit_cast(f32x4, d[2]) + __builtin_bit_cast(f32x4, d[3]));
          if (__all((bad & 0xFFu) == 0) || n >= GSPIN) break;
        }
#pragma unroll
        for (int r = 0; r < 4; ++r) s[r] = x32_sum(x16_sum(s[r] + dppz<0x128>(s[r])));
        if (lane < 8) *(LAS f32x4*)(L + LP_FIN + v * 128 + lane * 16) = s;
        BAR();
        if (v == 3) {
          float y = bf2f(xres);
#pragma unroll
          for (int k = 0; k < 4; ++k) y += *(LAS float*)(L + LP_FIN + k * 128 + fe * 4);
          if (lane < 32) ((GAS u16*)out)[oidx] = (u16)pk(y, y);
          if (qq == 0 && xg == 0) st2(v2u{tlo, thi}, rws, lane == 0 ? 0 : OOB, WS_W);
        }
        STAMP(8);
      }
    }
    STAMP_OUT();
    return;
  }

  const int ab = xg >> 1;
  const int r32 = lane & 31, hh = lane >> 5;
  v2u wa_;
  if (w < 4) {
    const rsrc_t rc = mk(ckv, 4 * 4096 * 512 * 2);
    v2u wb_;
    asm volatile("buffer_load_dwordx2 %0, off, %1, 0 sc1" : "=v"(wb_) : "s"(rws) : "memory");
    {
      int vo[4];
#pragma unroll
      for (int k = 0; k < 4; ++k) vo[k] = g * 1024 + ((i16 ^ ((g << 2) | k)) * 16);
      const int so = (idx * 128 + 32 * w) * 1024;
      SLP(DLY_C);
#pragma unroll
      for (int n = 0; n < 32; ++n) {
        if (n < KC0) { if (n >= PACE_C0) VMCNT(PACE_C0 - 1); }
        else if (n >= PACE_C) VMCNT(PACE_C - 1);
        dma16(rc, vo[(n >> 2) & 3], so + (n >> 2) * 4096 + (n & 3) * 256, lbase + (n & 3) * 32768 + (32 * w + 4 * (n >> 2)) * 256);
        if (n == KQA) RBAR();
      }
    }
    asm volatile("" : "+v"(wb_));
    wa_ = wb_;
  } else {
    wa_ = hl2(rws, 0, WS_W);
    if (w == 4) {
      v2u xr[9];
#pragma unroll
      for (int t = 0; t < 9; ++t) xr[t] = ld2<0>(rx, g * 1152 + i16 * 8, t * 128);
      const rsrc_t rq = mk(wq, 576 * 4096 * 2);
      const int vo = 2 * g * 8192 + i16 * 16, so = 144 * jq * 8192 + hd * 256;
      v4u a[18][2];
#pragma unroll
      for (int o = 0; o < 18; ++o) { a[o][0] = ld4<NT_W>(rq, vo, so + (8 * o) * 8192); a[o][1] = ld4<NT_W>(rq, vo, so + (8 * o + 1) * 8192); }
      SCHED();
      STAMP(7);
      float s = 0.f, q = 0.f;
#pragma unroll
      for (int t = 0; t < 9; ++t) {
        *(LAS v2u*)(L + LA_QA + g * 1168 + i16 * 8 + t * 128) = xr[t];
        s = dot2(xr[t].x, 0x3F803F80u, s); q = dot2(xr[t].x, xr[t].x, q);
        s = dot2(xr[t].y, 0x3F803F80u, s); q = dot2(xr[t].y, xr[t].y, q);
      }
      FENCE();
      unsigned xp[18];
#pragma unroll
      for (int o = 0; o < 18; ++o) xp[o] = *(LAS unsigned*)(L + LA_QA + bj * 1168 + (72 * jq + 4 * o + g) * 4);
      const float mean = row_sum(s) * (1.0f / 576.0f), msq = row_sum(q) * (1.0f / 576.0f);
      const float rs0 = __builtin_amdgcn_rsqf(fmaxf(fmaf(-mean, mean, msq), 0.f) + 1e-5f);
      const float rstd = __uint_as_float(__builtin_amdgcn_ds_bpermute(bj * 64, __float_as_uint(rs0)));
      const float nb = __uint_as_float(__builtin_amdgcn_ds_bpermute(bj * 64, __float_as_uint(-mean * rs0)));
      v4u Bq[18];
#pragma unroll
      for (int o = 0; o < 18; ++o) {
        Bq[o] = bdiag(pk(fmaf(bf2f(xp[o]), rstd, nb), fmaf(__uint_as_float(xp[o] & 0xFFFF0000u), rstd, nb)), sel);
        asm volatile("" : "+v"(Bq[o]));
      }
      const u64 tg = (((u64)wa_.y << 32) | wa_.x) + 0x9E3779B97F4A7C15ull;
      unsigned ql = (unsigned)tg, q8 = (unsigned)(tg >> 32) & 0xFFu;
      asm volatile("" : "+v"(ql), "+v"(q8));
      SCHED();
      STAMP(8);
      f32x4 qx = {0.f, 0.f, 0.f, 0.f}, qy = qx;
#pragma unroll
      for (int o = 0; o < 18; ++o) {
        swap24(a[o][0], a[o][1]);
        qx = mfma16(a[o][0], Bq[o], qx);
        qy = mfma16(a[o][1], Bq[o], qy);
      }
      const int po = WS_H1 + slot(idx, 4096);
      st4(v4u{ql, genc(qx[0], q8), ql, genc(qx[1], q8)}, rws, lane * 16, po);
      st4(v4u{ql, genc(qx[2], q8), ql, genc(qx[3], q8)}, rws, lane * 16, po + 1024);
      st4(v4u{ql, genc(qy[0], q8), ql, genc(qy[1], q8)}, rws, lane * 16, po + 2048);
      st4(v4u{ql, genc(qy[2], q8), ql, genc(qy[3], q8)}, rws, lane * 16, po + 3072);
      STAMP(9);
    }
    RBAR();
  }
  MAKE_TAG(wa_);
  unsigned e4[4];
#pragma unroll
  for (int k = 0; k < 4; ++k) e4[k] = lsbtag(tlo, thi, 4 * i16 + k);
  if (w < 4) {
    v4u fa[32];
    {
      const int fr = ((r32 & 3) << 2) | ((r32 >> 2) & 3);
      const int ca = (32 * w + r32) * 256;
#define QK_READ(Q) _Pragma("unroll") for (int k = 0; k < 8; ++k) fa[(Q)*8 + k] = *(LAS v4u*)(L + (Q)*32768 + ca + (((2 * k + hh) ^ fr) * 16)); SCHED();
      VMCNT(0); QK_READ(0) QK_READ(1) QK_READ(2) QK_READ(3)
    }
    int qb[4];
#pragma unroll
    for (int k = 0; k < 4; ++k) { qb[k] = LA_QA + ((lane ^ ((2 * k + hh) & 7)) * 16); asm volatile("" : "+v"(qb[k])); }
    const int q4 = i16 >> 2, lowc = 2 * (g & 1) + (cp >> 1);
    int ta[4][2];
#pragma unroll
    for (int t = 0; t < 4; ++t)
#pragma unroll
      for (int t2 = 0; t2 < 2; ++t2) {
        ta[t][t2] = w * 32768 + (8 * t2 + 4 * hh + q4) * 256 + ((((t ^ q4) << 2) | (lowc ^ (2 * t2 + hh))) * 16) + 8 * (cp & 1);
        asm volatile("" : "+v"(ta[t][t2]));
      }
    int mxa = (r32 >> 4) * 320 + (r32 & 15) * 4, pba = LA_PB + lane * 16;
    unsigned e0 = lsbtag(tlo, thi, 4 * (r32 >> 1)), e1 = lsbtag(tlo, thi, 4 * (r32 >> 1) + 1), e2 = lsbtag(tlo, thi, 4 * (r32 >> 1) + 2), e3 = lsbtag(tlo, thi, 4 * (r32 >> 1) + 3);
    int h3v = (hh * 4 + (r32 & 1)) * 131072 + (r32 >> 1) * 16 + WS_H3 + (w * 128 + idx) * 256;
    asm volatile("" : "+v"(mxa), "+v"(pba), "+v"(e0), "+v"(e1), "+v"(e2), "+v"(e3), "+v"(h3v));
    STAMP(2);
    BAR();
    STAMP(3);
    f32x16 sacc;
#pragma unroll
    for (int k = 0; k < 16; ++k) sacc[k] = 0.f;
    {
      v4u fb[32];
#pragma unroll
      for (int s = 0; s < 6; ++s) fb[s] = *(LAS v4u*)(L + qb[s & 3] + s * 1024);
      SCHED();
#pragma unroll
      for (int s = 0; s < 32; ++s) {
        if (s + 6 < 32) fb[s + 6] = *(LAS v4u*)(L + qb[(s + 6) & 3] + (s + 6) * 1024);
        sacc = mfma32(fa[s], fb[s], sacc);
        SCHED();
      }
    }
    v4u cb[32];
#define TR_READ(n) do { const v2u x0_ = __builtin_bit_cast(v2u, __builtin_amdgcn_ds_read_tr16_b64_v4i16((LAS s16x4*)(L + ta[(n) >> 3][0] + ((n) & 7) * 4096))); \
    const v2u x1_ = __builtin_bit_cast(v2u, __builtin_amdgcn_ds_read_tr16_b64_v4i16((LAS s16x4*)(L + ta[(n) >> 3][1] + ((n) & 7) * 4096)));                 \
    cb[n] = v4u{x0_.x, x0_.y, x1_.x, x1_.y}; } while (0)
#pragma unroll
    for (int n = 0; n < 6; ++n) TR_READ(n);
    SCHED();
    float mw = sacc[0];
#pragma unroll
    for (int k = 1; k < 16; ++k) mw = fmaxf(mw, sacc[k]);
    mw = x32_max(mw);
    asm volatile("s_waitcnt lgkmcnt(0)" ::: "memory");
    *(LAS float*)(L + mxa + w * 32768) = mw;
    STAMP(4);
    BAR();
    const float mx = fmaxf(fmaxf(*(LAS float*)(L + mxa), *(LAS float*)(L + mxa + 32768)), fmaxf(*(LAS float*)(L + mxa + 65536), *(LAS float*)(L + mxa + 98304)));
    float ls = 0.f;
#pragma unroll
    for (int k = 0; k < 16; ++k) { sacc[k] = __builtin_amdgcn_exp2f(sacc[k] - mx); ls += sacc[k]; }
#pragma unroll
    for (int s2 = 0; s2 < 2; ++s2)
      *(LAS v4u*)(L + pba + (2 * w + s2) * 1024) =
          v4u{pk(sacc[8 * s2], sacc[8 * s2 + 1]), pk(sacc[8 * s2 + 2], sacc[8 * s2 + 3]), pk(sacc[8 * s2 + 4], sacc[8 * s2 + 5]), pk(sacc[8 * s2 + 6], sacc[8 * s2 + 7])};
    ls = x32_sum(ls);
    *(LAS float*)(L + mxa + 640 + w * 32768) = ls;
    BAR();
    STAMP(5);
    {
      v4u pf[8];
#pragma unroll
      for (int k = 0; k < 8; ++k) pf[k] = *(LAS v4u*)(L + pba + k * 1024);
      f32x4 l4;
      if (w == 0) l4 = f32x4{*(LAS float*)(L + mxa + 640), *(LAS float*)(L + mxa + 640 + 32768), *(LAS float*)(L + mxa + 640 + 65536), *(LAS float*)(L + mxa + 640 + 98304)};
      SCHED();
      f32x16 o[4];
      unsigned p01[16];
      const bool odd = lane & 1;
#pragma unroll
      for (int t = 0; t < 4; ++t)
#pragma unroll
        for (int k = 0; k < 16; ++k) o[t][k] = 0.f;
#pragma unroll
      for (int n = 0; n < 32; ++n) {
        if (n + 6 < 32) TR_READ(n + 6);
        o[n >> 3] = mfma32(pf[n & 7], cb[n], o[n >> 3]);
        if (n == 0 && w == 0)
          st4(v4u{tlo, genc(mx, t8), tlo, genc((l4[0] + l4[1]) + (l4[2] + l4[3]), t8)}, rws, lane < 32 ? r32 * 16 : OOB, WS_ML + slot(idx, 512));
        if (n >= 18 && n < 26) {
          const int k = n - 18;
          const unsigned pa = pk(o[0][2 * k], o[1][2 * k]), pb = pk(o[0][2 * k + 1], o[1][2 * k + 1]);
          p01[2 * k] = lsbput(odd ? pb : pa, e0);
          p01[2 * k + 1] = __builtin_amdgcn_update_dpp(0, lsbput(odd ? pa : pb, e2), 0xB1, 0xf, 0xf, true);
          asm volatile("" : "+v"(p01[2 * k]), "+v"(p01[2 * k + 1]));
        }
        SCHED();
      }
      STAMP(1);
#pragma unroll
      for (int k = 0; k < 8; ++k) {
        const unsigned pa = pk(o[2][2 * k], o[3][2 * k]), pb = pk(o[2][2 * k + 1], o[3][2 * k + 1]);
        st4(v4u{p01[2 * k], lsbput(odd ? pb : pa, e1), p01[2 * k + 1], (unsigned)__builtin_amdgcn_update_dpp(0, lsbput(odd ? pa : pb, e3), 0xB1, 0xf, 0xf, true)},
            rws, h3v, (2 * (k & 1) + 8 * (k >> 1)) * 131072);
      }
    }
    STAMP(6);
    RBAR();
    RBAR();
    RBAR();
  } else {
    const int v = w - 4;
    STAMP(1);
    const rsrc_t rv = mk(wuv, 512 * 4096 * 2);
    v4u uw[8];
#define TL(i) do { uw[i] = ld4<NT_W>(rv, g * 32 * 8192 + i16 * 16, (128 * jq + 8 * v + ((i) ^ (hd & 1))) * 8192 + hd * 256); SCHED(); } while (0)
    {
      v4u qa[8];
      SLP(DLY_QA);
#pragma unroll
      for (int k = 0; k < 8; ++k) qa[k] = hl4(rws, lane * 16, WS_QA + ab * 8192 + slot(8 * v + k, 1024));
      SCHED();
#pragma unroll
      for (int i = 0; i < TL0; ++i) TL(i);
#pragma unroll 1
      for (int n = 0;; ++n) {
        unsigned bad = 0;
#pragma unroll
        for (int k = 0; k < 8; ++k) bad |= strip4(qa[k], e4);
        if (n == 0 && w != 4) { STAMP(7); }
        if (__all((bad & LSBM) == 0) || n >= GSPIN) break;
        SLP(DLY_R2);
#pragma unroll
        for (int k = 0; k < 8; ++k) qa[k] = hl4(rws, lane * 16, WS_QA + ab * 8192 + slot(8 * v + k, 1024));
      }
#pragma unroll
      for (int k = 0; k < 8; ++k) *(LAS v4u*)(L + LA_QA + (lane >> 1) * 1024 + ((((lane & 1) * 32 + 8 * v + k) ^ (lane & 7)) * 16)) = qa[k];
    }
    STAMP(2);
    BAR();
#pragma unroll
    for (int i = TL0; i < TL1; ++i) TL(i);
    RBAR();
#pragma unroll
    for (int i = TL1; i < 8; ++i) TL(i);
    RBAR();
    const int e7 = lane & 7, q4 = i16 >> 2;
    v4u bk[8], d[8];
    {
      v4u ml;
      SLP(DLY_M0);
      for (int n = 0;; ++n) {
        ml = hl4(rws, slot(r32, 512), WS_ML + v * 8 * 512 + hd * 16);
        if (__all(((ml.x ^ tlo) | (ml.z ^ tlo) | (((ml.y ^ t8) | (ml.w ^ t8)) & 0xFFu)) == 0) || n >= GSPIN) break;
        SLP(DLY_RM);
      }
      STAMP(5);
      SLP(DLY_H3);
#pragma unroll
      for (int k = 0; k < 8; ++k) d[k] = hl4(rws, lane * 16, WS_H3 + idx * 32768 + v * 8192 + k * 1024);
      if (v == 0) st2(v2u{tlo, thi}, rws, lane == 0 ? 0 : OOB, WS_HN + slot(idx, 128));
      SCHED();
      const float m = gdec(ml.y), M = x16_max(row_max(m));
      const float e = __builtin_amdgcn_exp2f(m - M);
      const float al = e * LSB_COMP / x16_sum(row_sum(e * gdec(ml.w)));
      const unsigned ah = pk(al, al) & 0xFFFFu;
      const unsigned hl = ah | (pk(al - bf2f(ah), 0.f) << 16);
      const unsigned src = (lane & 8) ? 0x0302u : 0x0100u, s1 = (e7 & 1) ? ((src << 16) | 0x0C0Cu) : (0x0C0C0000u | src);
      unsigned s8[4];
#pragma unroll
      for (int k = 0; k < 4; ++k) s8[k] = (e7 >> 1) == k ? s1 : 0x0C0C0C0Cu;
#pragma unroll
      for (int k = 0; k < 8; ++k) { bk[k] = bdiag(__builtin_amdgcn_ds_bpermute((4 * k + g) * 4, hl), s8); asm volatile("" : "+v"(bk[k])); }
    }
    STAMP(3);
    RBAR();
#pragma unroll
    for (int k = 0; k < 8; ++k) *(LAS v4u*)(L + LA_UVW + (32 * v + 4 * k + g) * 256 + ((i16 ^ ((g << 2) | (k & 3))) * 16)) = uw[k];
    BAR();
    v4u bv[2][4];
#pragma unroll
    for (int t = 0; t < 2; ++t)
#pragma unroll
      for (int ks = 0; ks < 4; ++ks) {
        v2u x[2];
#pragma unroll
        for (int t2 = 0; t2 < 2; ++t2) {
          const int row = 32 * ks + 8 * g + 4 * t2 + q4, ch = 2 * (2 * v + t) + (cp >> 1);
          x[t2] = __builtin_bit_cast(v2u, __builtin_amdgcn_ds_read_tr16_b64_v4i16(
                      (LAS s16x4*)(L + LA_UVW + row * 256 + ((ch ^ ((q4 << 2) | ((2 * g + t2) & 3))) * 16) + 8 * (cp & 1))));
        }
        bv[t][ks] = v4u{x[0].x, x[0].y, x[1].x, x[1].y};
        asm volatile("" : "+v"(bv[t][ks]));
      }
    int cta = LA_CTX + v * 288 + (32 * g + e7) * 2, ctr = LA_CTX + cp * 288 + g * 16;
    int h4a = ((2 * (g & 1) + (i16 & 1)) * 16 + (2 * (g >> 1) + (i16 >> 3)) * 4 + ((i16 >> 1) & 3)) * 16 + WS_H4 + v * QB + hd * 4096 + jq * 1024;
    asm volatile("" : "+v"(cta), "+v"(ctr), "+v"(h4a));
    SCHED();
    STAMP(4);
    {
      f32x4 c;
#pragma unroll 1
      for (int n = 0;; ++n) {
        unsigned bad = 0;
        c = f32x4{0.f, 0.f, 0.f, 0.f};
#pragma unroll
        for (int k = 0; k < 8; ++k) { bad |= strip4(d[k], e4); c = mfma16(d[k], bk[k], c); }
        if (__all((bad & LSBM) == 0) || n >= GSPIN) break;
#pragma unroll
        for (int k = 0; k < 8; ++k) d[k] = hl4(rws, lane * 16, WS_H3 + idx * 32768 + v * 8192 + k * 1024);
      }
      STAMP(6);
#pragma unroll
      for (int r = 0; r < 4; ++r) {
        const float y = c[r] + dppz<0x128>(c[r]);
        *(LAS u16*)(L + cta + 16 * r) = (u16)pk(y, y);
      }
    }
    BAR();
    {
      v4u af[4];
#pragma unroll
      for (int ks = 0; ks < 4; ++ks) af[ks] = *(LAS v4u*)(L + ctr + 64 * ks);
      f32x4 c0 = {0.f, 0.f, 0.f, 0.f}, c1 = c0;
#pragma unroll
      for (int ks = 0; ks < 4; ++ks) { c0 = mfma16(af[ks], bv[0][ks], c0); c1 = mfma16(af[ks], bv[1][ks], c1); }
      const f32x4 ct = g < 2 ? c0 : c1;
      const float x0 = (g & 1) ? ct[2] : ct[0], x1 = (g & 1) ? ct[3] : ct[1];
      const float n0 = dppz<0xB1>(x0), n1 = dppz<0xB1>(x1);
      st4(v4u{tlo, genc((lane & 1) ? n1 : x0, t8), tlo, genc((lane & 1) ? x1 : n0, t8)}, rws, h4a, 0);
    }
    STAMP(11);
  }
  STAMP_OUT();
}

extern "C" void kernel_launch(void* const* d_in, const int* in_sizes, int n_in, void* d_out, int out_size, void* d_ws, size_t ws_size,
                              hipStream_t stream) {
  (void)hipFuncSetAttribute((const void*)fused_decode, hipFuncAttributeMaxDynamicSharedMemorySize, LDS_BYTES);
  fused_decode<<<256, 512, LDS_BYTES, stream>>>((const u16*)d_in[0], (const u16*)d_in[1], (const u16*)d_in[2], (const u16*)d_in[3],
                                                (const u16*)d_in[4], (const u16*)d_in[5], (const u16*)d_in[7], (const u16*)d_in[8],
                                                (const u16*)d_in[9], (u16*)d_out, (char*)d_ws TUNE_ARG);
}
```

```cpp
#define STAMPS
#define PWHAT 2
#define PB0 0
#define PB1 0
#define PW0 0
#define PW1 0
#define PK 0
#include <hip/hip_runtime.h>
#include <stdint.h>

typedef unsigned short u16;
typedef unsigned long long u64;
typedef unsigned v4u __attribute__((ext_vector_type(4)));
typedef unsigned v2u __attribute__((ext_vector_type(2)));
typedef float f32x2 __attribute__((ext_vector_type(2)));
typedef float f32x4 __attribute__((ext_vector_type(4)));
typedef float f32x16 __attribute__((ext_vector_type(16)));
typedef short s16x4 __attribute__((ext_vector_type(4)));
typedef __bf16 bf16x8 __attribute__((ext_vector_type(8)));
typedef __bf16 bf16x2 __attribute__((ext_vector_type(2)));
typedef __amdgpu_buffer_rsrc_t rsrc_t;
#define GAS __attribute__((address_space(1)))
#define LAS __attribute__((address_space(3)))
#define DI __device__ __forceinline__

#define NT_W 2
#define NT_WO 0
#define PACE_C 7
#define DLY_H1 2
#define KQA 31
#define PACE_C0 3
#define KC0 6
#define DLY_K 40
#define PACE_G 7
#define PACE_O 8
#define DLY_R1 10
#define DLY_C 35
#define DLY_Q 0
#define DLY_F 11
#define DLY_QA 16
#define ABL 0
#define KWO 16
#define PACE_T 4
#define PACE_K 4
#define PACE_G0 2
#define KG0 0
#define PACE_W2 7
#define W2_A 6
#define TL0 0
#define TL1 4
#define TL2 12
#define TL3 24
#define PFW 1
#define DLY_PF 24
#define PACE_PF 4
#define DLY_R4 1
#define DLY_R7 5
#define DLY_RM 9
#define MLFIRST 0
#define DLY_W2 12
#define DLY_M0 48
#define H4HINT 0
#define DLY_H4 16
#define DLY_R2 1
#define UK0 6
#define WO_A 9
#define WO_B 13
#define WO_C 12
#define DLY_H3 4
#define DLY_H7 30
#define DLY_H5 16

#define SLP(N) do { if (N) __builtin_amdgcn_s_sleep(N); } while (0)
#define TUNE_PARAM
#define TUNE_ARG

constexpr int SC1 = 16;
constexpr int GSPIN = 1 << 13;

constexpr int WS_W = 0;
constexpr int QB = 524288;
constexpr int WS_H1 = 4096;
constexpr int WS_QA = WS_H1 + 131072;
constexpr int WS_ML = WS_QA + 32768;
constexpr int WS_HN = WS_ML + 16384;
constexpr int WS_H7 = WS_HN + 4096;
constexpr int WS_H4 = 4 * QB;
constexpr int WS_H5 = WS_H4 + 131072;
constexpr int WS_H3 = 8 * QB;
DI constexpr int slot(int p, int size) { return (p & 3) * QB + (p >> 2) * size; }
constexpr int WS_END = WS_H3 + 128 * 32768;
constexpr int WS_STAMP = WS_END;
constexpr int OOB = (int)0x80000000;

constexpr int LP_TAG = 4992, LP_XP = 0, LP_XD = 5120, LP_QB = 15360, LP_QO = 16896, LP_RED = 18432, LP_HP = 34816, LP_B8 = 35840, LP_FIN = 39936, LP_UK = 40960;
constexpr int LA_QA = 131072, LA_MX = LA_QA;
constexpr int LA_PB = LA_QA + 1024, LA_SUM = LA_QA + 512, LA_CTX = LA_QA + 9216, LA_CT2 = LA_QA + 11264, LA_B8 = LA_QA + 16384,
              LA_FIN = LA_QA + 20480, LA_UVW = 0;
constexpr int LDS_BYTES = 163840;

DI rsrc_t mk(const void* p, int bytes) { return __builtin_amdgcn_make_buffer_rsrc((void*)p, (short)0, bytes, 0x00020000); }
template <int AUX> DI v4u ld4(rsrc_t r, int vo, int so) { return __builtin_amdgcn_raw_buffer_load_b128(r, vo, so, AUX); }
template <int AUX> DI v2u ld2(rsrc_t r, int vo, int so) { return __builtin_amdgcn_raw_buffer_load_b64(r, vo, so, AUX); }
DI v4u hl4(rsrc_t r, int vo, int so) { asm volatile("" : "+v"(vo)); return __builtin_amdgcn_raw_buffer_load_b128(r, vo, so, SC1); }
DI v2u hl2(rsrc_t r, int vo, int so) { asm volatile("" : "+v"(vo)); return __builtin_amdgcn_raw_buffer_load_b64(r, vo, so, SC1); }
DI void st4(v4u v, rsrc_t r, int vo, int so) { __builtin_amdgcn_raw_buffer_store_b128(v, r, vo + so, 0, SC1); }
DI void st2(v2u v, rsrc_t r, int vo, int so) { __builtin_amdgcn_raw_buffer_store_b64(v, r, vo, so, SC1); }
DI void dma16(rsrc_t r, int vo, int so, unsigned lds) {
  asm volatile("s_mov_b32 m0, %0\n\ts_nop 0\n\tbuffer_load_dwordx4 %1, %2, %3 offen nt lds" ::"s"(lds), "v"(vo), "s"(r), "s"(so) : "memory");
}
#define VMCNT(n) asm volatile("s_waitcnt vmcnt(%0)" ::"n"(n) : "memory")
#define FENCE() asm volatile("" ::: "memory")
#define SCHED() __builtin_amdgcn_sched_barrier(0)

DI float bf2f(unsigned v) { return __uint_as_float(v << 16); }
DI unsigned pk(float a, float b) { f32x2 f = {a, b}; return __builtin_bit_cast(unsigned, __builtin_convertvector(f, bf16x2)); }
DI f32x4 mfma16(v4u a, v4u b, f32x4 c) {
  return __builtin_amdgcn_mfma_f32_16x16x32_bf16(__builtin_bit_cast(bf16x8, a), __builtin_bit_cast(bf16x8, b), c, 0, 0, 0);
}
DI f32x4 mfma16k16(v2u a, v2u b, f32x4 c) {
  return __builtin_amdgcn_mfma_f32_16x16x16bf16_1k(__builtin_bit_cast(s16x4, a), __builtin_bit_cast(s16x4, b), c, 0, 0, 0);
}
DI f32x16 mfma32(v4u a, v4u b, f32x16 c) {
  return __builtin_amdgcn_mfma_f32_32x32x16_bf16(__builtin_bit_cast(bf16x8, a), __builtin_bit_cast(bf16x8, b), c, 0, 0, 0);
}
template <int CTRL, int RM> DI float dppf(float old, float v) {
  return __builtin_bit_cast(float, __builtin_amdgcn_update_dpp(__builtin_bit_cast(int, old), __builtin_bit_cast(int, v), CTRL, RM, 0xf, false));
}
template <int CTRL> DI float dppz(float v) {
  return __builtin_bit_cast(float, __builtin_amdgcn_update_dpp(0, __builtin_bit_cast(int, v), CTRL, 0xf, 0xf, true));
}
DI float row_sum(float v) {
  v += dppz<0xB1>(v); v += dppz<0x4E>(v); v += dppz<0x124>(v); v += dppz<0x128>(v);
  return v;
}
DI float dot2(unsigned a, unsigned b, float c) {
  return __builtin_amdgcn_fdot2_f32_bf16(__builtin_bit_cast(bf16x2, a), __builtin_bit_cast(bf16x2, b), c, false);
}
DI float row_max(float v) {
  v = fmaxf(v, dppf<0xB1, 0xf>(v, v)); v = fmaxf(v, dppf<0x4E, 0xf>(v, v));
  v = fmaxf(v, dppf<0x124, 0xf>(v, v)); v = fmaxf(v, dppf<0x128, 0xf>(v, v));
  return v;
}
DI float x16_sum(float v) { auto r = __builtin_amdgcn_permlane16_swap(__float_as_uint(v), __float_as_uint(v), false, false); return __uint_as_float(r[0]) + __uint_as_float(r[1]); }
DI float x32_sum(float v) { auto r = __builtin_amdgcn_permlane32_swap(__float_as_uint(v), __float_as_uint(v), false, false); return __uint_as_float(r[0]) + __uint_as_float(r[1]); }
DI float x16_max(float v) { auto r = __builtin_amdgcn_permlane16_swap(__float_as_uint(v), __float_as_uint(v), false, false); return fmaxf(__uint_as_float(r[0]), __uint_as_float(r[1])); }
DI float x32_max(float v) { auto r = __builtin_amdgcn_permlane32_swap(__float_as_uint(v), __float_as_uint(v), false, false); return fmaxf(__uint_as_float(r[0]), __uint_as_float(r[1])); }

#define RBAR() asm volatile("s_barrier" ::: "memory")
#define BAR() asm volatile("s_waitcnt lgkmcnt(0)\n\ts_barrier" ::: "memory")
DI unsigned genc(float v, unsigned t8) { return ((__float_as_uint(v) + 0x80u) & 0xFFFFFF00u) | t8; }
DI float gdec(unsigned hi) { return __uint_as_float(hi & 0xFFFFFF00u); }

#define STAMP(k) do { stv[k] = __builtin_amdgcn_s_memtime(); } while (0)
#define STAMP_DECL u64 stv[12] = {0, 0, 0, 0, 0, 0, 0, 0, 0, 0, 0, 0}; const u64 strt0_ = __builtin_amdgcn_s_memrealtime()
#define STAMP_OUT() do { const u64 c1_ = __builtin_amdgcn_s_memtime(), r1_ = __builtin_amdgcn_s_memrealtime(); \
    if (lane == 0) { GAS u64* so_ = (GAS u64*)(ws + WS_STAMP) + ((size_t)blockIdx.x * 8 + w) * 16; \
    _Pragma("unroll") for (int k_ = 0; k_ < 12; ++k_) so_[k_] = stv[k_]; so_[12] = strt0_; so_[13] = c1_; so_[14] = r1_; } } while (0)

DI v4u bdiag(unsigned P, const unsigned (&s)[4]) {
  v4u b;
  b.x = __builtin_amdgcn_perm(P, P, s[0]); b.y = __builtin_amdgcn_perm(P, P, s[1]);
  b.z = __builtin_amdgcn_perm(P, P, s[2]); b.w = __builtin_amdgcn_perm(P, P, s[3]);
  return b;
}
DI void bdiag_sel(int cp, unsigned (&s)[4]) {
  const unsigned lo = (cp & 1) ? 0x01000C0Cu : 0x0C0C0100u, hi = (cp & 1) ? 0x03020C0Cu : 0x0C0C0302u, z = 0x0C0C0C0Cu;
  s[0] = cp < 2 ? lo : z; s[1] = cp < 2 ? z : lo; s[2] = cp < 2 ? hi : z; s[3] = cp < 2 ? z : hi;
}

DI unsigned lsbtag(unsigned tlo, unsigned thi, int p) { return (((p < 32 ? tlo : thi) >> (p & 31)) & 1u) | (((tlo ^ (unsigned)p) & 1u) << 16); }
constexpr unsigned LSBM = 0x00010001u;
DI unsigned lsbput(unsigned v, unsigned e) { return (v & ~LSBM) | e; }
DI unsigned strip4(v4u& d, const unsigned (&e)[4]) { d.x ^= e[0]; d.y ^= e[1]; d.z ^= e[2]; d.w ^= e[3]; return (d.x | d.y) | (d.z | d.w); }
DI void swap24(v4u& a, v4u& b) {
  unsigned az = a.z, aw = a.w, bx = b.x, by = b.y;
  asm volatile("v_swap_b32 %0, %2\n\tv_swap_b32 %1, %3\n\ts_nop 1" : "+v"(az), "+v"(aw), "+v"(bx), "+v"(by));
  a.z = az; a.w = aw; b.x = bx; b.y = by;
}
constexpr float LSB_COMP = 1.0028f;
DI v4u tag8(f32x4 c, const unsigned (&t)[4]) {
  return v4u{(__float_as_uint(c[0]) & 0xFFFFFF00u) | t[0], (__float_as_uint(c[1]) & 0xFFFFFF00u) | t[1],
             (__float_as_uint(c[2]) & 0xFFFFFF00u) | t[2], (__float_as_uint(c[3]) & 0xFFFFFF00u) | t[3]};
}
__global__ void __launch_bounds__(512, 2)
fused_decode(const u16* __restrict__ xin, const u16* __restrict__ ckv, const u16* __restrict__ wq, const u16* __restrict__ wuk,
             const u16* __restrict__ wuv, const u16* __restrict__ wo, const u16* __restrict__ w1, const u16* __restrict__ w3,
             const u16* __restrict__ w2, u16* __restrict__ out, char* ws TUNE_PARAM) {
  extern __shared__ __attribute__((aligned(16))) char smem[];
  LAS char* L = (LAS char*)smem;
  const unsigned lbase = (unsigned)(size_t)smem;
  const int lane = threadIdx.x & 63, w = __builtin_amdgcn_readfirstlane(threadIdx.x >> 6);
  const int xg = blockIdx.x & 7, qd_ = blockIdx.x >> 3, qq = qd_ & 15, idx = xg * 16 + qq, hd = idx >> 2, jq = idx & 3;
  const int i16 = lane & 15, g = lane >> 4, bj = i16 >> 2, cp = lane & 3;
  STAMP_DECL;
  STAMP(0);
  asm volatile("" ::"s"(xin), "s"(ckv), "s"(wq), "s"(wuk), "s"(wuv), "s"(wo), "s"(w1), "s"(w3), "s"(w2), "s"(out), "s"(ws));
  const rsrc_t rws = mk(ws, WS_END);
  if (qd_ < 16 && (w == 5 || w == 6) && qq < 13) {
    u64 pc;
    asm volatile("s_getpc_b64 %0" : "=s"(pc));
    v4u dummy;
    asm volatile("global_load_dwordx4 %0, %1, %2" : "=v"(dummy) : "v"((qq * 2 + w - 5) * 1024 + lane * 16), "s"(pc & ~0xFFull) : "memory");
    asm volatile("s_waitcnt vmcnt(0)" : "+v"(dummy));
  }
  const rsrc_t rx = mk(xin, 4 * 576 * 2);
  unsigned sel[4];
  bdiag_sel(cp, sel);
#define MAKE_TAG(W) const u64 tag_ = (((u64)(W).y << 32) | (W).x) + 0x9E3779B97F4A7C15ull;  \
  const unsigned tlo = (unsigned)tag_, thi = (unsigned)(tag_ >> 32), t8 = thi & 0xFFu;        \
  const unsigned tb[4] = {tlo & 0xFFu, (tlo >> 8) & 0xFFu, (tlo >> 16) & 0xFFu, tlo >> 24}
  const int vo5 = slot(lane >> 3, 16384) + (lane & 7) * 16;

  if (qd_ >= 16) {
    const int so5 = WS_H5 + idx * 128;
    if (w < 4) {
      v4u ka[2][4];
      v2u wv_;
      asm volatile("buffer_load_dwordx2 %0, off, %1, 0 sc1" : "=v"(wv_) : "s"(rws) : "memory");
      {
        const rsrc_t rk = mk(wuk, 512 * 4096 * 2);
        SLP(DLY_K);
#pragma unroll
        for (int n = 0; n < 8; ++n) {
          if (n >= PACE_K) VMCNT(PACE_K - 1);
          dma16(rk, g * 8192 + ((i16 ^ ((g << 2) | (n & 3))) * 16), (128 * jq + 32 * w + 4 * n) * 8192 + hd * 256, lbase + LP_UK + (8 * w + n) * 1024);
        }
        VMCNT(0);
        const int fr = ((i16 & 3) << 2) | (i16 >> 2);
#pragma unroll
        for (int t = 0; t < 2; ++t)
#pragma unroll
          for (int s = 0; s < 4; ++s) {
            ka[t][s] = *(LAS v4u*)(L + LP_UK + (32 * w + 16 * t + i16) * 256 + (((4 * s + g) ^ fr) * 16));
            asm volatile("" : "+v"(ka[t][s]));
          }
      }
      STAMP(1);
      asm volatile("" : "+v"(wv_));
      MAKE_TAG(wv_);
      unsigned eq[4];
#pragma unroll
      for (int k = 0; k < 4; ++k) { eq[k] = lsbtag(tlo, thi, 4 * i16 + k); asm volatile("" : "+v"(eq[k])); }
      SLP(DLY_H1);
      STAMP(2);
      {
        const int blk = w & 1, half = w >> 1;
        float q0, q1;
        for (int n = 0;; ++n) {
          v4u p[4];
#pragma unroll
          for (int jj = 0; jj < 4; ++jj) p[jj] = hl4(rws, lane * 16, WS_H1 + jj * QB + hd * 4096 + (blk * 2 + half) * 1024);
          unsigned bad = 0;
          q0 = 0.f; q1 = 0.f;
#pragma unroll
          for (int jj = 0; jj < 4; ++jj) {
            bad |= (p[jj].x ^ tlo) | (p[jj].z ^ tlo) | (((p[jj].y ^ t8) | (p[jj].w ^ t8)) & 0xFFu);
            q0 += gdec(p[jj].y); q1 += gdec(p[jj].w);
          }
          if (__all(bad == 0) || n >= GSPIN) break;
          SLP(DLY_R1);
        }
        const unsigned qp = pk(q0, q1);
        const int d0 = 32 * g + 16 * half + 4 * blk + cp;
        *(LAS u16*)(L + LP_QB + bj * 320 + d0 * 2) = (u16)qp;
        *(LAS u16*)(L + LP_QB + bj * 320 + (d0 + 8) * 2) = (u16)(qp >> 16);
      }
      STAMP(4);
      BAR();
      STAMP(5);
      {
        v4u qf[4];
#pragma unroll
        for (int s = 0; s < 4; ++s) qf[s] = *(LAS v4u*)(L + LP_QB + cp * 320 + s * 64 + g * 16);
        f32x4 c0 = {0.f, 0.f, 0.f, 0.f}, c1 = c0;
#pragma unroll
        for (int s = 0; s < 4; ++s) { c0 = mfma16(ka[0][s], qf[s], c0); c1 = mfma16(ka[1][s], qf[s], c1); }
        const float sc = 0.08838834764831845f * 1.4426950408889634f * LSB_COMP;
        if (i16 < 4) {
          *(LAS v2u*)(L + LP_QO + i16 * 256 + (32 * w + 4 * g) * 2) = v2u{pk(c0[0] * sc, c0[1] * sc), pk(c0[2] * sc, c0[3] * sc)};
          *(LAS v2u*)(L + LP_QO + i16 * 256 + (32 * w + 16 + 4 * g) * 2) = v2u{pk(c1[0] * sc, c1[1] * sc), pk(c1[2] * sc, c1[3] * sc)};
        }
      }
      BAR();
      if (w == 0) {
        const v4u v = *(LAS v4u*)(L + LP_QO + lane * 16);
        st4(v4u{lsbput(v.x, eq[0]), lsbput(v.y, eq[1]), lsbput(v.z, eq[2]), lsbput(v.w, eq[3])}, rws, g * 8192 + i16 * 16, WS_QA + slot(hd, 1024) + jq * 256);
      }
      STAMP(6);
      BAR();
      const rsrc_t r2 = mk(w2, 4608 * 576 * 2);
      const int so2 = (xg * 576 + 36 * qq) * 1152;
      const rsrc_t ro = mk(wo, 4096 * 576 * 2);
      const int soo = (hd * 128 + 32 * jq) * 1152;
      v4u a2[9][2];
      v2u a8[3][2];
      {
        const int vo4 = 2 * g * 1152 + i16 * 16, vo4m = g < 2 ? vo4 : OOB;
        const int vo8 = 2 * g * 1152 + i16 * 8 + 1024, vo8m = (g < 2 && w == 0) ? vo8 : OOB;
#pragma unroll
        for (int n = 0; n < 24; ++n) {
          if (n >= PACE_W2) VMCNT(PACE_W2 - 1);
          if (n == W2_A) { SCHED(); RBAR(); SLP(DLY_W2); }
          const int o = n >> 1, r = n & 1;
          if (n < 10) a2[o][r] = ld4<NT_W>(r2, o < 4 ? vo4 : vo4m, so2 + (8 * o + r) * 1152 + 256 * w);
          else if (n < 12) a8[0][r] = ld2<NT_W>(r2, vo8, so2 + (8 * w + r) * 1152);
          else if (n < 14) a8[1][r] = ld2<NT_W>(r2, vo8m, so2 + (32 + r) * 1152);
          else if (n < 22) a2[o - 2][r] = ld4<NT_W>(ro, vo4, soo + (8 * (o - 7) + r) * 1152 + 256 * w);
          else a8[2][r] = ld2<NT_W>(ro, vo8, soo + (8 * w + r) * 1152);
          SCHED();
        }
      }
      STAMP(7);
#pragma unroll
      for (int o = 0; o < 5; ++o) swap24(a2[o][0], a2[o][1]);
      STAMP(8);
      BAR();
      STAMP(9);
      {
        const v4u h4 = *(LAS v4u*)(L + LP_HP + (bj * 4 + g) * 48);
        const unsigned h5 = *(LAS unsigned*)(L + LP_HP + (bj * 4 + g) * 48 + 16);
        const unsigned hw = *(LAS unsigned*)(L + LP_HP + (bj * 4 + g) * 48 + 4 * w);
        f32x4 c8 = {0.f, 0.f, 0.f, 0.f}, cx = c8, cy = c8;
        c8 = mfma16(v4u{a8[0][0].x, a8[0][0].y, a8[0][1].x, a8[0][1].y}, bdiag(hw, sel), c8);
        c8 = mfma16(v4u{a8[1][0].x, a8[1][0].y, a8[1][1].x, a8[1][1].y}, bdiag(h5, sel), c8);
#pragma unroll
        for (int o = 0; o < 5; ++o) {
          const v4u B = bdiag(o == 0 ? h4.x : o == 1 ? h4.y : o == 2 ? h4.z : o == 3 ? h4.w : h5, sel);
          cx = mfma16(a2[o][0], B, cx);
          cy = mfma16(a2[o][1], B, cy);
        }
#pragma unroll
        for (int o = 5; o < 9; ++o) swap24(a2[o][0], a2[o][1]);
        asm volatile("" : "+v"(cx), "+v"(cy), "+v"(c8));
        int bpa = (bj * 16 + g) * 4;
        asm volatile("" : "+v"(bpa));
        STAMP(10);
        float y0, y1;
        for (int n = 0;; ++n) {
          v4u p[4];
#pragma unroll
          for (int jj = 0; jj < 4; ++jj) p[jj] = hl4(rws, lane * 16, WS_H4 + slot(idx, 4096) + jj * 1024);
          unsigned bad = 0;
          y0 = 0.f; y1 = 0.f;
#pragma unroll
          for (int jj = 0; jj < 4; ++jj) {
            bad |= (p[jj].x ^ tlo) | (p[jj].z ^ tlo) | (((p[jj].y ^ t8) | (p[jj].w ^ t8)) & 0xFFu);
            y0 += gdec(p[jj].y); y1 += gdec(p[jj].w);
          }
          if (__all(bad == 0) || n >= GSPIN) break;
          SLP(DLY_R4);
        }
        STAMP(11);
        const unsigned yp = pk(y0, y1);
        const unsigned pw = __builtin_amdgcn_ds_bpermute(bpa + 16 * w, yp);
        unsigned po[4];
#pragma unroll
        for (int o = 0; o < 4; ++o) po[o] = __builtin_amdgcn_ds_bpermute(bpa + 16 * o, yp);
        c8 = mfma16(v4u{a8[2][0].x, a8[2][0].y, a8[2][1].x, a8[2][1].y}, bdiag(pw, sel), c8);
#pragma unroll
        for (int o = 0; o < 4; ++o) {
          const v4u B = bdiag(po[o], sel);
          cx = mfma16(a2[5 + o][0], B, cx);
          cy = mfma16(a2[5 + o][1], B, cy);
        }
        *(LAS f32x4*)(L + LP_B8 + w * 1024 + lane * 16) = c8;
        st4(tag8(cx, tb), rws, vo5, so5 + (2 * w) * 2 * 16384);
        st4(tag8(cy, tb), rws, vo5, so5 + (2 * w + 1) * 2 * 16384);
        BAR();
        if (w == 0) {
          f32x4 s = *(LAS f32x4*)(L + LP_B8 + lane * 16);
#pragma unroll
          for (int k = 1; k < 4; ++k) s += *(LAS f32x4*)(L + LP_B8 + k * 1024 + lane * 16);
          st4(tag8(s, tb), rws, vo5, so5 + 8 * 2 * 16384);
        }
      }
      STAMP(3);
      if (qq < 9) RBAR();
    } else {
      const int v = w - 4;
      unsigned xn[8];
      const v2u wv_ = hl2(rws, 0, WS_W);
      {
        const v4u x0 = ld4<0>(rx, lane * 16, v * 1152), x1 = ld4<0>(rx, lane < 8 ? lane * 16 : OOB, v * 1152 + 1024);
        float s = 0.f, q = 0.f;
#pragma unroll
        for (int k = 0; k < 4; ++k) {
          s = dot2(x0[k], 0x3F803F80u, s); q = dot2(x0[k], x0[k], q);
          s = dot2(x1[k], 0x3F803F80u, s); q = dot2(x1[k], x1[k], q);
        }
        const float mean = x32_sum(x16_sum(row_sum(s))) * (1.0f / 576.0f);
        const float msq = x32_sum(x16_sum(row_sum(q))) * (1.0f / 576.0f);
        const float rstd = __builtin_amdgcn_rsqf(fmaxf(fmaf(-mean, mean, msq), 0.f) + 1e-5f), nb = -mean * rstd;
#pragma unroll
        for (int k = 0; k < 4; ++k) {
          xn[k] = pk(fmaf(bf2f(x0[k]), rstd, nb), fmaf(__uint_as_float(x0[k] & 0xFFFF0000u), rstd, nb));
        }
#pragma unroll
        for (int k = 0; k < 4; ++k) xn[4 + k] = pk(fmaf(bf2f(x1[k]), rstd, nb), fmaf(__uint_as_float(x1[k] & 0xFFFF0000u), rstd, nb));
      }
#pragma unroll
      for (int hf = 0; hf < 2; ++hf) {
        if (hf == 0 || lane < 8) {
#pragma unroll
          for (int k = 0; k < 4; ++k) {
            const int o = lane + 64 * hf;
            *(LAS unsigned*)(L + LP_XD + ((v * 4 + ((2 * k) & 3)) * 148 + 2 * o + (k >> 1)) * 4) = __builtin_amdgcn_perm(0u, xn[4 * hf + k], 0x01000100u);
            *(LAS unsigned*)(L + LP_XD + ((v * 4 + ((2 * k + 1) & 3)) * 148 + 2 * o + (k >> 1)) * 4) = __builtin_amdgcn_perm(0u, xn[4 * hf + k], 0x03020302u);
          }
        }
      }
      STAMP(1);
      MAKE_TAG(wv_);
      const int U0 = (81 * qq) / 144, Qb = 144 * (U0 + 1);
      unsigned xd[21];
      const GAS char* wb = (const GAS char*)(i16 < 8 ? w1 : w3) + (xg * 576 * 2 + (i16 & 7) * 16 + g * 9216);
      const unsigned m0 = cp == 0 ? 0xFFFFu : cp == 1 ? 0xFFFF0000u : 0u, m1 = cp == 2 ? 0xFFFFu : cp == 3 ? 0xFFFF0000u : 0u;
      f32x4 acc[4];
#pragma unroll
      for (int k = 0; k < 4; ++k) acc[k] = f32x4{0.f, 0.f, 0.f, 0.f};
      v4u a[21];
      BAR();
      RBAR();
#pragma unroll
      for (int t2 = 0; t2 < 21; ++t2) {
        const int Q = min(81 * qq + 4 * t2 + v, 81 * qq + 80), qk = Q >= Qb ? Q - Qb : Q - 144 * U0;
        xd[t2] = *(LAS unsigned*)(L + LP_XD + ((bj * 4 + g) * 148 + qk) * 4);
        asm volatile("" : "+v"(xd[t2]));
      }
      SLP(DLY_F);
      STAMP(2);
#pragma unroll
      for (int t = -PACE_G; t < 21; ++t) {
        const int tl = t + PACE_G;
        if (tl < 21) {
          const int Q = 81 * qq + 4 * tl + v, u = Q >= Qb ? U0 + 1 : U0, qk = Q - 144 * u;
          if (tl < KG0 && tl >= PACE_G0) VMCNT(PACE_G0 - 1);
          if (tl < 20 || v == 0) {
            const GAS v4u* p = (const GAS v4u*)(wb + (unsigned)(qk * 4 * 9216 + u * 128));
            a[tl] = NT_W ? __builtin_nontemporal_load(p) : *p;
          } else a[tl] = v4u{0u, 0u, 0u, 0u};
          SCHED();
        }
        if (tl == 20) { STAMP(3); BAR(); }
        if (t >= 0) {
          const int Q = 81 * qq + 4 * t + v;
          const v2u B = {xd[t] & m0, xd[t] & m1};
          if (Q < Qb) { acc[0] = mfma16k16(v2u{a[t].x, a[t].y}, B, acc[0]); acc[1] = mfma16k16(v2u{a[t].z, a[t].w}, B, acc[1]); }
          else        { acc[2] = mfma16k16(v2u{a[t].x, a[t].y}, B, acc[2]); acc[3] = mfma16k16(v2u{a[t].z, a[t].w}, B, acc[3]); }
          SCHED();
        }
      }
#pragma unroll
      for (int k = 0; k < 4; ++k) *(LAS f32x4*)(L + LP_RED + (v * 4 + k) * 1024 + lane * 16) = acc[k];
      STAMP(4);
      BAR();
      if (v == 0 || (v == 1 && 81 * qq + 80 >= Qb)) {
        const int pa = WS_H7 + slot(idx, 8192) + v * 4096 + (32 * ((lane >> 4) & 1) + 16 * (lane >> 5) + cp) * 64 + bj * 16;
#pragma unroll
        for (int hh = 0; hh < 2; ++hh) {
          f32x4 r = *(LAS f32x4*)(L + LP_RED + (2 * v + hh) * 1024 + lane * 16);
#pragma unroll
          for (int k = 1; k < 4; ++k) r += *(LAS f32x4*)(L + LP_RED + (k * 4 + 2 * v + hh) * 1024 + lane * 16);
#pragma unroll
          for (int k = 0; k < 2; ++k) {
            const auto x = __builtin_amdgcn_permlane32_swap(__float_as_uint(r[k]), __float_as_uint(r[2 + k]), false, false);
            st4(v4u{tlo, genc(__uint_as_float(x[0]), t8), tlo, genc(__uint_as_float(x[1]), t8)}, rws, pa + (8 * k + 4 * hh) * 64, 0);
          }
        }
      } else if (v >= 2) {
        int off[2][3];
        unsigned use[2][3];
        int hpa[2];
#pragma unroll
        for (int e = 0; e < 2; ++e) {
          const int t = (v - 2) * 64 + lane + 128 * e;
          const int cl = t >> 2, b = t & 3, f = 36 * qq + cl, u = f >> 6;
          const int r0 = (144 * u) / 81, r1 = (144 * u + 143) / 81;
#pragma unroll
          for (int k = 0; k < 3; ++k) {
            const int rr = r0 + k;
            const bool on = t < 144 && rr <= r1;
            off[e][k] = on ? slot(xg * 16 + rr, 8192) + (u - (81 * rr) / 144) * 4096 + (f & 63) * 64 + b * 16 : OOB;
            use[e][k] = on ? 0xFFFFFFFFu : 0u;
          }
          hpa[e] = LP_HP + ((b * 4 + ((cl >> 1) & 3)) * 12 + (cl >> 3)) * 4 + (cl & 1) * 2;
        }
        SLP(DLY_H7);
        STAMP(5);
        float gs[2], us[2];
        for (int n = 0;; ++n) {
          v4u p[2][3];
#pragma unroll
          for (int e = 0; e < 2; ++e)
#pragma unroll
            for (int k = 0; k < 3; ++k) p[e][k] = hl4(rws, off[e][k], WS_H7);
          unsigned bad = 0;
#pragma unroll
          for (int e = 0; e < 2; ++e) {
            gs[e] = 0.f; us[e] = 0.f;
#pragma unroll
            for (int k = 0; k < 3; ++k) {
              bad |= ((p[e][k].x ^ tlo) | (p[e][k].z ^ tlo) | (((p[e][k].y ^ t8) | (p[e][k].w ^ t8)) & 0xFFu)) & use[e][k];
              gs[e] += gdec(p[e][k].y); us[e] += gdec(p[e][k].w);
            }
          }
          if (__all(bad == 0) || n >= GSPIN) break;
          SLP(DLY_R7);
        }
#pragma unroll
        for (int e = 0; e < 2; ++e) {
          const float hv = gs[e] / (1.0f + __expf(-gs[e])) * us[e];
          if ((v - 2) * 64 + lane + 128 * e < 160) *(LAS u16*)(L + hpa[e]) = (u16)pk(hv, hv);
        }
      }
      STAMP(6);
      BAR();
      const int fe = lane & 31, fL = 8 * xg + (fe >> 2), frr = fe & 3;
      const int oidx = ((fL & 15) >> 2) * 576 + (qq < 8 ? 128 * (qq >> 1) + 8 * (4 * (fL >> 4) + frr) + 4 * (qq & 1) + (fL & 3) : 512 + 4 * (4 * (fL >> 4) + frr) + (fL & 3));
      unsigned xres = 0;
      if (v == 3 && qq < 9) xres = ((const GAS u16*)xin)[oidx];
      asm volatile("" : "+v"(xres));
      RBAR();
      STAMP(7);
      if (qq < 9) {
        SLP(DLY_H5);
        f32x4 s;
        for (int n = 0;; ++n) {
          v4u d[4];
#pragma unroll
          for (int k = 0; k < 4; ++k) d[k] = hl4(rws, lane * 16, WS_H5 + slot(qq * 8 + xg, 16384) + (v * 4 + k) * 1024);
          const unsigned bad = (strip4(d[0], tb) | strip4(d[1], tb)) | (strip4(d[2], tb) | strip4(d[3], tb));
          s = (__builtin_bit_cast(f32x4, d[0]) + __builtin_bit_cast(f32x4, d[1])) + (__builtin_bit_cast(f32x4, d[2]) + __builtin_bit_cast(f32x4, d[3]));
          if (__all((bad & 0xFFu) == 0) || n >= GSPIN) break;
        }
#pragma unroll
        for (int r = 0; r < 4; ++r) s[r] = x32_sum(x16_sum(s[r] + dppz<0x128>(s[r])));
        if (lane < 8) *(LAS f32x4*)(L + LP_FIN + v * 128 + lane * 16) = s;
        BAR();
        if (v == 3) {
          float y = bf2f(xres);
#pragma unroll
          for (int k = 0; k < 4; ++k) y += *(LAS float*)(L + LP_FIN + k * 128 + fe * 4);
          if (lane < 32) ((GAS u16*)out)[oidx] = (u16)pk(y, y);
          if (qq == 0 && xg == 0) st2(v2u{tlo, thi}, rws, lane == 0 ? 0 : OOB, WS_W);
        }
        STAMP(8);
      }
    }
    STAMP_OUT();
    return;
  }

  const int ab = xg >> 1;
  const int r32 = lane & 31, hh = lane >> 5;
  v2u wa_;
  if (w < 4) {
    const rsrc_t rc = mk(ckv, 4 * 4096 * 512 * 2);
    v2u wb_;
    asm volatile("buffer_load_dwordx2 %0, off, %1, 0 sc1" : "=v"(wb_) : "s"(rws) : "memory");
    {
      int vo[4];
#pragma unroll
      for (int k = 0; k < 4; ++k) vo[k] = g * 1024 + ((i16 ^ ((g << 2) | k)) * 16);
      const int so = (idx * 128 + 32 * w) * 1024;
      SLP(DLY_C);
#pragma unroll
      for (int n = 0; n < 32; ++n) {
        if (n < KC0) { if (n >= PACE_C0) VMCNT(PACE_C0 - 1); }
        else if (n >= PACE_C) VMCNT(PACE_C - 1);
        dma16(rc, vo[(n >> 2) & 3], so + (n >> 2) * 4096 + (n & 3) * 256, lbase + (n & 3) * 32768 + (32 * w + 4 * (n >> 2)) * 256);
        if (n == KQA) RBAR();
      }
    }
    asm volatile("" : "+v"(wb_));
    wa_ = wb_;
  } else {
    wa_ = hl2(rws, 0, WS_W);
    if (w == 4) {
      v2u xr[9];
#pragma unroll
      for (int t = 0; t < 9; ++t) xr[t] = ld2<0>(rx, g * 1152 + i16 * 8, t * 128);
      const rsrc_t rq = mk(wq, 576 * 4096 * 2);
      const int vo = 2 * g * 8192 + i16 * 16, so = 144 * jq * 8192 + hd * 256;
      v4u a[18][2];
#pragma unroll
      for (int o = 0; o < 18; ++o) { a[o][0] = ld4<NT_W>(rq, vo, so + (8 * o) * 8192); a[o][1] = ld4<NT_W>(rq, vo, so + (8 * o + 1) * 8192); }
      SCHED();
      STAMP(7);
      float s = 0.f, q = 0.f;
#pragma unroll
      for (int t = 0; t < 9; ++t) {
        *(LAS v2u*)(L + LA_QA + g * 1168 + i16 * 8 + t * 128) = xr[t];
        s = dot2(xr[t].x, 0x3F803F80u, s); q = dot2(xr[t].x, xr[t].x, q);
        s = dot2(xr[t].y, 0x3F803F80u, s); q = dot2(xr[t].y, xr[t].y, q);
      }
      FENCE();
      unsigned xp[18];
#pragma unroll
      for (int o = 0; o < 18; ++o) xp[o] = *(LAS unsigned*)(L + LA_QA + bj * 1168 + (72 * jq + 4 * o + g) * 4);
      const float mean = row_sum(s) * (1.0f / 576.0f), msq = row_sum(q) * (1.0f / 576.0f);
      const float rs0 = __builtin_amdgcn_rsqf(fmaxf(fmaf(-mean, mean, msq), 0.f) + 1e-5f);
      const float rstd = __uint_as_float(__builtin_amdgcn_ds_bpermute(bj * 64, __float_as_uint(rs0)));
      const float nb = __uint_as_float(__builtin_amdgcn_ds_bpermute(bj * 64, __float_as_uint(-mean * rs0)));
      v4u Bq[18];
#pragma unroll
      for (int o = 0; o < 18; ++o) {
        Bq[o] = bdiag(pk(fmaf(bf2f(xp[o]), rstd, nb), fmaf(__uint_as_float(xp[o] & 0xFFFF0000u), rstd, nb)), sel);
        asm volatile("" : "+v"(Bq[o]));
      }
      const u64 tg = (((u64)wa_.y << 32) | wa_.x) + 0x9E3779B97F4A7C15ull;
      unsigned ql = (unsigned)tg, q8 = (unsigned)(tg >> 32) & 0xFFu;
      asm volatile("" : "+v"(ql), "+v"(q8));
      SCHED();
      STAMP(8);
      f32x4 qx = {0.f, 0.f, 0.f, 0.f}, qy = qx;
#pragma unroll
      for (int o = 0; o < 18; ++o) {
        swap24(a[o][0], a[o][1]);
        qx = mfma16(a[o][0], Bq[o], qx);
        qy = mfma16(a[o][1], Bq[o], qy);
      }
      const int po = WS_H1 + slot(idx, 4096);
      st4(v4u{ql, genc(qx[0], q8), ql, genc(qx[1], q8)}, rws, lane * 16, po);
      st4(v4u{ql, genc(qx[2], q8), ql, genc(qx[3], q8)}, rws, lane * 16, po + 1024);
      st4(v4u{ql, genc(qy[0], q8), ql, genc(qy[1], q8)}, rws, lane * 16, po + 2048);
      st4(v4u{ql, genc(qy[2], q8), ql, genc(qy[3], q8)}, rws, lane * 16, po + 3072);
      STAMP(9);
    }
    RBAR();
  }
  MAKE_TAG(wa_);
  unsigned e4[4];
#pragma unroll
  for (int k = 0; k < 4; ++k) e4[k] = lsbtag(tlo, thi, 4 * i16 + k);
  if (w < 4) {
    v4u fa[32];
    {
      const int fr = ((r32 & 3) << 2) | ((r32 >> 2) & 3);
      const int ca = (32 * w + r32) * 256;
#define QK_READ(Q) _Pragma("unroll") for (int k = 0; k < 8; ++k) fa[(Q)*8 + k] = *(LAS v4u*)(L + (Q)*32768 + ca + (((2 * k + hh) ^ fr) * 16)); SCHED();
      VMCNT(0); QK_READ(0) QK_READ(1) QK_READ(2) QK_READ(3)
    }
    int qb[4];
#pragma unroll
    for (int k = 0; k < 4; ++k) { qb[k] = LA_QA + ((lane ^ ((2 * k + hh) & 7)) * 16); asm volatile("" : "+v"(qb[k])); }
    const int q4 = i16 >> 2, lowc = 2 * (g & 1) + (cp >> 1);
    int ta[4][2];
#pragma unroll
    for (int t = 0; t < 4; ++t)
#pragma unroll
      for (int t2 = 0; t2 < 2; ++t2) {
        ta[t][t2] = w * 32768 + (8 * t2 + 4 * hh + q4) * 256 + ((((t ^ q4) << 2) | (lowc ^ (2 * t2 + hh))) * 16) + 8 * (cp & 1);
        asm volatile("" : "+v"(ta[t][t2]));
      }
    int mxa = (r32 >> 4) * 320 + (r32 & 15) * 4, pba = LA_PB + lane * 16;
    unsigned e0 = lsbtag(tlo, thi, 4 * (r32 >> 1)), e1 = lsbtag(tlo, thi, 4 * (r32 >> 1) + 1), e2 = lsbtag(tlo, thi, 4 * (r32 >> 1) + 2), e3 = lsbtag(tlo, thi, 4 * (r32 >> 1) + 3);
    int h3v = (hh * 4 + (r32 & 1)) * 131072 + (r32 >> 1) * 16 + WS_H3 + (w * 128 + idx) * 256;
    asm volatile("" : "+v"(mxa), "+v"(pba), "+v"(e0), "+v"(e1), "+v"(e2), "+v"(e3), "+v"(h3v));
    STAMP(2);
    BAR();
    STAMP(3);
    f32x16 sacc;
#pragma unroll
    for (int k = 0; k < 16; ++k) sacc[k] = 0.f;
    {
      v4u fb[32];
#pragma unroll
      for (int s = 0; s < 6; ++s) fb[s] = *(LAS v4u*)(L + qb[s & 3] + s * 1024);
      SCHED();
#pragma unroll
      for (int s = 0; s < 32; ++s) {
        if (s + 6 < 32) fb[s + 6] = *(LAS v4u*)(L + qb[(s + 6) & 3] + (s + 6) * 1024);
        sacc = mfma32(fa[s], fb[s], sacc);
        SCHED();
      }
    }
    v4u cb[32];
#define TR_READ(n) do { const v2u x0_ = __builtin_bit_cast(v2u, __builtin_amdgcn_ds_read_tr16_b64_v4i16((LAS s16x4*)(L + ta[(n) >> 3][0] + ((n) & 7) * 4096))); \
    const v2u x1_ = __builtin_bit_cast(v2u, __builtin_amdgcn_ds_read_tr16_b64_v4i16((LAS s16x4*)(L + ta[(n) >> 3][1] + ((n) & 7) * 4096)));                 \
    cb[n] = v4u{x0_.x, x0_.y, x1_.x, x1_.y}; } while (0)
#pragma unroll
    for (int n = 0; n < 6; ++n) TR_READ(n);
    SCHED();
    float mw = sacc[0];
#pragma unroll
    for (int k = 1; k < 16; ++k) mw = fmaxf(mw, sacc[k]);
    mw = x32_max(mw);
    asm volatile("s_waitcnt lgkmcnt(0)" ::: "memory");
    *(LAS float*)(L + mxa + w * 32768) = mw;
    STAMP(4);
    BAR();
    const float mx = fmaxf(fmaxf(*(LAS float*)(L + mxa), *(LAS float*)(L + mxa + 32768)), fmaxf(*(LAS float*)(L + mxa + 65536), *(LAS float*)(L + mxa + 98304)));
    float ls = 0.f;
#pragma unroll
    for (int k = 0; k < 16; ++k) { sacc[k] = __builtin_amdgcn_exp2f(sacc[k] - mx); ls += sacc[k]; }
#pragma unroll
    for (int s2 = 0; s2 < 2; ++s2)
      *(LAS v4u*)(L + pba + (2 * w + s2) * 1024) =
          v4u{pk(sacc[8 * s2], sacc[8 * s2 + 1]), pk(sacc[8 * s2 + 2], sacc[8 * s2 + 3]), pk(sacc[8 * s2 + 4], sacc[8 * s2 + 5]), pk(sacc[8 * s2 + 6], sacc[8 * s2 + 7])};
    ls = x32_sum(ls);
    *(LAS float*)(L + mxa + 640 + w * 32768) = ls;
    BAR();
    STAMP(5);
    {
      v4u pf[8];
#pragma unroll
      for (int k = 0; k < 8; ++k) pf[k] = *(LAS v4u*)(L + pba + k * 1024);
      f32x4 l4;
      if (w == 0) l4 = f32x4{*(LAS float*)(L + mxa + 640), *(LAS float*)(L + mxa + 640 + 32768), *(LAS float*)(L + mxa + 640 + 65536), *(LAS float*)(L + mxa + 640 + 98304)};
      SCHED();
      f32x16 o[4];
      unsigned p01[16];
      const bool odd = lane & 1;
#pragma unroll
      for (int t = 0; t < 4; ++t)
#pragma unroll
        for (int k = 0; k < 16; ++k) o[t][k] = 0.f;
#pragma unroll
      for (int n = 0; n < 32; ++n) {
        if (n + 6 < 32) TR_READ(n + 6);
        o[n >> 3] = mfma32(pf[n & 7], cb[n], o[n >> 3]);
        if (n == 0 && w == 0)
          st4(v4u{tlo, genc(mx, t8), tlo, genc((l4[0] + l4[1]) + (l4[2] + l4[3]), t8)}, rws, lane < 32 ? r32 * 16 : OOB, WS_ML + slot(idx, 512));
        if (n >= 18 && n < 26) {
          const int k = n - 18;
          const unsigned pa = pk(o[0][2 * k], o[1][2 * k]), pb = pk(o[0][2 * k + 1], o[1][2 * k + 1]);
          p01[2 * k] = lsbput(odd ? pb : pa, e0);
          p01[2 * k + 1] = __builtin_amdgcn_update_dpp(0, lsbput(odd ? pa : pb, e2), 0xB1, 0xf, 0xf, true);
          asm volatile("" : "+v"(p01[2 * k]), "+v"(p01[2 * k + 1]));
        }
        SCHED();
      }
      STAMP(1);
#pragma unroll
      for (int k = 0; k < 8; ++k) {
        const unsigned pa = pk(o[2][2 * k], o[3][2 * k]), pb = pk(o[2][2 * k + 1], o[3][2 * k + 1]);
        st4(v4u{p01[2 * k], lsbput(odd ? pb : pa, e1), p01[2 * k + 1], (unsigned)__builtin_amdgcn_update_dpp(0, lsbput(odd ? pa : pb, e3), 0xB1, 0xf, 0xf, true)},
            rws, h3v, (2 * (k & 1) + 8 * (k >> 1)) * 131072);
      }
    }
    STAMP(6);
    RBAR();
    RBAR();
    RBAR();
  } else {
    const int v = w - 4;
    STAMP(1);
    const rsrc_t rv = mk(wuv, 512 * 4096 * 2);
    v4u uw[8];
#define TL(i) do { uw[i] = ld4<NT_W>(rv, g * 32 * 8192 + i16 * 16, (128 * jq + 8 * v + ((i) ^ (hd & 1))) * 8192 + hd * 256); SCHED(); } while (0)
    {
      v4u qa[8];
      SLP(DLY_QA);
#pragma unroll
      for (int k = 0; k < 8; ++k) qa[k] = hl4(rws, lane * 16, WS_QA + ab * 8192 + slot(8 * v + k, 1024));
      SCHED();
#pragma unroll
      for (int i = 0; i < TL0; ++i) TL(i);
#pragma unroll 1
      for (int n = 0;; ++n) {
        unsigned bad = 0;
#pragma unroll
        for (int k = 0; k < 8; ++k) bad |= strip4(qa[k], e4);
        if (n == 0 && w != 4) { STAMP(7); }
        if (w != 4) stv[8] = stv[0] + (u64)(n + 1) * 2300;
        if (__all((bad & LSBM) == 0) || n >= GSPIN) break;
        SLP(DLY_R2);
#pragma unroll
        for (int k = 0; k < 8; ++k) qa[k] = hl4(rws, lane * 16, WS_QA + ab * 8192 + slot(8 * v + k, 1024));
      }
#pragma unroll
      for (int k = 0; k < 8; ++k) *(LAS v4u*)(L + LA_QA + (lane >> 1) * 1024 + ((((lane & 1) * 32 + 8 * v + k) ^ (lane & 7)) * 16)) = qa[k];
    }
    STAMP(2);
    BAR();
#pragma unroll
    for (int i = TL0; i < TL1; ++i) TL(i);
    RBAR();
#pragma unroll
    for (int i = TL1; i < 8; ++i) TL(i);
    RBAR();
    const int e7 = lane & 7, q4 = i16 >> 2;
    v4u bk[8], d[8];
    {
      v4u ml;
      SLP(DLY_M0);
      for (int n = 0;; ++n) {
        ml = hl4(rws, slot(r32, 512), WS_ML + v * 8 * 512 + hd * 16);
        if (__all(((ml.x ^ tlo) | (ml.z ^ tlo) | (((ml.y ^ t8) | (ml.w ^ t8)) & 0xFFu)) == 0) || n >= GSPIN) break;
        SLP(DLY_RM);
      }
      STAMP(5);
      SLP(DLY_H3);
#pragma unroll
      for (int k = 0; k < 8; ++k) d[k] = hl4(rws, lane * 16, WS_H3 + idx * 32768 + v * 8192 + k * 1024);
      if (v == 0) st2(v2u{tlo, thi}, rws, lane == 0 ? 0 : OOB, WS_HN + slot(idx, 128));
      SCHED();
      const float m = gdec(ml.y), M = x16_max(row_max(m));
      const float e = __builtin_amdgcn_exp2f(m - M);
      const float al = e * LSB_COMP / x16_sum(row_sum(e * gdec(ml.w)));
      const unsigned ah = pk(al, al) & 0xFFFFu;
      const unsigned hl = ah | (pk(al - bf2f(ah), 0.f) << 16);
      const unsigned src = (lane & 8) ? 0x0302u : 0x0100u, s1 = (e7 & 1) ? ((src << 16) | 0x0C0Cu) : (0x0C0C0000u | src);
      unsigned s8[4];
#pragma unroll
      for (int k = 0; k < 4; ++k) s8[k] = (e7 >> 1) == k ? s1 : 0x0C0C0C0Cu;
#pragma unroll
      for (int k = 0; k < 8; ++k) { bk[k] = bdiag(__builtin_amdgcn_ds_bpermute((4 * k + g) * 4, hl), s8); asm volatile("" : "+v"(bk[k])); }
    }
    STAMP(3);
    RBAR();
#pragma unroll
    for (int k = 0; k < 8; ++k) *(LAS v4u*)(L + LA_UVW + (32 * v + 4 * k + g) * 256 + ((i16 ^ ((g << 2) | (k & 3))) * 16)) = uw[k];
    BAR();
    v4u bv[2][4];
#pragma unroll
    for (int t = 0; t < 2; ++t)
#pragma unroll
      for (int ks = 0; ks < 4; ++ks) {
        v2u x[2];
#pragma unroll
        for (int t2 = 0; t2 < 2; ++t2) {
          const int row = 32 * ks + 8 * g + 4 * t2 + q4, ch = 2 * (2 * v + t) + (cp >> 1);
          x[t2] = __builtin_bit_cast(v2u, __builtin_amdgcn_ds_read_tr16_b64_v4i16(
                      (LAS s16x4*)(L + LA_UVW + row * 256 + ((ch ^ ((q4 << 2) | ((2 * g + t2) & 3))) * 16) + 8 * (cp & 1))));
        }
        bv[t][ks] = v4u{x[0].x, x[0].y, x[1].x, x[1].y};
        asm volatile("" : "+v"(bv[t][ks]));
      }
    int cta = LA_CTX + v * 288 + (32 * g + e7) * 2, ctr = LA_CTX + cp * 288 + g * 16;
    int h4a = ((2 * (g & 1) + (i16 & 1)) * 16 + (2 * (g >> 1) + (i16 >> 3)) * 4 + ((i16 >> 1) & 3)) * 16 + WS_H4 + v * QB + hd * 4096 + jq * 1024;
    asm volatile("" : "+v"(cta), "+v"(ctr), "+v"(h4a));
    SCHED();
    STAMP(4);
    {
      f32x4 c;
#pragma unroll 1
      for (int n = 0;; ++n) {
        unsigned bad = 0;
        c = f32x4{0.f, 0.f, 0.f, 0.f};
#pragma unroll
        for (int k = 0; k < 8; ++k) { bad |= strip4(d[k], e4); c = mfma16(d[k], bk[k], c); }
        stv[10] = stv[0] + (u64)(n + 1) * 2300;
        if (__all((bad & LSBM) == 0) || n >= GSPIN) break;
#pragma unroll
        for (int k = 0; k < 8; ++k) d[k] = hl4(rws, lane * 16, WS_H3 + idx * 32768 + v * 8192 + k * 1024);
      }
      STAMP(6);
#pragma unroll
      for (int r = 0; r < 4; ++r) {
        const float y = c[r] + dppz<0x128>(c[r]);
        *(LAS u16*)(L + cta + 16 * r) = (u16)pk(y, y);
      }
    }
    BAR();
    {
      v4u af[4];
#pragma unroll
      for (int ks = 0; ks < 4; ++ks) af[ks] = *(LAS v4u*)(L + ctr + 64 * ks);
      f32x4 c0 = {0.f, 0.f, 0.f, 0.f}, c1 = c0;
#pragma unroll
      for (int ks = 0; ks < 4; ++ks) { c0 = mfma16(af[ks], bv[0][ks], c0); c1 = mfma16(af[ks], bv[1][ks], c1); }
      const f32x4 ct = g < 2 ? c0 : c1;
      const float x0 = (g & 1) ? ct[2] : ct[0], x1 = (g & 1) ? ct[3] : ct[1];
      const float n0 = dppz<0xB1>(x0), n1 = dppz<0xB1>(x1);
      st4(v4u{tlo, genc((lane & 1) ? n1 : x0, t8), tlo, genc((lane & 1) ? x1 : n0, t8)}, rws, h4a, 0);
    }
    STAMP(11);
  }
  STAMP_OUT();
}

__global__ void k_spin(char* ws){
  const u64* o=(const u64*)(ws+WS_STAMP);
  int t=threadIdx.x;
  u64 mn=~0ull,mxe=0; double a=0,sum=0; int cnt=0;
  __shared__ u64 s0[256],s2[256]; __shared__ double s3[256],s4[256]; __shared__ int s5[256];
  for(int i=t;i<2048;i+=256){ u64 st=o[i*16+12], en=o[i*16+14]; mn=st<mn?st:mn; mxe=en>mxe?en:mxe; }
  s0[t]=mn; s2[t]=mxe; __syncthreads();
  for(int i=0;i<256;i++){ mn=s0[i]<mn?s0[i]:mn; mxe=s2[i]>mxe?s2[i]:mxe; }
  for(int i=t;i<2048;i+=256){ int blk=i>>3,w=i&7; if(blk>=PB0&&blk<PB1&&w>=PW0&&w<PW1&&o[i*16+PK]){ double v=(double)(o[i*16+12]-mn)+(double)(o[i*16+PK]-o[i*16])/22.8; a=v>a?v:a; sum+=v; cnt++; } }
  s3[t]=a;s4[t]=sum;s5[t]=cnt; __syncthreads();
  if(t==0){ for(int i=1;i<256;i++){ a=s3[i]>a?s3[i]:a; sum+=s4[i]; cnt+=s5[i]; }
    double T=(PWHAT==0)?(double)(mxe-mn):(PWHAT==2)?0.0:(PWHAT==3)?a:sum/(cnt?cnt:1);
    u64 TT=(u64)(T*100.0); if(TT>1000000) TT=1000000;
    u64 t0=__builtin_amdgcn_s_memrealtime();
    while(__builtin_amdgcn_s_memrealtime()-t0 < TT) __builtin_amdgcn_s_sleep(8);
  }
}
extern "C" void kernel_launch(void* const* d_in, const int* in_sizes, int n_in, void* d_out, int out_size, void* d_ws, size_t ws_size,
                              hipStream_t stream) {
  (void)hipFuncSetAttribute((const void*)fused_decode, hipFuncAttributeMaxDynamicSharedMemorySize, LDS_BYTES);
  fused_decode<<<256, 512, LDS_BYTES, stream>>>((const u16*)d_in[0], (const u16*)d_in[1], (const u16*)d_in[2], (const u16*)d_in[3],
                                                (const u16*)d_in[4], (const u16*)d_in[5], (const u16*)d_in[7], (const u16*)d_in[8],
                                                (const u16*)d_in[9], (u16*)d_out, (char*)d_ws TUNE_ARG);
  k_spin<<<1,256,0,stream>>>((char*)d_ws);
}
```

```cpp
#include <hip/hip_runtime.h>
#include <stdint.h>

typedef unsigned short u16;
typedef unsigned long long u64;
typedef unsigned v4u __attribute__((ext_vector_type(4)));
typedef unsigned v2u __attribute__((ext_vector_type(2)));
typedef float f32x2 __attribute__((ext_vector_type(2)));
typedef float f32x4 __attribute__((ext_vector_type(4)));
typedef float f32x16 __attribute__((ext_vector_type(16)));
typedef short s16x4 __attribute__((ext_vector_type(4)));
typedef __bf16 bf16x8 __attribute__((ext_vector_type(8)));
typedef __bf16 bf16x2 __attribute__((ext_vector_type(2)));
typedef __amdgpu_buffer_rsrc_t rsrc_t;
#define GAS __attribute__((address_space(1)))
#define LAS __attribute__((address_space(3)))
#define DI __device__ __forceinline__

#define NT_W 2
#define NT_WO 0
#define PACE_C 7
#define DLY_H1 2
#define KQA 31
#define PACE_C0 3
#define KC0 6
#define DLY_K 16
#define PACE_G 4
#define PACE_O 8
#define DLY_R1 7
#define DLY_C 40
#define DLY_Q 0
#define DLY_F 86
#define DLY_QA 8
#define ABL 0
#define KWO 16
#define PACE_T 4
#define PACE_K 4
#define PACE_G0 2
#define KG0 0
#define PACE_W2 6
#define W2_A 6
#define TL0 0
#define TL1 4
#define TL2 12
#define TL3 24
#define PFW 1
#define DLY_PF 24
#define PACE_PF 4
#define DLY_R4 2
#define DLY_R7 2
#define DLY_RM 4
#define MLFIRST 0
#define DLY_W2 1
#define DLY_M0 12
#define H4HINT 0
#define DLY_H4 12
#define PFWAVE 7
#define KC1 32
#define PACE_C1 12
#define WO_E 10
#define PT 9
#define T_LATE 199
#define T_H7 190
#define DLY_R2 12
#define UK0 6
#define WO_A 9
#define WO_B 13
#define WO_C 12
#define DLY_H3 1
#define DLY_H7 19
#define DLY_H5 18

#define SLP(N) do { if (N) __builtin_amdgcn_s_sleep(N); } while (0)
#define TV(N) (N)
#define TUNE_PARAM
#define TUNE_ARG

constexpr int SC1 = 16;
constexpr int GSPIN = 1 << 13;

constexpr int WS_W = 0;
constexpr int QB = 524288;
constexpr int WS_H1 = 4096;
constexpr int WS_QA = WS_H1 + 131072;
constexpr int WS_ML = WS_QA + 32768;
constexpr int WS_HN = WS_ML + 16384;
constexpr int WS_H7 = WS_HN + 4096;
constexpr int WS_H4 = 4 * QB;
constexpr int WS_H5 = WS_H4 + 131072;
constexpr int WS_H3 = 8 * QB;
DI constexpr int slot(int p, int size) { return (p & 3) * QB + (p >> 2) * size; }
constexpr int WS_END = WS_H3 + 128 * 32768;
constexpr int WS_STAMP = WS_END;
constexpr int OOB = (int)0x80000000;

constexpr int LP_TAG = 4992, LP_XP = 0, LP_XD = 5120, LP_QB = 15360, LP_QO = 16896, LP_RED = 18432, LP_HP = 34816, LP_B8 = 35840, LP_FIN = 39936, LP_UK = 40960;
constexpr int LA_QA = 131072, LA_MX = LA_QA;
constexpr int LA_PB = LA_QA + 1024, LA_SUM = LA_QA + 512, LA_CTX = LA_QA + 9216, LA_CT2 = LA_QA + 11264, LA_B8 = LA_QA + 16384,
              LA_FIN = LA_QA + 20480, LA_UVW = 0;
constexpr int LDS_BYTES = 163840;

DI rsrc_t mk(const void* p, int bytes) { return __builtin_amdgcn_make_buffer_rsrc((void*)p, (short)0, bytes, 0x00020000); }
template <int AUX> DI v4u ld4(rsrc_t r, int vo, int so) { return __builtin_amdgcn_raw_buffer_load_b128(r, vo, so, AUX); }
template <int AUX> DI v2u ld2(rsrc_t r, int vo, int so) { return __builtin_amdgcn_raw_buffer_load_b64(r, vo, so, AUX); }
DI v4u hl4(rsrc_t r, int vo, int so) { asm volatile("" : "+v"(vo)); return __builtin_amdgcn_raw_buffer_load_b128(r, vo, so, SC1); }
DI v2u hl2(rsrc_t r, int vo, int so) { asm volatile("" : "+v"(vo)); return __builtin_amdgcn_raw_buffer_load_b64(r, vo, so, SC1); }
DI void touch(rsrc_t r, int vo, int so) { v4u t; asm volatile("buffer_load_dwordx4 %0, %1, %2, %3 offen sc1\n\ts_waitcnt vmcnt(0)" : "=&v"(t) : "v"(vo), "s"(r), "s"(so) : "memory"); }
DI void st4(v4u v, rsrc_t r, int vo, int so) { __builtin_amdgcn_raw_buffer_store_b128(v, r, vo + so, 0, SC1); }
DI void st2(v2u v, rsrc_t r, int vo, int so) { __builtin_amdgcn_raw_buffer_store_b64(v, r, vo, so, SC1); }
DI void dma16(rsrc_t r, int vo, int so, unsigned lds) {
  asm volatile("s_mov_b32 m0, %0\n\ts_nop 0\n\tbuffer_load_dwordx4 %1, %2, %3 offen nt lds" ::"s"(lds), "v"(vo), "s"(r), "s"(so) : "memory");
}
#define VMCNT(n) asm volatile("s_waitcnt vmcnt(%0)" ::"n"(n) : "memory")
#define FENCE() asm volatile("" ::: "memory")
#define SCHED() __builtin_amdgcn_sched_barrier(0)

DI float bf2f(unsigned v) { return __uint_as_float(v << 16); }
DI unsigned pk(float a, float b) { f32x2 f = {a, b}; return __builtin_bit_cast(unsigned, __builtin_convertvector(f, bf16x2)); }
DI f32x4 mfma16(v4u a, v4u b, f32x4 c) {
  return __builtin_amdgcn_mfma_f32_16x16x32_bf16(__builtin_bit_cast(bf16x8, a), __builtin_bit_cast(bf16x8, b), c, 0, 0, 0);
}
DI f32x4 mfma16k16(v2u a, v2u b, f32x4 c) {
  return __builtin_amdgcn_mfma_f32_16x16x16bf16_1k(__builtin_bit_cast(s16x4, a), __builtin_bit_cast(s16x4, b), c, 0, 0, 0);
}
DI f32x16 mfma32(v4u a, v4u b, f32x16 c) {
  return __builtin_amdgcn_mfma_f32_32x32x16_bf16(__builtin_bit_cast(bf16x8, a), __builtin_bit_cast(bf16x8, b), c, 0, 0, 0);
}
template <int CTRL, int RM> DI float dppf(float old, float v) {
  return __builtin_bit_cast(float, __builtin_amdgcn_update_dpp(__builtin_bit_cast(int, old), __builtin_bit_cast(int, v), CTRL, RM, 0xf, false));
}
template <int CTRL> DI float dppz(float v) {
  return __builtin_bit_cast(float, __builtin_amdgcn_update_dpp(0, __builtin_bit_cast(int, v), CTRL, 0xf, 0xf, true));
}
DI float row_sum(float v) {
  v += dppz<0xB1>(v); v += dppz<0x4E>(v); v += dppz<0x124>(v); v += dppz<0x128>(v);
  return v;
}
DI float dot2(unsigned a, unsigned b, float c) {
  return __builtin_amdgcn_fdot2_f32_bf16(__builtin_bit_cast(bf16x2, a), __builtin_bit_cast(bf16x2, b), c, false);
}
DI float row_max(float v) {
  v = fmaxf(v, dppf<0xB1, 0xf>(v, v)); v = fmaxf(v, dppf<0x4E, 0xf>(v, v));
  v = fmaxf(v, dppf<0x124, 0xf>(v, v)); v = fmaxf(v, dppf<0x128, 0xf>(v, v));
  return v;
}
DI float x16_sum(float v) { auto r = __builtin_amdgcn_permlane16_swap(__float_as_uint(v), __float_as_uint(v), false, false); return __uint_as_float(r[0]) + __uint_as_float(r[1]); }
DI float x32_sum(float v) { auto r = __builtin_amdgcn_permlane32_swap(__float_as_uint(v), __float_as_uint(v), false, false); return __uint_as_float(r[0]) + __uint_as_float(r[1]); }
DI float x16_max(float v) { auto r = __builtin_amdgcn_permlane16_swap(__float_as_uint(v), __float_as_uint(v), false, false); return fmaxf(__uint_as_float(r[0]), __uint_as_float(r[1])); }
DI float x32_max(float v) { auto r = __builtin_amdgcn_permlane32_swap(__float_as_uint(v), __float_as_uint(v), false, false); return fmaxf(__uint_as_float(r[0]), __uint_as_float(r[1])); }

#define RBAR() asm volatile("s_barrier" ::: "memory")
#define BAR() asm volatile("s_waitcnt lgkmcnt(0)\n\ts_barrier" ::: "memory")
DI unsigned genc(float v, unsigned t8) { return ((__float_as_uint(v) + 0x80u) & 0xFFFFFF00u) | t8; }
DI float gdec(unsigned hi) { return __uint_as_float(hi & 0xFFFFFF00u); }

#define STAMP(k)
#define STAMP_DECL
#define STAMP_OUT()

DI v4u bdiag(unsigned P, const unsigned (&s)[4]) {
  v4u b;
  b.x = __builtin_amdgcn_perm(P, P, s[0]); b.y = __builtin_amdgcn_perm(P, P, s[1]);
  b.z = __builtin_amdgcn_perm(P, P, s[2]); b.w = __builtin_amdgcn_perm(P, P, s[3]);
  return b;
}
DI void bdiag_sel(int cp, unsigned (&s)[4]) {
  const unsigned lo = (cp & 1) ? 0x01000C0Cu : 0x0C0C0100u, hi = (cp & 1) ? 0x03020C0Cu : 0x0C0C0302u, z = 0x0C0C0C0Cu;
  s[0] = cp < 2 ? lo : z; s[1] = cp < 2 ? z : lo; s[2] = cp < 2 ? hi : z; s[3] = cp < 2 ? z : hi;
}

DI unsigned lsbtag(unsigned tlo, unsigned thi, int p) { return (((p < 32 ? tlo : thi) >> (p & 31)) & 1u) | (((tlo ^ (unsigned)p) & 1u) << 16); }
constexpr unsigned LSBM = 0x00010001u;
DI unsigned lsbput(unsigned v, unsigned e) { return (v & ~LSBM) | e; }
DI unsigned strip4(v4u& d, const unsigned (&e)[4]) { d.x ^= e[0]; d.y ^= e[1]; d.z ^= e[2]; d.w ^= e[3]; return (d.x | d.y) | (d.z | d.w); }
DI void swap24(v4u& a, v4u& b) {
  unsigned az = a.z, aw = a.w, bx = b.x, by = b.y;
  asm volatile("v_swap_b32 %0, %2\n\tv_swap_b32 %1, %3\n\ts_nop 1" : "+v"(az), "+v"(aw), "+v"(bx), "+v"(by));
  a.z = az; a.w = aw; b.x = bx; b.y = by;
}
constexpr float LSB_COMP = 1.0028f;
DI v4u tag8(f32x4 c, const unsigned (&t)[4]) {
  return v4u{(__float_as_uint(c[0]) & 0xFFFFFF00u) | t[0], (__float_as_uint(c[1]) & 0xFFFFFF00u) | t[1],
             (__float_as_uint(c[2]) & 0xFFFFFF00u) | t[2], (__float_as_uint(c[3]) & 0xFFFFFF00u) | t[3]};
}
extern "C" __global__ void __launch_bounds__(512, 2)
fused_decode(const u16* __restrict__ xin, const u16* __restrict__ ckv, const u16* __restrict__ wq, const u16* __restrict__ wuk,
             const u16* __restrict__ wuv, const u16* __restrict__ wo, const u16* __restrict__ w1, const u16* __restrict__ w3,
             const u16* __restrict__ w2, u16* __restrict__ out, char* ws TUNE_PARAM) {
  extern __shared__ __attribute__((aligned(16))) char smem[];
  LAS char* L = (LAS char*)smem;
  const unsigned lbase = (unsigned)(size_t)smem;
  const int lane = threadIdx.x & 63, w = __builtin_amdgcn_readfirstlane(threadIdx.x >> 6);
  const int xg = blockIdx.x & 7, qd_ = blockIdx.x >> 3, qq = qd_ & 15, idx = xg * 16 + qq, hd = idx >> 2, jq = idx & 3;
  const int i16 = lane & 15, g = lane >> 4, bj = i16 >> 2, cp = lane & 3;
  STAMP_DECL;
  STAMP(0);
  const u64 t_start = __builtin_amdgcn_s_memrealtime();
  if (w == PFWAVE) {
    u64 pc; unsigned back, len;
    asm volatile("s_getpc_b64 %0\n.Lpf_here:\n\ts_mov_b32 %1, .Lpf_here-fused_decode\n\ts_mov_b32 %2, fused_decode_end-fused_decode" : "=s"(pc), "=s"(back), "=s"(len));
    const rsrc_t rcode = mk((const void*)(pc - back), (int)min(len, 32768u));
    v4u dummy;
    asm volatile("s_nop 4\n\tbuffer_load_dwordx4 %0, %1, %2, 0 offen\n\ts_waitcnt vmcnt(0)" : "=&v"(dummy) : "v"(qd_ * 1024 + lane * 16), "s"(rcode) : "memory");
  }
  asm volatile("" ::"s"(xin), "s"(ckv), "s"(wq), "s"(wuk), "s"(wuv), "s"(wo), "s"(w1), "s"(w3), "s"(w2), "s"(out), "s"(ws));
  const rsrc_t rws = mk(ws, WS_END);
  const rsrc_t rx = mk(xin, 4 * 576 * 2);
  unsigned sel[4];
  bdiag_sel(cp, sel);
#define MAKE_TAG(W) const u64 tag_ = (((u64)(W).y << 32) | (W).x) + 0x9E3779B97F4A7C15ull;  \
  const unsigned tlo = (unsigned)tag_, thi = (unsigned)(tag_ >> 32), t8 = thi & 0xFFu;        \
  const unsigned tb[4] = {tlo & 0xFFu, (tlo >> 8) & 0xFFu, (tlo >> 16) & 0xFFu, tlo >> 24}
  const int vo5 = slot(lane >> 3, 16384) + (lane & 7) * 16;

  if (qd_ >= 16) {
    const int so5 = WS_H5 + idx * 128;
    if (w < 4) {
      v4u ka[2][4];
      v2u wv_;
      asm volatile("buffer_load_dwordx2 %0, off, %1, 0 sc1" : "=v"(wv_) : "s"(rws) : "memory");
      {
        const rsrc_t rk = mk(wuk, 512 * 4096 * 2);
        SLP(DLY_K);
#pragma unroll
        for (int n = 0; n < 8; ++n) {
          if (n >= PACE_K) VMCNT(PACE_K - 1);
          dma16(rk, g * 8192 + ((i16 ^ ((g << 2) | (n & 3))) * 16), (128 * jq + 32 * w + 4 * n) * 8192 + hd * 256, lbase + LP_UK + (8 * w + n) * 1024);
        }
        VMCNT(0);
        const int fr = ((i16 & 3) << 2) | (i16 >> 2);
#pragma unroll
        for (int t = 0; t < 2; ++t)
#pragma unroll
          for (int s = 0; s < 4; ++s) {
            ka[t][s] = *(LAS v4u*)(L + LP_UK + (32 * w + 16 * t + i16) * 256 + (((4 * s + g) ^ fr) * 16));
            asm volatile("" : "+v"(ka[t][s]));
          }
      }
      STAMP(1);
      asm volatile("" : "+v"(wv_));
      MAKE_TAG(wv_);
      unsigned eq[4];
#pragma unroll
      for (int k = 0; k < 4; ++k) { eq[k] = lsbtag(tlo, thi, 4 * i16 + k); asm volatile("" : "+v"(eq[k])); }
      SLP(DLY_H1);
      STAMP(2);
      {
        const int blk = w & 1, half = w >> 1;
        float q0, q1;
        for (int n = 0;; ++n) {
          v4u p[4];
#pragma unroll
          for (int jj = 0; jj < 4; ++jj) p[jj] = hl4(rws, lane * 16, WS_H1 + jj * QB + hd * 4096 + (blk * 2 + half) * 1024);
          unsigned bad = 0;
          q0 = 0.f; q1 = 0.f;
#pragma unroll
          for (int jj = 0; jj < 4; ++jj) {
            bad |= (p[jj].x ^ tlo) | (p[jj].z ^ tlo) | (((p[jj].y ^ t8) | (p[jj].w ^ t8)) & 0xFFu);
            q0 += gdec(p[jj].y); q1 += gdec(p[jj].w);
          }
          if (__all(bad == 0) || n >= GSPIN) break;
          SLP(DLY_R1);
        }
        const unsigned qp = pk(q0, q1);
        const int d0 = 32 * g + 16 * half + 4 * blk + cp;
        *(LAS u16*)(L + LP_QB + bj * 320 + d0 * 2) = (u16)qp;
        *(LAS u16*)(L + LP_QB + bj * 320 + (d0 + 8) * 2) = (u16)(qp >> 16);
      }
      STAMP(4);
      BAR();
      STAMP(5);
      {
        v4u qf[4];
#pragma unroll
        for (int s = 0; s < 4; ++s) qf[s] = *(LAS v4u*)(L + LP_QB + cp * 320 + s * 64 + g * 16);
        f32x4 c0 = {0.f, 0.f, 0.f, 0.f}, c1 = c0;
#pragma unroll
        for (int s = 0; s < 4; ++s) { c0 = mfma16(ka[0][s], qf[s], c0); c1 = mfma16(ka[1][s], qf[s], c1); }
        const float sc = 0.08838834764831845f * 1.4426950408889634f * LSB_COMP;
        if (i16 < 4) {
          *(LAS v2u*)(L + LP_QO + i16 * 256 + (32 * w + 4 * g) * 2) = v2u{pk(c0[0] * sc, c0[1] * sc), pk(c0[2] * sc, c0[3] * sc)};
          *(LAS v2u*)(L + LP_QO + i16 * 256 + (32 * w + 16 + 4 * g) * 2) = v2u{pk(c1[0] * sc, c1[1] * sc), pk(c1[2] * sc, c1[3] * sc)};
        }
      }
      BAR();
      if (w == 0) {
        const v4u v = *(LAS v4u*)(L + LP_QO + lane * 16);
        st4(v4u{lsbput(v.x, eq[0]), lsbput(v.y, eq[1]), lsbput(v.z, eq[2]), lsbput(v.w, eq[3])}, rws, g * 8192 + i16 * 16, WS_QA + slot(hd, 1024) + jq * 256);
      }
      STAMP(6);
      BAR();
      const rsrc_t r2 = mk(w2, 4608 * 576 * 2);
      const int so2 = (xg * 576 + 36 * qq) * 1152;
      const rsrc_t ro = mk(wo, 4096 * 576 * 2);
      const int soo = (hd * 128 + 32 * jq) * 1152;
      v4u a2[9][2];
      v2u a8[3][2];
      {
        const int vo4 = 2 * g * 1152 + i16 * 16, vo4m = g < 2 ? vo4 : OOB;
        const int vo8 = 2 * g * 1152 + i16 * 8 + 1024, vo8m = (g < 2 && w == 0) ? vo8 : OOB;
#pragma unroll
        for (int n = 0; n < 14 + WO_E; ++n) {
          if (n >= PACE_W2) VMCNT(PACE_W2 - 1);
          if (n == W2_A) { SCHED(); RBAR(); SLP(DLY_W2); }
          const int o = n >> 1, r = n & 1;
          if (n < 10) a2[o][r] = ld4<NT_W>(r2, o < 4 ? vo4 : vo4m, so2 + (8 * o + r) * 1152 + 256 * w);
          else if (n < 12) a8[0][r] = ld2<NT_W>(r2, vo8, so2 + (8 * w + r) * 1152);
          else if (n < 14) a8[1][r] = ld2<NT_W>(r2, vo8m, so2 + (32 + r) * 1152);
          else if (n < 22) a2[o - 2][r] = ld4<NT_W>(ro, vo4, soo + (8 * (o - 7) + r) * 1152 + 256 * w);
          else a8[2][r] = ld2<NT_W>(ro, vo8, soo + (8 * w + r) * 1152);
          SCHED();
        }
      }
      STAMP(7);
#pragma unroll
      for (int o = 0; o < 5; ++o) swap24(a2[o][0], a2[o][1]);
      STAMP(8);
      RBAR();
      STAMP(9);
      {
        const int vo4 = 2 * g * 1152 + i16 * 16, vo8 = 2 * g * 1152 + i16 * 8 + 1024;
#pragma unroll
        for (int n = 14 + WO_E; n < 24; ++n) {
          const int o = n >> 1, r = n & 1;
          if (n < 22) a2[o - 2][r] = ld4<NT_W>(ro, vo4, soo + (8 * (o - 7) + r) * 1152 + 256 * w);
          else a8[2][r] = ld2<NT_W>(ro, vo8, soo + (8 * w + r) * 1152);
        }
        SCHED();
        const v4u h4 = *(LAS v4u*)(L + LP_HP + (bj * 4 + g) * 48);
        const unsigned h5 = *(LAS unsigned*)(L + LP_HP + (bj * 4 + g) * 48 + 16);
        const unsigned hw = *(LAS unsigned*)(L + LP_HP + (bj * 4 + g) * 48 + 4 * w);
        f32x4 c8 = {0.f, 0.f, 0.f, 0.f}, cx = c8, cy = c8;
        c8 = mfma16(v4u{a8[0][0].x, a8[0][0].y, a8[0][1].x, a8[0][1].y}, bdiag(hw, sel), c8);
        c8 = mfma16(v4u{a8[1][0].x, a8[1][0].y, a8[1][1].x, a8[1][1].y}, bdiag(h5, sel), c8);
#pragma unroll
        for (int o = 0; o < 5; ++o) {
          const v4u B = bdiag(o == 0 ? h4.x : o == 1 ? h4.y : o == 2 ? h4.z : o == 3 ? h4.w : h5, sel);
          cx = mfma16(a2[o][0], B, cx);
          cy = mfma16(a2[o][1], B, cy);
        }
#pragma unroll
        for (int o = 5; o < 9; ++o) swap24(a2[o][0], a2[o][1]);
        asm volatile("" : "+v"(cx), "+v"(cy), "+v"(c8));
        int bpa = (bj * 16 + g) * 4;
        asm volatile("" : "+v"(bpa));
        STAMP(10);
        float y0, y1;
        for (int n = 0;; ++n) {
          v4u p[4];
#pragma unroll
          for (int jj = 0; jj < 4; ++jj) p[jj] = hl4(rws, lane * 16, WS_H4 + slot(idx, 4096) + jj * 1024);
          unsigned bad = 0;
          y0 = 0.f; y1 = 0.f;
#pragma unroll
          for (int jj = 0; jj < 4; ++jj) {
            bad |= (p[jj].x ^ tlo) | (p[jj].z ^ tlo) | (((p[jj].y ^ t8) | (p[jj].w ^ t8)) & 0xFFu);
            y0 += gdec(p[jj].y); y1 += gdec(p[jj].w);
          }
          if (__all(bad == 0) || n >= GSPIN) break;
          SLP(DLY_R4);
        }
        STAMP(11);
        const unsigned yp = pk(y0, y1);
        const unsigned pw = __builtin_amdgcn_ds_bpermute(bpa + 16 * w, yp);
        unsigned po[4];
#pragma unroll
        for (int o = 0; o < 4; ++o) po[o] = __builtin_amdgcn_ds_bpermute(bpa + 16 * o, yp);
        c8 = mfma16(v4u{a8[2][0].x, a8[2][0].y, a8[2][1].x, a8[2][1].y}, bdiag(pw, sel), c8);
#pragma unroll
        for (int o = 0; o < 4; ++o) {
          const v4u B = bdiag(po[o], sel);
          cx = mfma16(a2[5 + o][0], B, cx);
          cy = mfma16(a2[5 + o][1], B, cy);
        }
        *(LAS f32x4*)(L + LP_B8 + w * 1024 + lane * 16) = c8;
        st4(tag8(cx, tb), rws, vo5, so5 + (2 * w) * 2 * 16384);
        st4(tag8(cy, tb), rws, vo5, so5 + (2 * w + 1) * 2 * 16384);
        BAR();
        if (w == 0) {
          f32x4 s = *(LAS f32x4*)(L + LP_B8 + lane * 16);
#pragma unroll
          for (int k = 1; k < 4; ++k) s += *(LAS f32x4*)(L + LP_B8 + k * 1024 + lane * 16);
          st4(tag8(s, tb), rws, vo5, so5 + 8 * 2 * 16384);
        }
      }
      STAMP(3);
      if (qq < 9) RBAR();
    } else {
      const int v = w - 4;
      unsigned xn[8];
      const v2u wv_ = hl2(rws, 0, WS_W);
      v4u pt1;
      if (PT & 1) asm volatile("buffer_load_dwordx4 %0, %1, %2, %3 offen sc1" : "=v"(pt1) : "v"(lane * 16), "s"(rws), "s"(WS_H1 + slot(idx, 4096) + v * 1024) : "memory");
      {
        const v4u x0 = ld4<0>(rx, lane * 16, v * 1152), x1 = ld4<0>(rx, lane < 8 ? lane * 16 : OOB, v * 1152 + 1024);
        float s = 0.f, q = 0.f;
#pragma unroll
        for (int k = 0; k < 4; ++k) {
          s = dot2(x0[k], 0x3F803F80u, s); q = dot2(x0[k], x0[k], q);
          s = dot2(x1[k], 0x3F803F80u, s); q = dot2(x1[k], x1[k], q);
        }
        const float mean = x32_sum(x16_sum(row_sum(s))) * (1.0f / 576.0f);
        const float msq = x32_sum(x16_sum(row_sum(q))) * (1.0f / 576.0f);
        const float rstd = __builtin_amdgcn_rsqf(fmaxf(fmaf(-mean, mean, msq), 0.f) + 1e-5f), nb = -mean * rstd;
#pragma unroll
        for (int k = 0; k < 4; ++k) {
          xn[k] = pk(fmaf(bf2f(x0[k]), rstd, nb), fmaf(__uint_as_float(x0[k] & 0xFFFF0000u), rstd, nb));
        }
#pragma unroll
        for (int k = 0; k < 4; ++k) xn[4 + k] = pk(fmaf(bf2f(x1[k]), rstd, nb), fmaf(__uint_as_float(x1[k] & 0xFFFF0000u), rstd, nb));
      }
      if (PT & 1) { VMCNT(0); asm volatile("" : "+v"(pt1)); }
#pragma unroll
      for (int hf = 0; hf < 2; ++hf) {
        if (hf == 0 || lane < 8) {
#pragma unroll
          for (int k = 0; k < 4; ++k) {
            const int o = lane + 64 * hf;
            *(LAS unsigned*)(L + LP_XD + ((v * 4 + ((2 * k) & 3)) * 148 + 2 * o + (k >> 1)) * 4) = __builtin_amdgcn_perm(0u, xn[4 * hf + k], 0x01000100u);
            *(LAS unsigned*)(L + LP_XD + ((v * 4 + ((2 * k + 1) & 3)) * 148 + 2 * o + (k >> 1)) * 4) = __builtin_amdgcn_perm(0u, xn[4 * hf + k], 0x03020302u);
          }
        }
      }
      STAMP(1);
      if (PT & 8) touch(rws, lane * 16, WS_H4 + slot(idx, 4096) + v * 1024);
      MAKE_TAG(wv_);
      const int U0 = (81 * qq) / 144, Qb = 144 * (U0 + 1);
      unsigned xd[21];
      const GAS char* wb = (const GAS char*)(i16 < 8 ? w1 : w3) + (xg * 576 * 2 + (i16 & 7) * 16 + g * 9216);
      const unsigned m0 = cp == 0 ? 0xFFFFu : cp == 1 ? 0xFFFF0000u : 0u, m1 = cp == 2 ? 0xFFFFu : cp == 3 ? 0xFFFF0000u : 0u;
      f32x4 acc[4];
#pragma unroll
      for (int k = 0; k < 4; ++k) acc[k] = f32x4{0.f, 0.f, 0.f, 0.f};
      v4u a[21];
        int off[2][3];
        unsigned use[2][3];
        int hpa[2];
#pragma unroll
        for (int e = 0; e < 2; ++e) {
          const int t = ((v - 2) & 1) * 64 + lane + 128 * e;
          const int cl = t >> 2, b = t & 3, f = 36 * qq + cl, u = f >> 6;
          const int r0 = (144 * u) / 81, r1 = (144 * u + 143) / 81;
#pragma unroll
          for (int k = 0; k < 3; ++k) {
            const int rr = r0 + k;
            const bool on = t < 144 && rr <= r1;
            off[e][k] = on ? slot(xg * 16 + rr, 8192) + (u - (81 * rr) / 144) * 4096 + (f & 63) * 64 + b * 16 : OOB;
            use[e][k] = on ? 0xFFFFFFFFu : 0u;
          }
          hpa[e] = LP_HP + ((b * 4 + ((cl >> 1) & 3)) * 12 + (cl >> 3)) * 4 + (cl & 1) * 2;
          asm volatile("" : "+v"(hpa[e]), "+v"(off[e][0]), "+v"(off[e][1]), "+v"(off[e][2]), "+v"(use[e][0]), "+v"(use[e][1]), "+v"(use[e][2]));
        }
      int pa = WS_H7 + slot(idx, 8192) + v * 4096 + (32 * ((lane >> 4) & 1) + 16 * (lane >> 5) + cp) * 64 + bj * 16;
      asm volatile("" : "+v"(pa));
      BAR();
      RBAR();
#pragma unroll
      for (int t2 = 0; t2 < 21; ++t2) {
        const int Q = min(81 * qq + 4 * t2 + v, 81 * qq + 80), qk = Q >= Qb ? Q - Qb : Q - 144 * U0;
        xd[t2] = *(LAS unsigned*)(L + LP_XD + ((bj * 4 + g) * 148 + qk) * 4);
        asm volatile("" : "+v"(xd[t2]));
      }
      if ((PT & 16) && v >= 2) {
        v4u t[6];
#pragma unroll
        for (int k = 0; k < 6; ++k) t[k] = hl4(rws, off[k / 3][k % 3], WS_H7);
#pragma unroll
        for (int k = 0; k < 6; ++k) asm volatile("" ::"v"(t[k]));
      }
      if ((PT & 32) && v < 2 && qq < 9) {
        v4u t[8];
#pragma unroll
        for (int k = 0; k < 8; ++k) t[k] = hl4(rws, lane * 16, WS_H5 + slot(qq * 8 + xg, 16384) + (v * 8 + k) * 1024);
#pragma unroll
        for (int k = 0; k < 8; ++k) asm volatile("" ::"v"(t[k]));
      }
#pragma unroll 1
      for (int n = 0; n < 4096 && (unsigned)(__builtin_amdgcn_s_memrealtime() - t_start) < (unsigned)(TV(DLY_F) * 5); ++n) __builtin_amdgcn_s_sleep(1);
      STAMP(2);
#pragma unroll
      for (int t = -PACE_G; t < 21; ++t) {
        const int tl = t + PACE_G;
        if (tl < 21) {
          const int Q = 81 * qq + 4 * tl + v, u = Q >= Qb ? U0 + 1 : U0, qk = Q - 144 * u;
          if (tl < KG0 && tl >= PACE_G0) VMCNT(PACE_G0 - 1);
          if (tl < 20 || v == 0) {
            const GAS v4u* p = (const GAS v4u*)(wb + (unsigned)(qk * 4 * 9216 + u * 128));
            a[tl] = NT_W ? __builtin_nontemporal_load(p) : *p;
          } else a[tl] = v4u{0u, 0u, 0u, 0u};
          SCHED();
        }
        if (tl == 20) { STAMP(3); BAR(); }
        if (t >= 0) {
          const int Q = 81 * qq + 4 * t + v;
          const v2u B = {xd[t] & m0, xd[t] & m1};
          if (Q < Qb) { acc[0] = mfma16k16(v2u{a[t].x, a[t].y}, B, acc[0]); acc[1] = mfma16k16(v2u{a[t].z, a[t].w}, B, acc[1]); }
          else        { acc[2] = mfma16k16(v2u{a[t].x, a[t].y}, B, acc[2]); acc[3] = mfma16k16(v2u{a[t].z, a[t].w}, B, acc[3]); }
          SCHED();
        }
      }
#pragma unroll
      for (int k = 0; k < 4; ++k) *(LAS f32x4*)(L + LP_RED + (v * 4 + k) * 1024 + lane * 16) = acc[k];
      STAMP(4);
      BAR();
      STAMP(9);
      if (v == 0 || (v == 1 && 81 * qq + 80 >= Qb)) {
#pragma unroll
        for (int hh = 0; hh < 2; ++hh) {
          f32x4 r = *(LAS f32x4*)(L + LP_RED + (2 * v + hh) * 1024 + lane * 16);
#pragma unroll
          for (int k = 1; k < 4; ++k) r += *(LAS f32x4*)(L + LP_RED + (k * 4 + 2 * v + hh) * 1024 + lane * 16);
#pragma unroll
          for (int k = 0; k < 2; ++k) {
            const auto x = __builtin_amdgcn_permlane32_swap(__float_as_uint(r[k]), __float_as_uint(r[2 + k]), false, false);
            st4(v4u{tlo, genc(__uint_as_float(x[0]), t8), tlo, genc(__uint_as_float(x[1]), t8)}, rws, pa + (8 * k + 4 * hh) * 64, 0);
          }
        }
      } else if (v >= 2) {
        SLP(DLY_H7);
#pragma unroll 1
        for (int n = 0; n < 4096 && (unsigned)(__builtin_amdgcn_s_memrealtime() - t_start) < (unsigned)(TV(T_H7) * 5); ++n) __builtin_amdgcn_s_sleep(1);
        STAMP(5);
        float gs[2], us[2];
        for (int n = 0;; ++n) {
          v4u p[2][3];
#pragma unroll
          for (int e = 0; e < 2; ++e)
#pragma unroll
            for (int k = 0; k < 3; ++k) p[e][k] = hl4(rws, off[e][k], WS_H7);
          unsigned bad = 0;
#pragma unroll
          for (int e = 0; e < 2; ++e) {
            gs[e] = 0.f; us[e] = 0.f;
#pragma unroll
            for (int k = 0; k < 3; ++k) {
              bad |= ((p[e][k].x ^ tlo) | (p[e][k].z ^ tlo) | (((p[e][k].y ^ t8) | (p[e][k].w ^ t8)) & 0xFFu)) & use[e][k];
              gs[e] += gdec(p[e][k].y); us[e] += gdec(p[e][k].w);
            }
          }
          if (__all(bad == 0) || n >= GSPIN) break;
          SLP(DLY_R7);
        }
#pragma unroll
        for (int e = 0; e < 2; ++e) {
          const float hv = gs[e] / (1.0f + __expf(-gs[e])) * us[e];
          if ((v - 2) * 64 + lane + 128 * e < 160) *(LAS u16*)(L + hpa[e]) = (u16)pk(hv, hv);
        }
      }
      STAMP(6);
      BAR();
      const int fe = lane & 31, fL = 8 * xg + (fe >> 2), frr = fe & 3;
      const int oidx = ((fL & 15) >> 2) * 576 + (qq < 8 ? 128 * (qq >> 1) + 8 * (4 * (fL >> 4) + frr) + 4 * (qq & 1) + (fL & 3) : 512 + 4 * (4 * (fL >> 4) + frr) + (fL & 3));
      unsigned xres = 0;
      if (v == 3 && qq < 9) xres = ((const GAS u16*)xin)[oidx];
      const int h5o = WS_H5 + slot(qq * 8 + xg, 16384) + v * 4096;
      int fina = LP_FIN + v * 128 + (lane & 7) * 16 + g * 4;
      asm volatile("" : "+v"(xres), "+v"(fina));
      RBAR();
      STAMP(7);
      if (qq < 9) {
        SLP(DLY_H5);
        v4u d[4];
#pragma unroll
        for (int k = 0; k < 4; ++k) d[k] = hl4(rws, lane * 16, h5o + k * 1024);
        unsigned pend = 15u;
#pragma unroll 1
        for (int n = 0;; ++n) {
#pragma unroll
          for (int k = 0; k < 4; ++k)
            if (pend & (1u << k)) { if (__all((strip4(d[k], tb) & 0xFFu) == 0)) pend &= ~(1u << k); }
          if (pend == 0 || n >= GSPIN) break;
#pragma unroll
          for (int k = 0; k < 4; ++k)
            if (pend & (1u << k)) d[k] = hl4(rws, lane * 16, h5o + k * 1024);
        }
        f32x4 s = (__builtin_bit_cast(f32x4, d[0]) + __builtin_bit_cast(f32x4, d[1])) + (__builtin_bit_cast(f32x4, d[2]) + __builtin_bit_cast(f32x4, d[3]));
#pragma unroll
        for (int r = 0; r < 4; ++r) s[r] += dppz<0x128>(s[r]);
        const auto x01 = __builtin_amdgcn_permlane16_swap(__float_as_uint(s[0]), __float_as_uint(s[1]), false, false);
        const auto x23 = __builtin_amdgcn_permlane16_swap(__float_as_uint(s[2]), __float_as_uint(s[3]), false, false);
        const auto z = __builtin_amdgcn_permlane32_swap(__float_as_uint(__uint_as_float(x01[0]) + __uint_as_float(x01[1])),
                                                        __float_as_uint(__uint_as_float(x23[0]) + __uint_as_float(x23[1])), false, false);
        *(LAS float*)(L + fina) = __uint_as_float(z[0]) + __uint_as_float(z[1]);
        BAR();
        if (v == 3) {
          float y = bf2f(xres);
#pragma unroll
          for (int k = 0; k < 4; ++k) y += *(LAS float*)(L + LP_FIN + k * 128 + fe * 4);
          if (lane < 32) ((GAS u16*)out)[oidx] = (u16)pk(y, y);
          if (qq == 0 && xg == 0) st2(v2u{tlo, thi}, rws, lane == 0 ? 0 : OOB, WS_W);
        }
        STAMP(8);
      }
    }
    STAMP_OUT();
    return;
  }

  const int ab = xg >> 1;
  const int r32 = lane & 31, hh = lane >> 5;
  v2u wa_;
  if (w < 4) {
    const rsrc_t rc = mk(ckv, 4 * 4096 * 512 * 2);
    v2u wb_;
    asm volatile("buffer_load_dwordx2 %0, off, %1, 0 sc1" : "=v"(wb_) : "s"(rws) : "memory");
    {
      int vo[4];
#pragma unroll
      for (int k = 0; k < 4; ++k) vo[k] = g * 1024 + ((i16 ^ ((g << 2) | k)) * 16);
      const int so = (idx * 128 + 32 * w) * 1024;
      SLP(DLY_C);
#pragma unroll
      for (int n = 0; n < 32; ++n) {
        if (n < KC0) { if (n >= PACE_C0) VMCNT(PACE_C0 - 1); }
        else if (n >= KC1) { if (n >= KC1 + PACE_C1 - PACE_C) VMCNT(PACE_C1 - 1); }
        else if (n >= PACE_C) VMCNT(PACE_C - 1);
        dma16(rc, vo[(n >> 2) & 3], so + (n >> 2) * 4096 + (n & 3) * 256, lbase + (n & 3) * 32768 + (32 * w + 4 * (n >> 2)) * 256);
        if (n == KQA) RBAR();
      }
    }
    asm volatile("" : "+v"(wb_));
    wa_ = wb_;
  } else {
    wa_ = hl2(rws, 0, WS_W);
    if ((PT & 2) && (w == 5 || w == 6)) touch(rws, lane * 16, WS_QA + ab * 8192 + slot(2 * qq + w - 5, 1024));
    if ((PT & 4) && w >= 5) {
      if (w == 7) touch(rws, lane < 32 ? slot(8 * qq + (lane >> 2), 512) + (lane & 3) * 16 : OOB, WS_ML + xg * 64);
    }
    if (w == 4) {
      v2u xr[9];
#pragma unroll
      for (int t = 0; t < 9; ++t) xr[t] = ld2<0>(rx, g * 1152 + i16 * 8, t * 128);
      const rsrc_t rq = mk(wq, 576 * 4096 * 2);
      const int vo = 2 * g * 8192 + i16 * 16, so = 144 * jq * 8192 + hd * 256;
      v4u a[18][2];
#pragma unroll
      for (int o = 0; o < 18; ++o) { a[o][0] = ld4<NT_W>(rq, vo, so + (8 * o) * 8192); a[o][1] = ld4<NT_W>(rq, vo, so + (8 * o + 1) * 8192); }
      SCHED();
      STAMP(7);
      float s = 0.f, q = 0.f;
#pragma unroll
      for (int t = 0; t < 9; ++t) {
        *(LAS v2u*)(L + LA_QA + g * 1168 + i16 * 8 + t * 128) = xr[t];
        s = dot2(xr[t].x, 0x3F803F80u, s); q = dot2(xr[t].x, xr[t].x, q);
        s = dot2(xr[t].y, 0x3F803F80u, s); q = dot2(xr[t].y, xr[t].y, q);
      }
      FENCE();
      unsigned xp[18];
#pragma unroll
      for (int o = 0; o < 18; ++o) xp[o] = *(LAS unsigned*)(L + LA_QA + bj * 1168 + (72 * jq + 4 * o + g) * 4);
      const float mean = row_sum(s) * (1.0f / 576.0f), msq = row_sum(q) * (1.0f / 576.0f);
      const float rs0 = __builtin_amdgcn_rsqf(fmaxf(fmaf(-mean, mean, msq), 0.f) + 1e-5f);
      const float rstd = __uint_as_float(__builtin_amdgcn_ds_bpermute(bj * 64, __float_as_uint(rs0)));
      const float nb = __uint_as_float(__builtin_amdgcn_ds_bpermute(bj * 64, __float_as_uint(-mean * rs0)));
      v4u Bq[18];
#pragma unroll
      for (int o = 0; o < 18; ++o) {
        Bq[o] = bdiag(pk(fmaf(bf2f(xp[o]), rstd, nb), fmaf(__uint_as_float(xp[o] & 0xFFFF0000u), rstd, nb)), sel);
        asm volatile("" : "+v"(Bq[o]));
      }
      const u64 tg = (((u64)wa_.y << 32) | wa_.x) + 0x9E3779B97F4A7C15ull;
      unsigned ql = (unsigned)tg, q8 = (unsigned)(tg >> 32) & 0xFFu;
      asm volatile("" : "+v"(ql), "+v"(q8));
      SCHED();
      STAMP(8);
      f32x4 qx = {0.f, 0.f, 0.f, 0.f}, qy = qx;
#pragma unroll
      for (int o = 0; o < 18; ++o) {
        swap24(a[o][0], a[o][1]);
        qx = mfma16(a[o][0], Bq[o], qx);
        qy = mfma16(a[o][1], Bq[o], qy);
      }
      const int po = WS_H1 + slot(idx, 4096);
      st4(v4u{ql, genc(qx[0], q8), ql, genc(qx[1], q8)}, rws, lane * 16, po);
      st4(v4u{ql, genc(qx[2], q8), ql, genc(qx[3], q8)}, rws, lane * 16, po + 1024);
      st4(v4u{ql, genc(qy[0], q8), ql, genc(qy[1], q8)}, rws, lane * 16, po + 2048);
      st4(v4u{ql, genc(qy[2], q8), ql, genc(qy[3], q8)}, rws, lane * 16, po + 3072);
      STAMP(9);
    }
    RBAR();
  }
  MAKE_TAG(wa_);
  unsigned e4[4];
#pragma unroll
  for (int k = 0; k < 4; ++k) e4[k] = lsbtag(tlo, thi, 4 * i16 + k);
  if (w < 4) {
    v4u fa[32];
    {
      const int fr = ((r32 & 3) << 2) | ((r32 >> 2) & 3);
      const int ca = (32 * w + r32) * 256;
#define QK_READ(Q) _Pragma("unroll") for (int k = 0; k < 8; ++k) fa[(Q)*8 + k] = *(LAS v4u*)(L + (Q)*32768 + ca + (((2 * k + hh) ^ fr) * 16)); SCHED();
      VMCNT(0); QK_READ(0) QK_READ(1) QK_READ(2) QK_READ(3)
    }
    int qb[4];
#pragma unroll
    for (int k = 0; k < 4; ++k) { qb[k] = LA_QA + ((lane ^ ((2 * k + hh) & 7)) * 16); asm volatile("" : "+v"(qb[k])); }
    const int q4 = i16 >> 2, lowc = 2 * (g & 1) + (cp >> 1);
    int ta[4][2];
#pragma unroll
    for (int t = 0; t < 4; ++t)
#pragma unroll
      for (int t2 = 0; t2 < 2; ++t2) {
        ta[t][t2] = w * 32768 + (8 * t2 + 4 * hh + q4) * 256 + ((((t ^ q4) << 2) | (lowc ^ (2 * t2 + hh))) * 16) + 8 * (cp & 1);
        asm volatile("" : "+v"(ta[t][t2]));
      }
    int mxa = (r32 >> 4) * 320 + (r32 & 15) * 4, pba = LA_PB + lane * 16;
    unsigned e0 = lsbtag(tlo, thi, 4 * (r32 >> 1)), e1 = lsbtag(tlo, thi, 4 * (r32 >> 1) + 1), e2 = lsbtag(tlo, thi, 4 * (r32 >> 1) + 2), e3 = lsbtag(tlo, thi, 4 * (r32 >> 1) + 3);
    int h3v = (hh * 4 + (r32 & 1)) * 131072 + (r32 >> 1) * 16 + WS_H3 + (w * 128 + idx) * 256;
    asm volatile("" : "+v"(mxa), "+v"(pba), "+v"(e0), "+v"(e1), "+v"(e2), "+v"(e3), "+v"(h3v));
    STAMP(2);
    BAR();
    STAMP(3);
    v4u cb[32];
#define TR_READ(n) do { const v2u x0_ = __builtin_bit_cast(v2u, __builtin_amdgcn_ds_read_tr16_b64_v4i16((LAS s16x4*)(L + ta[(n) >> 3][0] + ((n) & 7) * 4096))); \
    const v2u x1_ = __builtin_bit_cast(v2u, __builtin_amdgcn_ds_read_tr16_b64_v4i16((LAS s16x4*)(L + ta[(n) >> 3][1] + ((n) & 7) * 4096)));                 \
    cb[n] = v4u{x0_.x, x0_.y, x1_.x, x1_.y}; } while (0)
    f32x16 sacc;
#pragma unroll
    for (int k = 0; k < 16; ++k) sacc[k] = 0.f;
    {
      v4u fb[32];
#pragma unroll
      for (int s = 0; s < 6; ++s) fb[s] = *(LAS v4u*)(L + qb[s & 3] + s * 1024);
      SCHED();
#pragma unroll
      for (int s = 0; s < 32; ++s) {
        if (s + 6 < 32) fb[s + 6] = *(LAS v4u*)(L + qb[(s + 6) & 3] + (s + 6) * 1024);
        if (s >= 26) TR_READ(s - 26);
        sacc = mfma32(fa[s], fb[s], sacc);
        SCHED();
      }
    }
    float mw = sacc[0];
#pragma unroll
    for (int k = 1; k < 16; ++k) mw = fmaxf(mw, sacc[k]);
    mw = x32_max(mw);
    asm volatile("s_waitcnt lgkmcnt(0)" ::: "memory");
    *(LAS float*)(L + mxa + w * 32768) = mw;
    STAMP(4);
    BAR();
    const float mx = fmaxf(fmaxf(*(LAS float*)(L + mxa), *(LAS float*)(L + mxa + 32768)), fmaxf(*(LAS float*)(L + mxa + 65536), *(LAS float*)(L + mxa + 98304)));
    float ls = 0.f;
#pragma unroll
    for (int k = 0; k < 16; ++k) { sacc[k] = __builtin_amdgcn_exp2f(sacc[k] - mx); ls += sacc[k]; }
#pragma unroll
    for (int s2 = 0; s2 < 2; ++s2)
      *(LAS v4u*)(L + pba + (2 * w + s2) * 1024) =
          v4u{pk(sacc[8 * s2], sacc[8 * s2 + 1]), pk(sacc[8 * s2 + 2], sacc[8 * s2 + 3]), pk(sacc[8 * s2 + 4], sacc[8 * s2 + 5]), pk(sacc[8 * s2 + 6], sacc[8 * s2 + 7])};
    ls = x32_sum(ls);
    *(LAS float*)(L + mxa + 640 + w * 32768) = ls;
    BAR();
    STAMP(5);
    {
      v4u pf[8];
#pragma unroll
      for (int k = 0; k < 8; ++k) pf[k] = *(LAS v4u*)(L + pba + k * 1024);
      f32x4 l4;
      if (w == 0) l4 = f32x4{*(LAS float*)(L + mxa + 640), *(LAS float*)(L + mxa + 640 + 32768), *(LAS float*)(L + mxa + 640 + 65536), *(LAS float*)(L + mxa + 640 + 98304)};
      SCHED();
      f32x16 o[4];
      unsigned p01[16], pq[16];
      const bool odd = lane & 1;
#pragma unroll
      for (int t = 0; t < 4; ++t)
#pragma unroll
        for (int k = 0; k < 16; ++k) o[t][k] = 0.f;
#pragma unroll
      for (int n = 0; n < 32; ++n) {
        if (n + 6 < 32) TR_READ(n + 6);
        o[n >> 3] = mfma32(pf[n & 7], cb[n], o[n >> 3]);
        if (n == 0 && w == 0)
          st4(v4u{tlo, genc(mx, t8), tlo, genc((l4[0] + l4[1]) + (l4[2] + l4[3]), t8)}, rws, lane < 32 ? r32 * 16 : OOB, WS_ML + slot(idx, 512));
        if (n >= 17 && n < 32 && n != 24) {
          const int j = n < 24 ? n - 17 : n - 18;
          if (j < 8) { pq[2 * j] = pk(o[0][2 * j], o[1][2 * j]); pq[2 * j + 1] = pk(o[0][2 * j + 1], o[1][2 * j + 1]); asm volatile("" : "+v"(pq[2 * j]), "+v"(pq[2 * j + 1])); }
          if (j >= 6) {
            const int k = j - 6;
            p01[2 * k] = lsbput(odd ? pq[2 * k + 1] : pq[2 * k], e0);
            p01[2 * k + 1] = __builtin_amdgcn_update_dpp(0, lsbput(odd ? pq[2 * k] : pq[2 * k + 1], e2), 0xB1, 0xf, 0xf, true);
            asm volatile("" : "+v"(p01[2 * k]), "+v"(p01[2 * k + 1]));
          }
        }
        SCHED();
      }
      STAMP(1);
#pragma unroll
      for (int k = 0; k < 8; ++k) {
        const unsigned pa = pk(o[2][2 * k], o[3][2 * k]), pb = pk(o[2][2 * k + 1], o[3][2 * k + 1]);
        st4(v4u{p01[2 * k], lsbput(odd ? pb : pa, e1), p01[2 * k + 1], (unsigned)__builtin_amdgcn_update_dpp(0, lsbput(odd ? pa : pb, e3), 0xB1, 0xf, 0xf, true)},
            rws, h3v, (2 * (k & 1) + 8 * (k >> 1)) * 131072);
      }
    }
    STAMP(6);
    RBAR();
    RBAR();
    RBAR();
  } else {
    const int v = w - 4;
    STAMP(1);
    const rsrc_t rv = mk(wuv, 512 * 4096 * 2);
    v4u uw[8];
#define TL(i) do { uw[i] = ld4<NT_W>(rv, g * 32 * 8192 + i16 * 16, (128 * jq + 8 * v + ((i) ^ (hd & 1))) * 8192 + hd * 256); SCHED(); } while (0)
    {
      v4u qa[8];
      SLP(DLY_QA);
#pragma unroll
      for (int k = 0; k < 8; ++k) qa[k] = hl4(rws, lane * 16, WS_QA + ab * 8192 + slot(8 * v + k, 1024));
      SCHED();
#pragma unroll
      for (int i = 0; i < TL0; ++i) TL(i);
#pragma unroll 1
      for (int n = 0;; ++n) {
        unsigned bad = 0;
#pragma unroll
        for (int k = 0; k < 8; ++k) bad |= strip4(qa[k], e4);
        if (n == 0 && w != 4) { STAMP(7); }
        if (__all((bad & LSBM) == 0) || n >= GSPIN) break;
        SLP(DLY_R2);
#pragma unroll
        for (int k = 0; k < 8; ++k) qa[k] = hl4(rws, lane * 16, WS_QA + ab * 8192 + slot(8 * v + k, 1024));
      }
#pragma unroll
      for (int k = 0; k < 8; ++k) *(LAS v4u*)(L + LA_QA + (lane >> 1) * 1024 + ((((lane & 1) * 32 + 8 * v + k) ^ (lane & 7)) * 16)) = qa[k];
    }
    STAMP(2);
    BAR();
#pragma unroll
    for (int i = TL0; i < TL1; ++i) TL(i);
    RBAR();
#pragma unroll
    for (int i = TL1; i < 8; ++i) TL(i);
    RBAR();
    const int e7 = lane & 7, q4 = i16 >> 2;
    int cta = LA_CTX + v * 288 + (32 * g + e7) * 2, ctr = LA_CTX + cp * 288 + g * 16;
    int h4a = ((2 * (g & 1) + (i16 & 1)) * 16 + (2 * (g >> 1) + (i16 >> 3)) * 4 + ((i16 >> 1) & 3)) * 16 + WS_H4 + v * QB + hd * 4096 + jq * 1024;
    const unsigned src = (lane & 8) ? 0x0302u : 0x0100u, s1 = (e7 & 1) ? ((src << 16) | 0x0C0Cu) : (0x0C0C0000u | src);
    unsigned s8[4];
    int uwa[4], bva[2][2];
#pragma unroll
    for (int k = 0; k < 4; ++k) {
      s8[k] = (e7 >> 1) == k ? s1 : 0x0C0C0C0Cu;
      uwa[k] = LA_UVW + (32 * v + g) * 256 + ((i16 ^ ((g << 2) | k)) * 16);
      bva[k >> 1][k & 1] = LA_UVW + (8 * g + 4 * (k & 1) + q4) * 256 + (((2 * (2 * v + (k >> 1)) + (cp >> 1)) ^ ((q4 << 2) | ((2 * g + (k & 1)) & 3))) * 16) + 8 * (cp & 1);
      asm volatile("" : "+v"(s8[k]), "+v"(uwa[k]), "+v"(bva[k >> 1][k & 1]));
    }
    asm volatile("" : "+v"(cta), "+v"(ctr), "+v"(h4a));
#define ML_LOAD() hl4(rws, slot(r32, 512), WS_ML + v * 8 * 512 + hd * 16)
#define ML_OK(V) __all((((V).x ^ tlo) | ((V).z ^ tlo) | ((((V).y ^ t8) | ((V).w ^ t8)) & 0xFFu)) == 0)
#define H3_LOADS() _Pragma("unroll") for (int k = 0; k < 8; ++k) d[k] = hl4(rws, lane * 16, WS_H3 + idx * 32768 + v * 8192 + k * 1024); SCHED()
    v4u d[8], bk[8];
    v4u ml;
    VMCNT(0);
    RBAR();
    const bool early = (unsigned)(__builtin_amdgcn_s_memrealtime() - t_start) > (unsigned)(TV(T_LATE) * 5);
    if (early) { ml = ML_LOAD(); H3_LOADS(); }
#pragma unroll
    for (int k = 0; k < 8; ++k) *(LAS v4u*)(L + uwa[k & 3] + k * 1024) = uw[k];
    BAR();
    v4u bv[2][4];
#pragma unroll
    for (int t = 0; t < 2; ++t)
#pragma unroll
      for (int ks = 0; ks < 4; ++ks) {
        const v2u x0 = __builtin_bit_cast(v2u, __builtin_amdgcn_ds_read_tr16_b64_v4i16((LAS s16x4*)(L + bva[t][0] + ks * 8192)));
        const v2u x1 = __builtin_bit_cast(v2u, __builtin_amdgcn_ds_read_tr16_b64_v4i16((LAS s16x4*)(L + bva[t][1] + ks * 8192)));
        bv[t][ks] = v4u{x0.x, x0.y, x1.x, x1.y};
      }
    SCHED();
    STAMP(4);
    {
      if (early) {
#pragma unroll 1
        for (int n = 0; n < GSPIN && !ML_OK(ml); ++n) ml = ML_LOAD();
      } else {
        SLP(DLY_M0);
        for (int n = 0;; ++n) {
          ml = ML_LOAD();
          if (ML_OK(ml) || n >= GSPIN) break;
          SLP(DLY_RM);
        }
        SLP(DLY_H3);
        H3_LOADS();
      }
      STAMP(5);
      const float m = gdec(ml.y), M = x16_max(row_max(m));
      const float e = __builtin_amdgcn_exp2f(m - M);
      const float al = e * LSB_COMP * __builtin_amdgcn_rcpf(x16_sum(row_sum(e * gdec(ml.w))));
      const unsigned ah = pk(al, al) & 0xFFFFu;
      const unsigned hl = ah | (pk(al - bf2f(ah), 0.f) << 16);
#pragma unroll
      for (int k = 0; k < 8; ++k) { bk[k] = bdiag(__builtin_amdgcn_ds_bpermute((4 * k + g) * 4, hl), s8); asm volatile("" : "+v"(bk[k])); }
      STAMP(3);
      f32x4 c;
#pragma unroll 1
      for (int n = 0;; ++n) {
        unsigned bad = 0;
        c = f32x4{0.f, 0.f, 0.f, 0.f};
#pragma unroll
        for (int k = 0; k < 8; ++k) { bad |= strip4(d[k], e4); c = mfma16(d[k], bk[k], c); }
        if (__all((bad & LSBM) == 0) || n >= GSPIN) break;
#pragma unroll
        for (int k = 0; k < 8; ++k) d[k] = hl4(rws, lane * 16, WS_H3 + idx * 32768 + v * 8192 + k * 1024);
      }
      STAMP(6);
#pragma unroll
      for (int r = 0; r < 4; ++r) {
        const float y = c[r] + dppz<0x128>(c[r]);
        *(LAS u16*)(L + cta + 16 * r) = (u16)pk(y, y);
      }
    }
    BAR();
    {
      v4u af[4];
#pragma unroll
      for (int ks = 0; ks < 4; ++ks) af[ks] = *(LAS v4u*)(L + ctr + 64 * ks);
      f32x4 c0 = {0.f, 0.f, 0.f, 0.f}, c1 = c0;
#pragma unroll
      for (int ks = 0; ks < 4; ++ks) { c0 = mfma16(af[ks], bv[0][ks], c0); c1 = mfma16(af[ks], bv[1][ks], c1); }
      const f32x4 ct = g < 2 ? c0 : c1;
      const float x0 = (g & 1) ? ct[2] : ct[0], x1 = (g & 1) ? ct[3] : ct[1];
      const float n0 = dppz<0xB1>(x0), n1 = dppz<0xB1>(x1);
      st4(v4u{tlo, genc((lane & 1) ? n1 : x0, t8), tlo, genc((lane & 1) ? x1 : n0, t8)}, rws, h4a, 0);
    }
    STAMP(11);
  }
  STAMP_OUT();
}

extern "C" __global__ void fused_decode_end() {}

extern "C" void kernel_launch(void* const* d_in, const int* in_sizes, int n_in, void* d_out, int out_size, void* d_ws, size_t ws_size,
                              hipStream_t stream) {
  (void)hipFuncSetAttribute((const void*)fused_decode, hipFuncAttributeMaxDynamicSharedMemorySize, LDS_BYTES);
  fused_decode<<<256, 512, LDS_BYTES, stream>>>((const u16*)d_in[0], (const u16*)d_in[1], (const u16*)d_in[2], (const u16*)d_in[3],
                                                (const u16*)d_in[4], (const u16*)d_in[5], (const u16*)d_in[7], (const u16*)d_in[8],
                                                (const u16*)d_in[9], (u16*)d_out, (char*)d_ws TUNE_ARG);
}
```

```cpp
#include <hip/hip_runtime.h>
#include <stdint.h>

typedef unsigned short u16;
typedef unsigned long long u64;
typedef unsigned v4u __attribute__((ext_vector_type(4)));
typedef unsigned v2u __attribute__((ext_vector_type(2)));
typedef float f32x2 __attribute__((ext_vector_type(2)));
typedef float f32x4 __attribute__((ext_vector_type(4)));
typedef float f32x16 __attribute__((ext_vector_type(16)));
typedef short s16x4 __attribute__((ext_vector_type(4)));
typedef __bf16 bf16x8 __attribute__((ext_vector_type(8)));
typedef __bf16 bf16x2 __attribute__((ext_vector_type(2)));
typedef __amdgpu_buffer_rsrc_t rsrc_t;
#define GAS __attribute__((address_space(1)))
#define LAS __attribute__((address_space(3)))
#define DI __device__ __forceinline__

#define NT_W 2
#define NT_WO 0
#define PACE_C 7
#define DLY_H1 2
#define KQA 31
#define PACE_C0 3
#define KC0 0
#define DLY_K 16
#define PACE_G 4
#define PACE_O 8
#define DLY_R1 7
#define DLY_C 40
#define DLY_Q 0
#define DLY_F 70
#define DLY_QA 8
#define ABL 0
#define KWO 16
#define PACE_T 4
#define PACE_K 4
#define PACE_G0 2
#define KG0 0
#define PACE_W2 6
#define W2_A 6
#define TL0 0
#define TL1 4
#define TL2 12
#define TL3 24
#define PFW 1
#define DLY_PF 24
#define PACE_PF 4
#define DLY_R4 2
#define DLY_R7 2
#define DLY_RM 4
#define MLFIRST 0
#define DLY_W2 1
#define DLY_M0 12
#define H4HINT 0
#define DLY_H4 12
#define PFWAVE 7
#define KC1 32
#define PACE_C1 12
#define WO_E 10
#define PT 9
#define T_LATE 400
#define T_H7 183
#define DLY_R2 12
#define UK0 6
#define WO_A 9
#define WO_B 13
#define WO_C 12
#define DLY_H3 1
#define DLY_H7 19
#define DLY_H5 18

#define SLP(N) do { if (N) __builtin_amdgcn_s_sleep(N); } while (0)
#define TV(N) (N)
#define TUNE_PARAM
#define TUNE_ARG

constexpr int SC1 = 16;
constexpr int GSPIN = 1 << 13;

constexpr int WS_W = 0;
constexpr int QB = 524288;
constexpr int WS_H1 = 4096;
constexpr int WS_QA = WS_H1 + 131072;
constexpr int WS_ML = WS_QA + 32768;
constexpr int WS_HN = WS_ML + 16384;
constexpr int WS_H7 = WS_HN + 4096;
constexpr int WS_H4 = 4 * QB;
constexpr int WS_H5 = WS_H4 + 131072;
constexpr int WS_H3 = 8 * QB;
DI constexpr int slot(int p, int size) { return (p & 3) * QB + (p >> 2) * size; }
constexpr int WS_END = WS_H3 + 128 * 32768;
constexpr int WS_STAMP = WS_END;
constexpr int OOB = (int)0x80000000;

constexpr int LP_TAG = 4992, LP_XP = 0, LP_XD = 5120, LP_QB = 15360, LP_QO = 16896, LP_RED = 18432, LP_HP = 34816, LP_B8 = 35840, LP_FIN = 39936, LP_UK = 40960;
constexpr int LA_QA = 131072, LA_MX = LA_QA;
constexpr int LA_PB = LA_QA + 1024, LA_SUM = LA_QA + 512, LA_CTX = LA_QA + 9216, LA_CT2 = LA_QA + 11264, LA_B8 = LA_QA + 16384,
              LA_FIN = LA_QA + 20480, LA_UVW = 0;
constexpr int LDS_BYTES = 163840;

DI rsrc_t mk(const void* p, int bytes) { return __builtin_amdgcn_make_buffer_rsrc((void*)p, (short)0, bytes, 0x00020000); }
template <int AUX> DI v4u ld4(rsrc_t r, int vo, int so) { return __builtin_amdgcn_raw_buffer_load_b128(r, vo, so, AUX); }
template <int AUX> DI v2u ld2(rsrc_t r, int vo, int so) { return __builtin_amdgcn_raw_buffer_load_b64(r, vo, so, AUX); }
DI v4u hl4(rsrc_t r, int vo, int so) { asm volatile("" : "+v"(vo)); return __builtin_amdgcn_raw_buffer_load_b128(r, vo, so, SC1); }
DI v2u hl2(rsrc_t r, int vo, int so) { asm volatile("" : "+v"(vo)); return __builtin_amdgcn_raw_buffer_load_b64(r, vo, so, SC1); }
DI void touch(rsrc_t r, int vo, int so) { v4u t; asm volatile("buffer_load_dwordx4 %0, %1, %2, %3 offen sc1\n\ts_waitcnt vmcnt(0)" : "=&v"(t) : "v"(vo), "s"(r), "s"(so) : "memory"); }
DI void st4(v4u v, rsrc_t r, int vo, int so) { __builtin_amdgcn_raw_buffer_store_b128(v, r, vo + so, 0, SC1); }
DI void st2(v2u v, rsrc_t r, int vo, int so) { __builtin_amdgcn_raw_buffer_store_b64(v, r, vo, so, SC1); }
DI void dma16(rsrc_t r, int vo, int so, unsigned lds) {
  asm volatile("s_mov_b32 m0, %0\n\ts_nop 0\n\tbuffer_load_dwordx4 %1, %2, %3 offen nt lds" ::"s"(lds), "v"(vo), "s"(r), "s"(so) : "memory");
}
#define VMCNT(n) asm volatile("s_waitcnt vmcnt(%0)" ::"n"(n) : "memory")
#define FENCE() asm volatile("" ::: "memory")
#define SCHED() __builtin_amdgcn_sched_barrier(0)

DI float bf2f(unsigned v) { return __uint_as_float(v << 16); }
DI unsigned pk(float a, float b) { f32x2 f = {a, b}; return __builtin_bit_cast(unsigned, __builtin_convertvector(f, bf16x2)); }
DI f32x4 mfma16(v4u a, v4u b, f32x4 c) {
  return __builtin_amdgcn_mfma_f32_16x16x32_bf16(__builtin_bit_cast(bf16x8, a), __builtin_bit_cast(bf16x8, b), c, 0, 0, 0);
}
DI f32x4 mfma16k16(v2u a, v2u b, f32x4 c) {
  return __builtin_amdgcn_mfma_f32_16x16x16bf16_1k(__builtin_bit_cast(s16x4, a), __builtin_bit_cast(s16x4, b), c, 0, 0, 0);
}
DI f32x16 mfma32(v4u a, v4u b, f32x16 c) {
  return __builtin_amdgcn_mfma_f32_32x32x16_bf16(__builtin_bit_cast(bf16x8, a), __builtin_bit_cast(bf16x8, b), c, 0, 0, 0);
}
template <int CTRL, int RM> DI float dppf(float old, float v) {
  return __builtin_bit_cast(float, __builtin_amdgcn_update_dpp(__builtin_bit_cast(int, old), __builtin_bit_cast(int, v), CTRL, RM, 0xf, false));
}
template <int CTRL> DI float dppz(float v) {
  return __builtin_bit_cast(float, __builtin_amdgcn_update_dpp(0, __builtin_bit_cast(int, v), CTRL, 0xf, 0xf, true));
}
DI float row_sum(float v) {
  v += dppz<0xB1>(v); v += dppz<0x4E>(v); v += dppz<0x124>(v); v += dppz<0x128>(v);
  return v;
}
DI float dot2(unsigned a, unsigned b, float c) {
  return __builtin_amdgcn_fdot2_f32_bf16(__builtin_bit_cast(bf16x2, a), __builtin_bit_cast(bf16x2, b), c, false);
}
DI float row_max(float v) {
  v = fmaxf(v, dppf<0xB1, 0xf>(v, v)); v = fmaxf(v, dppf<0x4E, 0xf>(v, v));
  v = fmaxf(v, dppf<0x124, 0xf>(v, v)); v = fmaxf(v, dppf<0x128, 0xf>(v, v));
  return v;
}
DI float x16_sum(float v) { auto r = __builtin_amdgcn_permlane16_swap(__float_as_uint(v), __float_as_uint(v), false, false); return __uint_as_float(r[0]) + __uint_as_float(r[1]); }
DI float x32_sum(float v) { auto r = __builtin_amdgcn_permlane32_swap(__float_as_uint(v), __float_as_uint(v), false, false); return __uint_as_float(r[0]) + __uint_as_float(r[1]); }
DI float x16_max(float v) { auto r = __builtin_amdgcn_permlane16_swap(__float_as_uint(v), __float_as_uint(v), false, false); return fmaxf(__uint_as_float(r[0]), __uint_as_float(r[1])); }
DI float x32_max(float v) { auto r = __builtin_amdgcn_permlane32_swap(__float_as_uint(v), __float_as_uint(v), false, false); return fmaxf(__uint_as_float(r[0]), __uint_as_float(r[1])); }

#define RBAR() asm volatile("s_barrier" ::: "memory")
#define BAR() asm volatile("s_waitcnt lgkmcnt(0)\n\ts_barrier" ::: "memory")
DI unsigned genc(float v, unsigned t8) { return ((__float_as_uint(v) + 0x80u) & 0xFFFFFF00u) | t8; }
DI float gdec(unsigned hi) { return __uint_as_float(hi & 0xFFFFFF00u); }

#define STAMP(k)
#define STAMP_DECL
#define STAMP_OUT()

DI v4u bdiag(unsigned P, const unsigned (&s)[4]) {
  v4u b;
  b.x = __builtin_amdgcn_perm(P, P, s[0]); b.y = __builtin_amdgcn_perm(P, P, s[1]);
  b.z = __builtin_amdgcn_perm(P, P, s[2]); b.w = __builtin_amdgcn_perm(P, P, s[3]);
  return b;
}
DI void bdiag_sel(int cp, unsigned (&s)[4]) {
  const unsigned lo = (cp & 1) ? 0x01000C0Cu : 0x0C0C0100u, hi = (cp & 1) ? 0x03020C0Cu : 0x0C0C0302u, z = 0x0C0C0C0Cu;
  s[0] = cp < 2 ? lo : z; s[1] = cp < 2 ? z : lo; s[2] = cp < 2 ? hi : z; s[3] = cp < 2 ? z : hi;
}

DI unsigned lsbtag(unsigned tlo, unsigned thi, int p) { return (((p < 32 ? tlo : thi) >> (p & 31)) & 1u) | (((tlo ^ (unsigned)p) & 1u) << 16); }
constexpr unsigned LSBM = 0x00010001u;
DI unsigned lsbput(unsigned v, unsigned e) { return (v & ~LSBM) | e; }
DI unsigned strip4(v4u& d, const unsigned (&e)[4]) { d.x ^= e[0]; d.y ^= e[1]; d.z ^= e[2]; d.w ^= e[3]; return (d.x | d.y) | (d.z | d.w); }
DI void swap24(v4u& a, v4u& b) {
  unsigned az = a.z, aw = a.w, bx = b.x, by = b.y;
  asm volatile("v_swap_b32 %0, %2\n\tv_swap_b32 %1, %3\n\ts_nop 1" : "+v"(az), "+v"(aw), "+v"(bx), "+v"(by));
  a.z = az; a.w = aw; b.x = bx; b.y = by;
}
constexpr float LSB_COMP = 1.0028f;
DI v4u tag8(f32x4 c, const unsigned (&t)[4]) {
  return v4u{(__float_as_uint(c[0]) & 0xFFFFFF00u) | t[0], (__float_as_uint(c[1]) & 0xFFFFFF00u) | t[1],
             (__float_as_uint(c[2]) & 0xFFFFFF00u) | t[2], (__float_as_uint(c[3]) & 0xFFFFFF00u) | t[3]};
}
extern "C" __global__ void __launch_bounds__(512, 2)
fused_decode(const u16* __restrict__ xin, const u16* __restrict__ ckv, const u16* __restrict__ wq, const u16* __restrict__ wuk,
             const u16* __restrict__ wuv, const u16* __restrict__ wo, const u16* __restrict__ w1, const u16* __restrict__ w3,
             const u16* __restrict__ w2, u16* __restrict__ out, char* ws TUNE_PARAM) {
  extern __shared__ __attribute__((aligned(16))) char smem[];
  LAS char* L = (LAS char*)smem;
  const unsigned lbase = (unsigned)(size_t)smem;
  const int lane = threadIdx.x & 63, w = __builtin_amdgcn_readfirstlane(threadIdx.x >> 6);
  const int xg = blockIdx.x & 7, qd_ = blockIdx.x >> 3, qq = qd_ & 15, idx = xg * 16 + qq, hd = idx >> 2, jq = idx & 3;
  const int i16 = lane & 15, g = lane >> 4, bj = i16 >> 2, cp = lane & 3;
  STAMP_DECL;
  STAMP(0);
  if (w == PFWAVE) {
    u64 pc; unsigned back, len;
    asm volatile("s_getpc_b64 %0\n.Lpf_here:\n\ts_mov_b32 %1, .Lpf_here-fused_decode\n\ts_mov_b32 %2, fused_decode_end-fused_decode" : "=s"(pc), "=s"(back), "=s"(len));
    const rsrc_t rcode = mk((const void*)(pc - back), (int)min(len, 32768u));
    v4u dummy;
    asm volatile("s_nop 4\n\tbuffer_load_dwordx4 %0, %1, %2, 0 offen\n\ts_waitcnt vmcnt(0)" : "=&v"(dummy) : "v"(qd_ * 1024 + lane * 16), "s"(rcode) : "memory");
  }
  asm volatile("" ::"s"(xin), "s"(ckv), "s"(wq), "s"(wuk), "s"(wuv), "s"(wo), "s"(w1), "s"(w3), "s"(w2), "s"(out), "s"(ws));
  u64 t_start;
  asm volatile("s_memrealtime %0\n\ts_waitcnt lgkmcnt(0)" : "=s"(t_start) : "s"(ws) : "memory");
  const rsrc_t rws = mk(ws, WS_END);
  const rsrc_t rx = mk(xin, 4 * 576 * 2);
  unsigned sel[4];
  bdiag_sel(cp, sel);
#define MAKE_TAG(W) const u64 tag_ = (((u64)(W).y << 32) | (W).x) + 0x9E3779B97F4A7C15ull;  \
  const unsigned tlo = (unsigned)tag_, thi = (unsigned)(tag_ >> 32), t8 = thi & 0xFFu;        \
  const unsigned tb[4] = {tlo & 0xFFu, (tlo >> 8) & 0xFFu, (tlo >> 16) & 0xFFu, tlo >> 24}
  const int vo5 = slot(lane >> 3, 16384) + (lane & 7) * 16;

  if (qd_ >= 16) {
    const int so5 = WS_H5 + idx * 128;
    if (w < 4) {
      v4u ka[2][4];
      v2u wv_;
      asm volatile("buffer_load_dwordx2 %0, off, %1, 0 sc1" : "=v"(wv_) : "s"(rws) : "memory");
      {
        const rsrc_t rk = mk(wuk, 512 * 4096 * 2);
        SLP(DLY_K);
#pragma unroll
        for (int n = 0; n < 8; ++n) {
          if (n >= PACE_K) VMCNT(PACE_K - 1);
          dma16(rk, g * 8192 + ((i16 ^ ((g << 2) | (n & 3))) * 16), (128 * jq + 32 * w + 4 * n) * 8192 + hd * 256, lbase + LP_UK + (8 * w + n) * 1024);
        }
        VMCNT(0);
        const int fr = ((i16 & 3) << 2) | (i16 >> 2);
#pragma unroll
        for (int t = 0; t < 2; ++t)
#pragma unroll
          for (int s = 0; s < 4; ++s) {
            ka[t][s] = *(LAS v4u*)(L + LP_UK + (32 * w + 16 * t + i16) * 256 + (((4 * s + g) ^ fr) * 16));
            asm volatile("" : "+v"(ka[t][s]));
          }
      }
      STAMP(1);
      asm volatile("" : "+v"(wv_));
      MAKE_TAG(wv_);
      unsigned eq[4];
#pragma unroll
      for (int k = 0; k < 4; ++k) { eq[k] = lsbtag(tlo, thi, 4 * i16 + k); asm volatile("" : "+v"(eq[k])); }
      SLP(DLY_H1);
      STAMP(2);
      {
        const int blk = w & 1, half = w >> 1;
        float q0, q1;
        for (int n = 0;; ++n) {
          v4u p[4];
#pragma unroll
          for (int jj = 0; jj < 4; ++jj) p[jj] = hl4(rws, lane * 16, WS_H1 + jj * QB + hd * 4096 + (blk * 2 + half) * 1024);
          unsigned bad = 0;
          q0 = 0.f; q1 = 0.f;
#pragma unroll
          for (int jj = 0; jj < 4; ++jj) {
            bad |= (p[jj].x ^ tlo) | (p[jj].z ^ tlo) | (((p[jj].y ^ t8) | (p[jj].w ^ t8)) & 0xFFu);
            q0 += gdec(p[jj].y); q1 += gdec(p[jj].w);
          }
          if (__all(bad == 0) || n >= GSPIN) break;
          SLP(DLY_R1);
        }
        const unsigned qp = pk(q0, q1);
        const int d0 = 32 * g + 16 * half + 4 * blk + cp;
        *(LAS u16*)(L + LP_QB + bj * 320 + d0 * 2) = (u16)qp;
        *(LAS u16*)(L + LP_QB + bj * 320 + (d0 + 8) * 2) = (u16)(qp >> 16);
      }
      STAMP(4);
      BAR();
      STAMP(5);
      {
        v4u qf[4];
#pragma unroll
        for (int s = 0; s < 4; ++s) qf[s] = *(LAS v4u*)(L + LP_QB + cp * 320 + s * 64 + g * 16);
        f32x4 c0 = {0.f, 0.f, 0.f, 0.f}, c1 = c0;
#pragma unroll
        for (int s = 0; s < 4; ++s) { c0 = mfma16(ka[0][s], qf[s], c0); c1 = mfma16(ka[1][s], qf[s], c1); }
        const float sc = 0.08838834764831845f * 1.4426950408889634f * LSB_COMP;
        if (i16 < 4) {
          *(LAS v2u*)(L + LP_QO + i16 * 256 + (32 * w + 4 * g) * 2) = v2u{pk(c0[0] * sc, c0[1] * sc), pk(c0[2] * sc, c0[3] * sc)};
          *(LAS v2u*)(L + LP_QO + i16 * 256 + (32 * w + 16 + 4 * g) * 2) = v2u{pk(c1[0] * sc, c1[1] * sc), pk(c1[2] * sc, c1[3] * sc)};
        }
      }
      BAR();
      if (w == 0) {
        const v4u v = *(LAS v4u*)(L + LP_QO + lane * 16);
        st4(v4u{lsbput(v.x, eq[0]), lsbput(v.y, eq[1]), lsbput(v.z, eq[2]), lsbput(v.w, eq[3])}, rws, g * 8192 + i16 * 16, WS_QA + slot(hd, 1024) + jq * 256);
      }
      STAMP(6);
      BAR();
      const rsrc_t r2 = mk(w2, 4608 * 576 * 2);
      const int so2 = (xg * 576 + 36 * qq) * 1152;
      const rsrc_t ro = mk(wo, 4096 * 576 * 2);
      const int soo = (hd * 128 + 32 * jq) * 1152;
      v4u a2[9][2];
      v2u a8[3][2];
      {
        const int vo4 = 2 * g * 1152 + i16 * 16, vo4m = g < 2 ? vo4 : OOB;
        const int vo8 = 2 * g * 1152 + i16 * 8 + 1024, vo8m = (g < 2 && w == 0) ? vo8 : OOB;
#pragma unroll
        for (int n = 0; n < 14 + WO_E; ++n) {
          if (n >= PACE_W2) VMCNT(PACE_W2 - 1);
          if (n == W2_A) { SCHED(); RBAR(); SLP(DLY_W2); }
          const int o = n >> 1, r = n & 1;
          if (n < 10) a2[o][r] = ld4<NT_W>(r2, o < 4 ? vo4 : vo4m, so2 + (8 * o + r) * 1152 + 256 * w);
          else if (n < 12) a8[0][r] = ld2<NT_W>(r2, vo8, so2 + (8 * w + r) * 1152);
          else if (n < 14) a8[1][r] = ld2<NT_W>(r2, vo8m, so2 + (32 + r) * 1152);
          else if (n < 22) a2[o - 2][r] = ld4<NT_W>(ro, vo4, soo + (8 * (o - 7) + r) * 1152 + 256 * w);
          else a8[2][r] = ld2<NT_W>(ro, vo8, soo + (8 * w + r) * 1152);
          SCHED();
        }
      }
      STAMP(7);
#pragma unroll
      for (int o = 0; o < 5; ++o) swap24(a2[o][0], a2[o][1]);
      STAMP(8);
      RBAR();
      STAMP(9);
      {
        const int vo4 = 2 * g * 1152 + i16 * 16, vo8 = 2 * g * 1152 + i16 * 8 + 1024;
#pragma unroll
        for (int n = 14 + WO_E; n < 24; ++n) {
          const int o = n >> 1, r = n & 1;
          if (n < 22) a2[o - 2][r] = ld4<NT_W>(ro, vo4, soo + (8 * (o - 7) + r) * 1152 + 256 * w);
          else a8[2][r] = ld2<NT_W>(ro, vo8, soo + (8 * w + r) * 1152);
        }
        SCHED();
        const v4u h4 = *(LAS v4u*)(L + LP_HP + (bj * 4 + g) * 48);
        const unsigned h5 = *(LAS unsigned*)(L + LP_HP + (bj * 4 + g) * 48 + 16);
        const unsigned hw = *(LAS unsigned*)(L + LP_HP + (bj * 4 + g) * 48 + 4 * w);
        f32x4 c8 = {0.f, 0.f, 0.f, 0.f}, cx = c8, cy = c8;
        c8 = mfma16(v4u{a8[0][0].x, a8[0][0].y, a8[0][1].x, a8[0][1].y}, bdiag(hw, sel), c8);
        c8 = mfma16(v4u{a8[1][0].x, a8[1][0].y, a8[1][1].x, a8[1][1].y}, bdiag(h5, sel), c8);
#pragma unroll
        for (int o = 0; o < 5; ++o) {
          const v4u B = bdiag(o == 0 ? h4.x : o == 1 ? h4.y : o == 2 ? h4.z : o == 3 ? h4.w : h5, sel);
          cx = mfma16(a2[o][0], B, cx);
          cy = mfma16(a2[o][1], B, cy);
        }
#pragma unroll
        for (int o = 5; o < 9; ++o) swap24(a2[o][0], a2[o][1]);
        asm volatile("" : "+v"(cx), "+v"(cy), "+v"(c8));
        int bpa = (bj * 16 + g) * 4;
        asm volatile("" : "+v"(bpa));
        STAMP(10);
        float y0, y1;
        for (int n = 0;; ++n) {
          v4u p[4];
#pragma unroll
          for (int jj = 0; jj < 4; ++jj) p[jj] = hl4(rws, lane * 16, WS_H4 + slot(idx, 4096) + jj * 1024);
          unsigned bad = 0;
          y0 = 0.f; y1 = 0.f;
#pragma unroll
          for (int jj = 0; jj < 4; ++jj) {
            bad |= (p[jj].x ^ tlo) | (p[jj].z ^ tlo) | (((p[jj].y ^ t8) | (p[jj].w ^ t8)) & 0xFFu);
            y0 += gdec(p[jj].y); y1 += gdec(p[jj].w);
          }
          if (__all(bad == 0) || n >= GSPIN) break;
          SLP(DLY_R4);
        }
        STAMP(11);
        const unsigned yp = pk(y0, y1);
        const unsigned pw = __builtin_amdgcn_ds_bpermute(bpa + 16 * w, yp);
        unsigned po[4];
#pragma unroll
        for (int o = 0; o < 4; ++o) po[o] = __builtin_amdgcn_ds_bpermute(bpa + 16 * o, yp);
        c8 = mfma16(v4u{a8[2][0].x, a8[2][0].y, a8[2][1].x, a8[2][1].y}, bdiag(pw, sel), c8);
#pragma unroll
        for (int o = 0; o < 4; ++o) {
          const v4u B = bdiag(po[o], sel);
          cx = mfma16(a2[5 + o][0], B, cx);
          cy = mfma16(a2[5 + o][1], B, cy);
        }
        *(LAS f32x4*)(L + LP_B8 + w * 1024 + lane * 16) = c8;
        st4(tag8(cx, tb), rws, vo5, so5 + (2 * w) * 2 * 16384);
        st4(tag8(cy, tb), rws, vo5, so5 + (2 * w + 1) * 2 * 16384);
        BAR();
        if (w == 0) {
          f32x4 s = *(LAS f32x4*)(L + LP_B8 + lane * 16);
#pragma unroll
          for (int k = 1; k < 4; ++k) s += *(LAS f32x4*)(L + LP_B8 + k * 1024 + lane * 16);
          st4(tag8(s, tb), rws, vo5, so5 + 8 * 2 * 16384);
        }
      }
      STAMP(3);
      if (qq < 9) RBAR();
    } else {
      const int v = w - 4;
      unsigned xn[8];
      const v2u wv_ = hl2(rws, 0, WS_W);
      v4u pt1;
      if (PT & 1) asm volatile("buffer_load_dwordx4 %0, %1, %2, %3 offen sc1" : "=v"(pt1) : "v"(lane * 16), "s"(rws), "s"(WS_H1 + slot(idx, 4096) + v * 1024) : "memory");
      {
        const v4u x0 = ld4<0>(rx, lane * 16, v * 1152), x1 = ld4<0>(rx, lane < 8 ? lane * 16 : OOB, v * 1152 + 1024);
        float s = 0.f, q = 0.f;
#pragma unroll
        for (int k = 0; k < 4; ++k) {
          s = dot2(x0[k], 0x3F803F80u, s); q = dot2(x0[k], x0[k], q);
          s = dot2(x1[k], 0x3F803F80u, s); q = dot2(x1[k], x1[k], q);
        }
        const float mean = x32_sum(x16_sum(row_sum(s))) * (1.0f / 576.0f);
        const float msq = x32_sum(x16_sum(row_sum(q))) * (1.0f / 576.0f);
        const float rstd = __builtin_amdgcn_rsqf(fmaxf(fmaf(-mean, mean, msq), 0.f) + 1e-5f), nb = -mean * rstd;
#pragma unroll
        for (int k = 0; k < 4; ++k) {
          xn[k] = pk(fmaf(bf2f(x0[k]), rstd, nb), fmaf(__uint_as_float(x0[k] & 0xFFFF0000u), rstd, nb));
        }
#pragma unroll
        for (int k = 0; k < 4; ++k) xn[4 + k] = pk(fmaf(bf2f(x1[k]), rstd, nb), fmaf(__uint_as_float(x1[k] & 0xFFFF0000u), rstd, nb));
      }
      if (PT & 1) { VMCNT(0); asm volatile("" : "+v"(pt1)); }
#pragma unroll
      for (int hf = 0; hf < 2; ++hf) {
        if (hf == 0 || lane < 8) {
#pragma unroll
          for (int k = 0; k < 4; ++k) {
            const int o = lane + 64 * hf;
            *(LAS unsigned*)(L + LP_XD + ((v * 4 + ((2 * k) & 3)) * 148 + 2 * o + (k >> 1)) * 4) = __builtin_amdgcn_perm(0u, xn[4 * hf + k], 0x01000100u);
            *(LAS unsigned*)(L + LP_XD + ((v * 4 + ((2 * k + 1) & 3)) * 148 + 2 * o + (k >> 1)) * 4) = __builtin_amdgcn_perm(0u, xn[4 * hf + k], 0x03020302u);
          }
        }
      }
      STAMP(1);
      if (PT & 8) touch(rws, lane * 16, WS_H4 + slot(idx, 4096) + v * 1024);
      MAKE_TAG(wv_);
      const int U0 = (81 * qq) / 144, Qb = 144 * (U0 + 1);
      unsigned xd[21];
      const GAS char* wb = (const GAS char*)(i16 < 8 ? w1 : w3) + (xg * 576 * 2 + (i16 & 7) * 16 + g * 9216);
      const unsigned m0 = cp == 0 ? 0xFFFFu : cp == 1 ? 0xFFFF0000u : 0u, m1 = cp == 2 ? 0xFFFFu : cp == 3 ? 0xFFFF0000u : 0u;
      f32x4 acc[4];
#pragma unroll
      for (int k = 0; k < 4; ++k) acc[k] = f32x4{0.f, 0.f, 0.f, 0.f};
      v4u a[21];
        int off[2][3];
        unsigned use[2][3];
        int hpa[2];
#pragma unroll
        for (int e = 0; e < 2; ++e) {
          const int t = ((v - 2) & 1) * 64 + lane + 128 * e;
          const int cl = t >> 2, b = t & 3, f = 36 * qq + cl, u = f >> 6;
          const int r0 = (144 * u) / 81, r1 = (144 * u + 143) / 81;
#pragma unroll
          for (int k = 0; k < 3; ++k) {
            const int rr = r0 + k;
            const bool on = t < 144 && rr <= r1;
            off[e][k] = on ? slot(xg * 16 + rr, 8192) + (u - (81 * rr) / 144) * 4096 + (f & 63) * 64 + b * 16 : OOB;
            use[e][k] = on ? 0xFFFFFFFFu : 0u;
          }
          hpa[e] = LP_HP + ((b * 4 + ((cl >> 1) & 3)) * 12 + (cl >> 3)) * 4 + (cl & 1) * 2;
          asm volatile("" : "+v"(hpa[e]), "+v"(off[e][0]), "+v"(off[e][1]), "+v"(off[e][2]), "+v"(use[e][0]), "+v"(use[e][1]), "+v"(use[e][2]));
        }
      int pa = WS_H7 + slot(idx, 8192) + v * 4096 + (32 * ((lane >> 4) & 1) + 16 * (lane >> 5) + cp) * 64 + bj * 16;
      asm volatile("" : "+v"(pa));
      BAR();
      RBAR();
#pragma unroll
      for (int t2 = 0; t2 < 21; ++t2) {
        const int Q = min(81 * qq + 4 * t2 + v, 81 * qq + 80), qk = Q >= Qb ? Q - Qb : Q - 144 * U0;
        xd[t2] = *(LAS unsigned*)(L + LP_XD + ((bj * 4 + g) * 148 + qk) * 4);
        asm volatile("" : "+v"(xd[t2]));
      }
      if ((PT & 16) && v >= 2) {
        v4u t[6];
#pragma unroll
        for (int k = 0; k < 6; ++k) t[k] = hl4(rws, off[k / 3][k % 3], WS_H7);
#pragma unroll
        for (int k = 0; k < 6; ++k) asm volatile("" ::"v"(t[k]));
      }
      if ((PT & 32) && v < 2 && qq < 9) {
        v4u t[8];
#pragma unroll
        for (int k = 0; k < 8; ++k) t[k] = hl4(rws, lane * 16, WS_H5 + slot(qq * 8 + xg, 16384) + (v * 8 + k) * 1024);
#pragma unroll
        for (int k = 0; k < 8; ++k) asm volatile("" ::"v"(t[k]));
      }
#pragma unroll 1
      for (int n = 0; n < 4096 && (unsigned)(__builtin_amdgcn_s_memrealtime() - t_start) < (unsigned)(TV(DLY_F) * 5); ++n) __builtin_amdgcn_s_sleep(1);
      STAMP(2);
#pragma unroll
      for (int t = -PACE_G; t < 21; ++t) {
        const int tl = t + PACE_G;
        if (tl < 21) {
          const int Q = 81 * qq + 4 * tl + v, u = Q >= Qb ? U0 + 1 : U0, qk = Q - 144 * u;
          if (tl < KG0 && tl >= PACE_G0) VMCNT(PACE_G0 - 1);
          if (tl < 20 || v == 0) {
            const GAS v4u* p = (const GAS v4u*)(wb + (unsigned)(qk * 4 * 9216 + u * 128));
            a[tl] = NT_W ? __builtin_nontemporal_load(p) : *p;
          } else a[tl] = v4u{0u, 0u, 0u, 0u};
          SCHED();
        }
        if (tl == 20) { STAMP(3); BAR(); }
        if (t >= 0) {
          const int Q = 81 * qq + 4 * t + v;
          const v2u B = {xd[t] & m0, xd[t] & m1};
          if (Q < Qb) { acc[0] = mfma16k16(v2u{a[t].x, a[t].y}, B, acc[0]); acc[1] = mfma16k16(v2u{a[t].z, a[t].w}, B, acc[1]); }
          else        { acc[2] = mfma16k16(v2u{a[t].x, a[t].y}, B, acc[2]); acc[3] = mfma16k16(v2u{a[t].z, a[t].w}, B, acc[3]); }
          SCHED();
        }
      }
#pragma unroll
      for (int k = 0; k < 4; ++k) *(LAS f32x4*)(L + LP_RED + (v * 4 + k) * 1024 + lane * 16) = acc[k];
      STAMP(4);
      BAR();
      STAMP(9);
      if (v == 0 || (v == 1 && 81 * qq + 80 >= Qb)) {
#pragma unroll
        for (int hh = 0; hh < 2; ++hh) {
          f32x4 r = *(LAS f32x4*)(L + LP_RED + (2 * v + hh) * 1024 + lane * 16);
#pragma unroll
          for (int k = 1; k < 4; ++k) r += *(LAS f32x4*)(L + LP_RED + (k * 4 + 2 * v + hh) * 1024 + lane * 16);
#pragma unroll
          for (int k = 0; k < 2; ++k) {
            const auto x = __builtin_amdgcn_permlane32_swap(__float_as_uint(r[k]), __float_as_uint(r[2 + k]), false, false);
            st4(v4u{tlo, genc(__uint_as_float(x[0]), t8), tlo, genc(__uint_as_float(x[1]), t8)}, rws, pa + (8 * k + 4 * hh) * 64, 0);
          }
        }
      } else if (v >= 2) {
        SLP(DLY_H7);
#pragma unroll 1
        for (int n = 0; n < 4096 && (unsigned)(__builtin_amdgcn_s_memrealtime() - t_start) < (unsigned)(TV(T_H7) * 5); ++n) __builtin_amdgcn_s_sleep(1);
        STAMP(5);
        float gs[2], us[2];
        for (int n = 0;; ++n) {
          v4u p[2][3];
#pragma unroll
          for (int e = 0; e < 2; ++e)
#pragma unroll
            for (int k = 0; k < 3; ++k) p[e][k] = hl4(rws, off[e][k], WS_H7);
          unsigned bad = 0;
#pragma unroll
          for (int e = 0; e < 2; ++e) {
            gs[e] = 0.f; us[e] = 0.f;
#pragma unroll
            for (int k = 0; k < 3; ++k) {
              bad |= ((p[e][k].x ^ tlo) | (p[e][k].z ^ tlo) | (((p[e][k].y ^ t8) | (p[e][k].w ^ t8)) & 0xFFu)) & use[e][k];
              gs[e] += gdec(p[e][k].y); us[e] += gdec(p[e][k].w);
            }
          }
          if (__all(bad == 0) || n >= GSPIN) break;
          SLP(DLY_R7);
        }
#pragma unroll
        for (int e = 0; e < 2; ++e) {
          const float hv = gs[e] / (1.0f + __expf(-gs[e])) * us[e];
          if ((v - 2) * 64 + lane + 128 * e < 160) *(LAS u16*)(L + hpa[e]) = (u16)pk(hv, hv);
        }
      }
      STAMP(6);
      BAR();
      const int fe = lane & 31, fL = 8 * xg + (fe >> 2), frr = fe & 3;
      const int oidx = ((fL & 15) >> 2) * 576 + (qq < 8 ? 128 * (qq >> 1) + 8 * (4 * (fL >> 4) + frr) + 4 * (qq & 1) + (fL & 3) : 512 + 4 * (4 * (fL >> 4) + frr) + (fL & 3));
      unsigned xres = 0;
      if (v == 3 && qq < 9) xres = ((const GAS u16*)xin)[oidx];
      const int h5o = WS_H5 + slot(qq * 8 + xg, 16384) + v * 4096;
      int fina = LP_FIN + v * 128 + (lane & 7) * 16 + g * 4;
      asm volatile("" : "+v"(xres), "+v"(fina));
      RBAR();
      STAMP(7);
      if (qq < 9) {
        SLP(DLY_H5);
        v4u d[4];
#pragma unroll
        for (int k = 0; k < 4; ++k) d[k] = hl4(rws, lane * 16, h5o + k * 1024);
        unsigned pend = 15u;
#pragma unroll 1
        for (int n = 0;; ++n) {
#pragma unroll
          for (int k = 0; k < 4; ++k)
            if (pend & (1u << k)) { if (__all((strip4(d[k], tb) & 0xFFu) == 0)) pend &= ~(1u << k); }
          if (pend == 0 || n >= GSPIN) break;
#pragma unroll
          for (int k = 0; k < 4; ++k)
            if (pend & (1u << k)) d[k] = hl4(rws, lane * 16, h5o + k * 1024);
        }
        f32x4 s = (__builtin_bit_cast(f32x4, d[0]) + __builtin_bit_cast(f32x4, d[1])) + (__builtin_bit_cast(f32x4, d[2]) + __builtin_bit_cast(f32x4, d[3]));
#pragma unroll
        for (int r = 0; r < 4; ++r) s[r] += dppz<0x128>(s[r]);
        const auto x01 = __builtin_amdgcn_permlane16_swap(__float_as_uint(s[0]), __float_as_uint(s[1]), false, false);
        const auto x23 = __builtin_amdgcn_permlane16_swap(__float_as_uint(s[2]), __float_as_uint(s[3]), false, false);
        const auto z = __builtin_amdgcn_permlane32_swap(__float_as_uint(__uint_as_float(x01[0]) + __uint_as_float(x01[1])),
                                                        __float_as_uint(__uint_as_float(x23[0]) + __uint_as_float(x23[1])), false, false);
        *(LAS float*)(L + fina) = __uint_as_float(z[0]) + __uint_as_float(z[1]);
        BAR();
        if (v == 3) {
          float y = bf2f(xres);
#pragma unroll
          for (int k = 0; k < 4; ++k) y += *(LAS float*)(L + LP_FIN + k * 128 + fe * 4);
          if (lane < 32) ((GAS u16*)out)[oidx] = (u16)pk(y, y);
          if (qq == 0 && xg == 0) st2(v2u{tlo, thi}, rws, lane == 0 ? 0 : OOB, WS_W);
        }
        STAMP(8);
      }
    }
    STAMP_OUT();
    return;
  }

  const int ab = xg >> 1;
  const int r32 = lane & 31, hh = lane >> 5;
  v2u wa_;
  if (w < 4) {
    const rsrc_t rc = mk(ckv, 4 * 4096 * 512 * 2);
    v2u wb_;
    asm volatile("buffer_load_dwordx2 %0, off, %1, 0 sc1" : "=v"(wb_) : "s"(rws) : "memory");
    {
      int vo[4];
#pragma unroll
      for (int k = 0; k < 4; ++k) vo[k] = g * 1024 + ((i16 ^ ((g << 2) | k)) * 16);
      const int so = (idx * 128 + 32 * w) * 1024;
      SLP(DLY_C);
#pragma unroll
      for (int n = 0; n < 32; ++n) {
        if (n < KC0) { if (n >= PACE_C0) VMCNT(PACE_C0 - 1); }
        else if (n >= KC1) { if (n >= KC1 + PACE_C1 - PACE_C) VMCNT(PACE_C1 - 1); }
        else if (n >= PACE_C) VMCNT(PACE_C - 1);
        dma16(rc, vo[(n >> 2) & 3], so + (n >> 2) * 4096 + (n & 3) * 256, lbase + (n & 3) * 32768 + (32 * w + 4 * (n >> 2)) * 256);
        if (n == KQA) RBAR();
      }
    }
    asm volatile("" : "+v"(wb_));
    wa_ = wb_;
  } else {
    wa_ = hl2(rws, 0, WS_W);
    if ((PT & 2) && (w == 5 || w == 6)) touch(rws, lane * 16, WS_QA + ab * 8192 + slot(2 * qq + w - 5, 1024));
    if ((PT & 4) && w >= 5) {
      if (w == 7) touch(rws, lane < 32 ? slot(8 * qq + (lane >> 2), 512) + (lane & 3) * 16 : OOB, WS_ML + xg * 64);
    }
    if (w == 4) {
      v2u xr[9];
#pragma unroll
      for (int t = 0; t < 9; ++t) xr[t] = ld2<0>(rx, g * 1152 + i16 * 8, t * 128);
      const rsrc_t rq = mk(wq, 576 * 4096 * 2);
      const int vo = 2 * g * 8192 + i16 * 16, so = 144 * jq * 8192 + hd * 256;
      v4u a[18][2];
#pragma unroll
      for (int o = 0; o < 18; ++o) { a[o][0] = ld4<NT_W>(rq, vo, so + (8 * o) * 8192); a[o][1] = ld4<NT_W>(rq, vo, so + (8 * o + 1) * 8192); }
      SCHED();
      STAMP(7);
      float s = 0.f, q = 0.f;
#pragma unroll
      for (int t = 0; t < 9; ++t) {
        *(LAS v2u*)(L + LA_QA + g * 1168 + i16 * 8 + t * 128) = xr[t];
        s = dot2(xr[t].x, 0x3F803F80u, s); q = dot2(xr[t].x, xr[t].x, q);
        s = dot2(xr[t].y, 0x3F803F80u, s); q = dot2(xr[t].y, xr[t].y, q);
      }
      FENCE();
      unsigned xp[18];
#pragma unroll
      for (int o = 0; o < 18; ++o) xp[o] = *(LAS unsigned*)(L + LA_QA + bj * 1168 + (72 * jq + 4 * o + g) * 4);
      const float mean = row_sum(s) * (1.0f / 576.0f), msq = row_sum(q) * (1.0f / 576.0f);
      const float rs0 = __builtin_amdgcn_rsqf(fmaxf(fmaf(-mean, mean, msq), 0.f) + 1e-5f);
      const float rstd = __uint_as_float(__builtin_amdgcn_ds_bpermute(bj * 64, __float_as_uint(rs0)));
      const float nb = __uint_as_float(__builtin_amdgcn_ds_bpermute(bj * 64, __float_as_uint(-mean * rs0)));
      v4u Bq[18];
#pragma unroll
      for (int o = 0; o < 18; ++o) {
        Bq[o] = bdiag(pk(fmaf(bf2f(xp[o]), rstd, nb), fmaf(__uint_as_float(xp[o] & 0xFFFF0000u), rstd, nb)), sel);
        asm volatile("" : "+v"(Bq[o]));
      }
      const u64 tg = (((u64)wa_.y << 32) | wa_.x) + 0x9E3779B97F4A7C15ull;
      unsigned ql = (unsigned)tg, q8 = (unsigned)(tg >> 32) & 0xFFu;
      asm volatile("" : "+v"(ql), "+v"(q8));
      SCHED();
      STAMP(8);
      f32x4 qx = {0.f, 0.f, 0.f, 0.f}, qy = qx;
#pragma unroll
      for (int o = 0; o < 18; ++o) {
        swap24(a[o][0], a[o][1]);
        qx = mfma16(a[o][0], Bq[o], qx);
        qy = mfma16(a[o][1], Bq[o], qy);
      }
      const int po = WS_H1 + slot(idx, 4096);
      st4(v4u{ql, genc(qx[0], q8), ql, genc(qx[1], q8)}, rws, lane * 16, po);
      st4(v4u{ql, genc(qx[2], q8), ql, genc(qx[3], q8)}, rws, lane * 16, po + 1024);
      st4(v4u{ql, genc(qy[0], q8), ql, genc(qy[1], q8)}, rws, lane * 16, po + 2048);
      st4(v4u{ql, genc(qy[2], q8), ql, genc(qy[3], q8)}, rws, lane * 16, po + 3072);
      STAMP(9);
    }
    RBAR();
  }
  MAKE_TAG(wa_);
  unsigned e4[4];
#pragma unroll
  for (int k = 0; k < 4; ++k) e4[k] = lsbtag(tlo, thi, 4 * i16 + k);
  if (w < 4) {
    v4u fa[32];
    {
      const int fr = ((r32 & 3) << 2) | ((r32 >> 2) & 3);
      const int ca = (32 * w + r32) * 256;
#define QK_READ(Q) _Pragma("unroll") for (int k = 0; k < 8; ++k) fa[(Q)*8 + k] = *(LAS v4u*)(L + (Q)*32768 + ca + (((2 * k + hh) ^ fr) * 16)); SCHED();
      VMCNT(0); QK_READ(0) QK_READ(1) QK_READ(2) QK_READ(3)
    }
    int qb[4];
#pragma unroll
    for (int k = 0; k < 4; ++k) { qb[k] = LA_QA + ((lane ^ ((2 * k + hh) & 7)) * 16); asm volatile("" : "+v"(qb[k])); }
    const int q4 = i16 >> 2, lowc = 2 * (g & 1) + (cp >> 1);
    int ta[4][2];
#pragma unroll
    for (int t = 0; t < 4; ++t)
#pragma unroll
      for (int t2 = 0; t2 < 2; ++t2) {
        ta[t][t2] = w * 32768 + (8 * t2 + 4 * hh + q4) * 256 + ((((t ^ q4) << 2) | (lowc ^ (2 * t2 + hh))) * 16) + 8 * (cp & 1);
        asm volatile("" : "+v"(ta[t][t2]));
      }
    int mxa = (r32 >> 4) * 320 + (r32 & 15) * 4, pba = LA_PB + lane * 16;
    unsigned e0 = lsbtag(tlo, thi, 4 * (r32 >> 1)), e1 = lsbtag(tlo, thi, 4 * (r32 >> 1) + 1), e2 = lsbtag(tlo, thi, 4 * (r32 >> 1) + 2), e3 = lsbtag(tlo, thi, 4 * (r32 >> 1) + 3);
    int h3v = (hh * 4 + (r32 & 1)) * 131072 + (r32 >> 1) * 16 + WS_H3 + (w * 128 + idx) * 256;
    asm volatile("" : "+v"(mxa), "+v"(pba), "+v"(e0), "+v"(e1), "+v"(e2), "+v"(e3), "+v"(h3v));
    STAMP(2);
    BAR();
    STAMP(3);
    v4u cb[32];
#define TR_READ(n) do { const v2u x0_ = __builtin_bit_cast(v2u, __builtin_amdgcn_ds_read_tr16_b64_v4i16((LAS s16x4*)(L + ta[(n) >> 3][0] + ((n) & 7) * 4096))); \
    const v2u x1_ = __builtin_bit_cast(v2u, __builtin_amdgcn_ds_read_tr16_b64_v4i16((LAS s16x4*)(L + ta[(n) >> 3][1] + ((n) & 7) * 4096)));                 \
    cb[n] = v4u{x0_.x, x0_.y, x1_.x, x1_.y}; } while (0)
    f32x16 sacc;
#pragma unroll
    for (int k = 0; k < 16; ++k) sacc[k] = 0.f;
    {
      v4u fb[32];
#pragma unroll
      for (int s = 0; s < 6; ++s) fb[s] = *(LAS v4u*)(L + qb[s & 3] + s * 1024);
      SCHED();
#pragma unroll
      for (int s = 0; s < 32; ++s) {
        if (s + 6 < 32) fb[s + 6] = *(LAS v4u*)(L + qb[(s + 6) & 3] + (s + 6) * 1024);
        if (s >= 26) TR_READ(s - 26);
        sacc = mfma32(fa[s], fb[s], sacc);
        SCHED();
      }
    }
    float mw = sacc[0];
#pragma unroll
    for (int k = 1; k < 16; ++k) mw = fmaxf(mw, sacc[k]);
    mw = x32_max(mw);
    asm volatile("s_waitcnt lgkmcnt(0)" ::: "memory");
    *(LAS float*)(L + mxa + w * 32768) = mw;
    STAMP(4);
    BAR();
    const float mx = fmaxf(fmaxf(*(LAS float*)(L + mxa), *(LAS float*)(L + mxa + 32768)), fmaxf(*(LAS float*)(L + mxa + 65536), *(LAS float*)(L + mxa + 98304)));
    float ls = 0.f;
#pragma unroll
    for (int k = 0; k < 16; ++k) { sacc[k] = __builtin_amdgcn_exp2f(sacc[k] - mx); ls += sacc[k]; }
#pragma unroll
    for (int s2 = 0; s2 < 2; ++s2)
      *(LAS v4u*)(L + pba + (2 * w + s2) * 1024) =
          v4u{pk(sacc[8 * s2], sacc[8 * s2 + 1]), pk(sacc[8 * s2 + 2], sacc[8 * s2 + 3]), pk(sacc[8 * s2 + 4], sacc[8 * s2 + 5]), pk(sacc[8 * s2 + 6], sacc[8 * s2 + 7])};
    ls = x32_sum(ls);
    *(LAS float*)(L + mxa + 640 + w * 32768) = ls;
    BAR();
    STAMP(5);
    {
      v4u pf[8];
#pragma unroll
      for (int k = 0; k < 8; ++k) pf[k] = *(LAS v4u*)(L + pba + k * 1024);
      f32x4 l4;
      if (w == 0) l4 = f32x4{*(LAS float*)(L + mxa + 640), *(LAS float*)(L + mxa + 640 + 32768), *(LAS float*)(L + mxa + 640 + 65536), *(LAS float*)(L + mxa + 640 + 98304)};
      SCHED();
      f32x16 o[4];
      unsigned p01[16], pq[16];
      const bool odd = lane & 1;
#pragma unroll
      for (int t = 0; t < 4; ++t)
#pragma unroll
        for (int k = 0; k < 16; ++k) o[t][k] = 0.f;
#pragma unroll
      for (int n = 0; n < 32; ++n) {
        if (n + 6 < 32) TR_READ(n + 6);
        o[n >> 3] = mfma32(pf[n & 7], cb[n], o[n >> 3]);
        if (n == 0 && w == 0)
          st4(v4u{tlo, genc(mx, t8), tlo, genc((l4[0] + l4[1]) + (l4[2] + l4[3]), t8)}, rws, lane < 32 ? r32 * 16 : OOB, WS_ML + slot(idx, 512));
        if (n >= 17 && n < 32 && n != 24) {
          const int j = n < 24 ? n - 17 : n - 18;
          if (j < 8) { pq[2 * j] = pk(o[0][2 * j], o[1][2 * j]); pq[2 * j + 1] = pk(o[0][2 * j + 1], o[1][2 * j + 1]); asm volatile("" : "+v"(pq[2 * j]), "+v"(pq[2 * j + 1])); }
          if (j >= 6) {
            const int k = j - 6;
            p01[2 * k] = lsbput(odd ? pq[2 * k + 1] : pq[2 * k], e0);
            p01[2 * k + 1] = __builtin_amdgcn_update_dpp(0, lsbput(odd ? pq[2 * k] : pq[2 * k + 1], e2), 0xB1, 0xf, 0xf, true);
            asm volatile("" : "+v"(p01[2 * k]), "+v"(p01[2 * k + 1]));
          }
        }
        SCHED();
      }
      STAMP(1);
#pragma unroll
      for (int k = 0; k < 8; ++k) {
        const unsigned pa = pk(o[2][2 * k], o[3][2 * k]), pb = pk(o[2][2 * k + 1], o[3][2 * k + 1]);
        st4(v4u{p01[2 * k], lsbput(odd ? pb : pa, e1), p01[2 * k + 1], (unsigned)__builtin_amdgcn_update_dpp(0, lsbput(odd ? pa : pb, e3), 0xB1, 0xf, 0xf, true)},
            rws, h3v, (2 * (k & 1) + 8 * (k >> 1)) * 131072);
      }
    }
    STAMP(6);
    RBAR();
    RBAR();
    RBAR();
  } else {
    const int v = w - 4;
    STAMP(1);
    const rsrc_t rv = mk(wuv, 512 * 4096 * 2);
    v4u uw[8];
#define TL(i) do { uw[i] = ld4<NT_W>(rv, g * 32 * 8192 + i16 * 16, (128 * jq + 8 * v + ((i) ^ (hd & 1))) * 8192 + hd * 256); SCHED(); } while (0)
    {
      v4u qa[8];
      SLP(DLY_QA);
#pragma unroll
      for (int k = 0; k < 8; ++k) qa[k] = hl4(rws, lane * 16, WS_QA + ab * 8192 + slot(8 * v + k, 1024));
      SCHED();
#pragma unroll
      for (int i = 0; i < TL0; ++i) TL(i);
#pragma unroll 1
      for (int n = 0;; ++n) {
        unsigned bad = 0;
#pragma unroll
        for (int k = 0; k < 8; ++k) bad |= strip4(qa[k], e4);
        if (n == 0 && w != 4) { STAMP(7); }
        if (__all((bad & LSBM) == 0) || n >= GSPIN) break;
        SLP(DLY_R2);
#pragma unroll
        for (int k = 0; k < 8; ++k) qa[k] = hl4(rws, lane * 16, WS_QA + ab * 8192 + slot(8 * v + k, 1024));
      }
#pragma unroll
      for (int k = 0; k < 8; ++k) *(LAS v4u*)(L + LA_QA + (lane >> 1) * 1024 + ((((lane & 1) * 32 + 8 * v + k) ^ (lane & 7)) * 16)) = qa[k];
    }
    STAMP(2);
    BAR();
#pragma unroll
    for (int i = TL0; i < TL1; ++i) TL(i);
    RBAR();
#pragma unroll
    for (int i = TL1; i < 8; ++i) TL(i);
    RBAR();
    const int e7 = lane & 7, q4 = i16 >> 2;
    int cta = LA_CTX + v * 288 + (32 * g + e7) * 2, ctr = LA_CTX + cp * 288 + g * 16;
    int h4a = ((2 * (g & 1) + (i16 & 1)) * 16 + (2 * (g >> 1) + (i16 >> 3)) * 4 + ((i16 >> 1) & 3)) * 16 + WS_H4 + v * QB + hd * 4096 + jq * 1024;
    const unsigned src = (lane & 8) ? 0x0302u : 0x0100u, s1 = (e7 & 1) ? ((src << 16) | 0x0C0Cu) : (0x0C0C0000u | src);
    unsigned s8[4];
    int uwa[4], bva[2][2];
#pragma unroll
    for (int k = 0; k < 4; ++k) {
      s8[k] = (e7 >> 1) == k ? s1 : 0x0C0C0C0Cu;
      uwa[k] = LA_UVW + (32 * v + g) * 256 + ((i16 ^ ((g << 2) | k)) * 16);
      bva[k >> 1][k & 1] = LA_UVW + (8 * g + 4 * (k & 1) + q4) * 256 + (((2 * (2 * v + (k >> 1)) + (cp >> 1)) ^ ((q4 << 2) | ((2 * g + (k & 1)) & 3))) * 16) + 8 * (cp & 1);
      asm volatile("" : "+v"(s8[k]), "+v"(uwa[k]), "+v"(bva[k >> 1][k & 1]));
    }
    asm volatile("" : "+v"(cta), "+v"(ctr), "+v"(h4a));
#define ML_LOAD() hl4(rws, slot(r32, 512), WS_ML + v * 8 * 512 + hd * 16)
#define ML_OK(V) __all((((V).x ^ tlo) | ((V).z ^ tlo) | ((((V).y ^ t8) | ((V).w ^ t8)) & 0xFFu)) == 0)
#define H3_LOADS() _Pragma("unroll") for (int k = 0; k < 8; ++k) d[k] = hl4(rws, lane * 16, WS_H3 + idx * 32768 + v * 8192 + k * 1024); SCHED()
    v4u d[8], bk[8];
    v4u ml;
    VMCNT(0);
    RBAR();
    const bool early = (unsigned)(__builtin_amdgcn_s_memrealtime() - t_start) > (unsigned)(TV(T_LATE) * 5);
    if (early) { ml = ML_LOAD(); H3_LOADS(); }
#pragma unroll
    for (int k = 0; k < 8; ++k) *(LAS v4u*)(L + uwa[k & 3] + k * 1024) = uw[k];
    BAR();
    v4u bv[2][4];
#pragma unroll
    for (int t = 0; t < 2; ++t)
#pragma unroll
      for (int ks = 0; ks < 4; ++ks) {
        const v2u x0 = __builtin_bit_cast(v2u, __builtin_amdgcn_ds_read_tr16_b64_v4i16((LAS s16x4*)(L + bva[t][0] + ks * 8192)));
        const v2u x1 = __builtin_bit_cast(v2u, __builtin_amdgcn_ds_read_tr16_b64_v4i16((LAS s16x4*)(L + bva[t][1] + ks * 8192)));
        bv[t][ks] = v4u{x0.x, x0.y, x1.x, x1.y};
      }
    SCHED();
    STAMP(4);
    {
      if (early) {
#pragma unroll 1
        for (int n = 0; n < GSPIN && !ML_OK(ml); ++n) ml = ML_LOAD();
      } else {
        SLP(DLY_M0);
        for (int n = 0;; ++n) {
          ml = ML_LOAD();
          if (ML_OK(ml) || n >= GSPIN) break;
          SLP(DLY_RM);
        }
        SLP(DLY_H3);
        H3_LOADS();
      }
      STAMP(5);
      const float m = gdec(ml.y), M = x16_max(row_max(m));
      const float e = __builtin_amdgcn_exp2f(m - M);
      const float al = e * LSB_COMP * __builtin_amdgcn_rcpf(x16_sum(row_sum(e * gdec(ml.w))));
      const unsigned ah = pk(al, al) & 0xFFFFu;
      const unsigned hl = ah | (pk(al - bf2f(ah), 0.f) << 16);
#pragma unroll
      for (int k = 0; k < 8; ++k) { bk[k] = bdiag(__builtin_amdgcn_ds_bpermute((4 * k + g) * 4, hl), s8); asm volatile("" : "+v"(bk[k])); }
      STAMP(3);
      f32x4 c;
#pragma unroll 1
      for (int n = 0;; ++n) {
        unsigned bad = 0;
        c = f32x4{0.f, 0.f, 0.f, 0.f};
#pragma unroll
        for (int k = 0; k < 8; ++k) { bad |= strip4(d[k], e4); c = mfma16(d[k], bk[k], c); }
        if (__all((bad & LSBM) == 0) || n >= GSPIN) break;
#pragma unroll
        for (int k = 0; k < 8; ++k) d[k] = hl4(rws, lane * 16, WS_H3 + idx * 32768 + v * 8192 + k * 1024);
      }
      STAMP(6);
#pragma unroll
      for (int r = 0; r < 4; ++r) {
        const float y = c[r] + dppz<0x128>(c[r]);
        *(LAS u16*)(L + cta + 16 * r) = (u16)pk(y, y);
      }
    }
    BAR();
    {
      v4u af[4];
#pragma unroll
      for (int ks = 0; ks < 4; ++ks) af[ks] = *(LAS v4u*)(L + ctr + 64 * ks);
      SCHED();
      f32x4 c0 = {0.f, 0.f, 0.f, 0.f}, c1 = c0;
#pragma unroll
      for (int ks = 0; ks < 4; ++ks) { c0 = mfma16(af[ks], bv[0][ks], c0); c1 = mfma16(af[ks], bv[1][ks], c1); }
      const f32x4 ct = g < 2 ? c0 : c1;
      const float x0 = (g & 1) ? ct[2] : ct[0], x1 = (g & 1) ? ct[3] : ct[1];
      const float n0 = dppz<0xB1>(x0), n1 = dppz<0xB1>(x1);
      st4(v4u{tlo, genc((lane & 1) ? n1 : x0, t8), tlo, genc((lane & 1) ? x1 : n0, t8)}, rws, h4a, 0);
    }
    STAMP(11);
  }
  STAMP_OUT();
}

extern "C" __global__ void fused_decode_end() {}

extern "C" void kernel_launch(void* const* d_in, const int* in_sizes, int n_in, void* d_out, int out_size, void* d_ws, size_t ws_size,
                              hipStream_t stream) {
  (void)hipFuncSetAttribute((const void*)fused_decode, hipFuncAttributeMaxDynamicSharedMemorySize, LDS_BYTES);
  fused_decode<<<256, 512, LDS_BYTES, stream>>>((const u16*)d_in[0], (const u16*)d_in[1], (const u16*)d_in[2], (const u16*)d_in[3],
                                                (const u16*)d_in[4], (const u16*)d_in[5], (const u16*)d_in[7], (const u16*)d_in[8],
                                                (const u16*)d_in[9], (u16*)d_out, (char*)d_ws TUNE_ARG);
}
```

```cpp
#include <hip/hip_runtime.h>
#include <stdint.h>

typedef unsigned short u16;
typedef unsigned long long u64;
typedef unsigned v4u __attribute__((ext_vector_type(4)));
typedef unsigned v2u __attribute__((ext_vector_type(2)));
typedef float f32x2 __attribute__((ext_vector_type(2)));
typedef float f32x4 __attribute__((ext_vector_type(4)));
typedef float f32x16 __attribute__((ext_vector_type(16)));
typedef short s16x4 __attribute__((ext_vector_type(4)));
typedef __bf16 bf16x8 __attribute__((ext_vector_type(8)));
typedef __bf16 bf16x2 __attribute__((ext_vector_type(2)));
typedef __amdgpu_buffer_rsrc_t rsrc_t;
#define GAS __attribute__((address_space(1)))
#define LAS __attribute__((address_space(3)))
#define DI __device__ __forceinline__

#define NT_W 2
#define NT_WO 0
#define PACE_C 7
#define DLY_H1 2
#define KQA 31
#define PACE_C0 3
#define KC0 0
#define DLY_K 16
#define PACE_G 4
#define PACE_O 8
#define DLY_R1 7
#define DLY_C 40
#define DLY_Q 0
#define DLY_F 70
#define DLY_QA 8
#define ABL 0
#define KWO 16
#define PACE_T 4
#define PACE_K 4
#define PACE_G0 2
#define KG0 0
#define PACE_W2 6
#define W2_A 6
#define TL0 0
#define TL1 4
#define TL2 12
#define TL3 24
#define PFW 1
#define DLY_PF 24
#define PACE_PF 4
#define DLY_R4 2
#define DLY_R7 2
#define DLY_RM 4
#define MLFIRST 0
#define DLY_W2 1
#define DLY_M0 12
#define H4HINT 0
#define DLY_H4 12
#define PFWAVE 7
#define KC1 32
#define PACE_C1 12
#define WO_E 10
#define PT 9
#define T_LATE 400
#define T_H7 183
#define ROT 0
#define UKX 1
#define CLS 131072
#define WM (CLS / 131072)
#define DLY_R2 12
#define UK0 6
#define WO_A 9
#define WO_B 13
#define WO_C 12
#define DLY_H3 1
#define DLY_H7 22
#define DLY_H5 18

#define SLP(N) do { if (N) __builtin_amdgcn_s_sleep(N); } while (0)
#define TV(N) (N)
#define TUNE_PARAM
#define TUNE_ARG

constexpr int SC1 = 16;
constexpr int GSPIN = 1 << 13;

constexpr int QB = 131072, WIN = 16 * QB;
constexpr int WS_H1 = 0;
constexpr int WS_H4 = 32768;
constexpr int WS_QA = 65536;
constexpr int WS_ML = 73728;
constexpr int WS_W = 81920;
constexpr int WS_H7 = WIN;
constexpr int WS_H5 = 2 * WIN;
constexpr int WS_H3 = 3 * WIN;
DI constexpr int slot(int p, int size) { return (p & 15) * QB + (p >> 4) * size; }
DI constexpr int blk5(int n) { return (n & 1) * 8 * QB + (n >> 1) * 16384; }
constexpr int WS_END = WS_H3 + 128 * 32768;
constexpr int WS_STAMP = WS_END;
constexpr int OOB = (int)0x80000000;

constexpr int LP_TAG = 4992, LP_XP = 0, LP_XD = 5120, LP_QB = 15360, LP_QO = 16896, LP_RED = 18432, LP_HP = 34816, LP_B8 = 35840, LP_FIN = 39936, LP_UK = 40960;
constexpr int LA_QA = 131072, LA_MX = LA_QA;
constexpr int LA_PB = LA_QA + 1024, LA_SUM = LA_QA + 512, LA_CTX = LA_QA + 9216, LA_CT2 = LA_QA + 11264, LA_B8 = LA_QA + 16384,
              LA_FIN = LA_QA + 20480, LA_UVW = 0;
constexpr int LDS_BYTES = 163840;

DI rsrc_t mk(const void* p, int bytes) { return __builtin_amdgcn_make_buffer_rsrc((void*)p, (short)0, bytes, 0x00020000); }
template <int AUX> DI v4u ld4(rsrc_t r, int vo, int so) { return __builtin_amdgcn_raw_buffer_load_b128(r, vo, so, AUX); }
template <int AUX> DI v2u ld2(rsrc_t r, int vo, int so) { return __builtin_amdgcn_raw_buffer_load_b64(r, vo, so, AUX); }
DI v4u hl4(rsrc_t r, int vo, int so) { asm volatile("" : "+v"(vo)); return __builtin_amdgcn_raw_buffer_load_b128(r, vo, so, SC1); }
DI v2u hl2(rsrc_t r, int vo, int so) { asm volatile("" : "+v"(vo)); return __builtin_amdgcn_raw_buffer_load_b64(r, vo, so, SC1); }
DI void touch(rsrc_t r, int vo, int so) { v4u t; asm volatile("buffer_load_dwordx4 %0, %1, %2, %3 offen sc1\n\ts_waitcnt vmcnt(0)" : "=&v"(t) : "v"(vo), "s"(r), "s"(so) : "memory"); }
DI void st4(v4u v, rsrc_t r, int vo, int so) { __builtin_amdgcn_raw_buffer_store_b128(v, r, vo + so, 0, SC1); }
DI void st2(v2u v, rsrc_t r, int vo, int so) { __builtin_amdgcn_raw_buffer_store_b64(v, r, vo, so, SC1); }
DI void dma16(rsrc_t r, int vo, int so, unsigned lds) {
  asm volatile("s_mov_b32 m0, %0\n\ts_nop 0\n\tbuffer_load_dwordx4 %1, %2, %3 offen nt lds" ::"s"(lds), "v"(vo), "s"(r), "s"(so) : "memory");
}
#define VMCNT(n) asm volatile("s_waitcnt vmcnt(%0)" ::"n"(n) : "memory")
#define FENCE() asm volatile("" ::: "memory")
#define SCHED() __builtin_amdgcn_sched_barrier(0)

DI float bf2f(unsigned v) { return __uint_as_float(v << 16); }
DI unsigned pk(float a, float b) { f32x2 f = {a, b}; return __builtin_bit_cast(unsigned, __builtin_convertvector(f, bf16x2)); }
DI f32x4 mfma16(v4u a, v4u b, f32x4 c) {
  return __builtin_amdgcn_mfma_f32_16x16x32_bf16(__builtin_bit_cast(bf16x8, a), __builtin_bit_cast(bf16x8, b), c, 0, 0, 0);
}
DI f32x4 mfma16k16(v2u a, v2u b, f32x4 c) {
  return __builtin_amdgcn_mfma_f32_16x16x16bf16_1k(__builtin_bit_cast(s16x4, a), __builtin_bit_cast(s16x4, b), c, 0, 0, 0);
}
DI f32x16 mfma32(v4u a, v4u b, f32x16 c) {
  return __builtin_amdgcn_mfma_f32_32x32x16_bf16(__builtin_bit_cast(bf16x8, a), __builtin_bit_cast(bf16x8, b), c, 0, 0, 0);
}
template <int CTRL, int RM> DI float dppf(float old, float v) {
  return __builtin_bit_cast(float, __builtin_amdgcn_update_dpp(__builtin_bit_cast(int, old), __builtin_bit_cast(int, v), CTRL, RM, 0xf, false));
}
template <int CTRL> DI float dppz(float v) {
  return __builtin_bit_cast(float, __builtin_amdgcn_update_dpp(0, __builtin_bit_cast(int, v), CTRL, 0xf, 0xf, true));
}
DI float row_sum(float v) {
  v += dppz<0xB1>(v); v += dppz<0x4E>(v); v += dppz<0x124>(v); v += dppz<0x128>(v);
  return v;
}
DI float dot2(unsigned a, unsigned b, float c) {
  return __builtin_amdgcn_fdot2_f32_bf16(__builtin_bit_cast(bf16x2, a), __builtin_bit_cast(bf16x2, b), c, false);
}
DI float row_max(float v) {
  v = fmaxf(v, dppf<0xB1, 0xf>(v, v)); v = fmaxf(v, dppf<0x4E, 0xf>(v, v));
  v = fmaxf(v, dppf<0x124, 0xf>(v, v)); v = fmaxf(v, dppf<0x128, 0xf>(v, v));
  return v;
}
DI float x16_sum(float v) { auto r = __builtin_amdgcn_permlane16_swap(__float_as_uint(v), __float_as_uint(v), false, false); return __uint_as_float(r[0]) + __uint_as_float(r[1]); }
DI float x32_sum(float v) { auto r = __builtin_amdgcn_permlane32_swap(__float_as_uint(v), __float_as_uint(v), false, false); return __uint_as_float(r[0]) + __uint_as_float(r[1]); }
DI float x16_max(float v) { auto r = __builtin_amdgcn_permlane16_swap(__float_as_uint(v), __float_as_uint(v), false, false); return fmaxf(__uint_as_float(r[0]), __uint_as_float(r[1])); }
DI float x32_max(float v) { auto r = __builtin_amdgcn_permlane32_swap(__float_as_uint(v), __float_as_uint(v), false, false); return fmaxf(__uint_as_float(r[0]), __uint_as_float(r[1])); }

#define RBAR() asm volatile("s_barrier" ::: "memory")
#define BAR() asm volatile("s_waitcnt lgkmcnt(0)\n\ts_barrier" ::: "memory")
DI unsigned genc(float v, unsigned t8) { return ((__float_as_uint(v) + 0x80u) & 0xFFFFFF00u) | t8; }
DI float gdec(unsigned hi) { return __uint_as_float(hi & 0xFFFFFF00u); }

#define STAMP(k)
#define STAMP_DECL
#define STAMP_OUT()

DI v4u bdiag(unsigned P, const unsigned (&s)[4]) {
  v4u b;
  b.x = __builtin_amdgcn_perm(P, P, s[0]); b.y = __builtin_amdgcn_perm(P, P, s[1]);
  b.z = __builtin_amdgcn_perm(P, P, s[2]); b.w = __builtin_amdgcn_perm(P, P, s[3]);
  return b;
}
DI void bdiag_sel(int cp, unsigned (&s)[4]) {
  const unsigned lo = (cp & 1) ? 0x01000C0Cu : 0x0C0C0100u, hi = (cp & 1) ? 0x03020C0Cu : 0x0C0C0302u, z = 0x0C0C0C0Cu;
  s[0] = cp < 2 ? lo : z; s[1] = cp < 2 ? z : lo; s[2] = cp < 2 ? hi : z; s[3] = cp < 2 ? z : hi;
}

DI unsigned lsbtag(unsigned tlo, unsigned thi, int p) { return (((p < 32 ? tlo : thi) >> (p & 31)) & 1u) | (((tlo ^ (unsigned)p) & 1u) << 16); }
constexpr unsigned LSBM = 0x00010001u;
DI unsigned lsbput(unsigned v, unsigned e) { return (v & ~LSBM) | e; }
DI unsigned strip4(v4u& d, const unsigned (&e)[4]) { d.x ^= e[0]; d.y ^= e[1]; d.z ^= e[2]; d.w ^= e[3]; return (d.x | d.y) | (d.z | d.w); }
DI void swap24(v4u& a, v4u& b) {
  unsigned az = a.z, aw = a.w, bx = b.x, by = b.y;
  asm volatile("v_swap_b32 %0, %2\n\tv_swap_b32 %1, %3\n\ts_nop 1" : "+v"(az), "+v"(aw), "+v"(bx), "+v"(by));
  a.z = az; a.w = aw; b.x = bx; b.y = by;
}
constexpr float LSB_COMP = 1.0028f;
DI v4u tag8(f32x4 c, const unsigned (&t)[4]) {
  return v4u{(__float_as_uint(c[0]) & 0xFFFFFF00u) | t[0], (__float_as_uint(c[1]) & 0xFFFFFF00u) | t[1],
             (__float_as_uint(c[2]) & 0xFFFFFF00u) | t[2], (__float_as_uint(c[3]) & 0xFFFFFF00u) | t[3]};
}
extern "C" __global__ void __launch_bounds__(512, 2)
fused_decode(const u16* __restrict__ xin, const u16* __restrict__ ckv, const u16* __restrict__ wq, const u16* __restrict__ wuk,
             const u16* __restrict__ wuv, const u16* __restrict__ wo, const u16* __restrict__ w1, const u16* __restrict__ w3,
             const u16* __restrict__ w2, u16* __restrict__ out, char* ws TUNE_PARAM) {
  extern __shared__ __attribute__((aligned(16))) char smem[];
  LAS char* L = (LAS char*)smem;
  const unsigned lbase = (unsigned)(size_t)smem;
  const int lane = threadIdx.x & 63, w = __builtin_amdgcn_readfirstlane(threadIdx.x >> 6);
  const int xg = blockIdx.x & 7, qd_ = blockIdx.x >> 3, qq = qd_ & 15, idx = xg * 16 + qq, hd = idx >> 2, jq = idx & 3;
  const int i16 = lane & 15, g = lane >> 4, bj = i16 >> 2, cp = lane & 3;
  STAMP_DECL;
  STAMP(0);
  if (w == PFWAVE) {
    u64 pc; unsigned back, len;
    asm volatile("s_getpc_b64 %0\n.Lpf_here:\n\ts_mov_b32 %1, .Lpf_here-fused_decode\n\ts_mov_b32 %2, fused_decode_end-fused_decode" : "=s"(pc), "=s"(back), "=s"(len));
    const rsrc_t rcode = mk((const void*)(pc - back), (int)min(len, 32768u));
    v4u dummy;
    asm volatile("s_nop 4\n\tbuffer_load_dwordx4 %0, %1, %2, 0 offen\n\ts_waitcnt vmcnt(0)" : "=&v"(dummy) : "v"(qd_ * 1024 + lane * 16), "s"(rcode) : "memory");
  }
  asm volatile("" ::"s"(xin), "s"(ckv), "s"(wq), "s"(wuk), "s"(wuv), "s"(wo), "s"(w1), "s"(w3), "s"(w2), "s"(out), "s"(ws));
  u64 t_start;
  asm volatile("s_memrealtime %0\n\ts_waitcnt lgkmcnt(0)" : "=s"(t_start) : "s"(ws) : "memory");
  const rsrc_t rws = mk(ws, WS_END);
  const rsrc_t rx = mk(xin, 4 * 576 * 2);
  unsigned sel[4];
  bdiag_sel(cp, sel);
#define MAKE_TAG(W) const u64 tag_ = (((u64)(W).y << 32) | (W).x) + 0x9E3779B97F4A7C15ull;  \
  const unsigned tlo = (unsigned)tag_, thi = (unsigned)(tag_ >> 32), t8 = thi & 0xFFu;        \
  const unsigned tb[4] = {tlo & 0xFFu, (tlo >> 8) & 0xFFu, (tlo >> 16) & 0xFFu, tlo >> 24}
  const int vo5 = (lane >> 3) * QB + (lane & 7) * 16;

  if (qd_ >= 16) {
    const int so5 = WS_H5 + idx * 128;
    if (w < 4) {
      v4u ka[2][4];
      v2u wv_;
      asm volatile("buffer_load_dwordx2 %0, off, %1, %2 sc1" : "=v"(wv_) : "s"(rws), "s"(WS_W) : "memory");
      {
        const rsrc_t rk = mk(wuk, 512 * 4096 * 2);
        const int ukx = UKX * 4 * (hd & 1);
        SLP(DLY_K);
#pragma unroll
        for (int n = 0; n < 8; ++n) {
          if (n >= PACE_K) VMCNT(PACE_K - 1);
          dma16(rk, g * 8192 + ((i16 ^ ((g << 2) | (n & 3))) * 16), (128 * jq + 32 * w + 4 * (n ^ ukx)) * 8192 + hd * 256, lbase + LP_UK + (8 * w + (n ^ ukx)) * 1024);
        }
        VMCNT(0);
        const int fr = ((i16 & 3) << 2) | (i16 >> 2);
#pragma unroll
        for (int t = 0; t < 2; ++t)
#pragma unroll
          for (int s = 0; s < 4; ++s) {
            ka[t][s] = *(LAS v4u*)(L + LP_UK + (32 * w + 16 * t + i16) * 256 + (((4 * s + g) ^ fr) * 16));
            asm volatile("" : "+v"(ka[t][s]));
          }
      }
      STAMP(1);
      asm volatile("" : "+v"(wv_));
      MAKE_TAG(wv_);
      unsigned eq[4];
#pragma unroll
      for (int k = 0; k < 4; ++k) { eq[k] = lsbtag(tlo, thi, 4 * i16 + k); asm volatile("" : "+v"(eq[k])); }
      SLP(DLY_H1);
      STAMP(2);
      {
        const int blk = w & 1, half = w >> 1;
        float q0, q1;
        for (int n = 0;; ++n) {
          v4u p[4];
#pragma unroll
          for (int jj = 0; jj < 4; ++jj) p[jj] = hl4(rws, lane * 16, WS_H1 + slot(hd * 4 + jj, 4096) + (blk * 2 + half) * 1024);
          unsigned bad = 0;
          q0 = 0.f; q1 = 0.f;
#pragma unroll
          for (int jj = 0; jj < 4; ++jj) {
            bad |= (p[jj].x ^ tlo) | (p[jj].z ^ tlo) | (((p[jj].y ^ t8) | (p[jj].w ^ t8)) & 0xFFu);
            q0 += gdec(p[jj].y); q1 += gdec(p[jj].w);
          }
          if (__all(bad == 0) || n >= GSPIN) break;
          SLP(DLY_R1);
        }
        const unsigned qp = pk(q0, q1);
        const int d0 = 32 * g + 16 * half + 4 * blk + cp;
        *(LAS u16*)(L + LP_QB + bj * 320 + d0 * 2) = (u16)qp;
        *(LAS u16*)(L + LP_QB + bj * 320 + (d0 + 8) * 2) = (u16)(qp >> 16);
      }
      STAMP(4);
      BAR();
      STAMP(5);
      {
        v4u qf[4];
#pragma unroll
        for (int s = 0; s < 4; ++s) qf[s] = *(LAS v4u*)(L + LP_QB + cp * 320 + s * 64 + g * 16);
        f32x4 c0 = {0.f, 0.f, 0.f, 0.f}, c1 = c0;
#pragma unroll
        for (int s = 0; s < 4; ++s) { c0 = mfma16(ka[0][s], qf[s], c0); c1 = mfma16(ka[1][s], qf[s], c1); }
        const float sc = 0.08838834764831845f * 1.4426950408889634f * LSB_COMP;
        if (i16 < 4) {
          *(LAS v2u*)(L + LP_QO + i16 * 256 + (32 * w + 4 * g) * 2) = v2u{pk(c0[0] * sc, c0[1] * sc), pk(c0[2] * sc, c0[3] * sc)};
          *(LAS v2u*)(L + LP_QO + i16 * 256 + (32 * w + 16 + 4 * g) * 2) = v2u{pk(c1[0] * sc, c1[1] * sc), pk(c1[2] * sc, c1[3] * sc)};
        }
      }
      BAR();
      if (w == 0) {
        const v4u v = *(LAS v4u*)(L + LP_QO + lane * 16);
        st4(v4u{lsbput(v.x, eq[0]), lsbput(v.y, eq[1]), lsbput(v.z, eq[2]), lsbput(v.w, eq[3])}, rws, g * 2048 + i16 * 16, WS_QA + slot(hd, 1024) + jq * 256);
      }
      STAMP(6);
      BAR();
      const rsrc_t r2 = mk(w2, 4608 * 576 * 2);
      const int so2 = (xg * 576 + 36 * qq) * 1152;
      const rsrc_t ro = mk(wo, 4096 * 576 * 2);
      const int soo = (hd * 128 + 32 * jq) * 1152;
      v4u a2[9][2];
      v2u a8[3][2];
      {
        const int vo4 = 2 * g * 1152 + i16 * 16, vo4m = g < 2 ? vo4 : OOB;
        const int vo8 = 2 * g * 1152 + i16 * 8 + 1024, vo8m = (g < 2 && w == 0) ? vo8 : OOB;
#pragma unroll
        for (int n = 0; n < 14 + WO_E; ++n) {
          if (n >= PACE_W2) VMCNT(PACE_W2 - 1);
          if (n == W2_A) { SCHED(); RBAR(); SLP(DLY_W2); }
          const int o = n >> 1, r = n & 1;
          if (n < 10) a2[o][r] = ld4<NT_W>(r2, o < 4 ? vo4 : vo4m, so2 + (8 * o + r) * 1152 + 256 * w);
          else if (n < 12) a8[0][r] = ld2<NT_W>(r2, vo8, so2 + (8 * w + r) * 1152);
          else if (n < 14) a8[1][r] = ld2<NT_W>(r2, vo8m, so2 + (32 + r) * 1152);
          else if (n < 22) a2[o - 2][r] = ld4<NT_W>(ro, vo4, soo + (8 * (o - 7) + r) * 1152 + 256 * w);
          else a8[2][r] = ld2<NT_W>(ro, vo8, soo + (8 * w + r) * 1152);
          SCHED();
        }
      }
      STAMP(7);
#pragma unroll
      for (int o = 0; o < 5; ++o) swap24(a2[o][0], a2[o][1]);
      STAMP(8);
      RBAR();
      STAMP(9);
      {
        const int vo4 = 2 * g * 1152 + i16 * 16, vo8 = 2 * g * 1152 + i16 * 8 + 1024;
#pragma unroll
        for (int n = 14 + WO_E; n < 24; ++n) {
          const int o = n >> 1, r = n & 1;
          if (n < 22) a2[o - 2][r] = ld4<NT_W>(ro, vo4, soo + (8 * (o - 7) + r) * 1152 + 256 * w);
          else a8[2][r] = ld2<NT_W>(ro, vo8, soo + (8 * w + r) * 1152);
        }
        SCHED();
        const v4u h4 = *(LAS v4u*)(L + LP_HP + (bj * 4 + g) * 48);
        const unsigned h5 = *(LAS unsigned*)(L + LP_HP + (bj * 4 + g) * 48 + 16);
        const unsigned hw = *(LAS unsigned*)(L + LP_HP + (bj * 4 + g) * 48 + 4 * w);
        f32x4 c8 = {0.f, 0.f, 0.f, 0.f}, cx = c8, cy = c8;
        c8 = mfma16(v4u{a8[0][0].x, a8[0][0].y, a8[0][1].x, a8[0][1].y}, bdiag(hw, sel), c8);
        c8 = mfma16(v4u{a8[1][0].x, a8[1][0].y, a8[1][1].x, a8[1][1].y}, bdiag(h5, sel), c8);
#pragma unroll
        for (int o = 0; o < 5; ++o) {
          const v4u B = bdiag(o == 0 ? h4.x : o == 1 ? h4.y : o == 2 ? h4.z : o == 3 ? h4.w : h5, sel);
          cx = mfma16(a2[o][0], B, cx);
          cy = mfma16(a2[o][1], B, cy);
        }
#pragma unroll
        for (int o = 5; o < 9; ++o) swap24(a2[o][0], a2[o][1]);
        asm volatile("" : "+v"(cx), "+v"(cy), "+v"(c8));
        int bpa = (bj * 16 + g) * 4;
        asm volatile("" : "+v"(bpa));
        STAMP(10);
        float y0, y1;
        for (int n = 0;; ++n) {
          v4u p[4];
#pragma unroll
          for (int jj = 0; jj < 4; ++jj) p[jj] = hl4(rws, lane * 16, WS_H4 + slot(idx, 4096) + jj * 1024);
          unsigned bad = 0;
          y0 = 0.f; y1 = 0.f;
#pragma unroll
          for (int jj = 0; jj < 4; ++jj) {
            bad |= (p[jj].x ^ tlo) | (p[jj].z ^ tlo) | (((p[jj].y ^ t8) | (p[jj].w ^ t8)) & 0xFFu);
            y0 += gdec(p[jj].y); y1 += gdec(p[jj].w);
          }
          if (__all(bad == 0) || n >= GSPIN) break;
          SLP(DLY_R4);
        }
        STAMP(11);
        const unsigned yp = pk(y0, y1);
        const unsigned pw = __builtin_amdgcn_ds_bpermute(bpa + 16 * w, yp);
        unsigned po[4];
#pragma unroll
        for (int o = 0; o < 4; ++o) po[o] = __builtin_amdgcn_ds_bpermute(bpa + 16 * o, yp);
        c8 = mfma16(v4u{a8[2][0].x, a8[2][0].y, a8[2][1].x, a8[2][1].y}, bdiag(pw, sel), c8);
#pragma unroll
        for (int o = 0; o < 4; ++o) {
          const v4u B = bdiag(po[o], sel);
          cx = mfma16(a2[5 + o][0], B, cx);
          cy = mfma16(a2[5 + o][1], B, cy);
        }
        *(LAS f32x4*)(L + LP_B8 + w * 1024 + lane * 16) = c8;
        st4(tag8(cx, tb), rws, vo5, so5 + blk5(2 * w));
        st4(tag8(cy, tb), rws, vo5, so5 + blk5(2 * w + 1));
        BAR();
        if (w == 0) {
          f32x4 s = *(LAS f32x4*)(L + LP_B8 + lane * 16);
#pragma unroll
          for (int k = 1; k < 4; ++k) s += *(LAS f32x4*)(L + LP_B8 + k * 1024 + lane * 16);
          st4(tag8(s, tb), rws, vo5, so5 + blk5(8));
        }
      }
      STAMP(3);
      if (qq < 9) RBAR();
    } else {
      const int v = w - 4;
      unsigned xn[8];
      const v2u wv_ = hl2(rws, 0, WS_W);
      v4u pt1;
      if (PT & 1) asm volatile("buffer_load_dwordx4 %0, %1, %2, %3 offen sc1" : "=v"(pt1) : "v"(lane * 16), "s"(rws), "s"(WS_H1 + slot(idx, 4096) + v * 1024) : "memory");
      {
        const v4u x0 = ld4<0>(rx, lane * 16, v * 1152), x1 = ld4<0>(rx, lane < 8 ? lane * 16 : OOB, v * 1152 + 1024);
        float s = 0.f, q = 0.f;
#pragma unroll
        for (int k = 0; k < 4; ++k) {
          s = dot2(x0[k], 0x3F803F80u, s); q = dot2(x0[k], x0[k], q);
          s = dot2(x1[k], 0x3F803F80u, s); q = dot2(x1[k], x1[k], q);
        }
        const float mean = x32_sum(x16_sum(row_sum(s))) * (1.0f / 576.0f);
        const float msq = x32_sum(x16_sum(row_sum(q))) * (1.0f / 576.0f);
        const float rstd = __builtin_amdgcn_rsqf(fmaxf(fmaf(-mean, mean, msq), 0.f) + 1e-5f), nb = -mean * rstd;
#pragma unroll
        for (int k = 0; k < 4; ++k) {
          xn[k] = pk(fmaf(bf2f(x0[k]), rstd, nb), fmaf(__uint_as_float(x0[k] & 0xFFFF0000u), rstd, nb));
        }
#pragma unroll
        for (int k = 0; k < 4; ++k) xn[4 + k] = pk(fmaf(bf2f(x1[k]), rstd, nb), fmaf(__uint_as_float(x1[k] & 0xFFFF0000u), rstd, nb));
      }
      if (PT & 1) { VMCNT(0); asm volatile("" : "+v"(pt1)); }
#pragma unroll
      for (int hf = 0; hf < 2; ++hf) {
        if (hf == 0 || lane < 8) {
#pragma unroll
          for (int k = 0; k < 4; ++k) {
            const int o = lane + 64 * hf;
            *(LAS unsigned*)(L + LP_XD + ((v * 4 + ((2 * k) & 3)) * 148 + 2 * o + (k >> 1)) * 4) = __builtin_amdgcn_perm(0u, xn[4 * hf + k], 0x01000100u);
            *(LAS unsigned*)(L + LP_XD + ((v * 4 + ((2 * k + 1) & 3)) * 148 + 2 * o + (k >> 1)) * 4) = __builtin_amdgcn_perm(0u, xn[4 * hf + k], 0x03020302u);
          }
        }
      }
      STAMP(1);
      if (PT & 8) touch(rws, lane * 16, WS_H4 + slot(idx, 4096) + v * 1024);
      MAKE_TAG(wv_);
      const int U0 = (81 * qq) / 144, Qb = 144 * (U0 + 1);
#define QOF(j) (81 * qq + ((j) + ROT * xg >= 81 ? (j) + ROT * xg - 81 : (j) + ROT * xg))
      unsigned xd[21];
      const GAS char* wb = (const GAS char*)(i16 < 8 ? w1 : w3) + (xg * 576 * 2 + (i16 & 7) * 16 + g * 9216);
      const unsigned m0 = cp == 0 ? 0xFFFFu : cp == 1 ? 0xFFFF0000u : 0u, m1 = cp == 2 ? 0xFFFFu : cp == 3 ? 0xFFFF0000u : 0u;
      f32x4 acc[4];
#pragma unroll
      for (int k = 0; k < 4; ++k) acc[k] = f32x4{0.f, 0.f, 0.f, 0.f};
      v4u a[21];
        int off[2][3];
        unsigned use[2][3];
        int hpa[2];
#pragma unroll
        for (int e = 0; e < 2; ++e) {
          const int t = ((v - 2) & 1) * 64 + lane + 128 * e;
          const int cl = t >> 2, b = t & 3, f = 36 * qq + cl, u = f >> 6;
          const int r0 = (144 * u) / 81, r1 = (144 * u + 143) / 81;
#pragma unroll
          for (int k = 0; k < 3; ++k) {
            const int rr = r0 + k;
            const bool on = t < 144 && rr <= r1;
            off[e][k] = on ? slot(xg * 16 + rr, 8192) + (u - (81 * rr) / 144) * 4096 + (f & 63) * 64 + b * 16 : OOB;
            use[e][k] = on ? 0xFFFFFFFFu : 0u;
          }
          hpa[e] = LP_HP + ((b * 4 + ((cl >> 1) & 3)) * 12 + (cl >> 3)) * 4 + (cl & 1) * 2;
          asm volatile("" : "+v"(hpa[e]), "+v"(off[e][0]), "+v"(off[e][1]), "+v"(off[e][2]), "+v"(use[e][0]), "+v"(use[e][1]), "+v"(use[e][2]));
        }
      int pa = WS_H7 + slot(idx, 8192) + v * 4096 + (32 * ((lane >> 4) & 1) + 16 * (lane >> 5) + cp) * 64 + bj * 16;
      asm volatile("" : "+v"(pa));
      BAR();
      RBAR();
#pragma unroll
      for (int t2 = 0; t2 < 21; ++t2) {
        const int Q = QOF(min(4 * t2 + v, 80)), qk = Q >= Qb ? Q - Qb : Q - 144 * U0;
        xd[t2] = *(LAS unsigned*)(L + LP_XD + ((bj * 4 + g) * 148 + qk) * 4);
        asm volatile("" : "+v"(xd[t2]));
      }
      if ((PT & 16) && v >= 2) {
        v4u t[6];
#pragma unroll
        for (int k = 0; k < 6; ++k) t[k] = hl4(rws, off[k / 3][k % 3], WS_H7);
#pragma unroll
        for (int k = 0; k < 6; ++k) asm volatile("" ::"v"(t[k]));
      }
      if ((PT & 32) && v < 2 && qq < 9) {
        v4u t[8];
#pragma unroll
        for (int k = 0; k < 8; ++k) t[k] = hl4(rws, lane * 16, WS_H5 + slot(qq * 8 + xg, 16384) + (v * 8 + k) * 1024);
#pragma unroll
        for (int k = 0; k < 8; ++k) asm volatile("" ::"v"(t[k]));
      }
#pragma unroll 1
      for (int n = 0; n < 4096 && (unsigned)(__builtin_amdgcn_s_memrealtime() - t_start) < (unsigned)(TV(DLY_F) * 5); ++n) __builtin_amdgcn_s_sleep(1);
      STAMP(2);
#pragma unroll
      for (int t = -PACE_G; t < 21; ++t) {
        const int tl = t + PACE_G;
        if (tl < 21) {
          const int Q = QOF(4 * tl + v), u = Q >= Qb ? U0 + 1 : U0, qk = Q - 144 * u;
          if (tl < KG0 && tl >= PACE_G0) VMCNT(PACE_G0 - 1);
          if (tl < 20 || v == 0) {
            const GAS v4u* p = (const GAS v4u*)(wb + (unsigned)(qk * 4 * 9216 + u * 128));
            a[tl] = NT_W ? __builtin_nontemporal_load(p) : *p;
          } else a[tl] = v4u{0u, 0u, 0u, 0u};
          SCHED();
        }
        if (tl == 20) { STAMP(3); BAR(); }
        if (t >= 0) {
          const int Q = QOF(min(4 * t + v, 80));
          const v2u B = {xd[t] & m0, xd[t] & m1};
          if (Q < Qb) { acc[0] = mfma16k16(v2u{a[t].x, a[t].y}, B, acc[0]); acc[1] = mfma16k16(v2u{a[t].z, a[t].w}, B, acc[1]); }
          else        { acc[2] = mfma16k16(v2u{a[t].x, a[t].y}, B, acc[2]); acc[3] = mfma16k16(v2u{a[t].z, a[t].w}, B, acc[3]); }
          SCHED();
        }
      }
#pragma unroll
      for (int k = 0; k < 4; ++k) *(LAS f32x4*)(L + LP_RED + (v * 4 + k) * 1024 + lane * 16) = acc[k];
      STAMP(4);
      BAR();
      STAMP(9);
      if (v == 0 || (v == 1 && 81 * qq + 80 >= Qb)) {
#pragma unroll
        for (int hh = 0; hh < 2; ++hh) {
          f32x4 r = *(LAS f32x4*)(L + LP_RED + (2 * v + hh) * 1024 + lane * 16);
#pragma unroll
          for (int k = 1; k < 4; ++k) r += *(LAS f32x4*)(L + LP_RED + (k * 4 + 2 * v + hh) * 1024 + lane * 16);
#pragma unroll
          for (int k = 0; k < 2; ++k) {
            const auto x = __builtin_amdgcn_permlane32_swap(__float_as_uint(r[k]), __float_as_uint(r[2 + k]), false, false);
            st4(v4u{tlo, genc(__uint_as_float(x[0]), t8), tlo, genc(__uint_as_float(x[1]), t8)}, rws, pa + (8 * k + 4 * hh) * 64, 0);
          }
        }
      } else if (v >= 2) {
        SLP(DLY_H7);
#pragma unroll 1
        for (int n = 0; n < 4096 && (unsigned)(__builtin_amdgcn_s_memrealtime() - t_start) < (unsigned)(TV(T_H7) * 5); ++n) __builtin_amdgcn_s_sleep(1);
        STAMP(5);
        float gs[2], us[2];
        for (int n = 0;; ++n) {
          v4u p[2][3];
#pragma unroll
          for (int e = 0; e < 2; ++e)
#pragma unroll
            for (int k = 0; k < 3; ++k) p[e][k] = hl4(rws, off[e][k], WS_H7);
          unsigned bad = 0;
#pragma unroll
          for (int e = 0; e < 2; ++e) {
            gs[e] = 0.f; us[e] = 0.f;
#pragma unroll
            for (int k = 0; k < 3; ++k) {
              bad |= ((p[e][k].x ^ tlo) | (p[e][k].z ^ tlo) | (((p[e][k].y ^ t8) | (p[e][k].w ^ t8)) & 0xFFu)) & use[e][k];
              gs[e] += gdec(p[e][k].y); us[e] += gdec(p[e][k].w);
            }
          }
          if (__all(bad == 0) || n >= GSPIN) break;
          SLP(DLY_R7);
        }
#pragma unroll
        for (int e = 0; e < 2; ++e) {
          const float hv = gs[e] / (1.0f + __expf(-gs[e])) * us[e];
          if ((v - 2) * 64 + lane + 128 * e < 160) *(LAS u16*)(L + hpa[e]) = (u16)pk(hv, hv);
        }
      }
      STAMP(6);
      BAR();
      const int fe = lane & 31, fL = 8 * xg + (fe >> 2), frr = fe & 3;
      const int oidx = ((fL & 15) >> 2) * 576 + (qq < 8 ? 128 * (qq >> 1) + 8 * (4 * (fL >> 4) + frr) + 4 * (qq & 1) + (fL & 3) : 512 + 4 * (4 * (fL >> 4) + frr) + (fL & 3));
      unsigned xres = 0;
      if (v == 3 && qq < 9) xres = ((const GAS u16*)xin)[oidx];
      const int h5o = WS_H5 + slot(qq * 8 + xg, 16384) + v * 4096;
      int fina = LP_FIN + v * 128 + (lane & 7) * 16 + g * 4;
      asm volatile("" : "+v"(xres), "+v"(fina));
      RBAR();
      STAMP(7);
      if (qq < 9) {
        SLP(DLY_H5);
        v4u d[4];
#pragma unroll
        for (int k = 0; k < 4; ++k) d[k] = hl4(rws, lane * 16, h5o + k * 1024);
        unsigned pend = 15u;
#pragma unroll 1
        for (int n = 0;; ++n) {
#pragma unroll
          for (int k = 0; k < 4; ++k)
            if (pend & (1u << k)) { if (__all((strip4(d[k], tb) & 0xFFu) == 0)) pend &= ~(1u << k); }
          if (pend == 0 || n >= GSPIN) break;
#pragma unroll
          for (int k = 0; k < 4; ++k)
            if (pend & (1u << k)) d[k] = hl4(rws, lane * 16, h5o + k * 1024);
        }
        f32x4 s = (__builtin_bit_cast(f32x4, d[0]) + __builtin_bit_cast(f32x4, d[1])) + (__builtin_bit_cast(f32x4, d[2]) + __builtin_bit_cast(f32x4, d[3]));
#pragma unroll
        for (int r = 0; r < 4; ++r) s[r] += dppz<0x128>(s[r]);
        const auto x01 = __builtin_amdgcn_permlane16_swap(__float_as_uint(s[0]), __float_as_uint(s[1]), false, false);
        const auto x23 = __builtin_amdgcn_permlane16_swap(__float_as_uint(s[2]), __float_as_uint(s[3]), false, false);
        const auto z = __builtin_amdgcn_permlane32_swap(__float_as_uint(__uint_as_float(x01[0]) + __uint_as_float(x01[1])),
                                                        __float_as_uint(__uint_as_float(x23[0]) + __uint_as_float(x23[1])), false, false);
        *(LAS float*)(L + fina) = __uint_as_float(z[0]) + __uint_as_float(z[1]);
        BAR();
        if (v == 3) {
          float y = bf2f(xres);
#pragma unroll
          for (int k = 0; k < 4; ++k) y += *(LAS float*)(L + LP_FIN + k * 128 + fe * 4);
          if (lane < 32) ((GAS u16*)out)[oidx] = (u16)pk(y, y);
          if (qq == 0 && xg == 0) st2(v2u{tlo, thi}, rws, lane == 0 ? 0 : OOB, WS_W);
        }
        STAMP(8);
      }
    }
    STAMP_OUT();
    return;
  }

  const int ab = xg >> 1;
  const int r32 = lane & 31, hh = lane >> 5;
  v2u wa_;
  if (w < 4) {
    const rsrc_t rc = mk(ckv, 4 * 4096 * 512 * 2);
    v2u wb_;
    asm volatile("buffer_load_dwordx2 %0, off, %1, %2 sc1" : "=v"(wb_) : "s"(rws), "s"(WS_W) : "memory");
    {
      int vo[4];
#pragma unroll
      for (int k = 0; k < 4; ++k) vo[k] = g * 1024 + ((i16 ^ ((g << 2) | k)) * 16);
      const int so = (idx * 128 + 32 * w) * 1024;
      SLP(DLY_C);
#pragma unroll
      for (int n = 0; n < 32; ++n) {
        if (n < KC0) { if (n >= PACE_C0) VMCNT(PACE_C0 - 1); }
        else if (n >= KC1) { if (n >= KC1 + PACE_C1 - PACE_C) VMCNT(PACE_C1 - 1); }
        else if (n >= PACE_C) VMCNT(PACE_C - 1);
        dma16(rc, vo[(n >> 2) & 3], so + (n >> 2) * 4096 + (n & 3) * 256, lbase + (n & 3) * 32768 + (32 * w + 4 * (n >> 2)) * 256);
        if (n == KQA) RBAR();
      }
    }
    asm volatile("" : "+v"(wb_));
    wa_ = wb_;
  } else {
    wa_ = hl2(rws, 0, WS_W);
    if ((PT & 2) && (w == 5 || w == 6)) touch(rws, lane * 16, WS_QA + ab * 2048 + slot(2 * qq + w - 5, 1024));
    if ((PT & 4) && w >= 5) {
      if (w == 7) touch(rws, lane < 32 ? slot(8 * qq + (lane >> 2), 512) + (lane & 3) * 16 : OOB, WS_ML + xg * 64);
    }
    if (w == 4) {
      v2u xr[9];
#pragma unroll
      for (int t = 0; t < 9; ++t) xr[t] = ld2<0>(rx, g * 1152 + i16 * 8, t * 128);
      const rsrc_t rq = mk(wq, 576 * 4096 * 2);
      const int vo = 2 * g * 8192 + i16 * 16, so = 8 * jq * 8192 + hd * 256;
      v4u a[18][2];
#pragma unroll
      for (int o = 0; o < 18; ++o) { a[o][0] = ld4<NT_W>(rq, vo, so + (32 * o) * 8192); a[o][1] = ld4<NT_W>(rq, vo, so + (32 * o + 1) * 8192); }
      SCHED();
      STAMP(7);
      float s = 0.f, q = 0.f;
#pragma unroll
      for (int t = 0; t < 9; ++t) {
        *(LAS v2u*)(L + LA_QA + g * 1168 + i16 * 8 + t * 128) = xr[t];
        s = dot2(xr[t].x, 0x3F803F80u, s); q = dot2(xr[t].x, xr[t].x, q);
        s = dot2(xr[t].y, 0x3F803F80u, s); q = dot2(xr[t].y, xr[t].y, q);
      }
      FENCE();
      unsigned xp[18];
#pragma unroll
      for (int o = 0; o < 18; ++o) xp[o] = *(LAS unsigned*)(L + LA_QA + bj * 1168 + (4 * jq + 16 * o + g) * 4);
      const float mean = row_sum(s) * (1.0f / 576.0f), msq = row_sum(q) * (1.0f / 576.0f);
      const float rs0 = __builtin_amdgcn_rsqf(fmaxf(fmaf(-mean, mean, msq), 0.f) + 1e-5f);
      const float rstd = __uint_as_float(__builtin_amdgcn_ds_bpermute(bj * 64, __float_as_uint(rs0)));
      const float nb = __uint_as_float(__builtin_amdgcn_ds_bpermute(bj * 64, __float_as_uint(-mean * rs0)));
      v4u Bq[18];
#pragma unroll
      for (int o = 0; o < 18; ++o) {
        Bq[o] = bdiag(pk(fmaf(bf2f(xp[o]), rstd, nb), fmaf(__uint_as_float(xp[o] & 0xFFFF0000u), rstd, nb)), sel);
        asm volatile("" : "+v"(Bq[o]));
      }
      const u64 tg = (((u64)wa_.y << 32) | wa_.x) + 0x9E3779B97F4A7C15ull;
      unsigned ql = (unsigned)tg, q8 = (unsigned)(tg >> 32) & 0xFFu;
      asm volatile("" : "+v"(ql), "+v"(q8));
      SCHED();
      STAMP(8);
      f32x4 qx = {0.f, 0.f, 0.f, 0.f}, qy = qx;
#pragma unroll
      for (int o = 0; o < 18; ++o) {
        swap24(a[o][0], a[o][1]);
        qx = mfma16(a[o][0], Bq[o], qx);
        qy = mfma16(a[o][1], Bq[o], qy);
      }
      const int po = WS_H1 + slot(idx, 4096);
      st4(v4u{ql, genc(qx[0], q8), ql, genc(qx[1], q8)}, rws, lane * 16, po);
      st4(v4u{ql, genc(qx[2], q8), ql, genc(qx[3], q8)}, rws, lane * 16, po + 1024);
      st4(v4u{ql, genc(qy[0], q8), ql, genc(qy[1], q8)}, rws, lane * 16, po + 2048);
      st4(v4u{ql, genc(qy[2], q8), ql, genc(qy[3], q8)}, rws, lane * 16, po + 3072);
      STAMP(9);
    }
    RBAR();
  }
  MAKE_TAG(wa_);
  unsigned e4[4];
#pragma unroll
  for (int k = 0; k < 4; ++k) e4[k] = lsbtag(tlo, thi, 4 * i16 + k);
  if (w < 4) {
    v4u fa[32];
    {
      const int fr = ((r32 & 3) << 2) | ((r32 >> 2) & 3);
      const int ca = (32 * w + r32) * 256;
#define QK_READ(Q) _Pragma("unroll") for (int k = 0; k < 8; ++k) fa[(Q)*8 + k] = *(LAS v4u*)(L + (Q)*32768 + ca + (((2 * k + hh) ^ fr) * 16)); SCHED();
      VMCNT(0); QK_READ(0) QK_READ(1) QK_READ(2) QK_READ(3)
    }
    int qb[4];
#pragma unroll
    for (int k = 0; k < 4; ++k) { qb[k] = LA_QA + ((lane ^ ((2 * k + hh) & 7)) * 16); asm volatile("" : "+v"(qb[k])); }
    const int q4 = i16 >> 2, lowc = 2 * (g & 1) + (cp >> 1);
    int ta[4][2];
#pragma unroll
    for (int t = 0; t < 4; ++t)
#pragma unroll
      for (int t2 = 0; t2 < 2; ++t2) {
        ta[t][t2] = w * 32768 + (8 * t2 + 4 * hh + q4) * 256 + ((((t ^ q4) << 2) | (lowc ^ (2 * t2 + hh))) * 16) + 8 * (cp & 1);
        asm volatile("" : "+v"(ta[t][t2]));
      }
    int mxa = (r32 >> 4) * 320 + (r32 & 15) * 4, pba = LA_PB + lane * 16;
    unsigned e0 = lsbtag(tlo, thi, 4 * (r32 >> 1)), e1 = lsbtag(tlo, thi, 4 * (r32 >> 1) + 1), e2 = lsbtag(tlo, thi, 4 * (r32 >> 1) + 2), e3 = lsbtag(tlo, thi, 4 * (r32 >> 1) + 3);
    const int sp = idx & 31;
    int h3v = ((hh * 4 + (r32 & 1)) ^ (sp & 5)) * QB + (r32 >> 1) * 16 + WS_H3 + (sp * 16 + ab * 4 + w) * 256;
    asm volatile("" : "+v"(mxa), "+v"(pba), "+v"(e0), "+v"(e1), "+v"(e2), "+v"(e3), "+v"(h3v));
    STAMP(2);
    BAR();
    STAMP(3);
    v4u cb[32];
#define TR_READ(n) do { const v2u x0_ = __builtin_bit_cast(v2u, __builtin_amdgcn_ds_read_tr16_b64_v4i16((LAS s16x4*)(L + ta[(n) >> 3][0] + ((n) & 7) * 4096))); \
    const v2u x1_ = __builtin_bit_cast(v2u, __builtin_amdgcn_ds_read_tr16_b64_v4i16((LAS s16x4*)(L + ta[(n) >> 3][1] + ((n) & 7) * 4096)));                 \
    cb[n] = v4u{x0_.x, x0_.y, x1_.x, x1_.y}; } while (0)
    f32x16 sacc;
#pragma unroll
    for (int k = 0; k < 16; ++k) sacc[k] = 0.f;
    {
      v4u fb[32];
#pragma unroll
      for (int s = 0; s < 6; ++s) fb[s] = *(LAS v4u*)(L + qb[s & 3] + s * 1024);
      SCHED();
#pragma unroll
      for (int s = 0; s < 32; ++s) {
        if (s + 6 < 32) fb[s + 6] = *(LAS v4u*)(L + qb[(s + 6) & 3] + (s + 6) * 1024);
        if (s >= 26) TR_READ(s - 26);
        sacc = mfma32(fa[s], fb[s], sacc);
        SCHED();
      }
    }
    float mw = sacc[0];
#pragma unroll
    for (int k = 1; k < 16; ++k) mw = fmaxf(mw, sacc[k]);
    mw = x32_max(mw);
    asm volatile("s_waitcnt lgkmcnt(0)" ::: "memory");
    *(LAS float*)(L + mxa + w * 32768) = mw;
    STAMP(4);
    BAR();
    const float mx = fmaxf(fmaxf(*(LAS float*)(L + mxa), *(LAS float*)(L + mxa + 32768)), fmaxf(*(LAS float*)(L + mxa + 65536), *(LAS float*)(L + mxa + 98304)));
    float ls = 0.f;
#pragma unroll
    for (int k = 0; k < 16; ++k) { sacc[k] = __builtin_amdgcn_exp2f(sacc[k] - mx); ls += sacc[k]; }
#pragma unroll
    for (int s2 = 0; s2 < 2; ++s2)
      *(LAS v4u*)(L + pba + (2 * w + s2) * 1024) =
          v4u{pk(sacc[8 * s2], sacc[8 * s2 + 1]), pk(sacc[8 * s2 + 2], sacc[8 * s2 + 3]), pk(sacc[8 * s2 + 4], sacc[8 * s2 + 5]), pk(sacc[8 * s2 + 6], sacc[8 * s2 + 7])};
    ls = x32_sum(ls);
    *(LAS float*)(L + mxa + 640 + w * 32768) = ls;
    BAR();
    STAMP(5);
    {
      v4u pf[8];
#pragma unroll
      for (int k = 0; k < 8; ++k) pf[k] = *(LAS v4u*)(L + pba + k * 1024);
      f32x4 l4;
      if (w == 0) l4 = f32x4{*(LAS float*)(L + mxa + 640), *(LAS float*)(L + mxa + 640 + 32768), *(LAS float*)(L + mxa + 640 + 65536), *(LAS float*)(L + mxa + 640 + 98304)};
      SCHED();
      f32x16 o[4];
      unsigned p01[16], pq[16];
      const bool odd = lane & 1;
#pragma unroll
      for (int t = 0; t < 4; ++t)
#pragma unroll
        for (int k = 0; k < 16; ++k) o[t][k] = 0.f;
#pragma unroll
      for (int n = 0; n < 32; ++n) {
        if (n + 6 < 32) TR_READ(n + 6);
        o[n >> 3] = mfma32(pf[n & 7], cb[n], o[n >> 3]);
        if (n == 0 && w == 0)
          st4(v4u{tlo, genc(mx, t8), tlo, genc((l4[0] + l4[1]) + (l4[2] + l4[3]), t8)}, rws, lane < 32 ? r32 * 16 : OOB, WS_ML + slot(idx, 512));
        if (n >= 17 && n < 32 && n != 24) {
          const int j = n < 24 ? n - 17 : n - 18;
          if (j < 8) { pq[2 * j] = pk(o[0][2 * j], o[1][2 * j]); pq[2 * j + 1] = pk(o[0][2 * j + 1], o[1][2 * j + 1]); asm volatile("" : "+v"(pq[2 * j]), "+v"(pq[2 * j + 1])); }
          if (j >= 6) {
            const int k = j - 6;
            p01[2 * k] = lsbput(odd ? pq[2 * k + 1] : pq[2 * k], e0);
            p01[2 * k + 1] = __builtin_amdgcn_update_dpp(0, lsbput(odd ? pq[2 * k] : pq[2 * k + 1], e2), 0xB1, 0xf, 0xf, true);
            asm volatile("" : "+v"(p01[2 * k]), "+v"(p01[2 * k + 1]));
          }
        }
        SCHED();
      }
      STAMP(1);
#pragma unroll
      for (int k = 0; k < 8; ++k) {
        const unsigned pa = pk(o[2][2 * k], o[3][2 * k]), pb = pk(o[2][2 * k + 1], o[3][2 * k + 1]);
        st4(v4u{p01[2 * k], lsbput(odd ? pb : pa, e1), p01[2 * k + 1], (unsigned)__builtin_amdgcn_update_dpp(0, lsbput(odd ? pa : pb, e3), 0xB1, 0xf, 0xf, true)},
            rws, h3v, ((2 * (k & 1) + 8 * (k >> 1)) ^ (sp & 26)) * QB);
      }
    }
    STAMP(6);
    RBAR();
    RBAR();
    RBAR();
  } else {
    const int v = w - 4;
    STAMP(1);
    const rsrc_t rv = mk(wuv, 512 * 4096 * 2);
    v4u uw[8];
#define TL(i) do { uw[i] = ld4<NT_W>(rv, g * 32 * 8192 + i16 * 16, (128 * jq + 8 * v + ((i) ^ (hd & 1))) * 8192 + hd * 256); SCHED(); } while (0)
    {
      v4u qa[8];
      SLP(DLY_QA);
#pragma unroll
      for (int k = 0; k < 8; ++k) qa[k] = hl4(rws, lane * 16, WS_QA + ab * 2048 + slot(8 * v + k, 1024));
      SCHED();
#pragma unroll
      for (int i = 0; i < TL0; ++i) TL(i);
#pragma unroll 1
      for (int n = 0;; ++n) {
        unsigned bad = 0;
#pragma unroll
        for (int k = 0; k < 8; ++k) bad |= strip4(qa[k], e4);
        if (n == 0 && w != 4) { STAMP(7); }
        if (__all((bad & LSBM) == 0) || n >= GSPIN) break;
        SLP(DLY_R2);
#pragma unroll
        for (int k = 0; k < 8; ++k) qa[k] = hl4(rws, lane * 16, WS_QA + ab * 2048 + slot(8 * v + k, 1024));
      }
#pragma unroll
      for (int k = 0; k < 8; ++k) *(LAS v4u*)(L + LA_QA + (lane >> 1) * 1024 + ((((lane & 1) * 32 + 8 * v + k) ^ (lane & 7)) * 16)) = qa[k];
    }
    STAMP(2);
    BAR();
#pragma unroll
    for (int i = TL0; i < TL1; ++i) TL(i);
    RBAR();
#pragma unroll
    for (int i = TL1; i < 8; ++i) TL(i);
    RBAR();
    const int e7 = lane & 7, q4 = i16 >> 2;
    int cta = LA_CTX + v * 288 + (32 * g + e7) * 2, ctr = LA_CTX + cp * 288 + g * 16;
    int h4a = ((2 * (g & 1) + (i16 & 1)) * 16 + (2 * (g >> 1) + (i16 >> 3)) * 4 + ((i16 >> 1) & 3)) * 16 + WS_H4 + slot(hd * 4 + v, 4096) + jq * 1024;
    const unsigned src = (lane & 8) ? 0x0302u : 0x0100u, s1 = (e7 & 1) ? ((src << 16) | 0x0C0Cu) : (0x0C0C0000u | src);
    unsigned s8[4];
    int uwa[4], bva[2][2];
#pragma unroll
    for (int k = 0; k < 4; ++k) {
      s8[k] = (e7 >> 1) == k ? s1 : 0x0C0C0C0Cu;
      uwa[k] = LA_UVW + (32 * v + g) * 256 + ((i16 ^ ((g << 2) | k)) * 16);
      bva[k >> 1][k & 1] = LA_UVW + (8 * g + 4 * (k & 1) + q4) * 256 + (((2 * (2 * v + (k >> 1)) + (cp >> 1)) ^ ((q4 << 2) | ((2 * g + (k & 1)) & 3))) * 16) + 8 * (cp & 1);
      asm volatile("" : "+v"(s8[k]), "+v"(uwa[k]), "+v"(bva[k >> 1][k & 1]));
    }
    asm volatile("" : "+v"(cta), "+v"(ctr), "+v"(h4a));
    const int h3l = ((hd & 3) ^ g) * QB + g * 4096 + i16 * 16;
#define ML_LOAD() hl4(rws, slot(r32, 512), WS_ML + v * 1024 + hd * 16)
#define ML_OK(V) __all((((V).x ^ tlo) | ((V).z ^ tlo) | ((((V).y ^ t8) | ((V).w ^ t8)) & 0xFFu)) == 0)
#define H3_LOADS() _Pragma("unroll") for (int k = 0; k < 8; ++k) d[k] = hl4(rws, h3l, WS_H3 + ((hd >> 2) ^ k) * 4 * QB + k * 16384 + (v * 4 + jq) * 256); SCHED()
    v4u d[8], bk[8];
    v4u ml;
    VMCNT(0);
    RBAR();
    const bool early = (unsigned)(__builtin_amdgcn_s_memrealtime() - t_start) > (unsigned)(TV(T_LATE) * 5);
    if (early) { ml = ML_LOAD(); H3_LOADS(); }
#pragma unroll
    for (int k = 0; k < 8; ++k) *(LAS v4u*)(L + uwa[k & 3] + k * 1024) = uw[k];
    BAR();
    v4u bv[2][4];
#pragma unroll
    for (int t = 0; t < 2; ++t)
#pragma unroll
      for (int ks = 0; ks < 4; ++ks) {
        const v2u x0 = __builtin_bit_cast(v2u, __builtin_amdgcn_ds_read_tr16_b64_v4i16((LAS s16x4*)(L + bva[t][0] + ks * 8192)));
        const v2u x1 = __builtin_bit_cast(v2u, __builtin_amdgcn_ds_read_tr16_b64_v4i16((LAS s16x4*)(L + bva[t][1] + ks * 8192)));
        bv[t][ks] = v4u{x0.x, x0.y, x1.x, x1.y};
      }
    SCHED();
    STAMP(4);
    {
      if (early) {
#pragma unroll 1
        for (int n = 0; n < GSPIN && !ML_OK(ml); ++n) ml = ML_LOAD();
      } else {
        SLP(DLY_M0);
        for (int n = 0;; ++n) {
          ml = ML_LOAD();
          if (ML_OK(ml) || n >= GSPIN) break;
          SLP(DLY_RM);
        }
        SLP(DLY_H3);
        H3_LOADS();
      }
      STAMP(5);
      const float m = gdec(ml.y), M = x16_max(row_max(m));
      const float e = __builtin_amdgcn_exp2f(m - M);
      const float al = e * LSB_COMP * __builtin_amdgcn_rcpf(x16_sum(row_sum(e * gdec(ml.w))));
      const unsigned ah = pk(al, al) & 0xFFFFu;
      const unsigned hl = ah | (pk(al - bf2f(ah), 0.f) << 16);
#pragma unroll
      for (int k = 0; k < 8; ++k) { bk[k] = bdiag(__builtin_amdgcn_ds_bpermute((4 * k + g) * 4, hl), s8); asm volatile("" : "+v"(bk[k])); }
      STAMP(3);
      f32x4 c;
#pragma unroll 1
      for (int n = 0;; ++n) {
        unsigned bad = 0;
        c = f32x4{0.f, 0.f, 0.f, 0.f};
#pragma unroll
        for (int k = 0; k < 8; ++k) { bad |= strip4(d[k], e4); c = mfma16(d[k], bk[k], c); }
        if (__all((bad & LSBM) == 0) || n >= GSPIN) break;
#pragma unroll
        for (int k = 0; k < 8; ++k) d[k] = hl4(rws, h3l, WS_H3 + ((hd >> 2) ^ k) * 4 * QB + k * 16384 + (v * 4 + jq) * 256);
      }
      STAMP(6);
#pragma unroll
      for (int r = 0; r < 4; ++r) {
        const float y = c[r] + dppz<0x128>(c[r]);
        *(LAS u16*)(L + cta + 16 * r) = (u16)pk(y, y);
      }
    }
    BAR();
    {
      v4u af[4];
#pragma unroll
      for (int ks = 0; ks < 4; ++ks) af[ks] = *(LAS v4u*)(L + ctr + 64 * ks);
      SCHED();
      f32x4 c0 = {0.f, 0.f, 0.f, 0.f}, c1 = c0;
#pragma unroll
      for (int ks = 0; ks < 4; ++ks) { c0 = mfma16(af[ks], bv[0][ks], c0); c1 = mfma16(af[ks], bv[1][ks], c1); }
      const f32x4 ct = g < 2 ? c0 : c1;
      const float x0 = (g & 1) ? ct[2] : ct[0], x1 = (g & 1) ? ct[3] : ct[1];
      const float n0 = dppz<0xB1>(x0), n1 = dppz<0xB1>(x1);
      st4(v4u{tlo, genc((lane & 1) ? n1 : x0, t8), tlo, genc((lane & 1) ? x1 : n0, t8)}, rws, h4a, 0);
    }
    STAMP(11);
  }
  STAMP_OUT();
}

extern "C" __global__ void fused_decode_end() {}

extern "C" void kernel_launch(void* const* d_in, const int* in_sizes, int n_in, void* d_out, int out_size, void* d_ws, size_t ws_size,
                              hipStream_t stream) {
  (void)hipFuncSetAttribute((const void*)fused_decode, hipFuncAttributeMaxDynamicSharedMemorySize, LDS_BYTES);
  fused_decode<<<256, 512, LDS_BYTES, stream>>>((const u16*)d_in[0], (const u16*)d_in[1], (const u16*)d_in[2], (const u16*)d_in[3],
                                                (const u16*)d_in[4], (const u16*)d_in[5], (const u16*)d_in[7], (const u16*)d_in[8],
                                                (const u16*)d_in[9], (u16*)d_out, (char*)d_ws TUNE_ARG);
}
```

```cpp
#include <hip/hip_runtime.h>
#include <stdint.h>

typedef unsigned short u16;
typedef unsigned long long u64;
typedef unsigned v4u __attribute__((ext_vector_type(4)));
typedef unsigned v2u __attribute__((ext_vector_type(2)));
typedef float f32x2 __attribute__((ext_vector_type(2)));
typedef float f32x4 __attribute__((ext_vector_type(4)));
typedef float f32x16 __attribute__((ext_vector_type(16)));
typedef short s16x4 __attribute__((ext_vector_type(4)));
typedef __bf16 bf16x8 __attribute__((ext_vector_type(8)));
typedef __bf16 bf16x2 __attribute__((ext_vector_type(2)));
typedef __amdgpu_buffer_rsrc_t rsrc_t;
#define GAS __attribute__((address_space(1)))
#define LAS __attribute__((address_space(3)))
#define DI __device__ __forceinline__

#define BLOCK_ID blockIdx.x
constexpr int NT = 2;
constexpr int PACE_C = 7;
constexpr int PACE_K = 4;
constexpr int PACE_G = 4;
constexpr int PACE_W = 6;
#define DLY_C 40
#define DLY_K 16
#define DLY_H1 2
#define DLY_R1 7
#define DLY_QA 8
#define DLY_R2 12
#define DLY_M0 12
#define DLY_RM 4
#define DLY_H3 1
#define DLY_H7 22
#define DLY_R7 2
#define DLY_R4 2
#define DLY_H5 18
constexpr unsigned T_F = 350, T_H7 = 915;
#define SLP(N) __builtin_amdgcn_s_sleep(N)

constexpr int SC1 = 16;
constexpr int GSPIN = 1 << 13;

constexpr int QB = 131072, WIN = 16 * QB;
constexpr int WS_H1 = 0;
constexpr int WS_H4 = 32768;
constexpr int WS_QA = 65536;
constexpr int WS_ML = 73728;
constexpr int WS_W = 81920;
constexpr int WS_H7 = WIN;
constexpr int WS_H5 = 2 * WIN;
constexpr int WS_H3 = 3 * WIN;
DI constexpr int slot(int p, int size) { return (p & 15) * QB + (p >> 4) * size; }
DI constexpr int blk5(int n) { return (n & 1) * 8 * QB + (n >> 1) * 16384; }
constexpr int WS_END = WS_H3 + 128 * 32768;
constexpr int OOB = (int)0x80000000;

constexpr int LP_XD = 5120, LP_QB = 15360, LP_QO = 16896, LP_RED = 18432, LP_HP = 34816, LP_B8 = 35840, LP_FIN = 39936, LP_UK = 40960;
constexpr int LA_QA = 131072, LA_PB = LA_QA + 1024, LA_CTX = LA_QA + 9216, LA_UVW = 0;
constexpr int LDS_BYTES = 163840;

DI rsrc_t mk(const void* p, int bytes) { return __builtin_amdgcn_make_buffer_rsrc((void*)p, (short)0, bytes, 0x00020000); }
template <int AUX> DI v4u ld4(rsrc_t r, int vo, int so) { return __builtin_amdgcn_raw_buffer_load_b128(r, vo, so, AUX); }
template <int AUX> DI v2u ld2(rsrc_t r, int vo, int so) { return __builtin_amdgcn_raw_buffer_load_b64(r, vo, so, AUX); }
DI v4u hl4(rsrc_t r, int vo, int so) { asm volatile("" : "+v"(vo)); return __builtin_amdgcn_raw_buffer_load_b128(r, vo, so, SC1); }
DI v2u hl2(rsrc_t r, int vo, int so) { asm volatile("" : "+v"(vo)); return __builtin_amdgcn_raw_buffer_load_b64(r, vo, so, SC1); }
DI void touch(rsrc_t r, int vo, int so) { v4u t; asm volatile("buffer_load_dwordx4 %0, %1, %2, %3 offen sc1\n\ts_waitcnt vmcnt(0)" : "=&v"(t) : "v"(vo), "s"(r), "s"(so) : "memory"); }
DI void st4(v4u v, rsrc_t r, int vo, int so) { __builtin_amdgcn_raw_buffer_store_b128(v, r, vo + so, 0, SC1); }
DI void st2(v2u v, rsrc_t r, int vo, int so) { __builtin_amdgcn_raw_buffer_store_b64(v, r, vo, so, SC1); }
DI void dma16(rsrc_t r, int vo, int so, unsigned lds) {
  asm volatile("s_mov_b32 m0, %0\n\ts_nop 0\n\tbuffer_load_dwordx4 %1, %2, %3 offen nt lds" ::"s"(lds), "v"(vo), "s"(r), "s"(so) : "memory");
}
#define VMCNT(n) asm volatile("s_waitcnt vmcnt(%0)" ::"n"(n) : "memory")
#define FENCE() asm volatile("" ::: "memory")
#define SCHED() __builtin_amdgcn_sched_barrier(0)

DI float bf2f(unsigned v) { return __uint_as_float(v << 16); }
DI unsigned pk(float a, float b) { f32x2 f = {a, b}; return __builtin_bit_cast(unsigned, __builtin_convertvector(f, bf16x2)); }
DI f32x4 mfma16(v4u a, v4u b, f32x4 c) {
  return __builtin_amdgcn_mfma_f32_16x16x32_bf16(__builtin_bit_cast(bf16x8, a), __builtin_bit_cast(bf16x8, b), c, 0, 0, 0);
}
DI f32x4 mfma16k16(v2u a, v2u b, f32x4 c) {
  return __builtin_amdgcn_mfma_f32_16x16x16bf16_1k(__builtin_bit_cast(s16x4, a), __builtin_bit_cast(s16x4, b), c, 0, 0, 0);
}
DI f32x16 mfma32(v4u a, v4u b, f32x16 c) {
  return __builtin_amdgcn_mfma_f32_32x32x16_bf16(__builtin_bit_cast(bf16x8, a), __builtin_bit_cast(bf16x8, b), c, 0, 0, 0);
}
template <int CTRL, int RM> DI float dppf(float old, float v) {
  return __builtin_bit_cast(float, __builtin_amdgcn_update_dpp(__builtin_bit_cast(int, old), __builtin_bit_cast(int, v), CTRL, RM, 0xf, false));
}
template <int CTRL> DI float dppz(float v) {
  return __builtin_bit_cast(float, __builtin_amdgcn_update_dpp(0, __builtin_bit_cast(int, v), CTRL, 0xf, 0xf, true));
}
DI float row_sum(float v) {
  v += dppz<0xB1>(v); v += dppz<0x4E>(v); v += dppz<0x124>(v); v += dppz<0x128>(v);
  return v;
}
DI float dot2(unsigned a, unsigned b, float c) {
  return __builtin_amdgcn_fdot2_f32_bf16(__builtin_bit_cast(bf16x2, a), __builtin_bit_cast(bf16x2, b), c, false);
}
DI float row_max(float v) {
  v = fmaxf(v, dppf<0xB1, 0xf>(v, v)); v = fmaxf(v, dppf<0x4E, 0xf>(v, v));
  v = fmaxf(v, dppf<0x124, 0xf>(v, v)); v = fmaxf(v, dppf<0x128, 0xf>(v, v));
  return v;
}
DI float x16_sum(float v) { auto r = __builtin_amdgcn_permlane16_swap(__float_as_uint(v), __float_as_uint(v), false, false); return __uint_as_float(r[0]) + __uint_as_float(r[1]); }
DI float x32_sum(float v) { auto r = __builtin_amdgcn_permlane32_swap(__float_as_uint(v), __float_as_uint(v), false, false); return __uint_as_float(r[0]) + __uint_as_float(r[1]); }
DI float x16_max(float v) { auto r = __builtin_amdgcn_permlane16_swap(__float_as_uint(v), __float_as_uint(v), false, false); return fmaxf(__uint_as_float(r[0]), __uint_as_float(r[1])); }
DI float x32_max(float v) { auto r = __builtin_amdgcn_permlane32_swap(__float_as_uint(v), __float_as_uint(v), false, false); return fmaxf(__uint_as_float(r[0]), __uint_as_float(r[1])); }

#define RBAR() asm volatile("s_barrier" ::: "memory")
#define BAR() asm volatile("s_waitcnt lgkmcnt(0)\n\ts_barrier" ::: "memory")
DI unsigned genc(float v, unsigned t8) { return ((__float_as_uint(v) + 0x80u) & 0xFFFFFF00u) | t8; }
DI float gdec(unsigned hi) { return __uint_as_float(hi & 0xFFFFFF00u); }

#define STAMP_DECL
#define STAMP_OUT()
DI void wait_clock(u64 t0, unsigned ticks) {
#pragma unroll 1
  for (int n = 0; n < 4096 && (unsigned)(__builtin_amdgcn_s_memrealtime() - t0) < ticks; ++n) __builtin_amdgcn_s_sleep(1);
}

DI v4u bdiag(unsigned P, const unsigned (&s)[4]) {
  v4u b;
  b.x = __builtin_amdgcn_perm(P, P, s[0]); b.y = __builtin_amdgcn_perm(P, P, s[1]);
  b.z = __builtin_amdgcn_perm(P, P, s[2]); b.w = __builtin_amdgcn_perm(P, P, s[3]);
  return b;
}
DI void bdiag_sel(int cp, unsigned (&s)[4]) {
  const unsigned lo = (cp & 1) ? 0x01000C0Cu : 0x0C0C0100u, hi = (cp & 1) ? 0x03020C0Cu : 0x0C0C0302u, z = 0x0C0C0C0Cu;
  s[0] = cp < 2 ? lo : z; s[1] = cp < 2 ? z : lo; s[2] = cp < 2 ? hi : z; s[3] = cp < 2 ? z : hi;
}

DI unsigned lsbtag(unsigned tlo, unsigned thi, int p) { return (((p < 32 ? tlo : thi) >> (p & 31)) & 1u) | (((tlo ^ (unsigned)p) & 1u) << 16); }
constexpr unsigned LSBM = 0x00010001u;
DI unsigned lsbput(unsigned v, unsigned e) { return (v & ~LSBM) | e; }
DI unsigned strip4(v4u& d, const unsigned (&e)[4]) { d.x ^= e[0]; d.y ^= e[1]; d.z ^= e[2]; d.w ^= e[3]; return (d.x | d.y) | (d.z | d.w); }
DI void swap24(v4u& a, v4u& b) {
  unsigned az = a.z, aw = a.w, bx = b.x, by = b.y;
  asm volatile("v_swap_b32 %0, %2\n\tv_swap_b32 %1, %3\n\ts_nop 1" : "+v"(az), "+v"(aw), "+v"(bx), "+v"(by));
  a.z = az; a.w = aw; b.x = bx; b.y = by;
}
constexpr float LSB_COMP = 1.0028f;
DI v4u tag8(f32x4 c, const unsigned (&t)[4]) {
  return v4u{(__float_as_uint(c[0]) & 0xFFFFFF00u) | t[0], (__float_as_uint(c[1]) & 0xFFFFFF00u) | t[1],
             (__float_as_uint(c[2]) & 0xFFFFFF00u) | t[2], (__float_as_uint(c[3]) & 0xFFFFFF00u) | t[3]};
}
extern "C" __global__ void __launch_bounds__(512, 2)
fused_decode(const u16* __restrict__ xin, const u16* __restrict__ ckv, const u16* __restrict__ wq, const u16* __restrict__ wuk,
             const u16* __restrict__ wuv, const u16* __restrict__ wo, const u16* __restrict__ w1, const u16* __restrict__ w3,
             const u16* __restrict__ w2, u16* __restrict__ out, char* ws) {
  extern __shared__ __attribute__((aligned(16))) char smem[];
  LAS char* L = (LAS char*)smem;
  const unsigned lbase = (unsigned)(size_t)smem;
  const int lane = threadIdx.x & 63, w = __builtin_amdgcn_readfirstlane(threadIdx.x >> 6);
  const int bid = BLOCK_ID, xg = bid & 7, qd_ = bid >> 3, qq = qd_ & 15, idx = xg * 16 + qq, hd = idx >> 2, jq = idx & 3;
  const int i16 = lane & 15, g = lane >> 4, bj = i16 >> 2, cp = lane & 3;
  STAMP_DECL;
  if (w == 7) {
    u64 pc; unsigned back, len;
    asm volatile("s_getpc_b64 %0\n.Lpf_here:\n\ts_mov_b32 %1, .Lpf_here-fused_decode\n\ts_mov_b32 %2, fused_decode_end-fused_decode" : "=s"(pc), "=s"(back), "=s"(len));
    const rsrc_t rcode = mk((const void*)(pc - back), (int)min(len, 32768u));
    v4u dummy;
    asm volatile("s_nop 4\n\tbuffer_load_dwordx4 %0, %1, %2, 0 offen\n\ts_waitcnt vmcnt(0)" : "=&v"(dummy) : "v"(qd_ * 1024 + lane * 16), "s"(rcode) : "memory");
  }
  asm volatile("" ::"s"(xin), "s"(ckv), "s"(wq), "s"(wuk), "s"(wuv), "s"(wo), "s"(w1), "s"(w3), "s"(w2), "s"(out), "s"(ws));
  u64 t_start;
  asm volatile("s_memrealtime %0\n\ts_waitcnt lgkmcnt(0)" : "=s"(t_start) : "s"(ws) : "memory");
  const rsrc_t rws = mk(ws, WS_END);
  const rsrc_t rx = mk(xin, 4 * 576 * 2);
  unsigned sel[4];
  bdiag_sel(cp, sel);
#define MAKE_TAG(W) const u64 tag_ = (((u64)(W).y << 32) | (W).x) + 0x9E3779B97F4A7C15ull;  \
  const unsigned tlo = (unsigned)tag_, thi = (unsigned)(tag_ >> 32), t8 = thi & 0xFFu;        \
  const unsigned tb[4] = {tlo & 0xFFu, (tlo >> 8) & 0xFFu, (tlo >> 16) & 0xFFu, tlo >> 24}
  const int vo5 = (lane >> 3) * QB + (lane & 7) * 16;

  if (qd_ >= 16) {
    const int so5 = WS_H5 + idx * 128;
    if (w < 4) {
      v4u ka[2][4];
      v2u wv_;
      asm volatile("buffer_load_dwordx2 %0, off, %1, %2 sc1" : "=v"(wv_) : "s"(rws), "s"(WS_W) : "memory");
      {
        const rsrc_t rk = mk(wuk, 512 * 4096 * 2);
        const int ukx = 4 * (hd & 1);
        SLP(DLY_K);
#pragma unroll
        for (int n = 0; n < 8; ++n) {
          if (n >= PACE_K) VMCNT(PACE_K - 1);
          dma16(rk, g * 8192 + ((i16 ^ ((g << 2) | (n & 3))) * 16), (128 * jq + 32 * w + 4 * (n ^ ukx)) * 8192 + hd * 256, lbase + LP_UK + (8 * w + (n ^ ukx)) * 1024);
        }
        VMCNT(0);
        const int fr = ((i16 & 3) << 2) | (i16 >> 2);
#pragma unroll
        for (int t = 0; t < 2; ++t)
#pragma unroll
          for (int s = 0; s < 4; ++s) {
            ka[t][s] = *(LAS v4u*)(L + LP_UK + (32 * w + 16 * t + i16) * 256 + (((4 * s + g) ^ fr) * 16));
            asm volatile("" : "+v"(ka[t][s]));
          }
      }
      asm volatile("" : "+v"(wv_));
      MAKE_TAG(wv_);
      unsigned eq[4];
#pragma unroll
      for (int k = 0; k < 4; ++k) { eq[k] = lsbtag(tlo, thi, 4 * i16 + k); asm volatile("" : "+v"(eq[k])); }
      SLP(DLY_H1);
      {
        const int blk = w & 1, half = w >> 1;
        float q0, q1;
        for (int n = 0;; ++n) {
          v4u p[4];
#pragma unroll
          for (int jj = 0; jj < 4; ++jj) p[jj] = hl4(rws, lane * 16, WS_H1 + slot(hd * 4 + jj, 4096) + (blk * 2 + half) * 1024);
          unsigned bad = 0;
          q0 = 0.f; q1 = 0.f;
#pragma unroll
          for (int jj = 0; jj < 4; ++jj) {
            bad |= (p[jj].x ^ tlo) | (p[jj].z ^ tlo) | (((p[jj].y ^ t8) | (p[jj].w ^ t8)) & 0xFFu);
            q0 += gdec(p[jj].y); q1 += gdec(p[jj].w);
          }
          if (__all(bad == 0) || n >= GSPIN) break;
          SLP(DLY_R1);
        }
        const unsigned qp = pk(q0, q1);
        const int d0 = 32 * g + 16 * half + 4 * blk + cp;
        *(LAS u16*)(L + LP_QB + bj * 320 + d0 * 2) = (u16)qp;
        *(LAS u16*)(L + LP_QB + bj * 320 + (d0 + 8) * 2) = (u16)(qp >> 16);
      }
      BAR();
      {
        v4u qf[4];
#pragma unroll
        for (int s = 0; s < 4; ++s) qf[s] = *(LAS v4u*)(L + LP_QB + cp * 320 + s * 64 + g * 16);
        f32x4 c0 = {0.f, 0.f, 0.f, 0.f}, c1 = c0;
#pragma unroll
        for (int s = 0; s < 4; ++s) { c0 = mfma16(ka[0][s], qf[s], c0); c1 = mfma16(ka[1][s], qf[s], c1); }
        const float sc = 0.08838834764831845f * 1.4426950408889634f * LSB_COMP;
        if (i16 < 4) {
          *(LAS v2u*)(L + LP_QO + i16 * 256 + (32 * w + 4 * g) * 2) = v2u{pk(c0[0] * sc, c0[1] * sc), pk(c0[2] * sc, c0[3] * sc)};
          *(LAS v2u*)(L + LP_QO + i16 * 256 + (32 * w + 16 + 4 * g) * 2) = v2u{pk(c1[0] * sc, c1[1] * sc), pk(c1[2] * sc, c1[3] * sc)};
        }
      }
      BAR();
      if (w == 0) {
        const v4u v = *(LAS v4u*)(L + LP_QO + lane * 16);
        st4(v4u{lsbput(v.x, eq[0]), lsbput(v.y, eq[1]), lsbput(v.z, eq[2]), lsbput(v.w, eq[3])}, rws, g * 2048 + i16 * 16, WS_QA + slot(hd, 1024) + jq * 256);
      }
      BAR();
      const rsrc_t r2 = mk(w2, 4608 * 576 * 2);
      const int so2 = (xg * 576 + 36 * qq) * 1152;
      const rsrc_t ro = mk(wo, 4096 * 576 * 2);
      const int soo = (hd * 128 + 32 * jq) * 1152;
      v4u a2[9][2];
      v2u a8[3][2];
      {
        const int vo4 = 2 * g * 1152 + i16 * 16, vo4m = g < 2 ? vo4 : OOB;
        const int vo8 = 2 * g * 1152 + i16 * 8 + 1024, vo8m = (g < 2 && w == 0) ? vo8 : OOB;
#pragma unroll
        for (int n = 0; n < 24; ++n) {
          if (n >= PACE_W) VMCNT(PACE_W - 1);
          if (n == PACE_W) { SCHED(); RBAR(); SLP(1); }
          const int o = n >> 1, r = n & 1;
          if (n < 10) a2[o][r] = ld4<NT>(r2, o < 4 ? vo4 : vo4m, so2 + (8 * o + r) * 1152 + 256 * w);
          else if (n < 12) a8[0][r] = ld2<NT>(r2, vo8, so2 + (8 * w + r) * 1152);
          else if (n < 14) a8[1][r] = ld2<NT>(r2, vo8m, so2 + (32 + r) * 1152);
          else if (n < 22) a2[o - 2][r] = ld4<NT>(ro, vo4, soo + (8 * (o - 7) + r) * 1152 + 256 * w);
          else a8[2][r] = ld2<NT>(ro, vo8, soo + (8 * w + r) * 1152);
          SCHED();
        }
      }
#pragma unroll
      for (int o = 0; o < 5; ++o) swap24(a2[o][0], a2[o][1]);
      RBAR();
      {
        const v4u h4 = *(LAS v4u*)(L + LP_HP + (bj * 4 + g) * 48);
        const unsigned h5 = *(LAS unsigned*)(L + LP_HP + (bj * 4 + g) * 48 + 16);
        const unsigned hw = *(LAS unsigned*)(L + LP_HP + (bj * 4 + g) * 48 + 4 * w);
        f32x4 c8 = {0.f, 0.f, 0.f, 0.f}, cx = c8, cy = c8;
        c8 = mfma16(v4u{a8[0][0].x, a8[0][0].y, a8[0][1].x, a8[0][1].y}, bdiag(hw, sel), c8);
        c8 = mfma16(v4u{a8[1][0].x, a8[1][0].y, a8[1][1].x, a8[1][1].y}, bdiag(h5, sel), c8);
#pragma unroll
        for (int o = 0; o < 5; ++o) {
          const v4u B = bdiag(o == 0 ? h4.x : o == 1 ? h4.y : o == 2 ? h4.z : o == 3 ? h4.w : h5, sel);
          cx = mfma16(a2[o][0], B, cx);
          cy = mfma16(a2[o][1], B, cy);
        }
#pragma unroll
        for (int o = 5; o < 9; ++o) swap24(a2[o][0], a2[o][1]);
        asm volatile("" : "+v"(cx), "+v"(cy), "+v"(c8));
        int bpa = (bj * 16 + g) * 4;
        asm volatile("" : "+v"(bpa));
        float y0, y1;
        for (int n = 0;; ++n) {
          v4u p[4];
#pragma unroll
          for (int jj = 0; jj < 4; ++jj) p[jj] = hl4(rws, lane * 16, WS_H4 + slot(idx, 4096) + jj * 1024);
          unsigned bad = 0;
          y0 = 0.f; y1 = 0.f;
#pragma unroll
          for (int jj = 0; jj < 4; ++jj) {
            bad |= (p[jj].x ^ tlo) | (p[jj].z ^ tlo) | (((p[jj].y ^ t8) | (p[jj].w ^ t8)) & 0xFFu);
            y0 += gdec(p[jj].y); y1 += gdec(p[jj].w);
          }
          if (__all(bad == 0) || n >= GSPIN) break;
          SLP(DLY_R4);
        }
        const unsigned yp = pk(y0, y1);
        const unsigned pw = __builtin_amdgcn_ds_bpermute(bpa + 16 * w, yp);
        unsigned po[4];
#pragma unroll
        for (int o = 0; o < 4; ++o) po[o] = __builtin_amdgcn_ds_bpermute(bpa + 16 * o, yp);
        c8 = mfma16(v4u{a8[2][0].x, a8[2][0].y, a8[2][1].x, a8[2][1].y}, bdiag(pw, sel), c8);
#pragma unroll
        for (int o = 0; o < 4; ++o) {
          const v4u B = bdiag(po[o], sel);
          cx = mfma16(a2[5 + o][0], B, cx);
          cy = mfma16(a2[5 + o][1], B, cy);
        }
        *(LAS f32x4*)(L + LP_B8 + w * 1024 + lane * 16) = c8;
        st4(tag8(cx, tb), rws, vo5, so5 + blk5(2 * w));
        st4(tag8(cy, tb), rws, vo5, so5 + blk5(2 * w + 1));
        BAR();
        if (w == 0) {
          f32x4 s = *(LAS f32x4*)(L + LP_B8 + lane * 16);
#pragma unroll
          for (int k = 1; k < 4; ++k) s += *(LAS f32x4*)(L + LP_B8 + k * 1024 + lane * 16);
          st4(tag8(s, tb), rws, vo5, so5 + blk5(8));
        }
      }
      if (qq < 9) RBAR();
    } else {
      const int v = w - 4;
      unsigned xn[8];
      const v2u wv_ = hl2(rws, 0, WS_W);
      v4u pt1;
      asm volatile("buffer_load_dwordx4 %0, %1, %2, %3 offen sc1" : "=v"(pt1) : "v"(lane * 16), "s"(rws), "s"(WS_H1 + slot(idx, 4096) + v * 1024) : "memory");
      {
        const v4u x0 = ld4<0>(rx, lane * 16, v * 1152), x1 = ld4<0>(rx, lane < 8 ? lane * 16 : OOB, v * 1152 + 1024);
        float s = 0.f, q = 0.f;
#pragma unroll
        for (int k = 0; k < 4; ++k) {
          s = dot2(x0[k], 0x3F803F80u, s); q = dot2(x0[k], x0[k], q);
          s = dot2(x1[k], 0x3F803F80u, s); q = dot2(x1[k], x1[k], q);
        }
        const float mean = x32_sum(x16_sum(row_sum(s))) * (1.0f / 576.0f);
        const float msq = x32_sum(x16_sum(row_sum(q))) * (1.0f / 576.0f);
        const float rstd = __builtin_amdgcn_rsqf(fmaxf(fmaf(-mean, mean, msq), 0.f) + 1e-5f), nb = -mean * rstd;
#pragma unroll
        for (int k = 0; k < 4; ++k) {
          xn[k] = pk(fmaf(bf2f(x0[k]), rstd, nb), fmaf(__uint_as_float(x0[k] & 0xFFFF0000u), rstd, nb));
        }
#pragma unroll
        for (int k = 0; k < 4; ++k) xn[4 + k] = pk(fmaf(bf2f(x1[k]), rstd, nb), fmaf(__uint_as_float(x1[k] & 0xFFFF0000u), rstd, nb));
      }
      VMCNT(0);
      asm volatile("" : "+v"(pt1));
#pragma unroll
      for (int hf = 0; hf < 2; ++hf) {
        if (hf == 0 || lane < 8) {
#pragma unroll
          for (int k = 0; k < 4; ++k) {
            const int o = lane + 64 * hf;
            *(LAS unsigned*)(L + LP_XD + ((v * 4 + ((2 * k) & 3)) * 148 + 2 * o + (k >> 1)) * 4) = __builtin_amdgcn_perm(0u, xn[4 * hf + k], 0x01000100u);
            *(LAS unsigned*)(L + LP_XD + ((v * 4 + ((2 * k + 1) & 3)) * 148 + 2 * o + (k >> 1)) * 4) = __builtin_amdgcn_perm(0u, xn[4 * hf + k], 0x03020302u);
          }
        }
      }
      touch(rws, lane * 16, WS_H4 + slot(idx, 4096) + v * 1024);
      MAKE_TAG(wv_);
      const int U0 = (81 * qq) / 144, Qb = 144 * (U0 + 1);
      unsigned xd[21];
      const GAS char* wb = (const GAS char*)(i16 < 8 ? w1 : w3) + (xg * 576 * 2 + (i16 & 7) * 16 + g * 9216);
      const unsigned m0 = cp == 0 ? 0xFFFFu : cp == 1 ? 0xFFFF0000u : 0u, m1 = cp == 2 ? 0xFFFFu : cp == 3 ? 0xFFFF0000u : 0u;
      f32x4 acc[4];
#pragma unroll
      for (int k = 0; k < 4; ++k) acc[k] = f32x4{0.f, 0.f, 0.f, 0.f};
      v4u a[21];
        int off[2][3];
        unsigned use[2][3];
        int hpa[2];
#pragma unroll
        for (int e = 0; e < 2; ++e) {
          const int t = ((v - 2) & 1) * 64 + lane + 128 * e;
          const int cl = t >> 2, b = t & 3, f = 36 * qq + cl, u = f >> 6;
          const int r0 = (144 * u) / 81, r1 = (144 * u + 143) / 81;
#pragma unroll
          for (int k = 0; k < 3; ++k) {
            const int rr = r0 + k;
            const bool on = t < 144 && rr <= r1;
            off[e][k] = on ? slot(xg * 16 + rr, 8192) + (u - (81 * rr) / 144) * 4096 + (f & 63) * 64 + b * 16 : OOB;
            use[e][k] = on ? 0xFFFFFFFFu : 0u;
          }
          hpa[e] = LP_HP + ((b * 4 + ((cl >> 1) & 3)) * 12 + (cl >> 3)) * 4 + (cl & 1) * 2;
          asm volatile("" : "+v"(hpa[e]), "+v"(off[e][0]), "+v"(off[e][1]), "+v"(off[e][2]), "+v"(use[e][0]), "+v"(use[e][1]), "+v"(use[e][2]));
        }
      int pa = WS_H7 + slot(idx, 8192) + v * 4096 + (32 * ((lane >> 4) & 1) + 16 * (lane >> 5) + cp) * 64 + bj * 16;
      asm volatile("" : "+v"(pa));
      BAR();
      RBAR();
#pragma unroll
      for (int t2 = 0; t2 < 21; ++t2) {
        const int Q = 81 * qq + min(4 * t2 + v, 80), qk = Q >= Qb ? Q - Qb : Q - 144 * U0;
        xd[t2] = *(LAS unsigned*)(L + LP_XD + ((bj * 4 + g) * 148 + qk) * 4);
        asm volatile("" : "+v"(xd[t2]));
      }
      wait_clock(t_start, T_F);
#pragma unroll
      for (int t = -PACE_G; t < 21; ++t) {
        const int tl = t + PACE_G;
        if (tl < 21) {
          const int Q = 81 * qq + 4 * tl + v, u = Q >= Qb ? U0 + 1 : U0, qk = Q - 144 * u;
          if (tl < 20 || v == 0) {
            const GAS v4u* p = (const GAS v4u*)(wb + (unsigned)(qk * 4 * 9216 + u * 128));
            a[tl] = __builtin_nontemporal_load(p);
          } else a[tl] = v4u{0u, 0u, 0u, 0u};
          SCHED();
        }
        if (tl == 20) BAR();
        if (t >= 0) {
          const int Q = 81 * qq + min(4 * t + v, 80);
          const v2u B = {xd[t] & m0, xd[t] & m1};
          if (Q < Qb) { acc[0] = mfma16k16(v2u{a[t].x, a[t].y}, B, acc[0]); acc[1] = mfma16k16(v2u{a[t].z, a[t].w}, B, acc[1]); }
          else        { acc[2] = mfma16k16(v2u{a[t].x, a[t].y}, B, acc[2]); acc[3] = mfma16k16(v2u{a[t].z, a[t].w}, B, acc[3]); }
          SCHED();
        }
      }
#pragma unroll
      for (int k = 0; k < 4; ++k) *(LAS f32x4*)(L + LP_RED + (v * 4 + k) * 1024 + lane * 16) = acc[k];
      BAR();
      if (v == 0 || (v == 1 && 81 * qq + 80 >= Qb)) {
#pragma unroll
        for (int hh = 0; hh < 2; ++hh) {
          f32x4 r = *(LAS f32x4*)(L + LP_RED + (2 * v + hh) * 1024 + lane * 16);
#pragma unroll
          for (int k = 1; k < 4; ++k) r += *(LAS f32x4*)(L + LP_RED + (k * 4 + 2 * v + hh) * 1024 + lane * 16);
#pragma unroll
          for (int k = 0; k < 2; ++k) {
            const auto x = __builtin_amdgcn_permlane32_swap(__float_as_uint(r[k]), __float_as_uint(r[2 + k]), false, false);
            st4(v4u{tlo, genc(__uint_as_float(x[0]), t8), tlo, genc(__uint_as_float(x[1]), t8)}, rws, pa + (8 * k + 4 * hh) * 64, 0);
          }
        }
      } else if (v >= 2) {
        SLP(DLY_H7);
        wait_clock(t_start, T_H7);
        float gs[2], us[2];
        for (int n = 0;; ++n) {
          v4u p[2][3];
#pragma unroll
          for (int e = 0; e < 2; ++e)
#pragma unroll
            for (int k = 0; k < 3; ++k) p[e][k] = hl4(rws, off[e][k], WS_H7);
          unsigned bad = 0;
#pragma unroll
          for (int e = 0; e < 2; ++e) {
            gs[e] = 0.f; us[e] = 0.f;
#pragma unroll
            for (int k = 0; k < 3; ++k) {
              bad |= ((p[e][k].x ^ tlo) | (p[e][k].z ^ tlo) | (((p[e][k].y ^ t8) | (p[e][k].w ^ t8)) & 0xFFu)) & use[e][k];
              gs[e] += gdec(p[e][k].y); us[e] += gdec(p[e][k].w);
            }
          }
          if (__all(bad == 0) || n >= GSPIN) break;
          SLP(DLY_R7);
        }
#pragma unroll
        for (int e = 0; e < 2; ++e) {
          const float hv = gs[e] * __builtin_amdgcn_rcpf(1.0f + __expf(-gs[e])) * us[e];
          if ((v - 2) * 64 + lane + 128 * e < 160) *(LAS u16*)(L + hpa[e]) = (u16)pk(hv, hv);
        }
      }
      BAR();
      const int fe = lane & 31, fL = 8 * xg + (fe >> 2), frr = fe & 3;
      const int oidx = ((fL & 15) >> 2) * 576 + (qq < 8 ? 128 * (qq >> 1) + 8 * (4 * (fL >> 4) + frr) + 4 * (qq & 1) + (fL & 3) : 512 + 4 * (4 * (fL >> 4) + frr) + (fL & 3));
      unsigned xres = 0;
      if (v == 3 && qq < 9) xres = ((const GAS u16*)xin)[oidx];
      const int h5o = WS_H5 + slot(qq * 8 + xg, 16384) + v * 4096;
      int fina = LP_FIN + v * 128 + (lane & 7) * 16 + g * 4;
      asm volatile("" : "+v"(xres), "+v"(fina));
      RBAR();
      if (qq < 9) {
        SLP(DLY_H5);
        v4u d[4];
#pragma unroll
        for (int k = 0; k < 4; ++k) d[k] = hl4(rws, lane * 16, h5o + k * 1024);
        unsigned pend = 15u;
#pragma unroll 1
        for (int n = 0;; ++n) {
#pragma unroll
          for (int k = 0; k < 4; ++k)
            if (pend & (1u << k)) { if (__all((strip4(d[k], tb) & 0xFFu) == 0)) pend &= ~(1u << k); }
          if (pend == 0 || n >= GSPIN) break;
#pragma unroll
          for (int k = 0; k < 4; ++k)
            if (pend & (1u << k)) d[k] = hl4(rws, lane * 16, h5o + k * 1024);
        }
        f32x4 s = (__builtin_bit_cast(f32x4, d[0]) + __builtin_bit_cast(f32x4, d[1])) + (__builtin_bit_cast(f32x4, d[2]) + __builtin_bit_cast(f32x4, d[3]));
#pragma unroll
        for (int r = 0; r < 4; ++r) s[r] += dppz<0x128>(s[r]);
        const auto x01 = __builtin_amdgcn_permlane16_swap(__float_as_uint(s[0]), __float_as_uint(s[1]), false, false);
        const auto x23 = __builtin_amdgcn_permlane16_swap(__float_as_uint(s[2]), __float_as_uint(s[3]), false, false);
        const auto z = __builtin_amdgcn_permlane32_swap(__float_as_uint(__uint_as_float(x01[0]) + __uint_as_float(x01[1])),
                                                        __float_as_uint(__uint_as_float(x23[0]) + __uint_as_float(x23[1])), false, false);
        *(LAS float*)(L + fina) = __uint_as_float(z[0]) + __uint_as_float(z[1]);
        BAR();
        if (v == 3) {
          float y = bf2f(xres);
#pragma unroll
          for (int k = 0; k < 4; ++k) y += *(LAS float*)(L + LP_FIN + k * 128 + fe * 4);
          if (lane < 32) ((GAS u16*)out)[oidx] = (u16)pk(y, y);
          if (qq == 0 && xg == 0) st2(v2u{tlo, thi}, rws, lane == 0 ? 0 : OOB, WS_W);
        }
      }
    }
    STAMP_OUT();
    return;
  }

  const int ab = xg >> 1;
  const int r32 = lane & 31, hh = lane >> 5;
  v2u wa_;
  if (w < 4) {
    const rsrc_t rc = mk(ckv, 4 * 4096 * 512 * 2);
    v2u wb_;
    asm volatile("buffer_load_dwordx2 %0, off, %1, %2 sc1" : "=v"(wb_) : "s"(rws), "s"(WS_W) : "memory");
    {
      int vo[4];
#pragma unroll
      for (int k = 0; k < 4; ++k) vo[k] = g * 1024 + ((i16 ^ ((g << 2) | k)) * 16);
      const int so = (idx * 128 + 32 * w) * 1024;
      SLP(DLY_C);
#pragma unroll
      for (int n = 0; n < 32; ++n) {
        if (n >= PACE_C) VMCNT(PACE_C - 1);
        dma16(rc, vo[(n >> 2) & 3], so + (n >> 2) * 4096 + (n & 3) * 256, lbase + (n & 3) * 32768 + (32 * w + 4 * (n >> 2)) * 256);
      }
    }
    RBAR();
    asm volatile("" : "+v"(wb_));
    wa_ = wb_;
  } else {
    wa_ = hl2(rws, 0, WS_W);
    if (w == 4) {
      v2u xr[9];
#pragma unroll
      for (int t = 0; t < 9; ++t) xr[t] = ld2<0>(rx, g * 1152 + i16 * 8, t * 128);
      const rsrc_t rq = mk(wq, 576 * 4096 * 2);
      const int vo = 2 * g * 8192 + i16 * 16, so = 8 * jq * 8192 + hd * 256;
      v4u a[18][2];
#pragma unroll
      for (int o = 0; o < 18; ++o) { a[o][0] = ld4<NT>(rq, vo, so + (32 * o) * 8192); a[o][1] = ld4<NT>(rq, vo, so + (32 * o + 1) * 8192); }
      SCHED();
      float s = 0.f, q = 0.f;
#pragma unroll
      for (int t = 0; t < 9; ++t) {
        *(LAS v2u*)(L + LA_QA + g * 1168 + i16 * 8 + t * 128) = xr[t];
        s = dot2(xr[t].x, 0x3F803F80u, s); q = dot2(xr[t].x, xr[t].x, q);
        s = dot2(xr[t].y, 0x3F803F80u, s); q = dot2(xr[t].y, xr[t].y, q);
      }
      FENCE();
      unsigned xp[18];
#pragma unroll
      for (int o = 0; o < 18; ++o) xp[o] = *(LAS unsigned*)(L + LA_QA + bj * 1168 + (4 * jq + 16 * o + g) * 4);
      const float mean = row_sum(s) * (1.0f / 576.0f), msq = row_sum(q) * (1.0f / 576.0f);
      const float rs0 = __builtin_amdgcn_rsqf(fmaxf(fmaf(-mean, mean, msq), 0.f) + 1e-5f);
      const float rstd = __uint_as_float(__builtin_amdgcn_ds_bpermute(bj * 64, __float_as_uint(rs0)));
      const float nb = __uint_as_float(__builtin_amdgcn_ds_bpermute(bj * 64, __float_as_uint(-mean * rs0)));
      v4u Bq[18];
#pragma unroll
      for (int o = 0; o < 18; ++o) {
        Bq[o] = bdiag(pk(fmaf(bf2f(xp[o]), rstd, nb), fmaf(__uint_as_float(xp[o] & 0xFFFF0000u), rstd, nb)), sel);
        asm volatile("" : "+v"(Bq[o]));
      }
      const u64 tg = (((u64)wa_.y << 32) | wa_.x) + 0x9E3779B97F4A7C15ull;
      unsigned ql = (unsigned)tg, q8 = (unsigned)(tg >> 32) & 0xFFu;
      asm volatile("" : "+v"(ql), "+v"(q8));
      SCHED();
      f32x4 qx = {0.f, 0.f, 0.f, 0.f}, qy = qx;
#pragma unroll
      for (int o = 0; o < 18; ++o) {
        swap24(a[o][0], a[o][1]);
        qx = mfma16(a[o][0], Bq[o], qx);
        qy = mfma16(a[o][1], Bq[o], qy);
      }
      const int po = WS_H1 + slot(idx, 4096);
      st4(v4u{ql, genc(qx[0], q8), ql, genc(qx[1], q8)}, rws, lane * 16, po);
      st4(v4u{ql, genc(qx[2], q8), ql, genc(qx[3], q8)}, rws, lane * 16, po + 1024);
      st4(v4u{ql, genc(qy[0], q8), ql, genc(qy[1], q8)}, rws, lane * 16, po + 2048);
      st4(v4u{ql, genc(qy[2], q8), ql, genc(qy[3], q8)}, rws, lane * 16, po + 3072);
    }
    RBAR();
  }
  MAKE_TAG(wa_);
  unsigned e4[4];
#pragma unroll
  for (int k = 0; k < 4; ++k) e4[k] = lsbtag(tlo, thi, 4 * i16 + k);
  if (w < 4) {
    v4u fa[32];
    {
      const int fr = ((r32 & 3) << 2) | ((r32 >> 2) & 3);
      const int ca = (32 * w + r32) * 256;
#define QK_READ(Q) _Pragma("unroll") for (int k = 0; k < 8; ++k) fa[(Q)*8 + k] = *(LAS v4u*)(L + (Q)*32768 + ca + (((2 * k + hh) ^ fr) * 16)); SCHED();
      VMCNT(0); QK_READ(0) QK_READ(1) QK_READ(2) QK_READ(3)
    }
    int qb[4];
#pragma unroll
    for (int k = 0; k < 4; ++k) { qb[k] = LA_QA + ((lane ^ ((2 * k + hh) & 7)) * 16); asm volatile("" : "+v"(qb[k])); }
    const int q4 = i16 >> 2, lowc = 2 * (g & 1) + (cp >> 1);
    int ta[4][2];
#pragma unroll
    for (int t = 0; t < 4; ++t)
#pragma unroll
      for (int t2 = 0; t2 < 2; ++t2) {
        ta[t][t2] = w * 32768 + (8 * t2 + 4 * hh + q4) * 256 + ((((t ^ q4) << 2) | (lowc ^ (2 * t2 + hh))) * 16) + 8 * (cp & 1);
        asm volatile("" : "+v"(ta[t][t2]));
      }
    int mxa = (r32 >> 4) * 320 + (r32 & 15) * 4, pba = LA_PB + lane * 16;
    unsigned e0 = lsbtag(tlo, thi, 4 * (r32 >> 1)), e1 = lsbtag(tlo, thi, 4 * (r32 >> 1) + 1), e2 = lsbtag(tlo, thi, 4 * (r32 >> 1) + 2), e3 = lsbtag(tlo, thi, 4 * (r32 >> 1) + 3);
    const int sp = idx & 31;
    int h3v = ((hh * 4 + (r32 & 1)) ^ (sp & 5)) * QB + (r32 >> 1) * 16 + WS_H3 + (sp * 16 + ab * 4 + w) * 256;
    asm volatile("" : "+v"(mxa), "+v"(pba), "+v"(e0), "+v"(e1), "+v"(e2), "+v"(e3), "+v"(h3v));
    BAR();
    v4u cb[32];
#define TR_READ(n) do { const v2u x0_ = __builtin_bit_cast(v2u, __builtin_amdgcn_ds_read_tr16_b64_v4i16((LAS s16x4*)(L + ta[(n) >> 3][0] + ((n) & 7) * 4096))); \
    const v2u x1_ = __builtin_bit_cast(v2u, __builtin_amdgcn_ds_read_tr16_b64_v4i16((LAS s16x4*)(L + ta[(n) >> 3][1] + ((n) & 7) * 4096)));                 \
    cb[n] = v4u{x0_.x, x0_.y, x1_.x, x1_.y}; } while (0)
    f32x16 sacc;
#pragma unroll
    for (int k = 0; k < 16; ++k) sacc[k] = 0.f;
    {
      v4u fb[32];
#pragma unroll
      for (int s = 0; s < 6; ++s) fb[s] = *(LAS v4u*)(L + qb[s & 3] + s * 1024);
      SCHED();
#pragma unroll
      for (int s = 0; s < 32; ++s) {
        if (s + 6 < 32) fb[s + 6] = *(LAS v4u*)(L + qb[(s + 6) & 3] + (s + 6) * 1024);
        if (s >= 26) TR_READ(s - 26);
        sacc = mfma32(fa[s], fb[s], sacc);
        SCHED();
      }
    }
    float mw = sacc[0];
#pragma unroll
    for (int k = 1; k < 16; ++k) mw = fmaxf(mw, sacc[k]);
    mw = x32_max(mw);
    asm volatile("s_waitcnt lgkmcnt(0)" ::: "memory");
    *(LAS float*)(L + mxa + w * 32768) = mw;
    BAR();
    const float mx = gdec(genc(fmaxf(fmaxf(*(LAS float*)(L + mxa), *(LAS float*)(L + mxa + 32768)), fmaxf(*(LAS float*)(L + mxa + 65536), *(LAS float*)(L + mxa + 98304))), 0u));
    float ls = 0.f;
#pragma unroll
    for (int k = 0; k < 16; ++k) { sacc[k] = __builtin_amdgcn_exp2f(sacc[k] - mx); ls += sacc[k]; }
#pragma unroll
    for (int s2 = 0; s2 < 2; ++s2)
      *(LAS v4u*)(L + pba + (2 * w + s2) * 1024) =
          v4u{pk(sacc[8 * s2], sacc[8 * s2 + 1]), pk(sacc[8 * s2 + 2], sacc[8 * s2 + 3]), pk(sacc[8 * s2 + 4], sacc[8 * s2 + 5]), pk(sacc[8 * s2 + 6], sacc[8 * s2 + 7])};
    ls = x32_sum(ls);
    *(LAS float*)(L + mxa + 640 + w * 32768) = ls;
    BAR();
    {
      v4u pf[8];
#pragma unroll
      for (int k = 0; k < 8; ++k) pf[k] = *(LAS v4u*)(L + pba + k * 1024);
      f32x4 l4;
      if (w == 0) l4 = f32x4{*(LAS float*)(L + mxa + 640), *(LAS float*)(L + mxa + 640 + 32768), *(LAS float*)(L + mxa + 640 + 65536), *(LAS float*)(L + mxa + 640 + 98304)};
      SCHED();
      f32x16 o[4];
      unsigned p01[16], pq[16];
      const bool odd = lane & 1;
#pragma unroll
      for (int t = 0; t < 4; ++t)
#pragma unroll
        for (int k = 0; k < 16; ++k) o[t][k] = 0.f;
#pragma unroll
      for (int n = 0; n < 32; ++n) {
        if (n + 6 < 32) TR_READ(n + 6);
        o[n >> 3] = mfma32(pf[n & 7], cb[n], o[n >> 3]);
        if (n == 0 && w == 0)
          st4(v4u{tlo, genc(mx, t8), tlo, genc((l4[0] + l4[1]) + (l4[2] + l4[3]), t8)}, rws, lane < 32 ? r32 * 16 : OOB, WS_ML + slot(idx, 512));
        if (n >= 17 && n < 32 && n != 24) {
          const int j = n < 24 ? n - 17 : n - 18;
          if (j < 8) { pq[2 * j] = pk(o[0][2 * j], o[1][2 * j]); pq[2 * j + 1] = pk(o[0][2 * j + 1], o[1][2 * j + 1]); asm volatile("" : "+v"(pq[2 * j]), "+v"(pq[2 * j + 1])); }
          if (j >= 6) {
            const int k = j - 6;
            p01[2 * k] = lsbput(odd ? pq[2 * k + 1] : pq[2 * k], e0);
            p01[2 * k + 1] = __builtin_amdgcn_update_dpp(0, lsbput(odd ? pq[2 * k] : pq[2 * k + 1], e2), 0xB1, 0xf, 0xf, true);
            asm volatile("" : "+v"(p01[2 * k]), "+v"(p01[2 * k + 1]));
          }
        }
        SCHED();
      }
#pragma unroll
      for (int k = 0; k < 8; ++k) {
        const unsigned pa = pk(o[2][2 * k], o[3][2 * k]), pb = pk(o[2][2 * k + 1], o[3][2 * k + 1]);
        st4(v4u{p01[2 * k], lsbput(odd ? pb : pa, e1), p01[2 * k + 1], (unsigned)__builtin_amdgcn_update_dpp(0, lsbput(odd ? pa : pb, e3), 0xB1, 0xf, 0xf, true)},
            rws, h3v, ((2 * (k & 1) + 8 * (k >> 1)) ^ (sp & 26)) * QB);
      }
    }
    RBAR();
    RBAR();
    RBAR();
  } else {
    const int v = w - 4;
    const rsrc_t rv = mk(wuv, 512 * 4096 * 2);
    v4u uw[8];
#define TL(i) do { uw[i] = ld4<NT>(rv, g * 32 * 8192 + i16 * 16, (128 * jq + 8 * v + ((i) ^ (hd & 1))) * 8192 + hd * 256); SCHED(); } while (0)
    {
      v4u qa[8];
      SLP(DLY_QA);
#pragma unroll
      for (int k = 0; k < 8; ++k) qa[k] = hl4(rws, lane * 16, WS_QA + ab * 2048 + slot(8 * v + k, 1024));
      SCHED();
#pragma unroll 1
      for (int n = 0;; ++n) {
        unsigned bad = 0;
#pragma unroll
        for (int k = 0; k < 8; ++k) bad |= strip4(qa[k], e4);
        if (__all((bad & LSBM) == 0) || n >= GSPIN) break;
        SLP(DLY_R2);
#pragma unroll
        for (int k = 0; k < 8; ++k) qa[k] = hl4(rws, lane * 16, WS_QA + ab * 2048 + slot(8 * v + k, 1024));
      }
#pragma unroll
      for (int k = 0; k < 8; ++k) *(LAS v4u*)(L + LA_QA + (lane >> 1) * 1024 + ((((lane & 1) * 32 + 8 * v + k) ^ (lane & 7)) * 16)) = qa[k];
    }
    BAR();
#pragma unroll
    for (int i = 0; i < 4; ++i) TL(i);
    RBAR();
#pragma unroll
    for (int i = 4; i < 8; ++i) TL(i);
    RBAR();
    const int e7 = lane & 7, q4 = i16 >> 2;
    int cta = LA_CTX + v * 288 + (32 * g + e7) * 2, ctr = LA_CTX + cp * 288 + g * 16;
    int h4a = ((2 * (g & 1) + (i16 & 1)) * 16 + (2 * (g >> 1) + (i16 >> 3)) * 4 + ((i16 >> 1) & 3)) * 16 + WS_H4 + slot(hd * 4 + v, 4096) + jq * 1024;
    const unsigned src = (lane & 8) ? 0x0302u : 0x0100u, s1 = (e7 & 1) ? ((src << 16) | 0x0C0Cu) : (0x0C0C0000u | src);
    unsigned s8[4];
    int uwa[4], bva[2][2];
#pragma unroll
    for (int k = 0; k < 4; ++k) {
      s8[k] = (e7 >> 1) == k ? s1 : 0x0C0C0C0Cu;
      uwa[k] = LA_UVW + (32 * v + g) * 256 + ((i16 ^ ((g << 2) | k)) * 16);
      bva[k >> 1][k & 1] = LA_UVW + (8 * g + 4 * (k & 1) + q4) * 256 + (((2 * (2 * v + (k >> 1)) + (cp >> 1)) ^ ((q4 << 2) | ((2 * g + (k & 1)) & 3))) * 16) + 8 * (cp & 1);
      asm volatile("" : "+v"(s8[k]), "+v"(uwa[k]), "+v"(bva[k >> 1][k & 1]));
    }
    asm volatile("" : "+v"(cta), "+v"(ctr), "+v"(h4a));
    const int h3l = ((hd & 3) ^ g) * QB + g * 4096 + i16 * 16;
#define ML_LOAD() hl4(rws, slot(r32, 512), WS_ML + v * 1024 + hd * 16)
#define ML_OK(V) __all((((V).x ^ tlo) | ((V).z ^ tlo) | ((((V).y ^ t8) | ((V).w ^ t8)) & 0xFFu)) == 0)
#define H3_LOADS() _Pragma("unroll") for (int k = 0; k < 8; ++k) d[k] = hl4(rws, h3l, WS_H3 + ((hd >> 2) ^ k) * 4 * QB + k * 16384 + (v * 4 + jq) * 256); SCHED()
    v4u d[8], bk[8];
    v4u ml;
    VMCNT(0);
    RBAR();
#pragma unroll
    for (int k = 0; k < 8; ++k) *(LAS v4u*)(L + uwa[k & 3] + k * 1024) = uw[k];
    BAR();
    v4u bv[2][4];
#pragma unroll
    for (int t = 0; t < 2; ++t)
#pragma unroll
      for (int ks = 0; ks < 4; ++ks) {
        const v2u x0 = __builtin_bit_cast(v2u, __builtin_amdgcn_ds_read_tr16_b64_v4i16((LAS s16x4*)(L + bva[t][0] + ks * 8192)));
        const v2u x1 = __builtin_bit_cast(v2u, __builtin_amdgcn_ds_read_tr16_b64_v4i16((LAS s16x4*)(L + bva[t][1] + ks * 8192)));
        bv[t][ks] = v4u{x0.x, x0.y, x1.x, x1.y};
      }
    SCHED();
    {
      SLP(DLY_M0);
      for (int n = 0;; ++n) {
        ml = ML_LOAD();
        if (ML_OK(ml) || n >= GSPIN) break;
        SLP(DLY_RM);
      }
      SLP(DLY_H3);
      H3_LOADS();
      const float m = gdec(ml.y), M = x16_max(row_max(m));
      const float e = __builtin_amdgcn_exp2f(m - M);
      const float al = e * LSB_COMP * __builtin_amdgcn_rcpf(x16_sum(row_sum(e * gdec(ml.w))));
      const unsigned ah = pk(al, al) & 0xFFFFu;
      const unsigned hl = ah | (pk(al - bf2f(ah), 0.f) << 16);
#pragma unroll
      for (int k = 0; k < 8; ++k) { bk[k] = bdiag(__builtin_amdgcn_ds_bpermute((4 * k + g) * 4, hl), s8); asm volatile("" : "+v"(bk[k])); }
      f32x4 c;
#pragma unroll 1
      for (int n = 0;; ++n) {
        unsigned bad = 0;
        c = f32x4{0.f, 0.f, 0.f, 0.f};
#pragma unroll
        for (int k = 0; k < 8; ++k) { bad |= strip4(d[k], e4); c = mfma16(d[k], bk[k], c); }
        if (__all((bad & LSBM) == 0) || n >= GSPIN) break;
#pragma unroll
        for (int k = 0; k < 8; ++k) d[k] = hl4(rws, h3l, WS_H3 + ((hd >> 2) ^ k) * 4 * QB + k * 16384 + (v * 4 + jq) * 256);
      }
#pragma unroll
      for (int r = 0; r < 4; ++r) {
        const float y = c[r] + dppz<0x128>(c[r]);
        *(LAS u16*)(L + cta + 16 * r) = (u16)pk(y, y);
      }
    }
    BAR();
    {
      v4u af[4];
#pragma unroll
      for (int ks = 0; ks < 4; ++ks) af[ks] = *(LAS v4u*)(L + ctr + 64 * ks);
      SCHED();
      f32x4 c0 = {0.f, 0.f, 0.f, 0.f}, c1 = c0;
#pragma unroll
      for (int ks = 0; ks < 4; ++ks) { c0 = mfma16(af[ks], bv[0][ks], c0); c1 = mfma16(af[ks], bv[1][ks], c1); }
      const f32x4 ct = g < 2 ? c0 : c1;
      const float x0 = (g & 1) ? ct[2] : ct[0], x1 = (g & 1) ? ct[3] : ct[1];
      const float n0 = dppz<0xB1>(x0), n1 = dppz<0xB1>(x1);
      st4(v4u{tlo, genc((lane & 1) ? n1 : x0, t8), tlo, genc((lane & 1) ? x1 : n0, t8)}, rws, h4a, 0);
    }
  }
  STAMP_OUT();
}

extern "C" __global__ void fused_decode_end() {}

extern "C" void kernel_launch(void* const* d_in, const int* in_sizes, int n_in, void* d_out, int out_size, void* d_ws, size_t ws_size,
                              hipStream_t stream) {
  (void)hipFuncSetAttribute((const void*)fused_decode, hipFuncAttributeMaxDynamicSharedMemorySize, LDS_BYTES);
  fused_decode<<<256, 512, LDS_BYTES, stream>>>((const u16*)d_in[0], (const u16*)d_in[1], (const u16*)d_in[2], (const u16*)d_in[3],
                                                (const u16*)d_in[4], (const u16*)d_in[5], (const u16*)d_in[7], (const u16*)d_in[8],
                                                (const u16*)d_in[9], (u16*)d_out, (char*)d_ws);
}
```

```cpp
#define DIAG_SPAN
#define PW 0
#define PWHAT 0
#include <hip/hip_runtime.h>
#include <stdint.h>

typedef unsigned short u16;
typedef unsigned long long u64;
typedef unsigned v4u __attribute__((ext_vector_type(4)));
typedef unsigned v2u __attribute__((ext_vector_type(2)));
typedef float f32x2 __attribute__((ext_vector_type(2)));
typedef float f32x4 __attribute__((ext_vector_type(4)));
typedef float f32x16 __attribute__((ext_vector_type(16)));
typedef short s16x4 __attribute__((ext_vector_type(4)));
typedef __bf16 bf16x8 __attribute__((ext_vector_type(8)));
typedef __bf16 bf16x2 __attribute__((ext_vector_type(2)));
typedef __amdgpu_buffer_rsrc_t rsrc_t;
#define GAS __attribute__((address_space(1)))
#define LAS __attribute__((address_space(3)))
#define DI __device__ __forceinline__

#define BLOCK_ID blockIdx.x
constexpr int NT = 2;
constexpr int PACE_C = 7;
constexpr int PACE_K = 4;
constexpr int PACE_G = 4;
constexpr int PACE_W = 6;
#define DLY_C 40
#define DLY_K 16
#define DLY_H1 2
#define DLY_R1 7
#define DLY_QA 8
#define DLY_R2 12
#define DLY_M0 12
#define DLY_RM 4
#define DLY_H3 1
#define DLY_H7 22
#define DLY_R7 2
#define DLY_R4 2
#define DLY_H5 18
constexpr unsigned T_F = 350, T_H7 = 915;
#define SLP(N) __builtin_amdgcn_s_sleep(N)

constexpr int SC1 = 16;
constexpr int GSPIN = 1 << 13;

constexpr int QB = 131072, WIN = 16 * QB;
constexpr int WS_H1 = 0;
constexpr int WS_H4 = 32768;
constexpr int WS_QA = 65536;
constexpr int WS_ML = 73728;
constexpr int WS_W = 81920;
constexpr int WS_H7 = WIN;
constexpr int WS_H5 = 2 * WIN;
constexpr int WS_H3 = 3 * WIN;
DI constexpr int slot(int p, int size) { return (p & 15) * QB + (p >> 4) * size; }
DI constexpr int blk5(int n) { return (n & 1) * 8 * QB + (n >> 1) * 16384; }
constexpr int WS_END = WS_H3 + 128 * 32768;
constexpr int OOB = (int)0x80000000;

constexpr int LP_XD = 5120, LP_QB = 15360, LP_QO = 16896, LP_RED = 18432, LP_HP = 34816, LP_B8 = 35840, LP_FIN = 39936, LP_UK = 40960;
constexpr int LA_QA = 131072, LA_PB = LA_QA + 1024, LA_CTX = LA_QA + 9216, LA_UVW = 0;
constexpr int LDS_BYTES = 163840;

DI rsrc_t mk(const void* p, int bytes) { return __builtin_amdgcn_make_buffer_rsrc((void*)p, (short)0, bytes, 0x00020000); }
template <int AUX> DI v4u ld4(rsrc_t r, int vo, int so) { return __builtin_amdgcn_raw_buffer_load_b128(r, vo, so, AUX); }
template <int AUX> DI v2u ld2(rsrc_t r, int vo, int so) { return __builtin_amdgcn_raw_buffer_load_b64(r, vo, so, AUX); }
DI v4u hl4(rsrc_t r, int vo, int so) { asm volatile("" : "+v"(vo)); return __builtin_amdgcn_raw_buffer_load_b128(r, vo, so, SC1); }
DI v2u hl2(rsrc_t r, int vo, int so) { asm volatile("" : "+v"(vo)); return __builtin_amdgcn_raw_buffer_load_b64(r, vo, so, SC1); }
DI void touch(rsrc_t r, int vo, int so) { v4u t; asm volatile("buffer_load_dwordx4 %0, %1, %2, %3 offen sc1\n\ts_waitcnt vmcnt(0)" : "=&v"(t) : "v"(vo), "s"(r), "s"(so) : "memory"); }
DI void st4(v4u v, rsrc_t r, int vo, int so) { __builtin_amdgcn_raw_buffer_store_b128(v, r, vo + so, 0, SC1); }
DI void st2(v2u v, rsrc_t r, int vo, int so) { __builtin_amdgcn_raw_buffer_store_b64(v, r, vo, so, SC1); }
DI void dma16(rsrc_t r, int vo, int so, unsigned lds) {
  asm volatile("s_mov_b32 m0, %0\n\ts_nop 0\n\tbuffer_load_dwordx4 %1, %2, %3 offen nt lds" ::"s"(lds), "v"(vo), "s"(r), "s"(so) : "memory");
}
#define VMCNT(n) asm volatile("s_waitcnt vmcnt(%0)" ::"n"(n) : "memory")
#define FENCE() asm volatile("" ::: "memory")
#define SCHED() __builtin_amdgcn_sched_barrier(0)

DI float bf2f(unsigned v) { return __uint_as_float(v << 16); }
DI unsigned pk(float a, float b) { f32x2 f = {a, b}; return __builtin_bit_cast(unsigned, __builtin_convertvector(f, bf16x2)); }
DI f32x4 mfma16(v4u a, v4u b, f32x4 c) {
  return __builtin_amdgcn_mfma_f32_16x16x32_bf16(__builtin_bit_cast(bf16x8, a), __builtin_bit_cast(bf16x8, b), c, 0, 0, 0);
}
DI f32x4 mfma16k16(v2u a, v2u b, f32x4 c) {
  return __builtin_amdgcn_mfma_f32_16x16x16bf16_1k(__builtin_bit_cast(s16x4, a), __builtin_bit_cast(s16x4, b), c, 0, 0, 0);
}
DI f32x16 mfma32(v4u a, v4u b, f32x16 c) {
  return __builtin_amdgcn_mfma_f32_32x32x16_bf16(__builtin_bit_cast(bf16x8, a), __builtin_bit_cast(bf16x8, b), c, 0, 0, 0);
}
template <int CTRL, int RM> DI float dppf(float old, float v) {
  return __builtin_bit_cast(float, __builtin_amdgcn_update_dpp(__builtin_bit_cast(int, old), __builtin_bit_cast(int, v), CTRL, RM, 0xf, false));
}
template <int CTRL> DI float dppz(float v) {
  return __builtin_bit_cast(float, __builtin_amdgcn_update_dpp(0, __builtin_bit_cast(int, v), CTRL, 0xf, 0xf, true));
}
DI float row_sum(float v) {
  v += dppz<0xB1>(v); v += dppz<0x4E>(v); v += dppz<0x124>(v); v += dppz<0x128>(v);
  return v;
}
DI float dot2(unsigned a, unsigned b, float c) {
  return __builtin_amdgcn_fdot2_f32_bf16(__builtin_bit_cast(bf16x2, a), __builtin_bit_cast(bf16x2, b), c, false);
}
DI float row_max(float v) {
  v = fmaxf(v, dppf<0xB1, 0xf>(v, v)); v = fmaxf(v, dppf<0x4E, 0xf>(v, v));
  v = fmaxf(v, dppf<0x124, 0xf>(v, v)); v = fmaxf(v, dppf<0x128, 0xf>(v, v));
  return v;
}
DI float x16_sum(float v) { auto r = __builtin_amdgcn_permlane16_swap(__float_as_uint(v), __float_as_uint(v), false, false); return __uint_as_float(r[0]) + __uint_as_float(r[1]); }
DI float x32_sum(float v) { auto r = __builtin_amdgcn_permlane32_swap(__float_as_uint(v), __float_as_uint(v), false, false); return __uint_as_float(r[0]) + __uint_as_float(r[1]); }
DI float x16_max(float v) { auto r = __builtin_amdgcn_permlane16_swap(__float_as_uint(v), __float_as_uint(v), false, false); return fmaxf(__uint_as_float(r[0]), __uint_as_float(r[1])); }
DI float x32_max(float v) { auto r = __builtin_amdgcn_permlane32_swap(__float_as_uint(v), __float_as_uint(v), false, false); return fmaxf(__uint_as_float(r[0]), __uint_as_float(r[1])); }

#define RBAR() asm volatile("s_barrier" ::: "memory")
#define BAR() asm volatile("s_waitcnt lgkmcnt(0)\n\ts_barrier" ::: "memory")
DI unsigned genc(float v, unsigned t8) { return ((__float_as_uint(v) + 0x80u) & 0xFFFFFF00u) | t8; }
DI float gdec(unsigned hi) { return __uint_as_float(hi & 0xFFFFFF00u); }

#define STAMP_DECL const u64 strt0_ = __builtin_amdgcn_s_memrealtime()
#define STAMP_OUT() do { const u64 r1_ = __builtin_amdgcn_s_memrealtime(); \
    if (lane == 0) { GAS u64* so_ = (GAS u64*)(ws + WS_END) + ((size_t)blockIdx.x * 8 + w) * 2; so_[0] = strt0_; so_[1] = PW == 0 ? r1_ : t_start; } } while (0)
DI void wait_clock(u64 t0, unsigned ticks) {
#pragma unroll 1
  for (int n = 0; n < 4096 && (unsigned)(__builtin_amdgcn_s_memrealtime() - t0) < ticks; ++n) __builtin_amdgcn_s_sleep(1);
}

DI v4u bdiag(unsigned P, const unsigned (&s)[4]) {
  v4u b;
  b.x = __builtin_amdgcn_perm(P, P, s[0]); b.y = __builtin_amdgcn_perm(P, P, s[1]);
  b.z = __builtin_amdgcn_perm(P, P, s[2]); b.w = __builtin_amdgcn_perm(P, P, s[3]);
  return b;
}
DI void bdiag_sel(int cp, unsigned (&s)[4]) {
  const unsigned lo = (cp & 1) ? 0x01000C0Cu : 0x0C0C0100u, hi = (cp & 1) ? 0x03020C0Cu : 0x0C0C0302u, z = 0x0C0C0C0Cu;
  s[0] = cp < 2 ? lo : z; s[1] = cp < 2 ? z : lo; s[2] = cp < 2 ? hi : z; s[3] = cp < 2 ? z : hi;
}

DI unsigned lsbtag(unsigned tlo, unsigned thi, int p) { return (((p < 32 ? tlo : thi) >> (p & 31)) & 1u) | (((tlo ^ (unsigned)p) & 1u) << 16); }
constexpr unsigned LSBM = 0x00010001u;
DI unsigned lsbput(unsigned v, unsigned e) { return (v & ~LSBM) | e; }
DI unsigned strip4(v4u& d, const unsigned (&e)[4]) { d.x ^= e[0]; d.y ^= e[1]; d.z ^= e[2]; d.w ^= e[3]; return (d.x | d.y) | (d.z | d.w); }
DI void swap24(v4u& a, v4u& b) {
  unsigned az = a.z, aw = a.w, bx = b.x, by = b.y;
  asm volatile("v_swap_b32 %0, %2\n\tv_swap_b32 %1, %3\n\ts_nop 1" : "+v"(az), "+v"(aw), "+v"(bx), "+v"(by));
  a.z = az; a.w = aw; b.x = bx; b.y = by;
}
constexpr float LSB_COMP = 1.0028f;
DI v4u tag8(f32x4 c, const unsigned (&t)[4]) {
  return v4u{(__float_as_uint(c[0]) & 0xFFFFFF00u) | t[0], (__float_as_uint(c[1]) & 0xFFFFFF00u) | t[1],
             (__float_as_uint(c[2]) & 0xFFFFFF00u) | t[2], (__float_as_uint(c[3]) & 0xFFFFFF00u) | t[3]};
}
extern "C" __global__ void __launch_bounds__(512, 2)
fused_decode(const u16* __restrict__ xin, const u16* __restrict__ ckv, const u16* __restrict__ wq, const u16* __restrict__ wuk,
             const u16* __restrict__ wuv, const u16* __restrict__ wo, const u16* __restrict__ w1, const u16* __restrict__ w3,
             const u16* __restrict__ w2, u16* __restrict__ out, char* ws) {
  extern __shared__ __attribute__((aligned(16))) char smem[];
  LAS char* L = (LAS char*)smem;
  const unsigned lbase = (unsigned)(size_t)smem;
  const int lane = threadIdx.x & 63, w = __builtin_amdgcn_readfirstlane(threadIdx.x >> 6);
  const int bid = BLOCK_ID, xg = bid & 7, qd_ = bid >> 3, qq = qd_ & 15, idx = xg * 16 + qq, hd = idx >> 2, jq = idx & 3;
  const int i16 = lane & 15, g = lane >> 4, bj = i16 >> 2, cp = lane & 3;
  STAMP_DECL;
  if (w == 7) {
    u64 pc; unsigned back, len;
    asm volatile("s_getpc_b64 %0\n.Lpf_here:\n\ts_mov_b32 %1, .Lpf_here-fused_decode\n\ts_mov_b32 %2, fused_decode_end-fused_decode" : "=s"(pc), "=s"(back), "=s"(len));
    const rsrc_t rcode = mk((const void*)(pc - back), (int)min(len, 32768u));
    v4u dummy;
    asm volatile("s_nop 4\n\tbuffer_load_dwordx4 %0, %1, %2, 0 offen\n\ts_waitcnt vmcnt(0)" : "=&v"(dummy) : "v"(qd_ * 1024 + lane * 16), "s"(rcode) : "memory");
  }
  asm volatile("" ::"s"(xin), "s"(ckv), "s"(wq), "s"(wuk), "s"(wuv), "s"(wo), "s"(w1), "s"(w3), "s"(w2), "s"(out), "s"(ws));
  u64 t_start;
  asm volatile("s_memrealtime %0\n\ts_waitcnt lgkmcnt(0)" : "=s"(t_start) : "s"(ws) : "memory");
  const rsrc_t rws = mk(ws, WS_END);
  const rsrc_t rx = mk(xin, 4 * 576 * 2);
  unsigned sel[4];
  bdiag_sel(cp, sel);
#define MAKE_TAG(W) const u64 tag_ = (((u64)(W).y << 32) | (W).x) + 0x9E3779B97F4A7C15ull;  \
  const unsigned tlo = (unsigned)tag_, thi = (unsigned)(tag_ >> 32), t8 = thi & 0xFFu;        \
  const unsigned tb[4] = {tlo & 0xFFu, (tlo >> 8) & 0xFFu, (tlo >> 16) & 0xFFu, tlo >> 24}
  const int vo5 = (lane >> 3) * QB + (lane & 7) * 16;

  if (qd_ >= 16) {
    const int so5 = WS_H5 + idx * 128;
    if (w < 4) {
      v4u ka[2][4];
      v2u wv_;
      asm volatile("buffer_load_dwordx2 %0, off, %1, %2 sc1" : "=v"(wv_) : "s"(rws), "s"(WS_W) : "memory");
      {
        const rsrc_t rk = mk(wuk, 512 * 4096 * 2);
        const int ukx = 4 * (hd & 1);
        SLP(DLY_K);
#pragma unroll
        for (int n = 0; n < 8; ++n) {
          if (n >= PACE_K) VMCNT(PACE_K - 1);
          dma16(rk, g * 8192 + ((i16 ^ ((g << 2) | (n & 3))) * 16), (128 * jq + 32 * w + 4 * (n ^ ukx)) * 8192 + hd * 256, lbase + LP_UK + (8 * w + (n ^ ukx)) * 1024);
        }
        VMCNT(0);
        const int fr = ((i16 & 3) << 2) | (i16 >> 2);
#pragma unroll
        for (int t = 0; t < 2; ++t)
#pragma unroll
          for (int s = 0; s < 4; ++s) {
            ka[t][s] = *(LAS v4u*)(L + LP_UK + (32 * w + 16 * t + i16) * 256 + (((4 * s + g) ^ fr) * 16));
            asm volatile("" : "+v"(ka[t][s]));
          }
      }
      asm volatile("" : "+v"(wv_));
      MAKE_TAG(wv_);
      unsigned eq[4];
#pragma unroll
      for (int k = 0; k < 4; ++k) { eq[k] = lsbtag(tlo, thi, 4 * i16 + k); asm volatile("" : "+v"(eq[k])); }
      SLP(DLY_H1);
      {
        const int blk = w & 1, half = w >> 1;
        float q0, q1;
        for (int n = 0;; ++n) {
          v4u p[4];
#pragma unroll
          for (int jj = 0; jj < 4; ++jj) p[jj] = hl4(rws, lane * 16, WS_H1 + slot(hd * 4 + jj, 4096) + (blk * 2 + half) * 1024);
          unsigned bad = 0;
          q0 = 0.f; q1 = 0.f;
#pragma unroll
          for (int jj = 0; jj < 4; ++jj) {
            bad |= (p[jj].x ^ tlo) | (p[jj].z ^ tlo) | (((p[jj].y ^ t8) | (p[jj].w ^ t8)) & 0xFFu);
            q0 += gdec(p[jj].y); q1 += gdec(p[jj].w);
          }
          if (__all(bad == 0) || n >= GSPIN) break;
          SLP(DLY_R1);
        }
        const unsigned qp = pk(q0, q1);
        const int d0 = 32 * g + 16 * half + 4 * blk + cp;
        *(LAS u16*)(L + LP_QB + bj * 320 + d0 * 2) = (u16)qp;
        *(LAS u16*)(L + LP_QB + bj * 320 + (d0 + 8) * 2) = (u16)(qp >> 16);
      }
      BAR();
      {
        v4u qf[4];
#pragma unroll
        for (int s = 0; s < 4; ++s) qf[s] = *(LAS v4u*)(L + LP_QB + cp * 320 + s * 64 + g * 16);
        f32x4 c0 = {0.f, 0.f, 0.f, 0.f}, c1 = c0;
#pragma unroll
        for (int s = 0; s < 4; ++s) { c0 = mfma16(ka[0][s], qf[s], c0); c1 = mfma16(ka[1][s], qf[s], c1); }
        const float sc = 0.08838834764831845f * 1.4426950408889634f * LSB_COMP;
        if (i16 < 4) {
          *(LAS v2u*)(L + LP_QO + i16 * 256 + (32 * w + 4 * g) * 2) = v2u{pk(c0[0] * sc, c0[1] * sc), pk(c0[2] * sc, c0[3] * sc)};
          *(LAS v2u*)(L + LP_QO + i16 * 256 + (32 * w + 16 + 4 * g) * 2) = v2u{pk(c1[0] * sc, c1[1] * sc), pk(c1[2] * sc, c1[3] * sc)};
        }
      }
      BAR();
      if (w == 0) {
        const v4u v = *(LAS v4u*)(L + LP_QO + lane * 16);
        st4(v4u{lsbput(v.x, eq[0]), lsbput(v.y, eq[1]), lsbput(v.z, eq[2]), lsbput(v.w, eq[3])}, rws, g * 2048 + i16 * 16, WS_QA + slot(hd, 1024) + jq * 256);
      }
      BAR();
      const rsrc_t r2 = mk(w2, 4608 * 576 * 2);
      const int so2 = (xg * 576 + 36 * qq) * 1152;
      const rsrc_t ro = mk(wo, 4096 * 576 * 2);
      const int soo = (hd * 128 + 32 * jq) * 1152;
      v4u a2[9][2];
      v2u a8[3][2];
      {
        const int vo4 = 2 * g * 1152 + i16 * 16, vo4m = g < 2 ? vo4 : OOB;
        const int vo8 = 2 * g * 1152 + i16 * 8 + 1024, vo8m = (g < 2 && w == 0) ? vo8 : OOB;
#pragma unroll
        for (int n = 0; n < 24; ++n) {
          if (n >= PACE_W) VMCNT(PACE_W - 1);
          if (n == PACE_W) { SCHED(); RBAR(); SLP(1); }
          const int o = n >> 1, r = n & 1;
          if (n < 10) a2[o][r] = ld4<NT>(r2, o < 4 ? vo4 : vo4m, so2 + (8 * o + r) * 1152 + 256 * w);
          else if (n < 12) a8[0][r] = ld2<NT>(r2, vo8, so2 + (8 * w + r) * 1152);
          else if (n < 14) a8[1][r] = ld2<NT>(r2, vo8m, so2 + (32 + r) * 1152);
          else if (n < 22) a2[o - 2][r] = ld4<NT>(ro, vo4, soo + (8 * (o - 7) + r) * 1152 + 256 * w);
          else a8[2][r] = ld2<NT>(ro, vo8, soo + (8 * w + r) * 1152);
          SCHED();
        }
      }
#pragma unroll
      for (int o = 0; o < 5; ++o) swap24(a2[o][0], a2[o][1]);
      RBAR();
      {
        const v4u h4 = *(LAS v4u*)(L + LP_HP + (bj * 4 + g) * 48);
        const unsigned h5 = *(LAS unsigned*)(L + LP_HP + (bj * 4 + g) * 48 + 16);
        const unsigned hw = *(LAS unsigned*)(L + LP_HP + (bj * 4 + g) * 48 + 4 * w);
        f32x4 c8 = {0.f, 0.f, 0.f, 0.f}, cx = c8, cy = c8;
        c8 = mfma16(v4u{a8[0][0].x, a8[0][0].y, a8[0][1].x, a8[0][1].y}, bdiag(hw, sel), c8);
        c8 = mfma16(v4u{a8[1][0].x, a8[1][0].y, a8[1][1].x, a8[1][1].y}, bdiag(h5, sel), c8);
#pragma unroll
        for (int o = 0; o < 5; ++o) {
          const v4u B = bdiag(o == 0 ? h4.x : o == 1 ? h4.y : o == 2 ? h4.z : o == 3 ? h4.w : h5, sel);
          cx = mfma16(a2[o][0], B, cx);
          cy = mfma16(a2[o][1], B, cy);
        }
#pragma unroll
        for (int o = 5; o < 9; ++o) swap24(a2[o][0], a2[o][1]);
        asm volatile("" : "+v"(cx), "+v"(cy), "+v"(c8));
        int bpa = (bj * 16 + g) * 4;
        asm volatile("" : "+v"(bpa));
        float y0, y1;
        for (int n = 0;; ++n) {
          v4u p[4];
#pragma unroll
          for (int jj = 0; jj < 4; ++jj) p[jj] = hl4(rws, lane * 16, WS_H4 + slot(idx, 4096) + jj * 1024);
          unsigned bad = 0;
          y0 = 0.f; y1 = 0.f;
#pragma unroll
          for (int jj = 0; jj < 4; ++jj) {
            bad |= (p[jj].x ^ tlo) | (p[jj].z ^ tlo) | (((p[jj].y ^ t8) | (p[jj].w ^ t8)) & 0xFFu);
            y0 += gdec(p[jj].y); y1 += gdec(p[jj].w);
          }
          if (__all(bad == 0) || n >= GSPIN) break;
          SLP(DLY_R4);
        }
        const unsigned yp = pk(y0, y1);
        const unsigned pw = __builtin_amdgcn_ds_bpermute(bpa + 16 * w, yp);
        unsigned po[4];
#pragma unroll
        for (int o = 0; o < 4; ++o) po[o] = __builtin_amdgcn_ds_bpermute(bpa + 16 * o, yp);
        c8 = mfma16(v4u{a8[2][0].x, a8[2][0].y, a8[2][1].x, a8[2][1].y}, bdiag(pw, sel), c8);
#pragma unroll
        for (int o = 0; o < 4; ++o) {
          const v4u B = bdiag(po[o], sel);
          cx = mfma16(a2[5 + o][0], B, cx);
          cy = mfma16(a2[5 + o][1], B, cy);
        }
        *(LAS f32x4*)(L + LP_B8 + w * 1024 + lane * 16) = c8;
        st4(tag8(cx, tb), rws, vo5, so5 + blk5(2 * w));
        st4(tag8(cy, tb), rws, vo5, so5 + blk5(2 * w + 1));
        BAR();
        if (w == 0) {
          f32x4 s = *(LAS f32x4*)(L + LP_B8 + lane * 16);
#pragma unroll
          for (int k = 1; k < 4; ++k) s += *(LAS f32x4*)(L + LP_B8 + k * 1024 + lane * 16);
          st4(tag8(s, tb), rws, vo5, so5 + blk5(8));
        }
      }
      if (qq < 9) RBAR();
    } else {
      const int v = w - 4;
      unsigned xn[8];
      const v2u wv_ = hl2(rws, 0, WS_W);
      v4u pt1;
      asm volatile("buffer_load_dwordx4 %0, %1, %2, %3 offen sc1" : "=v"(pt1) : "v"(lane * 16), "s"(rws), "s"(WS_H1 + slot(idx, 4096) + v * 1024) : "memory");
      {
        const v4u x0 = ld4<0>(rx, lane * 16, v * 1152), x1 = ld4<0>(rx, lane < 8 ? lane * 16 : OOB, v * 1152 + 1024);
        float s = 0.f, q = 0.f;
#pragma unroll
        for (int k = 0; k < 4; ++k) {
          s = dot2(x0[k], 0x3F803F80u, s); q = dot2(x0[k], x0[k], q);
          s = dot2(x1[k], 0x3F803F80u, s); q = dot2(x1[k], x1[k], q);
        }
        const float mean = x32_sum(x16_sum(row_sum(s))) * (1.0f / 576.0f);
        const float msq = x32_sum(x16_sum(row_sum(q))) * (1.0f / 576.0f);
        const float rstd = __builtin_amdgcn_rsqf(fmaxf(fmaf(-mean, mean, msq), 0.f) + 1e-5f), nb = -mean * rstd;
#pragma unroll
        for (int k = 0; k < 4; ++k) {
          xn[k] = pk(fmaf(bf2f(x0[k]), rstd, nb), fmaf(__uint_as_float(x0[k] & 0xFFFF0000u), rstd, nb));
        }
#pragma unroll
        for (int k = 0; k < 4; ++k) xn[4 + k] = pk(fmaf(bf2f(x1[k]), rstd, nb), fmaf(__uint_as_float(x1[k] & 0xFFFF0000u), rstd, nb));
      }
      VMCNT(0);
      asm volatile("" : "+v"(pt1));
#pragma unroll
      for (int hf = 0; hf < 2; ++hf) {
        if (hf == 0 || lane < 8) {
#pragma unroll
          for (int k = 0; k < 4; ++k) {
            const int o = lane + 64 * hf;
            *(LAS unsigned*)(L + LP_XD + ((v * 4 + ((2 * k) & 3)) * 148 + 2 * o + (k >> 1)) * 4) = __builtin_amdgcn_perm(0u, xn[4 * hf + k], 0x01000100u);
            *(LAS unsigned*)(L + LP_XD + ((v * 4 + ((2 * k + 1) & 3)) * 148 + 2 * o + (k >> 1)) * 4) = __builtin_amdgcn_perm(0u, xn[4 * hf + k], 0x03020302u);
          }
        }
      }
      touch(rws, lane * 16, WS_H4 + slot(idx, 4096) + v * 1024);
      MAKE_TAG(wv_);
      const int U0 = (81 * qq) / 144, Qb = 144 * (U0 + 1);
      unsigned xd[21];
      const GAS char* wb = (const GAS char*)(i16 < 8 ? w1 : w3) + (xg * 576 * 2 + (i16 & 7) * 16 + g * 9216);
      const unsigned m0 = cp == 0 ? 0xFFFFu : cp == 1 ? 0xFFFF0000u : 0u, m1 = cp == 2 ? 0xFFFFu : cp == 3 ? 0xFFFF0000u : 0u;
      f32x4 acc[4];
#pragma unroll
      for (int k = 0; k < 4; ++k) acc[k] = f32x4{0.f, 0.f, 0.f, 0.f};
      v4u a[21];
        int off[2][3];
        unsigned use[2][3];
        int hpa[2];
#pragma unroll
        for (int e = 0; e < 2; ++e) {
          const int t = ((v - 2) & 1) * 64 + lane + 128 * e;
          const int cl = t >> 2, b = t & 3, f = 36 * qq + cl, u = f >> 6;
          const int r0 = (144 * u) / 81, r1 = (144 * u + 143) / 81;
#pragma unroll
          for (int k = 0; k < 3; ++k) {
            const int rr = r0 + k;
            const bool on = t < 144 && rr <= r1;
            off[e][k] = on ? slot(xg * 16 + rr, 8192) + (u - (81 * rr) / 144) * 4096 + (f & 63) * 64 + b * 16 : OOB;
            use[e][k] = on ? 0xFFFFFFFFu : 0u;
          }
          hpa[e] = LP_HP + ((b * 4 + ((cl >> 1) & 3)) * 12 + (cl >> 3)) * 4 + (cl & 1) * 2;
          asm volatile("" : "+v"(hpa[e]), "+v"(off[e][0]), "+v"(off[e][1]), "+v"(off[e][2]), "+v"(use[e][0]), "+v"(use[e][1]), "+v"(use[e][2]));
        }
      int pa = WS_H7 + slot(idx, 8192) + v * 4096 + (32 * ((lane >> 4) & 1) + 16 * (lane >> 5) + cp) * 64 + bj * 16;
      asm volatile("" : "+v"(pa));
      BAR();
      RBAR();
#pragma unroll
      for (int t2 = 0; t2 < 21; ++t2) {
        const int Q = 81 * qq + min(4 * t2 + v, 80), qk = Q >= Qb ? Q - Qb : Q - 144 * U0;
        xd[t2] = *(LAS unsigned*)(L + LP_XD + ((bj * 4 + g) * 148 + qk) * 4);
        asm volatile("" : "+v"(xd[t2]));
      }
      wait_clock(t_start, T_F);
#pragma unroll
      for (int t = -PACE_G; t < 21; ++t) {
        const int tl = t + PACE_G;
        if (tl < 21) {
          const int Q = 81 * qq + 4 * tl + v, u = Q >= Qb ? U0 + 1 : U0, qk = Q - 144 * u;
          if (tl < 20 || v == 0) {
            const GAS v4u* p = (const GAS v4u*)(wb + (unsigned)(qk * 4 * 9216 + u * 128));
            a[tl] = __builtin_nontemporal_load(p);
          } else a[tl] = v4u{0u, 0u, 0u, 0u};
          SCHED();
        }
        if (tl == 20) BAR();
        if (t >= 0) {
          const int Q = 81 * qq + min(4 * t + v, 80);
          const v2u B = {xd[t] & m0, xd[t] & m1};
          if (Q < Qb) { acc[0] = mfma16k16(v2u{a[t].x, a[t].y}, B, acc[0]); acc[1] = mfma16k16(v2u{a[t].z, a[t].w}, B, acc[1]); }
          else        { acc[2] = mfma16k16(v2u{a[t].x, a[t].y}, B, acc[2]); acc[3] = mfma16k16(v2u{a[t].z, a[t].w}, B, acc[3]); }
          SCHED();
        }
      }
#pragma unroll
      for (int k = 0; k < 4; ++k) *(LAS f32x4*)(L + LP_RED + (v * 4 + k) * 1024 + lane * 16) = acc[k];
      BAR();
      if (v == 0 || (v == 1 && 81 * qq + 80 >= Qb)) {
#pragma unroll
        for (int hh = 0; hh < 2; ++hh) {
          f32x4 r = *(LAS f32x4*)(L + LP_RED + (2 * v + hh) * 1024 + lane * 16);
#pragma unroll
          for (int k = 1; k < 4; ++k) r += *(LAS f32x4*)(L + LP_RED + (k * 4 + 2 * v + hh) * 1024 + lane * 16);
#pragma unroll
          for (int k = 0; k < 2; ++k) {
            const auto x = __builtin_amdgcn_permlane32_swap(__float_as_uint(r[k]), __float_as_uint(r[2 + k]), false, false);
            st4(v4u{tlo, genc(__uint_as_float(x[0]), t8), tlo, genc(__uint_as_float(x[1]), t8)}, rws, pa + (8 * k + 4 * hh) * 64, 0);
          }
        }
      } else if (v >= 2) {
        SLP(DLY_H7);
        wait_clock(t_start, T_H7);
        float gs[2], us[2];
        for (int n = 0;; ++n) {
          v4u p[2][3];
#pragma unroll
          for (int e = 0; e < 2; ++e)
#pragma unroll
            for (int k = 0; k < 3; ++k) p[e][k] = hl4(rws, off[e][k], WS_H7);
          unsigned bad = 0;
#pragma unroll
          for (int e = 0; e < 2; ++e) {
            gs[e] = 0.f; us[e] = 0.f;
#pragma unroll
            for (int k = 0; k < 3; ++k) {
              bad |= ((p[e][k].x ^ tlo) | (p[e][k].z ^ tlo) | (((p[e][k].y ^ t8) | (p[e][k].w ^ t8)) & 0xFFu)) & use[e][k];
              gs[e] += gdec(p[e][k].y); us[e] += gdec(p[e][k].w);
            }
          }
          if (__all(bad == 0) || n >= GSPIN) break;
          SLP(DLY_R7);
        }
#pragma unroll
        for (int e = 0; e < 2; ++e) {
          const float hv = gs[e] * __builtin_amdgcn_rcpf(1.0f + __expf(-gs[e])) * us[e];
          if ((v - 2) * 64 + lane + 128 * e < 160) *(LAS u16*)(L + hpa[e]) = (u16)pk(hv, hv);
        }
      }
      BAR();
      const int fe = lane & 31, fL = 8 * xg + (fe >> 2), frr = fe & 3;
      const int oidx = ((fL & 15) >> 2) * 576 + (qq < 8 ? 128 * (qq >> 1) + 8 * (4 * (fL >> 4) + frr) + 4 * (qq & 1) + (fL & 3) : 512 + 4 * (4 * (fL >> 4) + frr) + (fL & 3));
      unsigned xres = 0;
      if (v == 3 && qq < 9) xres = ((const GAS u16*)xin)[oidx];
      const int h5o = WS_H5 + slot(qq * 8 + xg, 16384) + v * 4096;
      int fina = LP_FIN + v * 128 + (lane & 7) * 16 + g * 4;
      asm volatile("" : "+v"(xres), "+v"(fina));
      RBAR();
      if (qq < 9) {
        SLP(DLY_H5);
        v4u d[4];
#pragma unroll
        for (int k = 0; k < 4; ++k) d[k] = hl4(rws, lane * 16, h5o + k * 1024);
        unsigned pend = 15u;
#pragma unroll 1
        for (int n = 0;; ++n) {
#pragma unroll
          for (int k = 0; k < 4; ++k)
            if (pend & (1u << k)) { if (__all((strip4(d[k], tb) & 0xFFu) == 0)) pend &= ~(1u << k); }
          if (pend == 0 || n >= GSPIN) break;
#pragma unroll
          for (int k = 0; k < 4; ++k)
            if (pend & (1u << k)) d[k] = hl4(rws, lane * 16, h5o + k * 1024);
        }
        f32x4 s = (__builtin_bit_cast(f32x4, d[0]) + __builtin_bit_cast(f32x4, d[1])) + (__builtin_bit_cast(f32x4, d[2]) + __builtin_bit_cast(f32x4, d[3]));
#pragma unroll
        for (int r = 0; r < 4; ++r) s[r] += dppz<0x128>(s[r]);
        const auto x01 = __builtin_amdgcn_permlane16_swap(__float_as_uint(s[0]), __float_as_uint(s[1]), false, false);
        const auto x23 = __builtin_amdgcn_permlane16_swap(__float_as_uint(s[2]), __float_as_uint(s[3]), false, false);
        const auto z = __builtin_amdgcn_permlane32_swap(__float_as_uint(__uint_as_float(x01[0]) + __uint_as_float(x01[1])),
                                                        __float_as_uint(__uint_as_float(x23[0]) + __uint_as_float(x23[1])), false, false);
        *(LAS float*)(L + fina) = __uint_as_float(z[0]) + __uint_as_float(z[1]);
        BAR();
        if (v == 3) {
          float y = bf2f(xres);
#pragma unroll
          for (int k = 0; k < 4; ++k) y += *(LAS float*)(L + LP_FIN + k * 128 + fe * 4);
          if (lane < 32) ((GAS u16*)out)[oidx] = (u16)pk(y, y);
          if (qq == 0 && xg == 0) st2(v2u{tlo, thi}, rws, lane == 0 ? 0 : OOB, WS_W);
        }
      }
    }
    STAMP_OUT();
    return;
  }

  const int ab = xg >> 1;
  const int r32 = lane & 31, hh = lane >> 5;
  v2u wa_;
  if (w < 4) {
    const rsrc_t rc = mk(ckv, 4 * 4096 * 512 * 2);
    v2u wb_;
    asm volatile("buffer_load_dwordx2 %0, off, %1, %2 sc1" : "=v"(wb_) : "s"(rws), "s"(WS_W) : "memory");
    {
      int vo[4];
#pragma unroll
      for (int k = 0; k < 4; ++k) vo[k] = g * 1024 + ((i16 ^ ((g << 2) | k)) * 16);
      const int so = (idx * 128 + 32 * w) * 1024;
      SLP(DLY_C);
#pragma unroll
      for (int n = 0; n < 32; ++n) {
        if (n >= PACE_C) VMCNT(PACE_C - 1);
        dma16(rc, vo[(n >> 2) & 3], so + (n >> 2) * 4096 + (n & 3) * 256, lbase + (n & 3) * 32768 + (32 * w + 4 * (n >> 2)) * 256);
      }
    }
    RBAR();
    asm volatile("" : "+v"(wb_));
    wa_ = wb_;
  } else {
    wa_ = hl2(rws, 0, WS_W);
    if (w == 4) {
      v2u xr[9];
#pragma unroll
      for (int t = 0; t < 9; ++t) xr[t] = ld2<0>(rx, g * 1152 + i16 * 8, t * 128);
      const rsrc_t rq = mk(wq, 576 * 4096 * 2);
      const int vo = 2 * g * 8192 + i16 * 16, so = 8 * jq * 8192 + hd * 256;
      v4u a[18][2];
#pragma unroll
      for (int o = 0; o < 18; ++o) { a[o][0] = ld4<NT>(rq, vo, so + (32 * o) * 8192); a[o][1] = ld4<NT>(rq, vo, so + (32 * o + 1) * 8192); }
      SCHED();
      float s = 0.f, q = 0.f;
#pragma unroll
      for (int t = 0; t < 9; ++t) {
        *(LAS v2u*)(L + LA_QA + g * 1168 + i16 * 8 + t * 128) = xr[t];
        s = dot2(xr[t].x, 0x3F803F80u, s); q = dot2(xr[t].x, xr[t].x, q);
        s = dot2(xr[t].y, 0x3F803F80u, s); q = dot2(xr[t].y, xr[t].y, q);
      }
      FENCE();
      unsigned xp[18];
#pragma unroll
      for (int o = 0; o < 18; ++o) xp[o] = *(LAS unsigned*)(L + LA_QA + bj * 1168 + (4 * jq + 16 * o + g) * 4);
      const float mean = row_sum(s) * (1.0f / 576.0f), msq = row_sum(q) * (1.0f / 576.0f);
      const float rs0 = __builtin_amdgcn_rsqf(fmaxf(fmaf(-mean, mean, msq), 0.f) + 1e-5f);
      const float rstd = __uint_as_float(__builtin_amdgcn_ds_bpermute(bj * 64, __float_as_uint(rs0)));
      const float nb = __uint_as_float(__builtin_amdgcn_ds_bpermute(bj * 64, __float_as_uint(-mean * rs0)));
      v4u Bq[18];
#pragma unroll
      for (int o = 0; o < 18; ++o) {
        Bq[o] = bdiag(pk(fmaf(bf2f(xp[o]), rstd, nb), fmaf(__uint_as_float(xp[o] & 0xFFFF0000u), rstd, nb)), sel);
        asm volatile("" : "+v"(Bq[o]));
      }
      const u64 tg = (((u64)wa_.y << 32) | wa_.x) + 0x9E3779B97F4A7C15ull;
      unsigned ql = (unsigned)tg, q8 = (unsigned)(tg >> 32) & 0xFFu;
      asm volatile("" : "+v"(ql), "+v"(q8));
      SCHED();
      f32x4 qx = {0.f, 0.f, 0.f, 0.f}, qy = qx;
#pragma unroll
      for (int o = 0; o < 18; ++o) {
        swap24(a[o][0], a[o][1]);
        qx = mfma16(a[o][0], Bq[o], qx);
        qy = mfma16(a[o][1], Bq[o], qy);
      }
      const int po = WS_H1 + slot(idx, 4096);
      st4(v4u{ql, genc(qx[0], q8), ql, genc(qx[1], q8)}, rws, lane * 16, po);
      st4(v4u{ql, genc(qx[2], q8), ql, genc(qx[3], q8)}, rws, lane * 16, po + 1024);
      st4(v4u{ql, genc(qy[0], q8), ql, genc(qy[1], q8)}, rws, lane * 16, po + 2048);
      st4(v4u{ql, genc(qy[2], q8), ql, genc(qy[3], q8)}, rws, lane * 16, po + 3072);
    }
    RBAR();
  }
  MAKE_TAG(wa_);
  unsigned e4[4];
#pragma unroll
  for (int k = 0; k < 4; ++k) e4[k] = lsbtag(tlo, thi, 4 * i16 + k);
  if (w < 4) {
    v4u fa[32];
    {
      const int fr = ((r32 & 3) << 2) | ((r32 >> 2) & 3);
      const int ca = (32 * w + r32) * 256;
#define QK_READ(Q) _Pragma("unroll") for (int k = 0; k < 8; ++k) fa[(Q)*8 + k] = *(LAS v4u*)(L + (Q)*32768 + ca + (((2 * k + hh) ^ fr) * 16)); SCHED();
      VMCNT(0); QK_READ(0) QK_READ(1) QK_READ(2) QK_READ(3)
    }
    int qb[4];
#pragma unroll
    for (int k = 0; k < 4; ++k) { qb[k] = LA_QA + ((lane ^ ((2 * k + hh) & 7)) * 16); asm volatile("" : "+v"(qb[k])); }
    const int q4 = i16 >> 2, lowc = 2 * (g & 1) + (cp >> 1);
    int ta[4][2];
#pragma unroll
    for (int t = 0; t < 4; ++t)
#pragma unroll
      for (int t2 = 0; t2 < 2; ++t2) {
        ta[t][t2] = w * 32768 + (8 * t2 + 4 * hh + q4) * 256 + ((((t ^ q4) << 2) | (lowc ^ (2 * t2 + hh))) * 16) + 8 * (cp & 1);
        asm volatile("" : "+v"(ta[t][t2]));
      }
    int mxa = (r32 >> 4) * 320 + (r32 & 15) * 4, pba = LA_PB + lane * 16;
    unsigned e0 = lsbtag(tlo, thi, 4 * (r32 >> 1)), e1 = lsbtag(tlo, thi, 4 * (r32 >> 1) + 1), e2 = lsbtag(tlo, thi, 4 * (r32 >> 1) + 2), e3 = lsbtag(tlo, thi, 4 * (r32 >> 1) + 3);
    const int sp = idx & 31;
    int h3v = ((hh * 4 + (r32 & 1)) ^ (sp & 5)) * QB + (r32 >> 1) * 16 + WS_H3 + (sp * 16 + ab * 4 + w) * 256;
    asm volatile("" : "+v"(mxa), "+v"(pba), "+v"(e0), "+v"(e1), "+v"(e2), "+v"(e3), "+v"(h3v));
    BAR();
    v4u cb[32];
#define TR_READ(n) do { const v2u x0_ = __builtin_bit_cast(v2u, __builtin_amdgcn_ds_read_tr16_b64_v4i16((LAS s16x4*)(L + ta[(n) >> 3][0] + ((n) & 7) * 4096))); \
    const v2u x1_ = __builtin_bit_cast(v2u, __builtin_amdgcn_ds_read_tr16_b64_v4i16((LAS s16x4*)(L + ta[(n) >> 3][1] + ((n) & 7) * 4096)));                 \
    cb[n] = v4u{x0_.x, x0_.y, x1_.x, x1_.y}; } while (0)
    f32x16 sacc;
#pragma unroll
    for (int k = 0; k < 16; ++k) sacc[k] = 0.f;
    {
      v4u fb[32];
#pragma unroll
      for (int s = 0; s < 6; ++s) fb[s] = *(LAS v4u*)(L + qb[s & 3] + s * 1024);
      SCHED();
#pragma unroll
      for (int s = 0; s < 32; ++s) {
        if (s + 6 < 32) fb[s + 6] = *(LAS v4u*)(L + qb[(s + 6) & 3] + (s + 6) * 1024);
        if (s >= 26) TR_READ(s - 26);
        sacc = mfma32(fa[s], fb[s], sacc);
        SCHED();
      }
    }
    float mw = sacc[0];
#pragma unroll
    for (int k = 1; k < 16; ++k) mw = fmaxf(mw, sacc[k]);
    mw = x32_max(mw);
    asm volatile("s_waitcnt lgkmcnt(0)" ::: "memory");
    *(LAS float*)(L + mxa + w * 32768) = mw;
    BAR();
    const float mx = gdec(genc(fmaxf(fmaxf(*(LAS float*)(L + mxa), *(LAS float*)(L + mxa + 32768)), fmaxf(*(LAS float*)(L + mxa + 65536), *(LAS float*)(L + mxa + 98304))), 0u));
    float ls = 0.f;
#pragma unroll
    for (int k = 0; k < 16; ++k) { sacc[k] = __builtin_amdgcn_exp2f(sacc[k] - mx); ls += sacc[k]; }
#pragma unroll
    for (int s2 = 0; s2 < 2; ++s2)
      *(LAS v4u*)(L + pba + (2 * w + s2) * 1024) =
          v4u{pk(sacc[8 * s2], sacc[8 * s2 + 1]), pk(sacc[8 * s2 + 2], sacc[8 * s2 + 3]), pk(sacc[8 * s2 + 4], sacc[8 * s2 + 5]), pk(sacc[8 * s2 + 6], sacc[8 * s2 + 7])};
    ls = x32_sum(ls);
    *(LAS float*)(L + mxa + 640 + w * 32768) = ls;
    BAR();
    {
      v4u pf[8];
#pragma unroll
      for (int k = 0; k < 8; ++k) pf[k] = *(LAS v4u*)(L + pba + k * 1024);
      f32x4 l4;
      if (w == 0) l4 = f32x4{*(LAS float*)(L + mxa + 640), *(LAS float*)(L + mxa + 640 + 32768), *(LAS float*)(L + mxa + 640 + 65536), *(LAS float*)(L + mxa + 640 + 98304)};
      SCHED();
      f32x16 o[4];
      unsigned p01[16], pq[16];
      const bool odd = lane & 1;
#pragma unroll
      for (int t = 0; t < 4; ++t)
#pragma unroll
        for (int k = 0; k < 16; ++k) o[t][k] = 0.f;
#pragma unroll
      for (int n = 0; n < 32; ++n) {
        if (n + 6 < 32) TR_READ(n + 6);
        o[n >> 3] = mfma32(pf[n & 7], cb[n], o[n >> 3]);
        if (n == 0 && w == 0)
          st4(v4u{tlo, genc(mx, t8), tlo, genc((l4[0] + l4[1]) + (l4[2] + l4[3]), t8)}, rws, lane < 32 ? r32 * 16 : OOB, WS_ML + slot(idx, 512));
        if (n >= 17 && n < 32 && n != 24) {
          const int j = n < 24 ? n - 17 : n - 18;
          if (j < 8) { pq[2 * j] = pk(o[0][2 * j], o[1][2 * j]); pq[2 * j + 1] = pk(o[0][2 * j + 1], o[1][2 * j + 1]); asm volatile("" : "+v"(pq[2 * j]), "+v"(pq[2 * j + 1])); }
          if (j >= 6) {
            const int k = j - 6;
            p01[2 * k] = lsbput(odd ? pq[2 * k + 1] : pq[2 * k], e0);
            p01[2 * k + 1] = __builtin_amdgcn_update_dpp(0, lsbput(odd ? pq[2 * k] : pq[2 * k + 1], e2), 0xB1, 0xf, 0xf, true);
            asm volatile("" : "+v"(p01[2 * k]), "+v"(p01[2 * k + 1]));
          }
        }
        SCHED();
      }
#pragma unroll
      for (int k = 0; k < 8; ++k) {
        const unsigned pa = pk(o[2][2 * k], o[3][2 * k]), pb = pk(o[2][2 * k + 1], o[3][2 * k + 1]);
        st4(v4u{p01[2 * k], lsbput(odd ? pb : pa, e1), p01[2 * k + 1], (unsigned)__builtin_amdgcn_update_dpp(0, lsbput(odd ? pa : pb, e3), 0xB1, 0xf, 0xf, true)},
            rws, h3v, ((2 * (k & 1) + 8 * (k >> 1)) ^ (sp & 26)) * QB);
      }
    }
    RBAR();
    RBAR();
    RBAR();
  } else {
    const int v = w - 4;
    const rsrc_t rv = mk(wuv, 512 * 4096 * 2);
    v4u uw[8];
#define TL(i) do { uw[i] = ld4<NT>(rv, g * 32 * 8192 + i16 * 16, (128 * jq + 8 * v + ((i) ^ (hd & 1))) * 8192 + hd * 256); SCHED(); } while (0)
    {
      v4u qa[8];
      SLP(DLY_QA);
#pragma unroll
      for (int k = 0; k < 8; ++k) qa[k] = hl4(rws, lane * 16, WS_QA + ab * 2048 + slot(8 * v + k, 1024));
      SCHED();
#pragma unroll 1
      for (int n = 0;; ++n) {
        unsigned bad = 0;
#pragma unroll
        for (int k = 0; k < 8; ++k) bad |= strip4(qa[k], e4);
        if (__all((bad & LSBM) == 0) || n >= GSPIN) break;
        SLP(DLY_R2);
#pragma unroll
        for (int k = 0; k < 8; ++k) qa[k] = hl4(rws, lane * 16, WS_QA + ab * 2048 + slot(8 * v + k, 1024));
      }
#pragma unroll
      for (int k = 0; k < 8; ++k) *(LAS v4u*)(L + LA_QA + (lane >> 1) * 1024 + ((((lane & 1) * 32 + 8 * v + k) ^ (lane & 7)) * 16)) = qa[k];
    }
    BAR();
#pragma unroll
    for (int i = 0; i < 4; ++i) TL(i);
    RBAR();
#pragma unroll
    for (int i = 4; i < 8; ++i) TL(i);
    RBAR();
    const int e7 = lane & 7, q4 = i16 >> 2;
    int cta = LA_CTX + v * 288 + (32 * g + e7) * 2, ctr = LA_CTX + cp * 288 + g * 16;
    int h4a = ((2 * (g & 1) + (i16 & 1)) * 16 + (2 * (g >> 1) + (i16 >> 3)) * 4 + ((i16 >> 1) & 3)) * 16 + WS_H4 + slot(hd * 4 + v, 4096) + jq * 1024;
    const unsigned src = (lane & 8) ? 0x0302u : 0x0100u, s1 = (e7 & 1) ? ((src << 16) | 0x0C0Cu) : (0x0C0C0000u | src);
    unsigned s8[4];
    int uwa[4], bva[2][2];
#pragma unroll
    for (int k = 0; k < 4; ++k) {
      s8[k] = (e7 >> 1) == k ? s1 : 0x0C0C0C0Cu;
      uwa[k] = LA_UVW + (32 * v + g) * 256 + ((i16 ^ ((g << 2) | k)) * 16);
      bva[k >> 1][k & 1] = LA_UVW + (8 * g + 4 * (k & 1) + q4) * 256 + (((2 * (2 * v + (k >> 1)) + (cp >> 1)) ^ ((q4 << 2) | ((2 * g + (k & 1)) & 3))) * 16) + 8 * (cp & 1);
      asm volatile("" : "+v"(s8[k]), "+v"(uwa[k]), "+v"(bva[k >> 1][k & 1]));
    }
    asm volatile("" : "+v"(cta), "+v"(ctr), "+v"(h4a));
    const int h3l = ((hd & 3) ^ g) * QB + g * 4096 + i16 * 16;
#define ML_LOAD() hl4(rws, slot(r32, 512), WS_ML + v * 1024 + hd * 16)
#define ML_OK(V) __all((((V).x ^ tlo) | ((V).z ^ tlo) | ((((V).y ^ t8) | ((V).w ^ t8)) & 0xFFu)) == 0)
#define H3_LOADS() _Pragma("unroll") for (int k = 0; k < 8; ++k) d[k] = hl4(rws, h3l, WS_H3 + ((hd >> 2) ^ k) * 4 * QB + k * 16384 + (v * 4 + jq) * 256); SCHED()
    v4u d[8], bk[8];
    v4u ml;
    VMCNT(0);
    RBAR();
#pragma unroll
    for (int k = 0; k < 8; ++k) *(LAS v4u*)(L + uwa[k & 3] + k * 1024) = uw[k];
    BAR();
    v4u bv[2][4];
#pragma unroll
    for (int t = 0; t < 2; ++t)
#pragma unroll
      for (int ks = 0; ks < 4; ++ks) {
        const v2u x0 = __builtin_bit_cast(v2u, __builtin_amdgcn_ds_read_tr16_b64_v4i16((LAS s16x4*)(L + bva[t][0] + ks * 8192)));
        const v2u x1 = __builtin_bit_cast(v2u, __builtin_amdgcn_ds_read_tr16_b64_v4i16((LAS s16x4*)(L + bva[t][1] + ks * 8192)));
        bv[t][ks] = v4u{x0.x, x0.y, x1.x, x1.y};
      }
    SCHED();
    {
      SLP(DLY_M0);
      for (int n = 0;; ++n) {
        ml = ML_LOAD();
        if (ML_OK(ml) || n >= GSPIN) break;
        SLP(DLY_RM);
      }
      SLP(DLY_H3);
      H3_LOADS();
      const float m = gdec(ml.y), M = x16_max(row_max(m));
      const float e = __builtin_amdgcn_exp2f(m - M);
      const float al = e * LSB_COMP * __builtin_amdgcn_rcpf(x16_sum(row_sum(e * gdec(ml.w))));
      const unsigned ah = pk(al, al) & 0xFFFFu;
      const unsigned hl = ah | (pk(al - bf2f(ah), 0.f) << 16);
#pragma unroll
      for (int k = 0; k < 8; ++k) { bk[k] = bdiag(__builtin_amdgcn_ds_bpermute((4 * k + g) * 4, hl), s8); asm volatile("" : "+v"(bk[k])); }
      f32x4 c;
#pragma unroll 1
      for (int n = 0;; ++n) {
        unsigned bad = 0;
        c = f32x4{0.f, 0.f, 0.f, 0.f};
#pragma unroll
        for (int k = 0; k < 8; ++k) { bad |= strip4(d[k], e4); c = mfma16(d[k], bk[k], c); }
        if (__all((bad & LSBM) == 0) || n >= GSPIN) break;
#pragma unroll
        for (int k = 0; k < 8; ++k) d[k] = hl4(rws, h3l, WS_H3 + ((hd >> 2) ^ k) * 4 * QB + k * 16384 + (v * 4 + jq) * 256);
      }
#pragma unroll
      for (int r = 0; r < 4; ++r) {
        const float y = c[r] + dppz<0x128>(c[r]);
        *(LAS u16*)(L + cta + 16 * r) = (u16)pk(y, y);
      }
    }
    BAR();
    {
      v4u af[4];
#pragma unroll
      for (int ks = 0; ks < 4; ++ks) af[ks] = *(LAS v4u*)(L + ctr + 64 * ks);
      SCHED();
      f32x4 c0 = {0.f, 0.f, 0.f, 0.f}, c1 = c0;
#pragma unroll
      for (int ks = 0; ks < 4; ++ks) { c0 = mfma16(af[ks], bv[0][ks], c0); c1 = mfma16(af[ks], bv[1][ks], c1); }
      const f32x4 ct = g < 2 ? c0 : c1;
      const float x0 = (g & 1) ? ct[2] : ct[0], x1 = (g & 1) ? ct[3] : ct[1];
      const float n0 = dppz<0xB1>(x0), n1 = dppz<0xB1>(x1);
      st4(v4u{tlo, genc((lane & 1) ? n1 : x0, t8), tlo, genc((lane & 1) ? x1 : n0, t8)}, rws, h4a, 0);
    }
  }
  STAMP_OUT();
}

extern "C" __global__ void fused_decode_end() {}

__global__ void k_spin(char* ws){
  const u64* o=(const u64*)(ws+WS_END);
  int t=threadIdx.x; u64 mn=~0ull,mx=0,mxs=0; double sum=0;
  __shared__ u64 s0[256],s1[256],s2[256]; __shared__ double s3[256];
  for(int i=t;i<2048;i+=256){ u64 a=o[2*i],b=o[2*i+1]; mn=a<mn?a:mn; mx=b>mx?b:mx; mxs=a>mxs?a:mxs; if((i&7)!=7) sum+=(double)(b-a); }
  s0[t]=mn;s1[t]=mx;s2[t]=mxs;s3[t]=sum; __syncthreads();
  if(t==0){ for(int i=1;i<256;i++){ mn=s0[i]<mn?s0[i]:mn; mx=s1[i]>mx?s1[i]:mx; mxs=s2[i]>mxs?s2[i]:mxs; sum+=s3[i]; }
    double T= PWHAT==0?0.0: PWHAT==1?(double)(mx-mn): PWHAT==2? sum/1792.0 : (double)(mxs-mn);
    u64 TT=(u64)(T*100.0); if(TT>1000000) TT=1000000;
    u64 t0=__builtin_amdgcn_s_memrealtime();
    while(__builtin_amdgcn_s_memrealtime()-t0 < TT) __builtin_amdgcn_s_sleep(8);
  }
}
extern "C" void kernel_launch(void* const* d_in, const int* in_sizes, int n_in, void* d_out, int out_size, void* d_ws, size_t ws_size,
                              hipStream_t stream) {
  (void)hipFuncSetAttribute((const void*)fused_decode, hipFuncAttributeMaxDynamicSharedMemorySize, LDS_BYTES);
  fused_decode<<<256, 512, LDS_BYTES, stream>>>((const u16*)d_in[0], (const u16*)d_in[1], (const u16*)d_in[2], (const u16*)d_in[3],
                                                (const u16*)d_in[4], (const u16*)d_in[5], (const u16*)d_in[7], (const u16*)d_in[8],
                                                (const u16*)d_in[9], (u16*)d_out, (char*)d_ws);
  k_spin<<<1,256,0,stream>>>((char*)d_ws);
}
```

```cpp
#define DIAG_SPAN
#define SEL t<32
#include <hip/hip_runtime.h>
#include <stdint.h>

typedef unsigned short u16;
typedef unsigned long long u64;
typedef unsigned v4u __attribute__((ext_vector_type(4)));
typedef unsigned v2u __attribute__((ext_vector_type(2)));
typedef float f32x2 __attribute__((ext_vector_type(2)));
typedef float f32x4 __attribute__((ext_vector_type(4)));
typedef float f32x16 __attribute__((ext_vector_type(16)));
typedef short s16x4 __attribute__((ext_vector_type(4)));
typedef __bf16 bf16x8 __attribute__((ext_vector_type(8)));
typedef __bf16 bf16x2 __attribute__((ext_vector_type(2)));
typedef __amdgpu_buffer_rsrc_t rsrc_t;
#define GAS __attribute__((address_space(1)))
#define LAS __attribute__((address_space(3)))
#define DI __device__ __forceinline__

#define BLOCK_ID blockIdx.x
constexpr int NT = 2;
constexpr int PACE_C = 7;
constexpr int PACE_K = 4;
constexpr int PACE_G = 4;
constexpr int PACE_W = 6;
#define DLY_C 40
#define DLY_K 16
#define DLY_H1 2
#define DLY_R1 7
#define DLY_QA 8
#define DLY_R2 12
#define DLY_M0 12
#define DLY_RM 4
#define DLY_H3 1
#define DLY_H7 22
#define DLY_R7 2
#define DLY_R4 2
#define DLY_H5 18
constexpr unsigned T_F = 350, T_H7 = 915;
#define SLP(N) __builtin_amdgcn_s_sleep(N)

constexpr int SC1 = 16;
constexpr int GSPIN = 1 << 13;

constexpr int QB = 131072, WIN = 16 * QB;
constexpr int WS_H1 = 0;
constexpr int WS_H4 = 32768;
constexpr int WS_QA = 65536;
constexpr int WS_ML = 73728;
constexpr int WS_W = 81920;
constexpr int WS_H7 = WIN;
constexpr int WS_H5 = 2 * WIN;
constexpr int WS_H3 = 3 * WIN;
DI constexpr int slot(int p, int size) { return (p & 15) * QB + (p >> 4) * size; }
DI constexpr int blk5(int n) { return (n & 1) * 8 * QB + (n >> 1) * 16384; }
constexpr int WS_END = WS_H3 + 128 * 32768;
constexpr int OOB = (int)0x80000000;

constexpr int LP_XD = 5120, LP_QB = 15360, LP_QO = 16896, LP_RED = 18432, LP_HP = 34816, LP_B8 = 35840, LP_FIN = 39936, LP_UK = 40960;
constexpr int LA_QA = 131072, LA_PB = LA_QA + 1024, LA_CTX = LA_QA + 9216, LA_UVW = 0;
constexpr int LDS_BYTES = 163840;

DI rsrc_t mk(const void* p, int bytes) { return __builtin_amdgcn_make_buffer_rsrc((void*)p, (short)0, bytes, 0x00020000); }
template <int AUX> DI v4u ld4(rsrc_t r, int vo, int so) { return __builtin_amdgcn_raw_buffer_load_b128(r, vo, so, AUX); }
template <int AUX> DI v2u ld2(rsrc_t r, int vo, int so) { return __builtin_amdgcn_raw_buffer_load_b64(r, vo, so, AUX); }
DI v4u hl4(rsrc_t r, int vo, int so) { asm volatile("" : "+v"(vo)); return __builtin_amdgcn_raw_buffer_load_b128(r, vo, so, SC1); }
DI v2u hl2(rsrc_t r, int vo, int so) { asm volatile("" : "+v"(vo)); return __builtin_amdgcn_raw_buffer_load_b64(r, vo, so, SC1); }
DI void touch(rsrc_t r, int vo, int so) { v4u t; asm volatile("buffer_load_dwordx4 %0, %1, %2, %3 offen sc1\n\ts_waitcnt vmcnt(0)" : "=&v"(t) : "v"(vo), "s"(r), "s"(so) : "memory"); }
DI void st4(v4u v, rsrc_t r, int vo, int so) { __builtin_amdgcn_raw_buffer_store_b128(v, r, vo + so, 0, SC1); }
DI void st2(v2u v, rsrc_t r, int vo, int so) { __builtin_amdgcn_raw_buffer_store_b64(v, r, vo, so, SC1); }
DI void dma16(rsrc_t r, int vo, int so, unsigned lds) {
  asm volatile("s_mov_b32 m0, %0\n\ts_nop 0\n\tbuffer_load_dwordx4 %1, %2, %3 offen nt lds" ::"s"(lds), "v"(vo), "s"(r), "s"(so) : "memory");
}
#define VMCNT(n) asm volatile("s_waitcnt vmcnt(%0)" ::"n"(n) : "memory")
#define FENCE() asm volatile("" ::: "memory")
#define SCHED() __builtin_amdgcn_sched_barrier(0)

DI float bf2f(unsigned v) { return __uint_as_float(v << 16); }
DI unsigned pk(float a, float b) { f32x2 f = {a, b}; return __builtin_bit_cast(unsigned, __builtin_convertvector(f, bf16x2)); }
DI f32x4 mfma16(v4u a, v4u b, f32x4 c) {
  return __builtin_amdgcn_mfma_f32_16x16x32_bf16(__builtin_bit_cast(bf16x8, a), __builtin_bit_cast(bf16x8, b), c, 0, 0, 0);
}
DI f32x4 mfma16k16(v2u a, v2u b, f32x4 c) {
  return __builtin_amdgcn_mfma_f32_16x16x16bf16_1k(__builtin_bit_cast(s16x4, a), __builtin_bit_cast(s16x4, b), c, 0, 0, 0);
}
DI f32x16 mfma32(v4u a, v4u b, f32x16 c) {
  return __builtin_amdgcn_mfma_f32_32x32x16_bf16(__builtin_bit_cast(bf16x8, a), __builtin_bit_cast(bf16x8, b), c, 0, 0, 0);
}
template <int CTRL, int RM> DI float dppf(float old, float v) {
  return __builtin_bit_cast(float, __builtin_amdgcn_update_dpp(__builtin_bit_cast(int, old), __builtin_bit_cast(int, v), CTRL, RM, 0xf, false));
}
template <int CTRL> DI float dppz(float v) {
  return __builtin_bit_cast(float, __builtin_amdgcn_update_dpp(0, __builtin_bit_cast(int, v), CTRL, 0xf, 0xf, true));
}
DI float row_sum(float v) {
  v += dppz<0xB1>(v); v += dppz<0x4E>(v); v += dppz<0x124>(v); v += dppz<0x128>(v);
  return v;
}
DI float dot2(unsigned a, unsigned b, float c) {
  return __builtin_amdgcn_fdot2_f32_bf16(__builtin_bit_cast(bf16x2, a), __builtin_bit_cast(bf16x2, b), c, false);
}
DI float row_max(float v) {
  v = fmaxf(v, dppf<0xB1, 0xf>(v, v)); v = fmaxf(v, dppf<0x4E, 0xf>(v, v));
  v = fmaxf(v, dppf<0x124, 0xf>(v, v)); v = fmaxf(v, dppf<0x128, 0xf>(v, v));
  return v;
}
DI float x16_sum(float v) { auto r = __builtin_amdgcn_permlane16_swap(__float_as_uint(v), __float_as_uint(v), false, false); return __uint_as_float(r[0]) + __uint_as_float(r[1]); }
DI float x32_sum(float v) { auto r = __builtin_amdgcn_permlane32_swap(__float_as_uint(v), __float_as_uint(v), false, false); return __uint_as_float(r[0]) + __uint_as_float(r[1]); }
DI float x16_max(float v) { auto r = __builtin_amdgcn_permlane16_swap(__float_as_uint(v), __float_as_uint(v), false, false); return fmaxf(__uint_as_float(r[0]), __uint_as_float(r[1])); }
DI float x32_max(float v) { auto r = __builtin_amdgcn_permlane32_swap(__float_as_uint(v), __float_as_uint(v), false, false); return fmaxf(__uint_as_float(r[0]), __uint_as_float(r[1])); }

#define RBAR() asm volatile("s_barrier" ::: "memory")
#define BAR() asm volatile("s_waitcnt lgkmcnt(0)\n\ts_barrier" ::: "memory")
DI unsigned genc(float v, unsigned t8) { return ((__float_as_uint(v) + 0x80u) & 0xFFFFFF00u) | t8; }
DI float gdec(unsigned hi) { return __uint_as_float(hi & 0xFFFFFF00u); }

#define STAMP_DECL const u64 strt0_ = __builtin_amdgcn_s_memrealtime()
#define STAMP_OUT() do { const u64 r1_ = __builtin_amdgcn_s_memrealtime(); \
    if (lane == 0) { GAS u64* so_ = (GAS u64*)(ws + WS_END) + ((size_t)blockIdx.x * 8 + w) * 2; so_[0] = strt0_; so_[1] = r1_; } } while (0)
DI void wait_clock(u64 t0, unsigned ticks) {
#pragma unroll 1
  for (int n = 0; n < 4096 && (unsigned)(__builtin_amdgcn_s_memrealtime() - t0) < ticks; ++n) __builtin_amdgcn_s_sleep(1);
}

DI v4u bdiag(unsigned P, const unsigned (&s)[4]) {
  v4u b;
  b.x = __builtin_amdgcn_perm(P, P, s[0]); b.y = __builtin_amdgcn_perm(P, P, s[1]);
  b.z = __builtin_amdgcn_perm(P, P, s[2]); b.w = __builtin_amdgcn_perm(P, P, s[3]);
  return b;
}
DI void bdiag_sel(int cp, unsigned (&s)[4]) {
  const unsigned lo = (cp & 1) ? 0x01000C0Cu : 0x0C0C0100u, hi = (cp & 1) ? 0x03020C0Cu : 0x0C0C0302u, z = 0x0C0C0C0Cu;
  s[0] = cp < 2 ? lo : z; s[1] = cp < 2 ? z : lo; s[2] = cp < 2 ? hi : z; s[3] = cp < 2 ? z : hi;
}

DI unsigned lsbtag(unsigned tlo, unsigned thi, int p) { return (((p < 32 ? tlo : thi) >> (p & 31)) & 1u) | (((tlo ^ (unsigned)p) & 1u) << 16); }
constexpr unsigned LSBM = 0x00010001u;
DI unsigned lsbput(unsigned v, unsigned e) { return (v & ~LSBM) | e; }
DI unsigned strip4(v4u& d, const unsigned (&e)[4]) { d.x ^= e[0]; d.y ^= e[1]; d.z ^= e[2]; d.w ^= e[3]; return (d.x | d.y) | (d.z | d.w); }
DI void swap24(v4u& a, v4u& b) {
  unsigned az = a.z, aw = a.w, bx = b.x, by = b.y;
  asm volatile("v_swap_b32 %0, %2\n\tv_swap_b32 %1, %3\n\ts_nop 1" : "+v"(az), "+v"(aw), "+v"(bx), "+v"(by));
  a.z = az; a.w = aw; b.x = bx; b.y = by;
}
constexpr float LSB_COMP = 1.0028f;
DI v4u tag8(f32x4 c, const unsigned (&t)[4]) {
  return v4u{(__float_as_uint(c[0]) & 0xFFFFFF00u) | t[0], (__float_as_uint(c[1]) & 0xFFFFFF00u) | t[1],
             (__float_as_uint(c[2]) & 0xFFFFFF00u) | t[2], (__float_as_uint(c[3]) & 0xFFFFFF00u) | t[3]};
}
extern "C" __global__ void __launch_bounds__(512, 2)
fused_decode(const u16* __restrict__ xin, const u16* __restrict__ ckv, const u16* __restrict__ wq, const u16* __restrict__ wuk,
             const u16* __restrict__ wuv, const u16* __restrict__ wo, const u16* __restrict__ w1, const u16* __restrict__ w3,
             const u16* __restrict__ w2, u16* __restrict__ out, char* ws) {
  extern __shared__ __attribute__((aligned(16))) char smem[];
  LAS char* L = (LAS char*)smem;
  const unsigned lbase = (unsigned)(size_t)smem;
  const int lane = threadIdx.x & 63, w = __builtin_amdgcn_readfirstlane(threadIdx.x >> 6);
  const int bid = BLOCK_ID, xg = bid & 7, qd_ = bid >> 3, qq = qd_ & 15, idx = xg * 16 + qq, hd = idx >> 2, jq = idx & 3;
  const int i16 = lane & 15, g = lane >> 4, bj = i16 >> 2, cp = lane & 3;
  STAMP_DECL;
  if (w == 7) {
    u64 pc; unsigned back, len;
    asm volatile("s_getpc_b64 %0\n.Lpf_here:\n\ts_mov_b32 %1, .Lpf_here-fused_decode\n\ts_mov_b32 %2, fused_decode_end-fused_decode" : "=s"(pc), "=s"(back), "=s"(len));
    const rsrc_t rcode = mk((const void*)(pc - back), (int)min(len, 32768u));
    v4u dummy;
    asm volatile("s_nop 4\n\tbuffer_load_dwordx4 %0, %1, %2, 0 offen\n\ts_waitcnt vmcnt(0)" : "=&v"(dummy) : "v"(qd_ * 1024 + lane * 16), "s"(rcode) : "memory");
  }
  asm volatile("" ::"s"(xin), "s"(ckv), "s"(wq), "s"(wuk), "s"(wuv), "s"(wo), "s"(w1), "s"(w3), "s"(w2), "s"(out), "s"(ws));
  u64 t_start;
  asm volatile("s_memrealtime %0\n\ts_waitcnt lgkmcnt(0)" : "=s"(t_start) : "s"(ws) : "memory");
  const rsrc_t rws = mk(ws, WS_END);
  const rsrc_t rx = mk(xin, 4 * 576 * 2);
  unsigned sel[4];
  bdiag_sel(cp, sel);
#define MAKE_TAG(W) const u64 tag_ = (((u64)(W).y << 32) | (W).x) + 0x9E3779B97F4A7C15ull;  \
  const unsigned tlo = (unsigned)tag_, thi = (unsigned)(tag_ >> 32), t8 = thi & 0xFFu;        \
  const unsigned tb[4] = {tlo & 0xFFu, (tlo >> 8) & 0xFFu, (tlo >> 16) & 0xFFu, tlo >> 24}
  const int vo5 = (lane >> 3) * QB + (lane & 7) * 16;

  if (qd_ >= 16) {
    const int so5 = WS_H5 + idx * 128;
    if (w < 4) {
      v4u ka[2][4];
      v2u wv_;
      asm volatile("buffer_load_dwordx2 %0, off, %1, %2 sc1" : "=v"(wv_) : "s"(rws), "s"(WS_W) : "memory");
      {
        const rsrc_t rk = mk(wuk, 512 * 4096 * 2);
        const int ukx = 4 * (hd & 1);
        SLP(DLY_K);
#pragma unroll
        for (int n = 0; n < 8; ++n) {
          if (n >= PACE_K) VMCNT(PACE_K - 1);
          dma16(rk, g * 8192 + ((i16 ^ ((g << 2) | (n & 3))) * 16), (128 * jq + 32 * w + 4 * (n ^ ukx)) * 8192 + hd * 256, lbase + LP_UK + (8 * w + (n ^ ukx)) * 1024);
        }
        VMCNT(0);
        const int fr = ((i16 & 3) << 2) | (i16 >> 2);
#pragma unroll
        for (int t = 0; t < 2; ++t)
#pragma unroll
          for (int s = 0; s < 4; ++s) {
            ka[t][s] = *(LAS v4u*)(L + LP_UK + (32 * w + 16 * t + i16) * 256 + (((4 * s + g) ^ fr) * 16));
            asm volatile("" : "+v"(ka[t][s]));
          }
      }
      asm volatile("" : "+v"(wv_));
      MAKE_TAG(wv_);
      unsigned eq[4];
#pragma unroll
      for (int k = 0; k < 4; ++k) { eq[k] = lsbtag(tlo, thi, 4 * i16 + k); asm volatile("" : "+v"(eq[k])); }
      SLP(DLY_H1);
      {
        const int blk = w & 1, half = w >> 1;
        float q0, q1;
        for (int n = 0;; ++n) {
          v4u p[4];
#pragma unroll
          for (int jj = 0; jj < 4; ++jj) p[jj] = hl4(rws, lane * 16, WS_H1 + slot(hd * 4 + jj, 4096) + (blk * 2 + half) * 1024);
          unsigned bad = 0;
          q0 = 0.f; q1 = 0.f;
#pragma unroll
          for (int jj = 0; jj < 4; ++jj) {
            bad |= (p[jj].x ^ tlo) | (p[jj].z ^ tlo) | (((p[jj].y ^ t8) | (p[jj].w ^ t8)) & 0xFFu);
            q0 += gdec(p[jj].y); q1 += gdec(p[jj].w);
          }
          if (__all(bad == 0) || n >= GSPIN) break;
          SLP(DLY_R1);
        }
        const unsigned qp = pk(q0, q1);
        const int d0 = 32 * g + 16 * half + 4 * blk + cp;
        *(LAS u16*)(L + LP_QB + bj * 320 + d0 * 2) = (u16)qp;
        *(LAS u16*)(L + LP_QB + bj * 320 + (d0 + 8) * 2) = (u16)(qp >> 16);
      }
      BAR();
      {
        v4u qf[4];
#pragma unroll
        for (int s = 0; s < 4; ++s) qf[s] = *(LAS v4u*)(L + LP_QB + cp * 320 + s * 64 + g * 16);
        f32x4 c0 = {0.f, 0.f, 0.f, 0.f}, c1 = c0;
#pragma unroll
        for (int s = 0; s < 4; ++s) { c0 = mfma16(ka[0][s], qf[s], c0); c1 = mfma16(ka[1][s], qf[s], c1); }
        const float sc = 0.08838834764831845f * 1.4426950408889634f * LSB_COMP;
        if (i16 < 4) {
          *(LAS v2u*)(L + LP_QO + i16 * 256 + (32 * w + 4 * g) * 2) = v2u{pk(c0[0] * sc, c0[1] * sc), pk(c0[2] * sc, c0[3] * sc)};
          *(LAS v2u*)(L + LP_QO + i16 * 256 + (32 * w + 16 + 4 * g) * 2) = v2u{pk(c1[0] * sc, c1[1] * sc), pk(c1[2] * sc, c1[3] * sc)};
        }
      }
      BAR();
      if (w == 0) {
        const v4u v = *(LAS v4u*)(L + LP_QO + lane * 16);
        st4(v4u{lsbput(v.x, eq[0]), lsbput(v.y, eq[1]), lsbput(v.z, eq[2]), lsbput(v.w, eq[3])}, rws, g * 2048 + i16 * 16, WS_QA + slot(hd, 1024) + jq * 256);
      }
      BAR();
      const rsrc_t r2 = mk(w2, 4608 * 576 * 2);
      const int so2 = (xg * 576 + 36 * qq) * 1152;
      const rsrc_t ro = mk(wo, 4096 * 576 * 2);
      const int soo = (hd * 128 + 32 * jq) * 1152;
      v4u a2[9][2];
      v2u a8[3][2];
      {
        const int vo4 = 2 * g * 1152 + i16 * 16, vo4m = g < 2 ? vo4 : OOB;
        const int vo8 = 2 * g * 1152 + i16 * 8 + 1024, vo8m = (g < 2 && w == 0) ? vo8 : OOB;
#pragma unroll
        for (int n = 0; n < 24; ++n) {
          if (n >= PACE_W) VMCNT(PACE_W - 1);
          if (n == PACE_W) { SCHED(); RBAR(); SLP(1); }
          const int o = n >> 1, r = n & 1;
          if (n < 10) a2[o][r] = ld4<NT>(r2, o < 4 ? vo4 : vo4m, so2 + (8 * o + r) * 1152 + 256 * w);
          else if (n < 12) a8[0][r] = ld2<NT>(r2, vo8, so2 + (8 * w + r) * 1152);
          else if (n < 14) a8[1][r] = ld2<NT>(r2, vo8m, so2 + (32 + r) * 1152);
          else if (n < 22) a2[o - 2][r] = ld4<NT>(ro, vo4, soo + (8 * (o - 7) + r) * 1152 + 256 * w);
          else a8[2][r] = ld2<NT>(ro, vo8, soo + (8 * w + r) * 1152);
          SCHED();
        }
      }
#pragma unroll
      for (int o = 0; o < 5; ++o) swap24(a2[o][0], a2[o][1]);
      RBAR();
      {
        const v4u h4 = *(LAS v4u*)(L + LP_HP + (bj * 4 + g) * 48);
        const unsigned h5 = *(LAS unsigned*)(L + LP_HP + (bj * 4 + g) * 48 + 16);
        const unsigned hw = *(LAS unsigned*)(L + LP_HP + (bj * 4 + g) * 48 + 4 * w);
        f32x4 c8 = {0.f, 0.f, 0.f, 0.f}, cx = c8, cy = c8;
        c8 = mfma16(v4u{a8[0][0].x, a8[0][0].y, a8[0][1].x, a8[0][1].y}, bdiag(hw, sel), c8);
        c8 = mfma16(v4u{a8[1][0].x, a8[1][0].y, a8[1][1].x, a8[1][1].y}, bdiag(h5, sel), c8);
#pragma unroll
        for (int o = 0; o < 5; ++o) {
          const v4u B = bdiag(o == 0 ? h4.x : o == 1 ? h4.y : o == 2 ? h4.z : o == 3 ? h4.w : h5, sel);
          cx = mfma16(a2[o][0], B, cx);
          cy = mfma16(a2[o][1], B, cy);
        }
#pragma unroll
        for (int o = 5; o < 9; ++o) swap24(a2[o][0], a2[o][1]);
        asm volatile("" : "+v"(cx), "+v"(cy), "+v"(c8));
        int bpa = (bj * 16 + g) * 4;
        asm volatile("" : "+v"(bpa));
        float y0, y1;
        for (int n = 0;; ++n) {
          v4u p[4];
#pragma unroll
          for (int jj = 0; jj < 4; ++jj) p[jj] = hl4(rws, lane * 16, WS_H4 + slot(idx, 4096) + jj * 1024);
          unsigned bad = 0;
          y0 = 0.f; y1 = 0.f;
#pragma unroll
          for (int jj = 0; jj < 4; ++jj) {
            bad |= (p[jj].x ^ tlo) | (p[jj].z ^ tlo) | (((p[jj].y ^ t8) | (p[jj].w ^ t8)) & 0xFFu);
            y0 += gdec(p[jj].y); y1 += gdec(p[jj].w);
          }
          if (__all(bad == 0) || n >= GSPIN) break;
          SLP(DLY_R4);
        }
        const unsigned yp = pk(y0, y1);
        const unsigned pw = __builtin_amdgcn_ds_bpermute(bpa + 16 * w, yp);
        unsigned po[4];
#pragma unroll
        for (int o = 0; o < 4; ++o) po[o] = __builtin_amdgcn_ds_bpermute(bpa + 16 * o, yp);
        c8 = mfma16(v4u{a8[2][0].x, a8[2][0].y, a8[2][1].x, a8[2][1].y}, bdiag(pw, sel), c8);
#pragma unroll
        for (int o = 0; o < 4; ++o) {
          const v4u B = bdiag(po[o], sel);
          cx = mfma16(a2[5 + o][0], B, cx);
          cy = mfma16(a2[5 + o][1], B, cy);
        }
        *(LAS f32x4*)(L + LP_B8 + w * 1024 + lane * 16) = c8;
        st4(tag8(cx, tb), rws, vo5, so5 + blk5(2 * w));
        st4(tag8(cy, tb), rws, vo5, so5 + blk5(2 * w + 1));
        BAR();
        if (w == 0) {
          f32x4 s = *(LAS f32x4*)(L + LP_B8 + lane * 16);
#pragma unroll
          for (int k = 1; k < 4; ++k) s += *(LAS f32x4*)(L + LP_B8 + k * 1024 + lane * 16);
          st4(tag8(s, tb), rws, vo5, so5 + blk5(8));
        }
      }
      if (qq < 9) RBAR();
    } else {
      const int v = w - 4;
      unsigned xn[8];
      const v2u wv_ = hl2(rws, 0, WS_W);
      v4u pt1;
      asm volatile("buffer_load_dwordx4 %0, %1, %2, %3 offen sc1" : "=v"(pt1) : "v"(lane * 16), "s"(rws), "s"(WS_H1 + slot(idx, 4096) + v * 1024) : "memory");
      {
        const v4u x0 = ld4<0>(rx, lane * 16, v * 1152), x1 = ld4<0>(rx, lane < 8 ? lane * 16 : OOB, v * 1152 + 1024);
        float s = 0.f, q = 0.f;
#pragma unroll
        for (int k = 0; k < 4; ++k) {
          s = dot2(x0[k], 0x3F803F80u, s); q = dot2(x0[k], x0[k], q);
          s = dot2(x1[k], 0x3F803F80u, s); q = dot2(x1[k], x1[k], q);
        }
        const float mean = x32_sum(x16_sum(row_sum(s))) * (1.0f / 576.0f);
        const float msq = x32_sum(x16_sum(row_sum(q))) * (1.0f / 576.0f);
        const float rstd = __builtin_amdgcn_rsqf(fmaxf(fmaf(-mean, mean, msq), 0.f) + 1e-5f), nb = -mean * rstd;
#pragma unroll
        for (int k = 0; k < 4; ++k) {
          xn[k] = pk(fmaf(bf2f(x0[k]), rstd, nb), fmaf(__uint_as_float(x0[k] & 0xFFFF0000u), rstd, nb));
        }
#pragma unroll
        for (int k = 0; k < 4; ++k) xn[4 + k] = pk(fmaf(bf2f(x1[k]), rstd, nb), fmaf(__uint_as_float(x1[k] & 0xFFFF0000u), rstd, nb));
      }
      VMCNT(0);
      asm volatile("" : "+v"(pt1));
#pragma unroll
      for (int hf = 0; hf < 2; ++hf) {
        if (hf == 0 || lane < 8) {
#pragma unroll
          for (int k = 0; k < 4; ++k) {
            const int o = lane + 64 * hf;
            *(LAS unsigned*)(L + LP_XD + ((v * 4 + ((2 * k) & 3)) * 148 + 2 * o + (k >> 1)) * 4) = __builtin_amdgcn_perm(0u, xn[4 * hf + k], 0x01000100u);
            *(LAS unsigned*)(L + LP_XD + ((v * 4 + ((2 * k + 1) & 3)) * 148 + 2 * o + (k >> 1)) * 4) = __builtin_amdgcn_perm(0u, xn[4 * hf + k], 0x03020302u);
          }
        }
      }
      touch(rws, lane * 16, WS_H4 + slot(idx, 4096) + v * 1024);
      MAKE_TAG(wv_);
      const int U0 = (81 * qq) / 144, Qb = 144 * (U0 + 1);
      unsigned xd[21];
      const GAS char* wb = (const GAS char*)(i16 < 8 ? w1 : w3) + (xg * 576 * 2 + (i16 & 7) * 16 + g * 9216);
      const unsigned m0 = cp == 0 ? 0xFFFFu : cp == 1 ? 0xFFFF0000u : 0u, m1 = cp == 2 ? 0xFFFFu : cp == 3 ? 0xFFFF0000u : 0u;
      f32x4 acc[4];
#pragma unroll
      for (int k = 0; k < 4; ++k) acc[k] = f32x4{0.f, 0.f, 0.f, 0.f};
      v4u a[21];
        int off[2][3];
        unsigned use[2][3];
        int hpa[2];
#pragma unroll
        for (int e = 0; e < 2; ++e) {
          const int t = ((v - 2) & 1) * 64 + lane + 128 * e;
          const int cl = t >> 2, b = t & 3, f = 36 * qq + cl, u = f >> 6;
          const int r0 = (144 * u) / 81, r1 = (144 * u + 143) / 81;
#pragma unroll
          for (int k = 0; k < 3; ++k) {
            const int rr = r0 + k;
            const bool on = t < 144 && rr <= r1;
            off[e][k] = on ? slot(xg * 16 + rr, 8192) + (u - (81 * rr) / 144) * 4096 + (f & 63) * 64 + b * 16 : OOB;
            use[e][k] = on ? 0xFFFFFFFFu : 0u;
          }
          hpa[e] = LP_HP + ((b * 4 + ((cl >> 1) & 3)) * 12 + (cl >> 3)) * 4 + (cl & 1) * 2;
          asm volatile("" : "+v"(hpa[e]), "+v"(off[e][0]), "+v"(off[e][1]), "+v"(off[e][2]), "+v"(use[e][0]), "+v"(use[e][1]), "+v"(use[e][2]));
        }
      int pa = WS_H7 + slot(idx, 8192) + v * 4096 + (32 * ((lane >> 4) & 1) + 16 * (lane >> 5) + cp) * 64 + bj * 16;
      asm volatile("" : "+v"(pa));
      BAR();
      RBAR();
#pragma unroll
      for (int t2 = 0; t2 < 21; ++t2) {
        const int Q = 81 * qq + min(4 * t2 + v, 80), qk = Q >= Qb ? Q - Qb : Q - 144 * U0;
        xd[t2] = *(LAS unsigned*)(L + LP_XD + ((bj * 4 + g) * 148 + qk) * 4);
        asm volatile("" : "+v"(xd[t2]));
      }
      wait_clock(t_start, T_F);
#pragma unroll
      for (int t = -PACE_G; t < 21; ++t) {
        const int tl = t + PACE_G;
        if (tl < 21) {
          const int Q = 81 * qq + 4 * tl + v, u = Q >= Qb ? U0 + 1 : U0, qk = Q - 144 * u;
          if (tl < 20 || v == 0) {
            const GAS v4u* p = (const GAS v4u*)(wb + (unsigned)(qk * 4 * 9216 + u * 128));
            a[tl] = __builtin_nontemporal_load(p);
          } else a[tl] = v4u{0u, 0u, 0u, 0u};
          SCHED();
        }
        if (tl == 20) BAR();
        if (t >= 0) {
          const int Q = 81 * qq + min(4 * t + v, 80);
          const v2u B = {xd[t] & m0, xd[t] & m1};
          if (Q < Qb) { acc[0] = mfma16k16(v2u{a[t].x, a[t].y}, B, acc[0]); acc[1] = mfma16k16(v2u{a[t].z, a[t].w}, B, acc[1]); }
          else        { acc[2] = mfma16k16(v2u{a[t].x, a[t].y}, B, acc[2]); acc[3] = mfma16k16(v2u{a[t].z, a[t].w}, B, acc[3]); }
          SCHED();
        }
      }
#pragma unroll
      for (int k = 0; k < 4; ++k) *(LAS f32x4*)(L + LP_RED + (v * 4 + k) * 1024 + lane * 16) = acc[k];
      BAR();
      if (v == 0 || (v == 1 && 81 * qq + 80 >= Qb)) {
#pragma unroll
        for (int hh = 0; hh < 2; ++hh) {
          f32x4 r = *(LAS f32x4*)(L + LP_RED + (2 * v + hh) * 1024 + lane * 16);
#pragma unroll
          for (int k = 1; k < 4; ++k) r += *(LAS f32x4*)(L + LP_RED + (k * 4 + 2 * v + hh) * 1024 + lane * 16);
#pragma unroll
          for (int k = 0; k < 2; ++k) {
            const auto x = __builtin_amdgcn_permlane32_swap(__float_as_uint(r[k]), __float_as_uint(r[2 + k]), false, false);
            st4(v4u{tlo, genc(__uint_as_float(x[0]), t8), tlo, genc(__uint_as_float(x[1]), t8)}, rws, pa + (8 * k + 4 * hh) * 64, 0);
          }
        }
      } else if (v >= 2) {
        SLP(DLY_H7);
        wait_clock(t_start, T_H7);
        float gs[2], us[2];
        for (int n = 0;; ++n) {
          v4u p[2][3];
#pragma unroll
          for (int e = 0; e < 2; ++e)
#pragma unroll
            for (int k = 0; k < 3; ++k) p[e][k] = hl4(rws, off[e][k], WS_H7);
          unsigned bad = 0;
#pragma unroll
          for (int e = 0; e < 2; ++e) {
            gs[e] = 0.f; us[e] = 0.f;
#pragma unroll
            for (int k = 0; k < 3; ++k) {
              bad |= ((p[e][k].x ^ tlo) | (p[e][k].z ^ tlo) | (((p[e][k].y ^ t8) | (p[e][k].w ^ t8)) & 0xFFu)) & use[e][k];
              gs[e] += gdec(p[e][k].y); us[e] += gdec(p[e][k].w);
            }
          }
          if (__all(bad == 0) || n >= GSPIN) break;
          SLP(DLY_R7);
        }
#pragma unroll
        for (int e = 0; e < 2; ++e) {
          const float hv = gs[e] * __builtin_amdgcn_rcpf(1.0f + __expf(-gs[e])) * us[e];
          if ((v - 2) * 64 + lane + 128 * e < 160) *(LAS u16*)(L + hpa[e]) = (u16)pk(hv, hv);
        }
      }
      BAR();
      const int fe = lane & 31, fL = 8 * xg + (fe >> 2), frr = fe & 3;
      const int oidx = ((fL & 15) >> 2) * 576 + (qq < 8 ? 128 * (qq >> 1) + 8 * (4 * (fL >> 4) + frr) + 4 * (qq & 1) + (fL & 3) : 512 + 4 * (4 * (fL >> 4) + frr) + (fL & 3));
      unsigned xres = 0;
      if (v == 3 && qq < 9) xres = ((const GAS u16*)xin)[oidx];
      const int h5o = WS_H5 + slot(qq * 8 + xg, 16384) + v * 4096;
      int fina = LP_FIN + v * 128 + (lane & 7) * 16 + g * 4;
      asm volatile("" : "+v"(xres), "+v"(fina));
      RBAR();
      if (qq < 9) {
        SLP(DLY_H5);
        v4u d[4];
#pragma unroll
        for (int k = 0; k < 4; ++k) d[k] = hl4(rws, lane * 16, h5o + k * 1024);
        unsigned pend = 15u;
#pragma unroll 1
        for (int n = 0;; ++n) {
#pragma unroll
          for (int k = 0; k < 4; ++k)
            if (pend & (1u << k)) { if (__all((strip4(d[k], tb) & 0xFFu) == 0)) pend &= ~(1u << k); }
          if (pend == 0 || n >= GSPIN) break;
#pragma unroll
          for (int k = 0; k < 4; ++k)
            if (pend & (1u << k)) d[k] = hl4(rws, lane * 16, h5o + k * 1024);
        }
        f32x4 s = (__builtin_bit_cast(f32x4, d[0]) + __builtin_bit_cast(f32x4, d[1])) + (__builtin_bit_cast(f32x4, d[2]) + __builtin_bit_cast(f32x4, d[3]));
#pragma unroll
        for (int r = 0; r < 4; ++r) s[r] += dppz<0x128>(s[r]);
        const auto x01 = __builtin_amdgcn_permlane16_swap(__float_as_uint(s[0]), __float_as_uint(s[1]), false, false);
        const auto x23 = __builtin_amdgcn_permlane16_swap(__float_as_uint(s[2]), __float_as_uint(s[3]), false, false);
        const auto z = __builtin_amdgcn_permlane32_swap(__float_as_uint(__uint_as_float(x01[0]) + __uint_as_float(x01[1])),
                                                        __float_as_uint(__uint_as_float(x23[0]) + __uint_as_float(x23[1])), false, false);
        *(LAS float*)(L + fina) = __uint_as_float(z[0]) + __uint_as_float(z[1]);
        BAR();
        if (v == 3) {
          float y = bf2f(xres);
#pragma unroll
          for (int k = 0; k < 4; ++k) y += *(LAS float*)(L + LP_FIN + k * 128 + fe * 4);
          if (lane < 32) ((GAS u16*)out)[oidx] = (u16)pk(y, y);
          if (qq == 0 && xg == 0) st2(v2u{tlo, thi}, rws, lane == 0 ? 0 : OOB, WS_W);
        }
      }
    }
    STAMP_OUT();
    return;
  }

  const int ab = xg >> 1;
  const int r32 = lane & 31, hh = lane >> 5;
  v2u wa_;
  if (w < 4) {
    const rsrc_t rc = mk(ckv, 4 * 4096 * 512 * 2);
    v2u wb_;
    asm volatile("buffer_load_dwordx2 %0, off, %1, %2 sc1" : "=v"(wb_) : "s"(rws), "s"(WS_W) : "memory");
    {
      int vo[4];
#pragma unroll
      for (int k = 0; k < 4; ++k) vo[k] = g * 1024 + ((i16 ^ ((g << 2) | k)) * 16);
      const int so = (idx * 128 + 32 * w) * 1024;
      SLP(DLY_C);
#pragma unroll
      for (int n = 0; n < 32; ++n) {
        if (n >= PACE_C) VMCNT(PACE_C - 1);
        dma16(rc, vo[(n >> 2) & 3], so + (n >> 2) * 4096 + (n & 3) * 256, lbase + (n & 3) * 32768 + (32 * w + 4 * (n >> 2)) * 256);
      }
    }
    RBAR();
    asm volatile("" : "+v"(wb_));
    wa_ = wb_;
  } else {
    wa_ = hl2(rws, 0, WS_W);
    if (w == 4) {
      v2u xr[9];
#pragma unroll
      for (int t = 0; t < 9; ++t) xr[t] = ld2<0>(rx, g * 1152 + i16 * 8, t * 128);
      const rsrc_t rq = mk(wq, 576 * 4096 * 2);
      const int vo = 2 * g * 8192 + i16 * 16, so = 8 * jq * 8192 + hd * 256;
      v4u a[18][2];
#pragma unroll
      for (int o = 0; o < 18; ++o) { a[o][0] = ld4<NT>(rq, vo, so + (32 * o) * 8192); a[o][1] = ld4<NT>(rq, vo, so + (32 * o + 1) * 8192); }
      SCHED();
      float s = 0.f, q = 0.f;
#pragma unroll
      for (int t = 0; t < 9; ++t) {
        *(LAS v2u*)(L + LA_QA + g * 1168 + i16 * 8 + t * 128) = xr[t];
        s = dot2(xr[t].x, 0x3F803F80u, s); q = dot2(xr[t].x, xr[t].x, q);
        s = dot2(xr[t].y, 0x3F803F80u, s); q = dot2(xr[t].y, xr[t].y, q);
      }
      FENCE();
      unsigned xp[18];
#pragma unroll
      for (int o = 0; o < 18; ++o) xp[o] = *(LAS unsigned*)(L + LA_QA + bj * 1168 + (4 * jq + 16 * o + g) * 4);
      const float mean = row_sum(s) * (1.0f / 576.0f), msq = row_sum(q) * (1.0f / 576.0f);
      const float rs0 = __builtin_amdgcn_rsqf(fmaxf(fmaf(-mean, mean, msq), 0.f) + 1e-5f);
      const float rstd = __uint_as_float(__builtin_amdgcn_ds_bpermute(bj * 64, __float_as_uint(rs0)));
      const float nb = __uint_as_float(__builtin_amdgcn_ds_bpermute(bj * 64, __float_as_uint(-mean * rs0)));
      v4u Bq[18];
#pragma unroll
      for (int o = 0; o < 18; ++o) {
        Bq[o] = bdiag(pk(fmaf(bf2f(xp[o]), rstd, nb), fmaf(__uint_as_float(xp[o] & 0xFFFF0000u), rstd, nb)), sel);
        asm volatile("" : "+v"(Bq[o]));
      }
      const u64 tg = (((u64)wa_.y << 32) | wa_.x) + 0x9E3779B97F4A7C15ull;
      unsigned ql = (unsigned)tg, q8 = (unsigned)(tg >> 32) & 0xFFu;
      asm volatile("" : "+v"(ql), "+v"(q8));
      SCHED();
      f32x4 qx = {0.f, 0.f, 0.f, 0.f}, qy = qx;
#pragma unroll
      for (int o = 0; o < 18; ++o) {
        swap24(a[o][0], a[o][1]);
        qx = mfma16(a[o][0], Bq[o], qx);
        qy = mfma16(a[o][1], Bq[o], qy);
      }
      const int po = WS_H1 + slot(idx, 4096);
      st4(v4u{ql, genc(qx[0], q8), ql, genc(qx[1], q8)}, rws, lane * 16, po);
      st4(v4u{ql, genc(qx[2], q8), ql, genc(qx[3], q8)}, rws, lane * 16, po + 1024);
      st4(v4u{ql, genc(qy[0], q8), ql, genc(qy[1], q8)}, rws, lane * 16, po + 2048);
      st4(v4u{ql, genc(qy[2], q8), ql, genc(qy[3], q8)}, rws, lane * 16, po + 3072);
    }
    RBAR();
  }
  MAKE_TAG(wa_);
  unsigned e4[4];
#pragma unroll
  for (int k = 0; k < 4; ++k) e4[k] = lsbtag(tlo, thi, 4 * i16 + k);
  if (w < 4) {
    v4u fa[32];
    {
      const int fr = ((r32 & 3) << 2) | ((r32 >> 2) & 3);
      const int ca = (32 * w + r32) * 256;
#define QK_READ(Q) _Pragma("unroll") for (int k = 0; k < 8; ++k) fa[(Q)*8 + k] = *(LAS v4u*)(L + (Q)*32768 + ca + (((2 * k + hh) ^ fr) * 16)); SCHED();
      VMCNT(0); QK_READ(0) QK_READ(1) QK_READ(2) QK_READ(3)
    }
    int qb[4];
#pragma unroll
    for (int k = 0; k < 4; ++k) { qb[k] = LA_QA + ((lane ^ ((2 * k + hh) & 7)) * 16); asm volatile("" : "+v"(qb[k])); }
    const int q4 = i16 >> 2, lowc = 2 * (g & 1) + (cp >> 1);
    int ta[4][2];
#pragma unroll
    for (int t = 0; t < 4; ++t)
#pragma unroll
      for (int t2 = 0; t2 < 2; ++t2) {
        ta[t][t2] = w * 32768 + (8 * t2 + 4 * hh + q4) * 256 + ((((t ^ q4) << 2) | (lowc ^ (2 * t2 + hh))) * 16) + 8 * (cp & 1);
        asm volatile("" : "+v"(ta[t][t2]));
      }
    int mxa = (r32 >> 4) * 320 + (r32 & 15) * 4, pba = LA_PB + lane * 16;
    unsigned e0 = lsbtag(tlo, thi, 4 * (r32 >> 1)), e1 = lsbtag(tlo, thi, 4 * (r32 >> 1) + 1), e2 = lsbtag(tlo, thi, 4 * (r32 >> 1) + 2), e3 = lsbtag(tlo, thi, 4 * (r32 >> 1) + 3);
    const int sp = idx & 31;
    int h3v = ((hh * 4 + (r32 & 1)) ^ (sp & 5)) * QB + (r32 >> 1) * 16 + WS_H3 + (sp * 16 + ab * 4 + w) * 256;
    asm volatile("" : "+v"(mxa), "+v"(pba), "+v"(e0), "+v"(e1), "+v"(e2), "+v"(e3), "+v"(h3v));
    BAR();
    v4u cb[32];
#define TR_READ(n) do { const v2u x0_ = __builtin_bit_cast(v2u, __builtin_amdgcn_ds_read_tr16_b64_v4i16((LAS s16x4*)(L + ta[(n) >> 3][0] + ((n) & 7) * 4096))); \
    const v2u x1_ = __builtin_bit_cast(v2u, __builtin_amdgcn_ds_read_tr16_b64_v4i16((LAS s16x4*)(L + ta[(n) >> 3][1] + ((n) & 7) * 4096)));                 \
    cb[n] = v4u{x0_.x, x0_.y, x1_.x, x1_.y}; } while (0)
    f32x16 sacc;
#pragma unroll
    for (int k = 0; k < 16; ++k) sacc[k] = 0.f;
    {
      v4u fb[32];
#pragma unroll
      for (int s = 0; s < 6; ++s) fb[s] = *(LAS v4u*)(L + qb[s & 3] + s * 1024);
      SCHED();
#pragma unroll
      for (int s = 0; s < 32; ++s) {
        if (s + 6 < 32) fb[s + 6] = *(LAS v4u*)(L + qb[(s + 6) & 3] + (s + 6) * 1024);
        if (s >= 26) TR_READ(s - 26);
        sacc = mfma32(fa[s], fb[s], sacc);
        SCHED();
      }
    }
    float mw = sacc[0];
#pragma unroll
    for (int k = 1; k < 16; ++k) mw = fmaxf(mw, sacc[k]);
    mw = x32_max(mw);
    asm volatile("s_waitcnt lgkmcnt(0)" ::: "memory");
    *(LAS float*)(L + mxa + w * 32768) = mw;
    BAR();
    const float mx = gdec(genc(fmaxf(fmaxf(*(LAS float*)(L + mxa), *(LAS float*)(L + mxa + 32768)), fmaxf(*(LAS float*)(L + mxa + 65536), *(LAS float*)(L + mxa + 98304))), 0u));
    float ls = 0.f;
#pragma unroll
    for (int k = 0; k < 16; ++k) { sacc[k] = __builtin_amdgcn_exp2f(sacc[k] - mx); ls += sacc[k]; }
#pragma unroll
    for (int s2 = 0; s2 < 2; ++s2)
      *(LAS v4u*)(L + pba + (2 * w + s2) * 1024) =
          v4u{pk(sacc[8 * s2], sacc[8 * s2 + 1]), pk(sacc[8 * s2 + 2], sacc[8 * s2 + 3]), pk(sacc[8 * s2 + 4], sacc[8 * s2 + 5]), pk(sacc[8 * s2 + 6], sacc[8 * s2 + 7])};
    ls = x32_sum(ls);
    *(LAS float*)(L + mxa + 640 + w * 32768) = ls;
    BAR();
    {
      v4u pf[8];
#pragma unroll
      for (int k = 0; k < 8; ++k) pf[k] = *(LAS v4u*)(L + pba + k * 1024);
      f32x4 l4;
      if (w == 0) l4 = f32x4{*(LAS float*)(L + mxa + 640), *(LAS float*)(L + mxa + 640 + 32768), *(LAS float*)(L + mxa + 640 + 65536), *(LAS float*)(L + mxa + 640 + 98304)};
      SCHED();
      f32x16 o[4];
      unsigned p01[16], pq[16];
      const bool odd = lane & 1;
#pragma unroll
      for (int t = 0; t < 4; ++t)
#pragma unroll
        for (int k = 0; k < 16; ++k) o[t][k] = 0.f;
#pragma unroll
      for (int n = 0; n < 32; ++n) {
        if (n + 6 < 32) TR_READ(n + 6);
        o[n >> 3] = mfma32(pf[n & 7], cb[n], o[n >> 3]);
        if (n == 0 && w == 0)
          st4(v4u{tlo, genc(mx, t8), tlo, genc((l4[0] + l4[1]) + (l4[2] + l4[3]), t8)}, rws, lane < 32 ? r32 * 16 : OOB, WS_ML + slot(idx, 512));
        if (n >= 17 && n < 32 && n != 24) {
          const int j = n < 24 ? n - 17 : n - 18;
          if (j < 8) { pq[2 * j] = pk(o[0][2 * j], o[1][2 * j]); pq[2 * j + 1] = pk(o[0][2 * j + 1], o[1][2 * j + 1]); asm volatile("" : "+v"(pq[2 * j]), "+v"(pq[2 * j + 1])); }
          if (j >= 6) {
            const int k = j - 6;
            p01[2 * k] = lsbput(odd ? pq[2 * k + 1] : pq[2 * k], e0);
            p01[2 * k + 1] = __builtin_amdgcn_update_dpp(0, lsbput(odd ? pq[2 * k] : pq[2 * k + 1], e2), 0xB1, 0xf, 0xf, true);
            asm volatile("" : "+v"(p01[2 * k]), "+v"(p01[2 * k + 1]));
          }
        }
        SCHED();
      }
#pragma unroll
      for (int k = 0; k < 8; ++k) {
        const unsigned pa = pk(o[2][2 * k], o[3][2 * k]), pb = pk(o[2][2 * k + 1], o[3][2 * k + 1]);
        st4(v4u{p01[2 * k], lsbput(odd ? pb : pa, e1), p01[2 * k + 1], (unsigned)__builtin_amdgcn_update_dpp(0, lsbput(odd ? pa : pb, e3), 0xB1, 0xf, 0xf, true)},
            rws, h3v, ((2 * (k & 1) + 8 * (k >> 1)) ^ (sp & 26)) * QB);
      }
    }
    RBAR();
    RBAR();
    RBAR();
  } else {
    const int v = w - 4;
    const rsrc_t rv = mk(wuv, 512 * 4096 * 2);
    v4u uw[8];
#define TL(i) do { uw[i] = ld4<NT>(rv, g * 32 * 8192 + i16 * 16, (128 * jq + 8 * v + ((i) ^ (hd & 1))) * 8192 + hd * 256); SCHED(); } while (0)
    {
      v4u qa[8];
      SLP(DLY_QA);
#pragma unroll
      for (int k = 0; k < 8; ++k) qa[k] = hl4(rws, lane * 16, WS_QA + ab * 2048 + slot(8 * v + k, 1024));
      SCHED();
#pragma unroll 1
      for (int n = 0;; ++n) {
        unsigned bad = 0;
#pragma unroll
        for (int k = 0; k < 8; ++k) bad |= strip4(qa[k], e4);
        if (__all((bad & LSBM) == 0) || n >= GSPIN) break;
        SLP(DLY_R2);
#pragma unroll
        for (int k = 0; k < 8; ++k) qa[k] = hl4(rws, lane * 16, WS_QA + ab * 2048 + slot(8 * v + k, 1024));
      }
#pragma unroll
      for (int k = 0; k < 8; ++k) *(LAS v4u*)(L + LA_QA + (lane >> 1) * 1024 + ((((lane & 1) * 32 + 8 * v + k) ^ (lane & 7)) * 16)) = qa[k];
    }
    BAR();
#pragma unroll
    for (int i = 0; i < 4; ++i) TL(i);
    RBAR();
#pragma unroll
    for (int i = 4; i < 8; ++i) TL(i);
    RBAR();
    const int e7 = lane & 7, q4 = i16 >> 2;
    int cta = LA_CTX + v * 288 + (32 * g + e7) * 2, ctr = LA_CTX + cp * 288 + g * 16;
    int h4a = ((2 * (g & 1) + (i16 & 1)) * 16 + (2 * (g >> 1) + (i16 >> 3)) * 4 + ((i16 >> 1) & 3)) * 16 + WS_H4 + slot(hd * 4 + v, 4096) + jq * 1024;
    const unsigned src = (lane & 8) ? 0x0302u : 0x0100u, s1 = (e7 & 1) ? ((src << 16) | 0x0C0Cu) : (0x0C0C0000u | src);
    unsigned s8[4];
    int uwa[4], bva[2][2];
#pragma unroll
    for (int k = 0; k < 4; ++k) {
      s8[k] = (e7 >> 1) == k ? s1 : 0x0C0C0C0Cu;
      uwa[k] = LA_UVW + (32 * v + g) * 256 + ((i16 ^ ((g << 2) | k)) * 16);
      bva[k >> 1][k & 1] = LA_UVW + (8 * g + 4 * (k & 1) + q4) * 256 + (((2 * (2 * v + (k >> 1)) + (cp >> 1)) ^ ((q4 << 2) | ((2 * g + (k & 1)) & 3))) * 16) + 8 * (cp & 1);
      asm volatile("" : "+v"(s8[k]), "+v"(uwa[k]), "+v"(bva[k >> 1][k & 1]));
    }
    asm volatile("" : "+v"(cta), "+v"(ctr), "+v"(h4a));
    const int h3l = ((hd & 3) ^ g) * QB + g * 4096 + i16 * 16;
#define ML_LOAD() hl4(rws, slot(r32, 512), WS_ML + v * 1024 + hd * 16)
#define ML_OK(V) __all((((V).x ^ tlo) | ((V).z ^ tlo) | ((((V).y ^ t8) | ((V).w ^ t8)) & 0xFFu)) == 0)
#define H3_LOADS() _Pragma("unroll") for (int k = 0; k < 8; ++k) d[k] = hl4(rws, h3l, WS_H3 + ((hd >> 2) ^ k) * 4 * QB + k * 16384 + (v * 4 + jq) * 256); SCHED()
    v4u d[8], bk[8];
    v4u ml;
    VMCNT(0);
    RBAR();
#pragma unroll
    for (int k = 0; k < 8; ++k) *(LAS v4u*)(L + uwa[k & 3] + k * 1024) = uw[k];
    BAR();
    v4u bv[2][4];
#pragma unroll
    for (int t = 0; t < 2; ++t)
#pragma unroll
      for (int ks = 0; ks < 4; ++ks) {
        const v2u x0 = __builtin_bit_cast(v2u, __builtin_amdgcn_ds_read_tr16_b64_v4i16((LAS s16x4*)(L + bva[t][0] + ks * 8192)));
        const v2u x1 = __builtin_bit_cast(v2u, __builtin_amdgcn_ds_read_tr16_b64_v4i16((LAS s16x4*)(L + bva[t][1] + ks * 8192)));
        bv[t][ks] = v4u{x0.x, x0.y, x1.x, x1.y};
      }
    SCHED();
    {
      SLP(DLY_M0);
      for (int n = 0;; ++n) {
        ml = ML_LOAD();
        if (ML_OK(ml) || n >= GSPIN) break;
        SLP(DLY_RM);
      }
      SLP(DLY_H3);
      H3_LOADS();
      const float m = gdec(ml.y), M = x16_max(row_max(m));
      const float e = __builtin_amdgcn_exp2f(m - M);
      const float al = e * LSB_COMP * __builtin_amdgcn_rcpf(x16_sum(row_sum(e * gdec(ml.w))));
      const unsigned ah = pk(al, al) & 0xFFFFu;
      const unsigned hl = ah | (pk(al - bf2f(ah), 0.f) << 16);
#pragma unroll
      for (int k = 0; k < 8; ++k) { bk[k] = bdiag(__builtin_amdgcn_ds_bpermute((4 * k + g) * 4, hl), s8); asm volatile("" : "+v"(bk[k])); }
      f32x4 c;
#pragma unroll 1
      for (int n = 0;; ++n) {
        unsigned bad = 0;
        c = f32x4{0.f, 0.f, 0.f, 0.f};
#pragma unroll
        for (int k = 0; k < 8; ++k) { bad |= strip4(d[k], e4); c = mfma16(d[k], bk[k], c); }
        if (__all((bad & LSBM) == 0) || n >= GSPIN) break;
#pragma unroll
        for (int k = 0; k < 8; ++k) d[k] = hl4(rws, h3l, WS_H3 + ((hd >> 2) ^ k) * 4 * QB + k * 16384 + (v * 4 + jq) * 256);
      }
#pragma unroll
      for (int r = 0; r < 4; ++r) {
        const float y = c[r] + dppz<0x128>(c[r]);
        *(LAS u16*)(L + cta + 16 * r) = (u16)pk(y, y);
      }
    }
    BAR();
    {
      v4u af[4];
#pragma unroll
      for (int ks = 0; ks < 4; ++ks) af[ks] = *(LAS v4u*)(L + ctr + 64 * ks);
      SCHED();
      f32x4 c0 = {0.f, 0.f, 0.f, 0.f}, c1 = c0;
#pragma unroll
      for (int ks = 0; ks < 4; ++ks) { c0 = mfma16(af[ks], bv[0][ks], c0); c1 = mfma16(af[ks], bv[1][ks], c1); }
      const f32x4 ct = g < 2 ? c0 : c1;
      const float x0 = (g & 1) ? ct[2] : ct[0], x1 = (g & 1) ? ct[3] : ct[1];
      const float n0 = dppz<0xB1>(x0), n1 = dppz<0xB1>(x1);
      st4(v4u{tlo, genc((lane & 1) ? n1 : x0, t8), tlo, genc((lane & 1) ? x1 : n0, t8)}, rws, h4a, 0);
    }
  }
  STAMP_OUT();
}

extern "C" __global__ void fused_decode_end() {}

__global__ void k_spin(char* ws){
  const u64* o=(const u64*)(ws+WS_END);
  int t=threadIdx.x; u64 mn=~0ull;
  __shared__ u64 s0[256]; __shared__ double s3[256]; __shared__ int s4[256];
  for(int w=0;w<8;w++){ u64 a=o[2*(t*8+w)]; mn=a<mn?a:mn; }
  s0[t]=mn; u64 me=o[2*(t*8)]; __syncthreads();
  for(int i=0;i<256;i++) mn=s0[i]<mn?s0[i]:mn;
  bool on = (SEL);
  s3[t]= on? (double)(me-mn):0.0; s4[t]=on; __syncthreads();
  if(t==0){ double sum=0; int c=0; for(int i=0;i<256;i++){ sum+=s3[i]; c+=s4[i]; }
    u64 TT=(u64)(sum/c*100.0); if(TT>1000000) TT=1000000;
    u64 t0=__builtin_amdgcn_s_memrealtime();
    while(__builtin_amdgcn_s_memrealtime()-t0 < TT) __builtin_amdgcn_s_sleep(8);
  }
}
extern "C" void kernel_launch(void* const* d_in, const int* in_sizes, int n_in, void* d_out, int out_size, void* d_ws, size_t ws_size,
                              hipStream_t stream) {
  (void)hipFuncSetAttribute((const void*)fused_decode, hipFuncAttributeMaxDynamicSharedMemorySize, LDS_BYTES);
  fused_decode<<<256, 512, LDS_BYTES, stream>>>((const u16*)d_in[0], (const u16*)d_in[1], (const u16*)d_in[2], (const u16*)d_in[3],
                                                (const u16*)d_in[4], (const u16*)d_in[5], (const u16*)d_in[7], (const u16*)d_in[8],
                                                (const u16*)d_in[9], (u16*)d_out, (char*)d_ws);
  k_spin<<<1,256,0,stream>>>((char*)d_ws);
}
```

```cpp
#include <hip/hip_runtime.h>
#include <stdint.h>

typedef unsigned short u16;
typedef unsigned long long u64;
typedef unsigned v4u __attribute__((ext_vector_type(4)));
typedef unsigned v2u __attribute__((ext_vector_type(2)));
typedef float f32x2 __attribute__((ext_vector_type(2)));
typedef float f32x4 __attribute__((ext_vector_type(4)));
typedef float f32x16 __attribute__((ext_vector_type(16)));
typedef short s16x4 __attribute__((ext_vector_type(4)));
typedef __bf16 bf16x8 __attribute__((ext_vector_type(8)));
typedef __bf16 bf16x2 __attribute__((ext_vector_type(2)));
typedef __amdgpu_buffer_rsrc_t rsrc_t;
#define GAS __attribute__((address_space(1)))
#define LAS __attribute__((address_space(3)))
#define DI __device__ __forceinline__

#define BLOCK_ID blockIdx.x
constexpr int NT = 2;
constexpr int PACE_C = 7;
constexpr int PACE_K = 4;
constexpr int PACE_G = 4;
constexpr int PACE_W = 6;
#define DLY_C 40
#define DLY_K 12
#define DLY_H1 2
#define DLY_R1 7
#define DLY_QA 8
#define DLY_R2 12
#define DLY_M0 12
#define DLY_RM 4
#define DLY_H3 1
#define DLY_H7 22
#define DLY_R7 2
#define DLY_R4 2
#define DLY_H5 18
constexpr unsigned T_F = 350, T_H7 = 915;
#define SLP(N) __builtin_amdgcn_s_sleep(N)

constexpr int SC1 = 16;
constexpr int GSPIN = 1 << 13;

constexpr int QB = 131072, WIN = 16 * QB;
constexpr int WS_H1 = 0;
constexpr int WS_H4 = 32768;
constexpr int WS_QA = 65536;
constexpr int WS_ML = 73728;
constexpr int WS_W = 81920;
constexpr int WS_H7 = WIN;
constexpr int WS_H5 = 2 * WIN;
constexpr int WS_H3 = 3 * WIN;
DI constexpr int slot(int p, int size) { return (p & 15) * QB + (p >> 4) * size; }
DI constexpr int blk5(int n) { return (n & 1) * 8 * QB + (n >> 1) * 16384; }
constexpr int WS_END = WS_H3 + 128 * 32768;
constexpr int OOB = (int)0x80000000;

constexpr int LP_XD = 5120, LP_QB = 15360, LP_QO = 16896, LP_RED = 18432, LP_HP = 34816, LP_B8 = 35840, LP_FIN = 39936, LP_UK = 40960;
constexpr int LA_QA = 131072, LA_PB = LA_QA + 1024, LA_CTX = LA_QA + 9216, LA_UVW = 0;
constexpr int LDS_BYTES = 163840;

DI rsrc_t mk(const void* p, int bytes) { return __builtin_amdgcn_make_buffer_rsrc((void*)p, (short)0, bytes, 0x00020000); }
template <int AUX> DI v4u ld4(rsrc_t r, int vo, int so) { return __builtin_amdgcn_raw_buffer_load_b128(r, vo, so, AUX); }
template <int AUX> DI v2u ld2(rsrc_t r, int vo, int so) { return __builtin_amdgcn_raw_buffer_load_b64(r, vo, so, AUX); }
DI v4u hl4(rsrc_t r, int vo, int so) { asm volatile("" : "+v"(vo)); return __builtin_amdgcn_raw_buffer_load_b128(r, vo, so, SC1); }
DI v2u hl2(rsrc_t r, int vo, int so) { asm volatile("" : "+v"(vo)); return __builtin_amdgcn_raw_buffer_load_b64(r, vo, so, SC1); }
DI void touch(rsrc_t r, int vo, int so) { v4u t; asm volatile("buffer_load_dwordx4 %0, %1, %2, %3 offen sc1\n\ts_waitcnt vmcnt(0)" : "=&v"(t) : "v"(vo), "s"(r), "s"(so) : "memory"); }
DI void st4(v4u v, rsrc_t r, int vo, int so) { __builtin_amdgcn_raw_buffer_store_b128(v, r, vo + so, 0, SC1); }
DI void st2(v2u v, rsrc_t r, int vo, int so) { __builtin_amdgcn_raw_buffer_store_b64(v, r, vo, so, SC1); }
DI void dma16(rsrc_t r, int vo, int so, unsigned lds) {
  asm volatile("s_mov_b32 m0, %0\n\ts_nop 0\n\tbuffer_load_dwordx4 %1, %2, %3 offen nt lds" ::"s"(lds), "v"(vo), "s"(r), "s"(so) : "memory");
}
#define VMCNT(n) asm volatile("s_waitcnt vmcnt(%0)" ::"n"(n) : "memory")
#define FENCE() asm volatile("" ::: "memory")
#define SCHED() __builtin_amdgcn_sched_barrier(0)

DI float bf2f(unsigned v) { return __uint_as_float(v << 16); }
DI unsigned pk(float a, float b) { f32x2 f = {a, b}; return __builtin_bit_cast(unsigned, __builtin_convertvector(f, bf16x2)); }
DI f32x4 mfma16(v4u a, v4u b, f32x4 c) {
  return __builtin_amdgcn_mfma_f32_16x16x32_bf16(__builtin_bit_cast(bf16x8, a), __builtin_bit_cast(bf16x8, b), c, 0, 0, 0);
}
DI f32x4 mfma16k16(v2u a, v2u b, f32x4 c) {
  return __builtin_amdgcn_mfma_f32_16x16x16bf16_1k(__builtin_bit_cast(s16x4, a), __builtin_bit_cast(s16x4, b), c, 0, 0, 0);
}
DI f32x16 mfma32(v4u a, v4u b, f32x16 c) {
  return __builtin_amdgcn_mfma_f32_32x32x16_bf16(__builtin_bit_cast(bf16x8, a), __builtin_bit_cast(bf16x8, b), c, 0, 0, 0);
}
template <int CTRL, int RM> DI float dppf(float old, float v) {
  return __builtin_bit_cast(float, __builtin_amdgcn_update_dpp(__builtin_bit_cast(int, old), __builtin_bit_cast(int, v), CTRL, RM, 0xf, false));
}
template <int CTRL> DI float dppz(float v) {
  return __builtin_bit_cast(float, __builtin_amdgcn_update_dpp(0, __builtin_bit_cast(int, v), CTRL, 0xf, 0xf, true));
}
DI float row_sum(float v) {
  v += dppz<0xB1>(v); v += dppz<0x4E>(v); v += dppz<0x124>(v); v += dppz<0x128>(v);
  return v;
}
DI float dot2(unsigned a, unsigned b, float c) {
  return __builtin_amdgcn_fdot2_f32_bf16(__builtin_bit_cast(bf16x2, a), __builtin_bit_cast(bf16x2, b), c, false);
}
DI float row_max(float v) {
  v = fmaxf(v, dppf<0xB1, 0xf>(v, v)); v = fmaxf(v, dppf<0x4E, 0xf>(v, v));
  v = fmaxf(v, dppf<0x124, 0xf>(v, v)); v = fmaxf(v, dppf<0x128, 0xf>(v, v));
  return v;
}
DI float x16_sum(float v) { auto r = __builtin_amdgcn_permlane16_swap(__float_as_uint(v), __float_as_uint(v), false, false); return __uint_as_float(r[0]) + __uint_as_float(r[1]); }
DI float x32_sum(float v) { auto r = __builtin_amdgcn_permlane32_swap(__float_as_uint(v), __float_as_uint(v), false, false); return __uint_as_float(r[0]) + __uint_as_float(r[1]); }
DI float x16_max(float v) { auto r = __builtin_amdgcn_permlane16_swap(__float_as_uint(v), __float_as_uint(v), false, false); return fmaxf(__uint_as_float(r[0]), __uint_as_float(r[1])); }
DI float x32_max(float v) { auto r = __builtin_amdgcn_permlane32_swap(__float_as_uint(v), __float_as_uint(v), false, false); return fmaxf(__uint_as_float(r[0]), __uint_as_float(r[1])); }

#define RBAR() asm volatile("s_barrier" ::: "memory")
#define BAR() asm volatile("s_waitcnt lgkmcnt(0)\n\ts_barrier" ::: "memory")
DI unsigned genc(float v, unsigned t8) { return ((__float_as_uint(v) + 0x80u) & 0xFFFFFF00u) | t8; }
DI float gdec(unsigned hi) { return __uint_as_float(hi & 0xFFFFFF00u); }

#define STAMP_DECL
#define STAMP_OUT()
DI void wait_clock(u64 t0, unsigned ticks) {
#pragma unroll 1
  for (int n = 0; n < 4096 && (unsigned)(__builtin_amdgcn_s_memrealtime() - t0) < ticks; ++n) __builtin_amdgcn_s_sleep(1);
}

DI v4u bdiag(unsigned P, const unsigned (&s)[4]) {
  v4u b;
  b.x = __builtin_amdgcn_perm(P, P, s[0]); b.y = __builtin_amdgcn_perm(P, P, s[1]);
  b.z = __builtin_amdgcn_perm(P, P, s[2]); b.w = __builtin_amdgcn_perm(P, P, s[3]);
  return b;
}
DI void bdiag_sel(int cp, unsigned (&s)[4]) {
  const unsigned lo = (cp & 1) ? 0x01000C0Cu : 0x0C0C0100u, hi = (cp & 1) ? 0x03020C0Cu : 0x0C0C0302u, z = 0x0C0C0C0Cu;
  s[0] = cp < 2 ? lo : z; s[1] = cp < 2 ? z : lo; s[2] = cp < 2 ? hi : z; s[3] = cp < 2 ? z : hi;
}

DI unsigned lsbtag(unsigned tlo, unsigned thi, int p) { return (((p < 32 ? tlo : thi) >> (p & 31)) & 1u) | (((tlo ^ (unsigned)p) & 1u) << 16); }
constexpr unsigned LSBM = 0x00010001u;
DI unsigned pk7(float a, float b) { return pk(__uint_as_float(__float_as_uint(a) + 0x8000u), __uint_as_float(__float_as_uint(b) + 0x8000u)); }
DI unsigned lsbput(unsigned v, unsigned e) { return (v & ~LSBM) | e; }
DI unsigned strip4(v4u& d, const unsigned (&e)[4]) { d.x ^= e[0]; d.y ^= e[1]; d.z ^= e[2]; d.w ^= e[3]; return (d.x | d.y) | (d.z | d.w); }
DI void swap24(v4u& a, v4u& b) {
  unsigned az = a.z, aw = a.w, bx = b.x, by = b.y;
  asm volatile("v_swap_b32 %0, %2\n\tv_swap_b32 %1, %3\n\ts_nop 1" : "+v"(az), "+v"(aw), "+v"(bx), "+v"(by));
  a.z = az; a.w = aw; b.x = bx; b.y = by;
}
DI v4u tag8(f32x4 c, const unsigned (&t)[4]) {
  return v4u{(__float_as_uint(c[0]) & 0xFFFFFF00u) | t[0], (__float_as_uint(c[1]) & 0xFFFFFF00u) | t[1],
             (__float_as_uint(c[2]) & 0xFFFFFF00u) | t[2], (__float_as_uint(c[3]) & 0xFFFFFF00u) | t[3]};
}
extern "C" __global__ void __launch_bounds__(512, 2)
fused_decode(const u16* __restrict__ xin, const u16* __restrict__ ckv, const u16* __restrict__ wq, const u16* __restrict__ wuk,
             const u16* __restrict__ wuv, const u16* __restrict__ wo, const u16* __restrict__ w1, const u16* __restrict__ w3,
             const u16* __restrict__ w2, u16* __restrict__ out, char* ws) {
  extern __shared__ __attribute__((aligned(16))) char smem[];
  LAS char* L = (LAS char*)smem;
  const unsigned lbase = (unsigned)(size_t)smem;
  const int lane = threadIdx.x & 63, w = __builtin_amdgcn_readfirstlane(threadIdx.x >> 6);
  const int bid = BLOCK_ID, xg = bid & 7, qd_ = bid >> 3, qq = qd_ & 15, idx = xg * 16 + qq, hd = idx >> 2, jq = idx & 3;
  const int i16 = lane & 15, g = lane >> 4, bj = i16 >> 2, cp = lane & 3;
  STAMP_DECL;
  if (w == 7) {
    u64 pc; unsigned back, len;
    asm volatile("s_getpc_b64 %0\n.Lpf_here:\n\ts_mov_b32 %1, .Lpf_here-fused_decode\n\ts_mov_b32 %2, fused_decode_end-fused_decode" : "=s"(pc), "=s"(back), "=s"(len));
    const rsrc_t rcode = mk((const void*)(pc - back), (int)min(len, 32768u));
    v4u dummy;
    asm volatile("s_nop 4\n\tbuffer_load_dwordx4 %0, %1, %2, 0 offen\n\ts_waitcnt vmcnt(0)" : "=&v"(dummy) : "v"(qd_ * 1024 + lane * 16), "s"(rcode) : "memory");
  }
  asm volatile("" ::"s"(xin), "s"(ckv), "s"(wq), "s"(wuk), "s"(wuv), "s"(wo), "s"(w1), "s"(w3), "s"(w2), "s"(out), "s"(ws));
  u64 t_start;
  asm volatile("s_memrealtime %0\n\ts_waitcnt lgkmcnt(0)" : "=s"(t_start) : "s"(ws) : "memory");
  const rsrc_t rws = mk(ws, WS_END);
  const rsrc_t rx = mk(xin, 4 * 576 * 2);
  unsigned sel[4];
  bdiag_sel(cp, sel);
#define MAKE_TAG(W) const u64 tag_ = (((u64)(W).y << 32) | (W).x) + 0x9E3779B97F4A7C15ull;  \
  const unsigned tlo = (unsigned)tag_, thi = (unsigned)(tag_ >> 32), t8 = thi & 0xFFu;        \
  const unsigned tb[4] = {tlo & 0xFFu, (tlo >> 8) & 0xFFu, (tlo >> 16) & 0xFFu, tlo >> 24}
  const int vo5 = (lane >> 3) * QB + (lane & 7) * 16;

  if (qd_ >= 16) {
    const int so5 = WS_H5 + idx * 128;
    if (w < 4) {
      v4u ka[2][4];
      v2u wv_;
      asm volatile("buffer_load_dwordx2 %0, off, %1, %2 sc1" : "=v"(wv_) : "s"(rws), "s"(WS_W) : "memory");
      {
        const rsrc_t rk = mk(wuk, 512 * 4096 * 2);
        const int ukx = 4 * (hd & 1);
        SLP(DLY_K);
#pragma unroll
        for (int n = 0; n < 8; ++n) {
          if (n >= PACE_K) VMCNT(PACE_K - 1);
          dma16(rk, g * 8192 + ((i16 ^ ((g << 2) | (n & 3))) * 16), (128 * jq + 32 * w + 4 * (n ^ ukx)) * 8192 + hd * 256, lbase + LP_UK + (8 * w + (n ^ ukx)) * 1024);
        }
        VMCNT(0);
        const int fr = ((i16 & 3) << 2) | (i16 >> 2);
#pragma unroll
        for (int t = 0; t < 2; ++t)
#pragma unroll
          for (int s = 0; s < 4; ++s) {
            ka[t][s] = *(LAS v4u*)(L + LP_UK + (32 * w + 16 * t + i16) * 256 + (((4 * s + g) ^ fr) * 16));
            asm volatile("" : "+v"(ka[t][s]));
          }
      }
      asm volatile("" : "+v"(wv_));
      MAKE_TAG(wv_);
      unsigned eq[4];
#pragma unroll
      for (int k = 0; k < 4; ++k) { eq[k] = lsbtag(tlo, thi, 4 * i16 + k); asm volatile("" : "+v"(eq[k])); }
      SLP(DLY_H1);
      {
        const int blk = w & 1, half = w >> 1;
        float q0, q1;
        for (int n = 0;; ++n) {
          v4u p[4];
#pragma unroll
          for (int jj = 0; jj < 4; ++jj) p[jj] = hl4(rws, lane * 16, WS_H1 + slot(hd * 4 + jj, 4096) + (blk * 2 + half) * 1024);
          unsigned bad = 0;
          q0 = 0.f; q1 = 0.f;
#pragma unroll
          for (int jj = 0; jj < 4; ++jj) {
            bad |= (p[jj].x ^ tlo) | (p[jj].z ^ tlo) | (((p[jj].y ^ t8) | (p[jj].w ^ t8)) & 0xFFu);
            q0 += gdec(p[jj].y); q1 += gdec(p[jj].w);
          }
          if (__all(bad == 0) || n >= GSPIN) break;
          SLP(DLY_R1);
        }
        const unsigned qp = pk(q0, q1);
        const int d0 = 32 * g + 16 * half + 4 * blk + cp;
        *(LAS u16*)(L + LP_QB + bj * 320 + d0 * 2) = (u16)qp;
        *(LAS u16*)(L + LP_QB + bj * 320 + (d0 + 8) * 2) = (u16)(qp >> 16);
      }
      BAR();
      {
        v4u qf[4];
#pragma unroll
        for (int s = 0; s < 4; ++s) qf[s] = *(LAS v4u*)(L + LP_QB + cp * 320 + s * 64 + g * 16);
        f32x4 c0 = {0.f, 0.f, 0.f, 0.f}, c1 = c0;
#pragma unroll
        for (int s = 0; s < 4; ++s) { c0 = mfma16(ka[0][s], qf[s], c0); c1 = mfma16(ka[1][s], qf[s], c1); }
        const float sc = 0.08838834764831845f * 1.4426950408889634f;
        if (i16 < 4) {
          *(LAS v2u*)(L + LP_QO + i16 * 256 + (32 * w + 4 * g) * 2) = v2u{pk7(c0[0] * sc, c0[1] * sc), pk7(c0[2] * sc, c0[3] * sc)};
          *(LAS v2u*)(L + LP_QO + i16 * 256 + (32 * w + 16 + 4 * g) * 2) = v2u{pk7(c1[0] * sc, c1[1] * sc), pk7(c1[2] * sc, c1[3] * sc)};
        }
      }
      BAR();
      if (w == 0) {
        const v4u v = *(LAS v4u*)(L + LP_QO + lane * 16);
        st4(v4u{lsbput(v.x, eq[0]), lsbput(v.y, eq[1]), lsbput(v.z, eq[2]), lsbput(v.w, eq[3])}, rws, g * 2048 + i16 * 16, WS_QA + slot(hd, 1024) + jq * 256);
      }
      BAR();
      const rsrc_t r2 = mk(w2, 4608 * 576 * 2);
      const int so2 = (xg * 576 + 36 * qq) * 1152;
      const rsrc_t ro = mk(wo, 4096 * 576 * 2);
      const int soo = (hd * 128 + 32 * jq) * 1152;
      v4u a2[9][2];
      v2u a8[3][2];
      {
        const int vo4 = 2 * g * 1152 + i16 * 16, vo4m = g < 2 ? vo4 : OOB;
        const int vo8 = 2 * g * 1152 + i16 * 8 + 1024, vo8m = (g < 2 && w == 0) ? vo8 : OOB;
#pragma unroll
        for (int n = 0; n < 24; ++n) {
          if (n >= PACE_W) VMCNT(PACE_W - 1);
          if (n == PACE_W) { SCHED(); RBAR(); SLP(1); }
          const int o = n >> 1, r = n & 1;
          if (n < 10) a2[o][r] = ld4<NT>(r2, o < 4 ? vo4 : vo4m, so2 + (8 * o + r) * 1152 + 256 * w);
          else if (n < 12) a8[0][r] = ld2<NT>(r2, vo8, so2 + (8 * w + r) * 1152);
          else if (n < 14) a8[1][r] = ld2<NT>(r2, vo8m, so2 + (32 + r) * 1152);
          else if (n < 22) a2[o - 2][r] = ld4<NT>(ro, vo4, soo + (8 * (o - 7) + r) * 1152 + 256 * w);
          else a8[2][r] = ld2<NT>(ro, vo8, soo + (8 * w + r) * 1152);
          SCHED();
        }
      }
#pragma unroll
      for (int o = 0; o < 5; ++o) swap24(a2[o][0], a2[o][1]);
      RBAR();
      {
        const v4u h4 = *(LAS v4u*)(L + LP_HP + (bj * 4 + g) * 48);
        const unsigned h5 = *(LAS unsigned*)(L + LP_HP + (bj * 4 + g) * 48 + 16);
        const unsigned hw = *(LAS unsigned*)(L + LP_HP + (bj * 4 + g) * 48 + 4 * w);
        f32x4 c8 = {0.f, 0.f, 0.f, 0.f}, cx = c8, cy = c8;
        c8 = mfma16(v4u{a8[0][0].x, a8[0][0].y, a8[0][1].x, a8[0][1].y}, bdiag(hw, sel), c8);
        c8 = mfma16(v4u{a8[1][0].x, a8[1][0].y, a8[1][1].x, a8[1][1].y}, bdiag(h5, sel), c8);
#pragma unroll
        for (int o = 0; o < 5; ++o) {
          const v4u B = bdiag(o == 0 ? h4.x : o == 1 ? h4.y : o == 2 ? h4.z : o == 3 ? h4.w : h5, sel);
          cx = mfma16(a2[o][0], B, cx);
          cy = mfma16(a2[o][1], B, cy);
        }
#pragma unroll
        for (int o = 5; o < 9; ++o) swap24(a2[o][0], a2[o][1]);
        asm volatile("" : "+v"(cx), "+v"(cy), "+v"(c8));
        int bpa = (bj * 16 + g) * 4;
        asm volatile("" : "+v"(bpa));
        float y0, y1;
        for (int n = 0;; ++n) {
          v4u p[4];
#pragma unroll
          for (int jj = 0; jj < 4; ++jj) p[jj] = hl4(rws, lane * 16, WS_H4 + slot(idx, 4096) + jj * 1024);
          unsigned bad = 0;
          y0 = 0.f; y1 = 0.f;
#pragma unroll
          for (int jj = 0; jj < 4; ++jj) {
            bad |= (p[jj].x ^ tlo) | (p[jj].z ^ tlo) | (((p[jj].y ^ t8) | (p[jj].w ^ t8)) & 0xFFu);
            y0 += gdec(p[jj].y); y1 += gdec(p[jj].w);
          }
          if (__all(bad == 0) || n >= GSPIN) break;
          SLP(DLY_R4);
        }
        const unsigned yp = pk(y0, y1);
        const unsigned pw = __builtin_amdgcn_ds_bpermute(bpa + 16 * w, yp);
        unsigned po[4];
#pragma unroll
        for (int o = 0; o < 4; ++o) po[o] = __builtin_amdgcn_ds_bpermute(bpa + 16 * o, yp);
        c8 = mfma16(v4u{a8[2][0].x, a8[2][0].y, a8[2][1].x, a8[2][1].y}, bdiag(pw, sel), c8);
#pragma unroll
        for (int o = 0; o < 4; ++o) {
          const v4u B = bdiag(po[o], sel);
          cx = mfma16(a2[5 + o][0], B, cx);
          cy = mfma16(a2[5 + o][1], B, cy);
        }
        *(LAS f32x4*)(L + LP_B8 + w * 1024 + lane * 16) = c8;
        st4(tag8(cx, tb), rws, vo5, so5 + blk5(2 * w));
        st4(tag8(cy, tb), rws, vo5, so5 + blk5(2 * w + 1));
        BAR();
        if (w == 0) {
          f32x4 s = *(LAS f32x4*)(L + LP_B8 + lane * 16);
#pragma unroll
          for (int k = 1; k < 4; ++k) s += *(LAS f32x4*)(L + LP_B8 + k * 1024 + lane * 16);
          st4(tag8(s, tb), rws, vo5, so5 + blk5(8));
        }
      }
      if (qq < 9) RBAR();
    } else {
      const int v = w - 4;
      unsigned xn[8];
      const v2u wv_ = hl2(rws, 0, WS_W);
      v4u pt1;
      asm volatile("buffer_load_dwordx4 %0, %1, %2, %3 offen sc1" : "=v"(pt1) : "v"(lane * 16), "s"(rws), "s"(WS_H1 + slot(idx, 4096) + v * 1024) : "memory");
      {
        const v4u x0 = ld4<0>(rx, lane * 16, v * 1152), x1 = ld4<0>(rx, lane < 8 ? lane * 16 : OOB, v * 1152 + 1024);
        float s = 0.f, q = 0.f;
#pragma unroll
        for (int k = 0; k < 4; ++k) {
          s = dot2(x0[k], 0x3F803F80u, s); q = dot2(x0[k], x0[k], q);
          s = dot2(x1[k], 0x3F803F80u, s); q = dot2(x1[k], x1[k], q);
        }
        const float mean = x32_sum(x16_sum(row_sum(s))) * (1.0f / 576.0f);
        const float msq = x32_sum(x16_sum(row_sum(q))) * (1.0f / 576.0f);
        const float rstd = __builtin_amdgcn_rsqf(fmaxf(fmaf(-mean, mean, msq), 0.f) + 1e-5f), nb = -mean * rstd;
#pragma unroll
        for (int k = 0; k < 4; ++k) {
          xn[k] = pk(fmaf(bf2f(x0[k]), rstd, nb), fmaf(__uint_as_float(x0[k] & 0xFFFF0000u), rstd, nb));
        }
#pragma unroll
        for (int k = 0; k < 4; ++k) xn[4 + k] = pk(fmaf(bf2f(x1[k]), rstd, nb), fmaf(__uint_as_float(x1[k] & 0xFFFF0000u), rstd, nb));
      }
      VMCNT(0);
      asm volatile("" : "+v"(pt1));
#pragma unroll
      for (int hf = 0; hf < 2; ++hf) {
        if (hf == 0 || lane < 8) {
#pragma unroll
          for (int k = 0; k < 4; ++k) {
            const int o = lane + 64 * hf;
            *(LAS unsigned*)(L + LP_XD + ((v * 4 + ((2 * k) & 3)) * 148 + 2 * o + (k >> 1)) * 4) = __builtin_amdgcn_perm(0u, xn[4 * hf + k], 0x01000100u);
            *(LAS unsigned*)(L + LP_XD + ((v * 4 + ((2 * k + 1) & 3)) * 148 + 2 * o + (k >> 1)) * 4) = __builtin_amdgcn_perm(0u, xn[4 * hf + k], 0x03020302u);
          }
        }
      }
      touch(rws, lane * 16, WS_H4 + slot(idx, 4096) + v * 1024);
      MAKE_TAG(wv_);
      const int U0 = (81 * qq) / 144, Qb = 144 * (U0 + 1);
      unsigned xd[21];
      const GAS char* wb = (const GAS char*)(i16 < 8 ? w1 : w3) + (xg * 576 * 2 + (i16 & 7) * 16 + g * 9216);
      const unsigned m0 = cp == 0 ? 0xFFFFu : cp == 1 ? 0xFFFF0000u : 0u, m1 = cp == 2 ? 0xFFFFu : cp == 3 ? 0xFFFF0000u : 0u;
      f32x4 acc[4];
#pragma unroll
      for (int k = 0; k < 4; ++k) acc[k] = f32x4{0.f, 0.f, 0.f, 0.f};
      v4u a[21];
        int off[2][3];
        unsigned use[2][3];
        int hpa[2];
#pragma unroll
        for (int e = 0; e < 2; ++e) {
          const int t = ((v - 2) & 1) * 64 + lane + 128 * e;
          const int cl = t >> 2, b = t & 3, f = 36 * qq + cl, u = f >> 6;
          const int r0 = (144 * u) / 81, r1 = (144 * u + 143) / 81;
#pragma unroll
          for (int k = 0; k < 3; ++k) {
            const int rr = r0 + k;
            const bool on = t < 144 && rr <= r1;
            off[e][k] = on ? slot(xg * 16 + rr, 8192) + (u - (81 * rr) / 144) * 4096 + (f & 63) * 64 + b * 16 : OOB;
            use[e][k] = on ? 0xFFFFFFFFu : 0u;
          }
          hpa[e] = LP_HP + ((b * 4 + ((cl >> 1) & 3)) * 12 + (cl >> 3)) * 4 + (cl & 1) * 2;
          asm volatile("" : "+v"(hpa[e]), "+v"(off[e][0]), "+v"(off[e][1]), "+v"(off[e][2]), "+v"(use[e][0]), "+v"(use[e][1]), "+v"(use[e][2]));
        }
      int pa = WS_H7 + slot(idx, 8192) + v * 4096 + (32 * ((lane >> 4) & 1) + 16 * (lane >> 5) + cp) * 64 + bj * 16;
      asm volatile("" : "+v"(pa));
      BAR();
      RBAR();
#pragma unroll
      for (int t2 = 0; t2 < 21; ++t2) {
        const int Q = 81 * qq + min(4 * t2 + v, 80), qk = Q >= Qb ? Q - Qb : Q - 144 * U0;
        xd[t2] = *(LAS unsigned*)(L + LP_XD + ((bj * 4 + g) * 148 + qk) * 4);
        asm volatile("" : "+v"(xd[t2]));
      }
      wait_clock(t_start, T_F);
#pragma unroll
      for (int t = -PACE_G; t < 21; ++t) {
        const int tl = t + PACE_G;
        if (tl < 21) {
          const int Q = 81 * qq + 4 * tl + v, u = Q >= Qb ? U0 + 1 : U0, qk = Q - 144 * u;
          if (tl < 20 || v == 0) {
            const GAS v4u* p = (const GAS v4u*)(wb + (unsigned)(qk * 4 * 9216 + u * 128));
            a[tl] = __builtin_nontemporal_load(p);
          } else a[tl] = v4u{0u, 0u, 0u, 0u};
          SCHED();
        }
        if (tl == 20) BAR();
        if (t >= 0) {
          const int Q = 81 * qq + min(4 * t + v, 80);
          const v2u B = {xd[t] & m0, xd[t] & m1};
          if (Q < Qb) { acc[0] = mfma16k16(v2u{a[t].x, a[t].y}, B, acc[0]); acc[1] = mfma16k16(v2u{a[t].z, a[t].w}, B, acc[1]); }
          else        { acc[2] = mfma16k16(v2u{a[t].x, a[t].y}, B, acc[2]); acc[3] = mfma16k16(v2u{a[t].z, a[t].w}, B, acc[3]); }
          SCHED();
        }
      }
#pragma unroll
      for (int k = 0; k < 4; ++k) *(LAS f32x4*)(L + LP_RED + (v * 4 + k) * 1024 + lane * 16) = acc[k];
      BAR();
      if (v == 0 || (v == 1 && 81 * qq + 80 >= Qb)) {
#pragma unroll
        for (int hh = 0; hh < 2; ++hh) {
          f32x4 r = *(LAS f32x4*)(L + LP_RED + (2 * v + hh) * 1024 + lane * 16);
#pragma unroll
          for (int k = 1; k < 4; ++k) r += *(LAS f32x4*)(L + LP_RED + (k * 4 + 2 * v + hh) * 1024 + lane * 16);
#pragma unroll
          for (int k = 0; k < 2; ++k) {
            const auto x = __builtin_amdgcn_permlane32_swap(__float_as_uint(r[k]), __float_as_uint(r[2 + k]), false, false);
            st4(v4u{tlo, genc(__uint_as_float(x[0]), t8), tlo, genc(__uint_as_float(x[1]), t8)}, rws, pa + (8 * k + 4 * hh) * 64, 0);
          }
        }
      } else if (v >= 2) {
        SLP(DLY_H7);
        wait_clock(t_start, T_H7);
        float gs[2], us[2];
        for (int n = 0;; ++n) {
          v4u p[2][3];
#pragma unroll
          for (int e = 0; e < 2; ++e)
#pragma unroll
            for (int k = 0; k < 3; ++k) p[e][k] = hl4(rws, off[e][k], WS_H7);
          unsigned bad = 0;
#pragma unroll
          for (int e = 0; e < 2; ++e) {
            gs[e] = 0.f; us[e] = 0.f;
#pragma unroll
            for (int k = 0; k < 3; ++k) {
              bad |= ((p[e][k].x ^ tlo) | (p[e][k].z ^ tlo) | (((p[e][k].y ^ t8) | (p[e][k].w ^ t8)) & 0xFFu)) & use[e][k];
              gs[e] += gdec(p[e][k].y); us[e] += gdec(p[e][k].w);
            }
          }
          if (__all(bad == 0) || n >= GSPIN) break;
          SLP(DLY_R7);
        }
#pragma unroll
        for (int e = 0; e < 2; ++e) {
          const float hv = gs[e] * __builtin_amdgcn_rcpf(1.0f + __expf(-gs[e])) * us[e];
          if ((v - 2) * 64 + lane + 128 * e < 160) *(LAS u16*)(L + hpa[e]) = (u16)pk(hv, hv);
        }
      }
      BAR();
      const int fe = lane & 31, fL = 8 * xg + (fe >> 2), frr = fe & 3;
      const int oidx = ((fL & 15) >> 2) * 576 + (qq < 8 ? 128 * (qq >> 1) + 8 * (4 * (fL >> 4) + frr) + 4 * (qq & 1) + (fL & 3) : 512 + 4 * (4 * (fL >> 4) + frr) + (fL & 3));
      unsigned xres = 0;
      if (v == 3 && qq < 9) xres = ((const GAS u16*)xin)[oidx];
      const int h5o = WS_H5 + slot(qq * 8 + xg, 16384) + v * 4096;
      int fina = LP_FIN + v * 128 + (lane & 7) * 16 + g * 4;
      asm volatile("" : "+v"(xres), "+v"(fina));
      RBAR();
      if (qq < 9) {
        SLP(DLY_H5);
        v4u d[4];
#pragma unroll
        for (int k = 0; k < 4; ++k) d[k] = hl4(rws, lane * 16, h5o + k * 1024);
        unsigned pend = 15u;
#pragma unroll 1
        for (int n = 0;; ++n) {
#pragma unroll
          for (int k = 0; k < 4; ++k)
            if (pend & (1u << k)) { if (__all((strip4(d[k], tb) & 0xFFu) == 0)) pend &= ~(1u << k); }
          if (pend == 0 || n >= GSPIN) break;
#pragma unroll
          for (int k = 0; k < 4; ++k)
            if (pend & (1u << k)) d[k] = hl4(rws, lane * 16, h5o + k * 1024);
        }
        f32x4 s = (__builtin_bit_cast(f32x4, d[0]) + __builtin_bit_cast(f32x4, d[1])) + (__builtin_bit_cast(f32x4, d[2]) + __builtin_bit_cast(f32x4, d[3]));
#pragma unroll
        for (int r = 0; r < 4; ++r) s[r] += dppz<0x128>(s[r]);
        const auto x01 = __builtin_amdgcn_permlane16_swap(__float_as_uint(s[0]), __float_as_uint(s[1]), false, false);
        const auto x23 = __builtin_amdgcn_permlane16_swap(__float_as_uint(s[2]), __float_as_uint(s[3]), false, false);
        const auto z = __builtin_amdgcn_permlane32_swap(__float_as_uint(__uint_as_float(x01[0]) + __uint_as_float(x01[1])),
                                                        __float_as_uint(__uint_as_float(x23[0]) + __uint_as_float(x23[1])), false, false);
        *(LAS float*)(L + fina) = __uint_as_float(z[0]) + __uint_as_float(z[1]);
        BAR();
        if (v == 3) {
          float y = bf2f(xres);
#pragma unroll
          for (int k = 0; k < 4; ++k) y += *(LAS float*)(L + LP_FIN + k * 128 + fe * 4);
          if (lane < 32) ((GAS u16*)out)[oidx] = (u16)pk(y, y);
          if (qq == 0 && xg == 0) st2(v2u{tlo, thi}, rws, lane == 0 ? 0 : OOB, WS_W);
        }
      }
    }
    STAMP_OUT();
    return;
  }

  const int ab = xg >> 1;
  const int r32 = lane & 31, hh = lane >> 5;
  v2u wa_;
  if (w < 4) {
    const rsrc_t rc = mk(ckv, 4 * 4096 * 512 * 2);
    v2u wb_;
    asm volatile("buffer_load_dwordx2 %0, off, %1, %2 sc1" : "=v"(wb_) : "s"(rws), "s"(WS_W) : "memory");
    {
      int vo[4];
#pragma unroll
      for (int k = 0; k < 4; ++k) vo[k] = g * 1024 + ((i16 ^ ((g << 2) | k)) * 16);
      const int so = (idx * 128 + 32 * w) * 1024;
      SLP(DLY_C);
#pragma unroll
      for (int n = 0; n < 32; ++n) {
        if (n >= PACE_C) VMCNT(PACE_C - 1);
        dma16(rc, vo[(n >> 2) & 3], so + (n >> 2) * 4096 + (n & 3) * 256, lbase + (n & 3) * 32768 + (32 * w + 4 * (n >> 2)) * 256);
      }
    }
    RBAR();
    asm volatile("" : "+v"(wb_));
    wa_ = wb_;
  } else {
    wa_ = hl2(rws, 0, WS_W);
    if (w == 4) {
      v2u xr[9];
#pragma unroll
      for (int t = 0; t < 9; ++t) xr[t] = ld2<0>(rx, g * 1152 + i16 * 8, t * 128);
      const rsrc_t rq = mk(wq, 576 * 4096 * 2);
      const int vo = 2 * g * 8192 + i16 * 16, so = 8 * jq * 8192 + hd * 256;
      v4u a[18][2];
#pragma unroll
      for (int o = 0; o < 18; ++o) { a[o][0] = ld4<NT>(rq, vo, so + (32 * o) * 8192); a[o][1] = ld4<NT>(rq, vo, so + (32 * o + 1) * 8192); }
      SCHED();
      float s = 0.f, q = 0.f;
#pragma unroll
      for (int t = 0; t < 9; ++t) {
        *(LAS v2u*)(L + LA_QA + g * 1168 + i16 * 8 + t * 128) = xr[t];
        s = dot2(xr[t].x, 0x3F803F80u, s); q = dot2(xr[t].x, xr[t].x, q);
        s = dot2(xr[t].y, 0x3F803F80u, s); q = dot2(xr[t].y, xr[t].y, q);
      }
      FENCE();
      unsigned xp[18];
#pragma unroll
      for (int o = 0; o < 18; ++o) xp[o] = *(LAS unsigned*)(L + LA_QA + bj * 1168 + (4 * jq + 16 * o + g) * 4);
      const float mean = row_sum(s) * (1.0f / 576.0f), msq = row_sum(q) * (1.0f / 576.0f);
      const float rs0 = __builtin_amdgcn_rsqf(fmaxf(fmaf(-mean, mean, msq), 0.f) + 1e-5f);
      const float rstd = __uint_as_float(__builtin_amdgcn_ds_bpermute(bj * 64, __float_as_uint(rs0)));
      const float nb = __uint_as_float(__builtin_amdgcn_ds_bpermute(bj * 64, __float_as_uint(-mean * rs0)));
      v4u Bq[18];
#pragma unroll
      for (int o = 0; o < 18; ++o) {
        Bq[o] = bdiag(pk(fmaf(bf2f(xp[o]), rstd, nb), fmaf(__uint_as_float(xp[o] & 0xFFFF0000u), rstd, nb)), sel);
        asm volatile("" : "+v"(Bq[o]));
      }
      const u64 tg = (((u64)wa_.y << 32) | wa_.x) + 0x9E3779B97F4A7C15ull;
      unsigned ql = (unsigned)tg, q8 = (unsigned)(tg >> 32) & 0xFFu;
      asm volatile("" : "+v"(ql), "+v"(q8));
      SCHED();
      f32x4 qx = {0.f, 0.f, 0.f, 0.f}, qy = qx;
#pragma unroll
      for (int o = 0; o < 18; ++o) {
        swap24(a[o][0], a[o][1]);
        qx = mfma16(a[o][0], Bq[o], qx);
        qy = mfma16(a[o][1], Bq[o], qy);
      }
      const int po = WS_H1 + slot(idx, 4096);
      st4(v4u{ql, genc(qx[0], q8), ql, genc(qx[1], q8)}, rws, lane * 16, po);
      st4(v4u{ql, genc(qx[2], q8), ql, genc(qx[3], q8)}, rws, lane * 16, po + 1024);
      st4(v4u{ql, genc(qy[0], q8), ql, genc(qy[1], q8)}, rws, lane * 16, po + 2048);
      st4(v4u{ql, genc(qy[2], q8), ql, genc(qy[3], q8)}, rws, lane * 16, po + 3072);
    }
    RBAR();
  }
  MAKE_TAG(wa_);
  unsigned e4[4];
#pragma unroll
  for (int k = 0; k < 4; ++k) e4[k] = lsbtag(tlo, thi, 4 * i16 + k);
  if (w < 4) {
    v4u fa[32];
    {
      const int fr = ((r32 & 3) << 2) | ((r32 >> 2) & 3);
      const int ca = (32 * w + r32) * 256;
#define QK_READ(Q) _Pragma("unroll") for (int k = 0; k < 8; ++k) fa[(Q)*8 + k] = *(LAS v4u*)(L + (Q)*32768 + ca + (((2 * k + hh) ^ fr) * 16)); SCHED();
      VMCNT(0); QK_READ(0) QK_READ(1) QK_READ(2) QK_READ(3)
    }
    int qb[4];
#pragma unroll
    for (int k = 0; k < 4; ++k) { qb[k] = LA_QA + ((lane ^ ((2 * k + hh) & 7)) * 16); asm volatile("" : "+v"(qb[k])); }
    const int q4 = i16 >> 2, lowc = 2 * (g & 1) + (cp >> 1);
    int ta[4][2];
#pragma unroll
    for (int t = 0; t < 4; ++t)
#pragma unroll
      for (int t2 = 0; t2 < 2; ++t2) {
        ta[t][t2] = w * 32768 + (8 * t2 + 4 * hh + q4) * 256 + ((((t ^ q4) << 2) | (lowc ^ (2 * t2 + hh))) * 16) + 8 * (cp & 1);
        asm volatile("" : "+v"(ta[t][t2]));
      }
    int mxa = (r32 >> 4) * 320 + (r32 & 15) * 4, pba = LA_PB + lane * 16;
    unsigned e0 = lsbtag(tlo, thi, 4 * (r32 >> 1)), e1 = lsbtag(tlo, thi, 4 * (r32 >> 1) + 1), e2 = lsbtag(tlo, thi, 4 * (r32 >> 1) + 2), e3 = lsbtag(tlo, thi, 4 * (r32 >> 1) + 3);
    const int sp = idx & 31;
    int h3v = ((hh * 4 + (r32 & 1)) ^ (sp & 5)) * QB + (r32 >> 1) * 16 + WS_H3 + (sp * 16 + ab * 4 + w) * 256;
    asm volatile("" : "+v"(mxa), "+v"(pba), "+v"(e0), "+v"(e1), "+v"(e2), "+v"(e3), "+v"(h3v));
    BAR();
    v4u cb[32];
#define TR_READ(n) do { const v2u x0_ = __builtin_bit_cast(v2u, __builtin_amdgcn_ds_read_tr16_b64_v4i16((LAS s16x4*)(L + ta[(n) >> 3][0] + ((n) & 7) * 4096))); \
    const v2u x1_ = __builtin_bit_cast(v2u, __builtin_amdgcn_ds_read_tr16_b64_v4i16((LAS s16x4*)(L + ta[(n) >> 3][1] + ((n) & 7) * 4096)));                 \
    cb[n] = v4u{x0_.x, x0_.y, x1_.x, x1_.y}; } while (0)
    f32x16 sacc;
#pragma unroll
    for (int k = 0; k < 16; ++k) sacc[k] = 0.f;
    {
      v4u fb[32];
#pragma unroll
      for (int s = 0; s < 6; ++s) fb[s] = *(LAS v4u*)(L + qb[s & 3] + s * 1024);
      SCHED();
#pragma unroll
      for (int s = 0; s < 32; ++s) {
        if (s + 6 < 32) fb[s + 6] = *(LAS v4u*)(L + qb[(s + 6) & 3] + (s + 6) * 1024);
        if (s >= 26) TR_READ(s - 26);
        sacc = mfma32(fa[s], fb[s], sacc);
        SCHED();
      }
    }
    float mw = sacc[0];
#pragma unroll
    for (int k = 1; k < 16; ++k) mw = fmaxf(mw, sacc[k]);
    mw = x32_max(mw);
    asm volatile("s_waitcnt lgkmcnt(0)" ::: "memory");
    *(LAS float*)(L + mxa + w * 32768) = mw;
    BAR();
    const float mx = gdec(genc(fmaxf(fmaxf(*(LAS float*)(L + mxa), *(LAS float*)(L + mxa + 32768)), fmaxf(*(LAS float*)(L + mxa + 65536), *(LAS float*)(L + mxa + 98304))), 0u));
    float ls = 0.f;
#pragma unroll
    for (int k = 0; k < 16; ++k) { sacc[k] = __builtin_amdgcn_exp2f(sacc[k] - mx); ls += sacc[k]; }
#pragma unroll
    for (int s2 = 0; s2 < 2; ++s2)
      *(LAS v4u*)(L + pba + (2 * w + s2) * 1024) =
          v4u{pk(sacc[8 * s2], sacc[8 * s2 + 1]), pk(sacc[8 * s2 + 2], sacc[8 * s2 + 3]), pk(sacc[8 * s2 + 4], sacc[8 * s2 + 5]), pk(sacc[8 * s2 + 6], sacc[8 * s2 + 7])};
    ls = x32_sum(ls);
    *(LAS float*)(L + mxa + 640 + w * 32768) = ls;
    BAR();
    {
      v4u pf[8];
#pragma unroll
      for (int k = 0; k < 8; ++k) pf[k] = *(LAS v4u*)(L + pba + k * 1024);
      f32x4 l4;
      if (w == 0) l4 = f32x4{*(LAS float*)(L + mxa + 640), *(LAS float*)(L + mxa + 640 + 32768), *(LAS float*)(L + mxa + 640 + 65536), *(LAS float*)(L + mxa + 640 + 98304)};
      SCHED();
      f32x16 o[4];
      unsigned p01[16], pq[16];
      const bool odd = lane & 1;
#pragma unroll
      for (int t = 0; t < 4; ++t)
#pragma unroll
        for (int k = 0; k < 16; ++k) o[t][k] = 0.f;
#pragma unroll
      for (int n = 0; n < 32; ++n) {
        if (n + 6 < 32) TR_READ(n + 6);
        o[n >> 3] = mfma32(pf[n & 7], cb[n], o[n >> 3]);
        if (n == 0 && w == 0)
          st4(v4u{tlo, genc(mx, t8), tlo, genc((l4[0] + l4[1]) + (l4[2] + l4[3]), t8)}, rws, lane < 32 ? r32 * 16 : OOB, WS_ML + slot(idx, 512));
        if (n >= 17 && n < 32 && n != 24) {
          const int j = n < 24 ? n - 17 : n - 18;
          if (j < 8) { pq[2 * j] = pk7(o[0][2 * j], o[1][2 * j]); pq[2 * j + 1] = pk7(o[0][2 * j + 1], o[1][2 * j + 1]); asm volatile("" : "+v"(pq[2 * j]), "+v"(pq[2 * j + 1])); }
          if (j >= 6) {
            const int k = j - 6;
            p01[2 * k] = lsbput(odd ? pq[2 * k + 1] : pq[2 * k], e0);
            p01[2 * k + 1] = __builtin_amdgcn_update_dpp(0, lsbput(odd ? pq[2 * k] : pq[2 * k + 1], e2), 0xB1, 0xf, 0xf, true);
            asm volatile("" : "+v"(p01[2 * k]), "+v"(p01[2 * k + 1]));
          }
        }
        SCHED();
      }
#pragma unroll
      for (int k = 0; k < 8; ++k) {
        const unsigned pa = pk7(o[2][2 * k], o[3][2 * k]), pb = pk7(o[2][2 * k + 1], o[3][2 * k + 1]);
        st4(v4u{p01[2 * k], lsbput(odd ? pb : pa, e1), p01[2 * k + 1], (unsigned)__builtin_amdgcn_update_dpp(0, lsbput(odd ? pa : pb, e3), 0xB1, 0xf, 0xf, true)},
            rws, h3v, ((2 * (k & 1) + 8 * (k >> 1)) ^ (sp & 26)) * QB);
      }
    }
    RBAR();
    RBAR();
    RBAR();
  } else {
    const int v = w - 4;
    const rsrc_t rv = mk(wuv, 512 * 4096 * 2);
    v4u uw[8];
#define TL(i) do { uw[i] = ld4<NT>(rv, g * 32 * 8192 + i16 * 16, (128 * jq + 8 * v + ((i) ^ (hd & 1))) * 8192 + hd * 256); SCHED(); } while (0)
    {
      v4u qa[8];
      SLP(DLY_QA);
#pragma unroll
      for (int k = 0; k < 8; ++k) qa[k] = hl4(rws, lane * 16, WS_QA + ab * 2048 + slot(8 * v + k, 1024));
      SCHED();
#pragma unroll 1
      for (int n = 0;; ++n) {
        unsigned bad = 0;
#pragma unroll
        for (int k = 0; k < 8; ++k) bad |= strip4(qa[k], e4);
        if (__all((bad & LSBM) == 0) || n >= GSPIN) break;
        SLP(DLY_R2);
#pragma unroll
        for (int k = 0; k < 8; ++k) qa[k] = hl4(rws, lane * 16, WS_QA + ab * 2048 + slot(8 * v + k, 1024));
      }
#pragma unroll
      for (int k = 0; k < 8; ++k) *(LAS v4u*)(L + LA_QA + (lane >> 1) * 1024 + ((((lane & 1) * 32 + 8 * v + k) ^ (lane & 7)) * 16)) = qa[k];
    }
    BAR();
#pragma unroll
    for (int i = 0; i < 4; ++i) TL(i);
    RBAR();
#pragma unroll
    for (int i = 4; i < 8; ++i) TL(i);
    RBAR();
    const int e7 = lane & 7, q4 = i16 >> 2;
    int cta = LA_CTX + v * 288 + (32 * g + e7) * 2, ctr = LA_CTX + cp * 288 + g * 16;
    int h4a = ((2 * (g & 1) + (i16 & 1)) * 16 + (2 * (g >> 1) + (i16 >> 3)) * 4 + ((i16 >> 1) & 3)) * 16 + WS_H4 + slot(hd * 4 + v, 4096) + jq * 1024;
    const unsigned src = (lane & 8) ? 0x0302u : 0x0100u, s1 = (e7 & 1) ? ((src << 16) | 0x0C0Cu) : (0x0C0C0000u | src);
    unsigned s8[4];
    int uwa[4], bva[2][2];
#pragma unroll
    for (int k = 0; k < 4; ++k) {
      s8[k] = (e7 >> 1) == k ? s1 : 0x0C0C0C0Cu;
      uwa[k] = LA_UVW + (32 * v + g) * 256 + ((i16 ^ ((g << 2) | k)) * 16);
      bva[k >> 1][k & 1] = LA_UVW + (8 * g + 4 * (k & 1) + q4) * 256 + (((2 * (2 * v + (k >> 1)) + (cp >> 1)) ^ ((q4 << 2) | ((2 * g + (k & 1)) & 3))) * 16) + 8 * (cp & 1);
      asm volatile("" : "+v"(s8[k]), "+v"(uwa[k]), "+v"(bva[k >> 1][k & 1]));
    }
    asm volatile("" : "+v"(cta), "+v"(ctr), "+v"(h4a));
    const int h3l = ((hd & 3) ^ g) * QB + g * 4096 + i16 * 16;
#define ML_LOAD() hl4(rws, slot(r32, 512), WS_ML + v * 1024 + hd * 16)
#define ML_OK(V) __all((((V).x ^ tlo) | ((V).z ^ tlo) | ((((V).y ^ t8) | ((V).w ^ t8)) & 0xFFu)) == 0)
#define H3_LOADS() _Pragma("unroll") for (int k = 0; k < 8; ++k) d[k] = hl4(rws, h3l, WS_H3 + ((hd >> 2) ^ k) * 4 * QB + k * 16384 + (v * 4 + jq) * 256); SCHED()
    v4u d[8], bk[8];
    v4u ml;
    VMCNT(0);
    RBAR();
#pragma unroll
    for (int k = 0; k < 8; ++k) *(LAS v4u*)(L + uwa[k & 3] + k * 1024) = uw[k];
    BAR();
    v4u bv[2][4];
#pragma unroll
    for (int t = 0; t < 2; ++t)
#pragma unroll
      for (int ks = 0; ks < 4; ++ks) {
        const v2u x0 = __builtin_bit_cast(v2u, __builtin_amdgcn_ds_read_tr16_b64_v4i16((LAS s16x4*)(L + bva[t][0] + ks * 8192)));
        const v2u x1 = __builtin_bit_cast(v2u, __builtin_amdgcn_ds_read_tr16_b64_v4i16((LAS s16x4*)(L + bva[t][1] + ks * 8192)));
        bv[t][ks] = v4u{x0.x, x0.y, x1.x, x1.y};
      }
    SCHED();
    {
      SLP(DLY_M0);
      for (int n = 0;; ++n) {
        ml = ML_LOAD();
        if (ML_OK(ml) || n >= GSPIN) break;
        SLP(DLY_RM);
      }
      SLP(DLY_H3);
      H3_LOADS();
      const float m = gdec(ml.y), M = x16_max(row_max(m));
      const float e = __builtin_amdgcn_exp2f(m - M);
      const float al = e * __builtin_amdgcn_rcpf(x16_sum(row_sum(e * gdec(ml.w))));
      const unsigned ah = pk(al, al) & 0xFFFFu;
      const unsigned hl = ah | (pk(al - bf2f(ah), 0.f) << 16);
#pragma unroll
      for (int k = 0; k < 8; ++k) { bk[k] = bdiag(__builtin_amdgcn_ds_bpermute((4 * k + g) * 4, hl), s8); asm volatile("" : "+v"(bk[k])); }
      f32x4 c;
#pragma unroll 1
      for (int n = 0;; ++n) {
        unsigned bad = 0;
        c = f32x4{0.f, 0.f, 0.f, 0.f};
#pragma unroll
        for (int k = 0; k < 8; ++k) { bad |= strip4(d[k], e4); c = mfma16(d[k], bk[k], c); }
        if (__all((bad & LSBM) == 0) || n >= GSPIN) break;
#pragma unroll
        for (int k = 0; k < 8; ++k) d[k] = hl4(rws, h3l, WS_H3 + ((hd >> 2) ^ k) * 4 * QB + k * 16384 + (v * 4 + jq) * 256);
      }
#pragma unroll
      for (int r = 0; r < 4; ++r) {
        const float y = c[r] + dppz<0x128>(c[r]);
        *(LAS u16*)(L + cta + 16 * r) = (u16)pk(y, y);
      }
    }
    BAR();
    {
      v4u af[4];
#pragma unroll
      for (int ks = 0; ks < 4; ++ks) af[ks] = *(LAS v4u*)(L + ctr + 64 * ks);
      SCHED();
      f32x4 c0 = {0.f, 0.f, 0.f, 0.f}, c1 = c0;
#pragma unroll
      for (int ks = 0; ks < 4; ++ks) { c0 = mfma16(af[ks], bv[0][ks], c0); c1 = mfma16(af[ks], bv[1][ks], c1); }
      const f32x4 ct = g < 2 ? c0 : c1;
      const float x0 = (g & 1) ? ct[2] : ct[0], x1 = (g & 1) ? ct[3] : ct[1];
      const float n0 = dppz<0xB1>(x0), n1 = dppz<0xB1>(x1);
      st4(v4u{tlo, genc((lane & 1) ? n1 : x0, t8), tlo, genc((lane & 1) ? x1 : n0, t8)}, rws, h4a, 0);
    }
  }
  STAMP_OUT();
}

extern "C" __global__ void fused_decode_end() {}

extern "C" void kernel_launch(void* const* d_in, const int* in_sizes, int n_in, void* d_out, int out_size, void* d_ws, size_t ws_size,
                              hipStream_t stream) {
  (void)hipFuncSetAttribute((const void*)fused_decode, hipFuncAttributeMaxDynamicSharedMemorySize, LDS_BYTES);
  fused_decode<<<256, 512, LDS_BYTES, stream>>>((const u16*)d_in[0], (const u16*)d_in[1], (const u16*)d_in[2], (const u16*)d_in[3],
                                                (const u16*)d_in[4], (const u16*)d_in[5], (const u16*)d_in[7], (const u16*)d_in[8],
                                                (const u16*)d_in[9], (u16*)d_out, (char*)d_ws);
}
```

```cpp
#include <hip/hip_runtime.h>
#include <stdint.h>

typedef unsigned short u16;
typedef unsigned long long u64;
typedef unsigned v4u __attribute__((ext_vector_type(4)));
typedef unsigned v2u __attribute__((ext_vector_type(2)));
typedef float f32x2 __attribute__((ext_vector_type(2)));
typedef float f32x4 __attribute__((ext_vector_type(4)));
typedef float f32x16 __attribute__((ext_vector_type(16)));
typedef short s16x4 __attribute__((ext_vector_type(4)));
typedef __bf16 bf16x8 __attribute__((ext_vector_type(8)));
typedef __bf16 bf16x2 __attribute__((ext_vector_type(2)));
typedef __amdgpu_buffer_rsrc_t rsrc_t;
#define GAS __attribute__((address_space(1)))
#define LAS __attribute__((address_space(3)))
#define DI __device__ __forceinline__

constexpr int NT = 2;
constexpr int PACE_C = 7;
constexpr int PACE_K = 6;
constexpr int PACE_G = 4;
constexpr int PACE_W = 6;
#define DLY_C 40
#define DLY_K 12
#define DLY_H1 2
#define DLY_R1 7
#define DLY_QA 8
#define DLY_R2 12
#define DLY_M0 12
#define DLY_RM 4
#define DLY_H3 1
#define DLY_H7 22
#define DLY_R7 2
#define DLY_R4 2
#define DLY_H5 18
constexpr unsigned T_F = 350, T_H7 = 940;
#define SLP(N) __builtin_amdgcn_s_sleep(N)

constexpr int SC1 = 16;
constexpr int GSPIN = 1 << 13;

constexpr int QB = 131072, WIN = 16 * QB;
constexpr int WS_H1 = 0;
constexpr int WS_H4 = 32768;
constexpr int WS_QA = 65536;
constexpr int WS_ML = 73728;
constexpr int WS_W = 81920;
constexpr int WS_H7 = WIN;
constexpr int WS_H5 = 2 * WIN;
constexpr int WS_H3 = 3 * WIN;
DI constexpr int slot(int p, int size) { return (p & 15) * QB + (p >> 4) * size; }
DI constexpr int blk5(int n) { return (n & 1) * 8 * QB + (n >> 1) * 16384; }
constexpr int WS_END = WS_H3 + 128 * 32768;
constexpr int OOB = (int)0x80000000;

constexpr int LP_XD = 5120, LP_QB = 15360, LP_QO = 16896, LP_RED = 18432, LP_HP = 34816, LP_B8 = 35840, LP_FIN = 39936, LP_UK = 40960;
constexpr int LA_QA = 131072, LA_PB = LA_QA + 1024, LA_CTX = LA_QA + 9216, LA_UVW = 0;
constexpr int LDS_BYTES = 163840;

DI rsrc_t mk(const void* p, int bytes) { return __builtin_amdgcn_make_buffer_rsrc((void*)p, (short)0, bytes, 0x00020000); }
template <int AUX> DI v4u ld4(rsrc_t r, int vo, int so) { return __builtin_amdgcn_raw_buffer_load_b128(r, vo, so, AUX); }
template <int AUX> DI v2u ld2(rsrc_t r, int vo, int so) { return __builtin_amdgcn_raw_buffer_load_b64(r, vo, so, AUX); }
DI v4u hl4(rsrc_t r, int vo, int so) { asm volatile("" : "+v"(vo)); return __builtin_amdgcn_raw_buffer_load_b128(r, vo, so, SC1); }
DI v2u hl2(rsrc_t r, int vo, int so) { asm volatile("" : "+v"(vo)); return __builtin_amdgcn_raw_buffer_load_b64(r, vo, so, SC1); }
DI void touch(rsrc_t r, int vo, int so) { v4u t; asm volatile("buffer_load_dwordx4 %0, %1, %2, %3 offen sc1\n\ts_waitcnt vmcnt(0)" : "=&v"(t) : "v"(vo), "s"(r), "s"(so) : "memory"); }
DI void st4(v4u v, rsrc_t r, int vo, int so) { __builtin_amdgcn_raw_buffer_store_b128(v, r, vo + so, 0, SC1); }
DI void st2(v2u v, rsrc_t r, int vo, int so) { __builtin_amdgcn_raw_buffer_store_b64(v, r, vo, so, SC1); }
DI void dma16(rsrc_t r, int vo, int so, unsigned lds) {
  asm volatile("s_mov_b32 m0, %0\n\ts_nop 0\n\tbuffer_load_dwordx4 %1, %2, %3 offen nt lds" ::"s"(lds), "v"(vo), "s"(r), "s"(so) : "memory");
}
#define VMCNT(n) asm volatile("s_waitcnt vmcnt(%0)" ::"n"(n) : "memory")
#define FENCE() asm volatile("" ::: "memory")
#define SCHED() __builtin_amdgcn_sched_barrier(0)

DI float bf2f(unsigned v) { return __uint_as_float(v << 16); }
DI unsigned pk(float a, float b) { f32x2 f = {a, b}; return __builtin_bit_cast(unsigned, __builtin_convertvector(f, bf16x2)); }
DI f32x4 mfma16(v4u a, v4u b, f32x4 c) {
  return __builtin_amdgcn_mfma_f32_16x16x32_bf16(__builtin_bit_cast(bf16x8, a), __builtin_bit_cast(bf16x8, b), c, 0, 0, 0);
}
DI f32x4 mfma16k16(v2u a, v2u b, f32x4 c) {
  return __builtin_amdgcn_mfma_f32_16x16x16bf16_1k(__builtin_bit_cast(s16x4, a), __builtin_bit_cast(s16x4, b), c, 0, 0, 0);
}
DI f32x16 mfma32(v4u a, v4u b, f32x16 c) {
  return __builtin_amdgcn_mfma_f32_32x32x16_bf16(__builtin_bit_cast(bf16x8, a), __builtin_bit_cast(bf16x8, b), c, 0, 0, 0);
}
template <int CTRL, int RM> DI float dppf(float old, float v) {
  return __builtin_bit_cast(float, __builtin_amdgcn_update_dpp(__builtin_bit_cast(int, old), __builtin_bit_cast(int, v), CTRL, RM, 0xf, false));
}
template <int CTRL> DI float dppz(float v) {
  return __builtin_bit_cast(float, __builtin_amdgcn_update_dpp(0, __builtin_bit_cast(int, v), CTRL, 0xf, 0xf, true));
}
DI float row_sum(float v) {
  v += dppz<0xB1>(v); v += dppz<0x4E>(v); v += dppz<0x124>(v); v += dppz<0x128>(v);
  return v;
}
DI float dot2(unsigned a, unsigned b, float c) {
  return __builtin_amdgcn_fdot2_f32_bf16(__builtin_bit_cast(bf16x2, a), __builtin_bit_cast(bf16x2, b), c, false);
}
DI float row_max(float v) {
  v = fmaxf(v, dppf<0xB1, 0xf>(v, v)); v = fmaxf(v, dppf<0x4E, 0xf>(v, v));
  v = fmaxf(v, dppf<0x124, 0xf>(v, v)); v = fmaxf(v, dppf<0x128, 0xf>(v, v));
  return v;
}
DI float x16_sum(float v) { auto r = __builtin_amdgcn_permlane16_swap(__float_as_uint(v), __float_as_uint(v), false, false); return __uint_as_float(r[0]) + __uint_as_float(r[1]); }
DI float x32_sum(float v) { auto r = __builtin_amdgcn_permlane32_swap(__float_as_uint(v), __float_as_uint(v), false, false); return __uint_as_float(r[0]) + __uint_as_float(r[1]); }
DI float x16_max(float v) { auto r = __builtin_amdgcn_permlane16_swap(__float_as_uint(v), __float_as_uint(v), false, false); return fmaxf(__uint_as_float(r[0]), __uint_as_float(r[1])); }
DI float x32_max(float v) { auto r = __builtin_amdgcn_permlane32_swap(__float_as_uint(v), __float_as_uint(v), false, false); return fmaxf(__uint_as_float(r[0]), __uint_as_float(r[1])); }

#define RBAR() asm volatile("s_barrier" ::: "memory")
#define BAR() asm volatile("s_waitcnt lgkmcnt(0)\n\ts_barrier" ::: "memory")
DI unsigned genc(float v, unsigned t8) { return ((__float_as_uint(v) + 0x80u) & 0xFFFFFF00u) | t8; }
DI float gdec(unsigned hi) { return __uint_as_float(hi & 0xFFFFFF00u); }

DI void wait_clock(u64 t0, unsigned ticks) {
#pragma unroll 1
  for (int n = 0; n < 4096 && (unsigned)(__builtin_amdgcn_s_memrealtime() - t0) < ticks; ++n) __builtin_amdgcn_s_sleep(1);
}

DI v4u bdiag(unsigned P, const unsigned (&s)[4]) {
  v4u b;
  b.x = __builtin_amdgcn_perm(P, P, s[0]); b.y = __builtin_amdgcn_perm(P, P, s[1]);
  b.z = __builtin_amdgcn_perm(P, P, s[2]); b.w = __builtin_amdgcn_perm(P, P, s[3]);
  return b;
}
DI void bdiag_sel(int cp, unsigned (&s)[4]) {
  const unsigned lo = (cp & 1) ? 0x01000C0Cu : 0x0C0C0100u, hi = (cp & 1) ? 0x03020C0Cu : 0x0C0C0302u, z = 0x0C0C0C0Cu;
  s[0] = cp < 2 ? lo : z; s[1] = cp < 2 ? z : lo; s[2] = cp < 2 ? hi : z; s[3] = cp < 2 ? z : hi;
}

DI unsigned lsbtag(unsigned tlo, unsigned thi, int p) { return (((p < 32 ? tlo : thi) >> (p & 31)) & 1u) | (((tlo ^ (unsigned)p) & 1u) << 16); }
constexpr unsigned LSBM = 0x00010001u;
DI unsigned pk7(float a, float b) { return pk(__uint_as_float(__float_as_uint(a) + 0x8000u), __uint_as_float(__float_as_uint(b) + 0x8000u)); }
DI unsigned lsbput(unsigned v, unsigned e) { return (v & ~LSBM) | e; }
DI unsigned strip4(v4u& d, const unsigned (&e)[4]) { d.x ^= e[0]; d.y ^= e[1]; d.z ^= e[2]; d.w ^= e[3]; return (d.x | d.y) | (d.z | d.w); }
DI void swap24(v4u& a, v4u& b) {
  unsigned az = a.z, aw = a.w, bx = b.x, by = b.y;
  asm volatile("v_swap_b32 %0, %2\n\tv_swap_b32 %1, %3\n\ts_nop 1" : "+v"(az), "+v"(aw), "+v"(bx), "+v"(by));
  a.z = az; a.w = aw; b.x = bx; b.y = by;
}
DI v4u tag8(f32x4 c, const unsigned (&t)[4]) {
  return v4u{(__float_as_uint(c[0]) & 0xFFFFFF00u) | t[0], (__float_as_uint(c[1]) & 0xFFFFFF00u) | t[1],
             (__float_as_uint(c[2]) & 0xFFFFFF00u) | t[2], (__float_as_uint(c[3]) & 0xFFFFFF00u) | t[3]};
}
extern "C" __global__ void __launch_bounds__(512, 2)
fused_decode(const u16* __restrict__ xin, const u16* __restrict__ ckv, const u16* __restrict__ wq, const u16* __restrict__ wuk,
             const u16* __restrict__ wuv, const u16* __restrict__ wo, const u16* __restrict__ w1, const u16* __restrict__ w3,
             const u16* __restrict__ w2, u16* __restrict__ out, char* ws) {
  extern __shared__ __attribute__((aligned(16))) char smem[];
  LAS char* L = (LAS char*)smem;
  const unsigned lbase = (unsigned)(size_t)smem;
  const int lane = threadIdx.x & 63, w = __builtin_amdgcn_readfirstlane(threadIdx.x >> 6);
  const int xg = blockIdx.x & 7, qd_ = blockIdx.x >> 3, qq = qd_ & 15, idx = xg * 16 + qq, hd = idx >> 2, jq = idx & 3;
  const int i16 = lane & 15, g = lane >> 4, bj = i16 >> 2, cp = lane & 3;
  if (w == 7) {
    u64 pc; unsigned back, len;
    asm volatile("s_getpc_b64 %0\n.Lpf_here:\n\ts_mov_b32 %1, .Lpf_here-fused_decode\n\ts_mov_b32 %2, fused_decode_end-fused_decode" : "=s"(pc), "=s"(back), "=s"(len));
    const rsrc_t rcode = mk((const void*)(pc - back), (int)min(len, 32768u));
    v4u dummy;
    asm volatile("s_nop 4\n\tbuffer_load_dwordx4 %0, %1, %2, 0 offen\n\ts_waitcnt vmcnt(0)" : "=&v"(dummy) : "v"(qd_ * 1024 + lane * 16), "s"(rcode) : "memory");
  }
  asm volatile("" ::"s"(xin), "s"(ckv), "s"(wq), "s"(wuk), "s"(wuv), "s"(wo), "s"(w1), "s"(w3), "s"(w2), "s"(out), "s"(ws));
  u64 t_start;
  asm volatile("s_memrealtime %0\n\ts_waitcnt lgkmcnt(0)" : "=s"(t_start) : "s"(ws) : "memory");
  const rsrc_t rws = mk(ws, WS_END);
  const rsrc_t rx = mk(xin, 4 * 576 * 2);
  unsigned sel[4];
  bdiag_sel(cp, sel);
#define MAKE_TAG(W) const u64 tag_ = (((u64)(W).y << 32) | (W).x) + 0x9E3779B97F4A7C15ull;  \
  const unsigned tlo = (unsigned)tag_, thi = (unsigned)(tag_ >> 32), t8 = thi & 0xFFu;        \
  const unsigned tb[4] = {tlo & 0xFFu, (tlo >> 8) & 0xFFu, (tlo >> 16) & 0xFFu, tlo >> 24}
  const int vo5 = (lane >> 3) * QB + (lane & 7) * 16;

  if (qd_ >= 16) {
    const int so5 = WS_H5 + idx * 128;
    if (w < 4) {
      v4u ka[2][4];
      v2u wv_;
      asm volatile("buffer_load_dwordx2 %0, off, %1, %2 sc1" : "=v"(wv_) : "s"(rws), "s"(WS_W) : "memory");
      {
        const rsrc_t rk = mk(wuk, 512 * 4096 * 2);
        const int ukx = 4 * (hd & 1);
        SLP(DLY_K);
#pragma unroll
        for (int n = 0; n < 8; ++n) {
          if (n >= PACE_K) VMCNT(PACE_K - 1);
          dma16(rk, g * 8192 + ((i16 ^ ((g << 2) | (n & 3))) * 16), (128 * jq + 32 * w + 4 * (n ^ ukx)) * 8192 + hd * 256, lbase + LP_UK + (8 * w + (n ^ ukx)) * 1024);
        }
        VMCNT(0);
        const int fr = ((i16 & 3) << 2) | (i16 >> 2);
#pragma unroll
        for (int t = 0; t < 2; ++t)
#pragma unroll
          for (int s = 0; s < 4; ++s) {
            ka[t][s] = *(LAS v4u*)(L + LP_UK + (32 * w + 16 * t + i16) * 256 + (((4 * s + g) ^ fr) * 16));
            asm volatile("" : "+v"(ka[t][s]));
          }
      }
      asm volatile("" : "+v"(wv_));
      MAKE_TAG(wv_);
      unsigned eq[4];
#pragma unroll
      for (int k = 0; k < 4; ++k) { eq[k] = lsbtag(tlo, thi, 4 * i16 + k); asm volatile("" : "+v"(eq[k])); }
      SLP(DLY_H1);
      {
        const int blk = w & 1, half = w >> 1;
        float q0, q1;
        for (int n = 0;; ++n) {
          v4u p[4];
#pragma unroll
          for (int jj = 0; jj < 4; ++jj) p[jj] = hl4(rws, lane * 16, WS_H1 + slot(hd * 4 + jj, 4096) + (blk * 2 + half) * 1024);
          unsigned bad = 0;
          q0 = 0.f; q1 = 0.f;
#pragma unroll
          for (int jj = 0; jj < 4; ++jj) {
            bad |= (p[jj].x ^ tlo) | (p[jj].z ^ tlo) | (((p[jj].y ^ t8) | (p[jj].w ^ t8)) & 0xFFu);
            q0 += gdec(p[jj].y); q1 += gdec(p[jj].w);
          }
          if (__all(bad == 0) || n >= GSPIN) break;
          SLP(DLY_R1);
        }
        const unsigned qp = pk(q0, q1);
        const int d0 = 32 * g + 16 * half + 4 * blk + cp;
        *(LAS u16*)(L + LP_QB + bj * 320 + d0 * 2) = (u16)qp;
        *(LAS u16*)(L + LP_QB + bj * 320 + (d0 + 8) * 2) = (u16)(qp >> 16);
      }
      BAR();
      {
        v4u qf[4];
#pragma unroll
        for (int s = 0; s < 4; ++s) qf[s] = *(LAS v4u*)(L + LP_QB + cp * 320 + s * 64 + g * 16);
        f32x4 c0 = {0.f, 0.f, 0.f, 0.f}, c1 = c0;
#pragma unroll
        for (int s = 0; s < 4; ++s) { c0 = mfma16(ka[0][s], qf[s], c0); c1 = mfma16(ka[1][s], qf[s], c1); }
        const float sc = 0.08838834764831845f * 1.4426950408889634f;
        if (i16 < 4) {
          *(LAS v2u*)(L + LP_QO + i16 * 256 + (32 * w + 4 * g) * 2) = v2u{pk7(c0[0] * sc, c0[1] * sc), pk7(c0[2] * sc, c0[3] * sc)};
          *(LAS v2u*)(L + LP_QO + i16 * 256 + (32 * w + 16 + 4 * g) * 2) = v2u{pk7(c1[0] * sc, c1[1] * sc), pk7(c1[2] * sc, c1[3] * sc)};
        }
      }
      BAR();
      if (w == 0) {
        const v4u v = *(LAS v4u*)(L + LP_QO + lane * 16);
        st4(v4u{lsbput(v.x, eq[0]), lsbput(v.y, eq[1]), lsbput(v.z, eq[2]), lsbput(v.w, eq[3])}, rws, g * 2048 + i16 * 16, WS_QA + slot(hd, 1024) + jq * 256);
      }
      BAR();
      const rsrc_t r2 = mk(w2, 4608 * 576 * 2);
      const int so2 = (xg * 576 + 36 * qq) * 1152;
      const rsrc_t ro = mk(wo, 4096 * 576 * 2);
      const int soo = (hd * 128 + 32 * jq) * 1152;
      v4u a2[9][2];
      v2u a8[3][2];
      {
        const int vo4 = 2 * g * 1152 + i16 * 16, vo4m = g < 2 ? vo4 : OOB;
        const int vo8 = 2 * g * 1152 + i16 * 8 + 1024, vo8m = (g < 2 && w == 0) ? vo8 : OOB;
#pragma unroll
        for (int n = 0; n < 24; ++n) {
          if (n >= PACE_W) VMCNT(PACE_W - 1);
          if (n == PACE_W) { SCHED(); RBAR(); SLP(1); }
          const int o = n >> 1, r = n & 1;
          if (n < 10) a2[o][r] = ld4<NT>(r2, o < 4 ? vo4 : vo4m, so2 + (8 * o + r) * 1152 + 256 * w);
          else if (n < 12) a8[0][r] = ld2<NT>(r2, vo8, so2 + (8 * w + r) * 1152);
          else if (n < 14) a8[1][r] = ld2<NT>(r2, vo8m, so2 + (32 + r) * 1152);
          else if (n < 22) a2[o - 2][r] = ld4<NT>(ro, vo4, soo + (8 * (o - 7) + r) * 1152 + 256 * w);
          else a8[2][r] = ld2<NT>(ro, vo8, soo + (8 * w + r) * 1152);
          SCHED();
        }
      }
#pragma unroll
      for (int o = 0; o < 5; ++o) swap24(a2[o][0], a2[o][1]);
      RBAR();
      {
        const v4u h4 = *(LAS v4u*)(L + LP_HP + (bj * 4 + g) * 48);
        const unsigned h5 = *(LAS unsigned*)(L + LP_HP + (bj * 4 + g) * 48 + 16);
        const unsigned hw = *(LAS unsigned*)(L + LP_HP + (bj * 4 + g) * 48 + 4 * w);
        f32x4 c8 = {0.f, 0.f, 0.f, 0.f}, cx = c8, cy = c8;
        c8 = mfma16(v4u{a8[0][0].x, a8[0][0].y, a8[0][1].x, a8[0][1].y}, bdiag(hw, sel), c8);
        c8 = mfma16(v4u{a8[1][0].x, a8[1][0].y, a8[1][1].x, a8[1][1].y}, bdiag(h5, sel), c8);
#pragma unroll
        for (int o = 0; o < 5; ++o) {
          const v4u B = bdiag(o == 0 ? h4.x : o == 1 ? h4.y : o == 2 ? h4.z : o == 3 ? h4.w : h5, sel);
          cx = mfma16(a2[o][0], B, cx);
          cy = mfma16(a2[o][1], B, cy);
        }
#pragma unroll
        for (int o = 5; o < 9; ++o) swap24(a2[o][0], a2[o][1]);
        asm volatile("" : "+v"(cx), "+v"(cy), "+v"(c8));
        int bpa = (bj * 16 + g) * 4;
        asm volatile("" : "+v"(bpa));
        float y0, y1;
        for (int n = 0;; ++n) {
          v4u p[4];
#pragma unroll
          for (int jj = 0; jj < 4; ++jj) p[jj] = hl4(rws, lane * 16, WS_H4 + slot(idx, 4096) + jj * 1024);
          unsigned bad = 0;
          y0 = 0.f; y1 = 0.f;
#pragma unroll
          for (int jj = 0; jj < 4; ++jj) {
            bad |= (p[jj].x ^ tlo) | (p[jj].z ^ tlo) | (((p[jj].y ^ t8) | (p[jj].w ^ t8)) & 0xFFu);
            y0 += gdec(p[jj].y); y1 += gdec(p[jj].w);
          }
          if (__all(bad == 0) || n >= GSPIN) break;
          SLP(DLY_R4);
        }
        const unsigned yp = pk(y0, y1);
        const unsigned pw = __builtin_amdgcn_ds_bpermute(bpa + 16 * w, yp);
        unsigned po[4];
#pragma unroll
        for (int o = 0; o < 4; ++o) po[o] = __builtin_amdgcn_ds_bpermute(bpa + 16 * o, yp);
        c8 = mfma16(v4u{a8[2][0].x, a8[2][0].y, a8[2][1].x, a8[2][1].y}, bdiag(pw, sel), c8);
#pragma unroll
        for (int o = 0; o < 4; ++o) {
          const v4u B = bdiag(po[o], sel);
          cx = mfma16(a2[5 + o][0], B, cx);
          cy = mfma16(a2[5 + o][1], B, cy);
        }
        *(LAS f32x4*)(L + LP_B8 + w * 1024 + lane * 16) = c8;
        st4(tag8(cx, tb), rws, vo5, so5 + blk5(2 * w));
        st4(tag8(cy, tb), rws, vo5, so5 + blk5(2 * w + 1));
        BAR();
        if (w == 0) {
          f32x4 s = *(LAS f32x4*)(L + LP_B8 + lane * 16);
#pragma unroll
          for (int k = 1; k < 4; ++k) s += *(LAS f32x4*)(L + LP_B8 + k * 1024 + lane * 16);
          st4(tag8(s, tb), rws, vo5, so5 + blk5(8));
        }
      }
      if (qq < 9) RBAR();
    } else {
      const int v = w - 4;
      unsigned xn[8];
      const v2u wv_ = hl2(rws, 0, WS_W);
      v4u pt1;
      asm volatile("buffer_load_dwordx4 %0, %1, %2, %3 offen sc1" : "=v"(pt1) : "v"(lane * 16), "s"(rws), "s"(WS_H1 + slot(idx, 4096) + v * 1024) : "memory");
      {
        const v4u x0 = ld4<0>(rx, lane * 16, v * 1152), x1 = ld4<0>(rx, lane < 8 ? lane * 16 : OOB, v * 1152 + 1024);
        float s = 0.f, q = 0.f;
#pragma unroll
        for (int k = 0; k < 4; ++k) {
          s = dot2(x0[k], 0x3F803F80u, s); q = dot2(x0[k], x0[k], q);
          s = dot2(x1[k], 0x3F803F80u, s); q = dot2(x1[k], x1[k], q);
        }
        const float mean = x32_sum(x16_sum(row_sum(s))) * (1.0f / 576.0f);
        const float msq = x32_sum(x16_sum(row_sum(q))) * (1.0f / 576.0f);
        const float rstd = __builtin_amdgcn_rsqf(fmaxf(fmaf(-mean, mean, msq), 0.f) + 1e-5f), nb = -mean * rstd;
#pragma unroll
        for (int k = 0; k < 4; ++k) {
          xn[k] = pk(fmaf(bf2f(x0[k]), rstd, nb), fmaf(__uint_as_float(x0[k] & 0xFFFF0000u), rstd, nb));
        }
#pragma unroll
        for (int k = 0; k < 4; ++k) xn[4 + k] = pk(fmaf(bf2f(x1[k]), rstd, nb), fmaf(__uint_as_float(x1[k] & 0xFFFF0000u), rstd, nb));
      }
      VMCNT(0);
      asm volatile("" : "+v"(pt1));
#pragma unroll
      for (int hf = 0; hf < 2; ++hf) {
        if (hf == 0 || lane < 8) {
#pragma unroll
          for (int k = 0; k < 4; ++k) {
            const int o = lane + 64 * hf;
            *(LAS unsigned*)(L + LP_XD + ((v * 4 + ((2 * k) & 3)) * 148 + 2 * o + (k >> 1)) * 4) = __builtin_amdgcn_perm(0u, xn[4 * hf + k], 0x01000100u);
            *(LAS unsigned*)(L + LP_XD + ((v * 4 + ((2 * k + 1) & 3)) * 148 + 2 * o + (k >> 1)) * 4) = __builtin_amdgcn_perm(0u, xn[4 * hf + k], 0x03020302u);
          }
        }
      }
      touch(rws, lane * 16, WS_H4 + slot(idx, 4096) + v * 1024);
      MAKE_TAG(wv_);
      const int U0 = (81 * qq) / 144, Qb = 144 * (U0 + 1);
      unsigned xd[21];
      const GAS char* wb = (const GAS char*)(i16 < 8 ? w1 : w3) + (xg * 576 * 2 + (i16 & 7) * 16 + g * 9216);
      const unsigned m0 = cp == 0 ? 0xFFFFu : cp == 1 ? 0xFFFF0000u : 0u, m1 = cp == 2 ? 0xFFFFu : cp == 3 ? 0xFFFF0000u : 0u;
      f32x4 acc[4];
#pragma unroll
      for (int k = 0; k < 4; ++k) acc[k] = f32x4{0.f, 0.f, 0.f, 0.f};
      v4u a[21];
        int off[2][3];
        unsigned use[2][3];
        int hpa[2];
#pragma unroll
        for (int e = 0; e < 2; ++e) {
          const int t = ((v - 2) & 1) * 64 + lane + 128 * e;
          const int cl = t >> 2, b = t & 3, f = 36 * qq + cl, u = f >> 6;
          const int r0 = (144 * u) / 81, r1 = (144 * u + 143) / 81;
#pragma unroll
          for (int k = 0; k < 3; ++k) {
            const int rr = r0 + k;
            const bool on = t < 144 && rr <= r1;
            off[e][k] = on ? slot(xg * 16 + rr, 8192) + (u - (81 * rr) / 144) * 4096 + (f & 63) * 64 + b * 16 : OOB;
            use[e][k] = on ? 0xFFFFFFFFu : 0u;
          }
          hpa[e] = LP_HP + ((b * 4 + ((cl >> 1) & 3)) * 12 + (cl >> 3)) * 4 + (cl & 1) * 2;
          asm volatile("" : "+v"(hpa[e]), "+v"(off[e][0]), "+v"(off[e][1]), "+v"(off[e][2]), "+v"(use[e][0]), "+v"(use[e][1]), "+v"(use[e][2]));
        }
      int pa = WS_H7 + slot(idx, 8192) + v * 4096 + (32 * ((lane >> 4) & 1) + 16 * (lane >> 5) + cp) * 64 + bj * 16;
      asm volatile("" : "+v"(pa));
      BAR();
      RBAR();
#pragma unroll
      for (int t2 = 0; t2 < 21; ++t2) {
        const int Q = 81 * qq + min(4 * t2 + v, 80), qk = Q >= Qb ? Q - Qb : Q - 144 * U0;
        xd[t2] = *(LAS unsigned*)(L + LP_XD + ((bj * 4 + g) * 148 + qk) * 4);
        asm volatile("" : "+v"(xd[t2]));
      }
      wait_clock(t_start, T_F);
#pragma unroll
      for (int t = -PACE_G; t < 21; ++t) {
        const int tl = t + PACE_G;
        if (tl < 21) {
          const int Q = 81 * qq + 4 * tl + v, u = Q >= Qb ? U0 + 1 : U0, qk = Q - 144 * u;
          if (tl < 20 || v == 0) {
            const GAS v4u* p = (const GAS v4u*)(wb + (unsigned)(qk * 4 * 9216 + u * 128));
            a[tl] = __builtin_nontemporal_load(p);
          } else a[tl] = v4u{0u, 0u, 0u, 0u};
          SCHED();
        }
        if (tl == 20) BAR();
        if (t >= 0) {
          const int Q = 81 * qq + min(4 * t + v, 80);
          const v2u B = {xd[t] & m0, xd[t] & m1};
          if (Q < Qb) { acc[0] = mfma16k16(v2u{a[t].x, a[t].y}, B, acc[0]); acc[1] = mfma16k16(v2u{a[t].z, a[t].w}, B, acc[1]); }
          else        { acc[2] = mfma16k16(v2u{a[t].x, a[t].y}, B, acc[2]); acc[3] = mfma16k16(v2u{a[t].z, a[t].w}, B, acc[3]); }
          SCHED();
        }
      }
#pragma unroll
      for (int k = 0; k < 4; ++k) *(LAS f32x4*)(L + LP_RED + (v * 4 + k) * 1024 + lane * 16) = acc[k];
      BAR();
      if (v == 0 || (v == 1 && 81 * qq + 80 >= Qb)) {
#pragma unroll
        for (int hh = 0; hh < 2; ++hh) {
          f32x4 r = *(LAS f32x4*)(L + LP_RED + (2 * v + hh) * 1024 + lane * 16);
#pragma unroll
          for (int k = 1; k < 4; ++k) r += *(LAS f32x4*)(L + LP_RED + (k * 4 + 2 * v + hh) * 1024 + lane * 16);
#pragma unroll
          for (int k = 0; k < 2; ++k) {
            const auto x = __builtin_amdgcn_permlane32_swap(__float_as_uint(r[k]), __float_as_uint(r[2 + k]), false, false);
            st4(v4u{tlo, genc(__uint_as_float(x[0]), t8), tlo, genc(__uint_as_float(x[1]), t8)}, rws, pa + (8 * k + 4 * hh) * 64, 0);
          }
        }
      } else if (v >= 2) {
        SLP(DLY_H7);
        wait_clock(t_start, T_H7);
        float gs[2], us[2];
        for (int n = 0;; ++n) {
          v4u p[2][3];
#pragma unroll
          for (int e = 0; e < 2; ++e)
#pragma unroll
            for (int k = 0; k < 3; ++k) p[e][k] = hl4(rws, off[e][k], WS_H7);
          unsigned bad = 0;
#pragma unroll
          for (int e = 0; e < 2; ++e) {
            gs[e] = 0.f; us[e] = 0.f;
#pragma unroll
            for (int k = 0; k < 3; ++k) {
              bad |= ((p[e][k].x ^ tlo) | (p[e][k].z ^ tlo) | (((p[e][k].y ^ t8) | (p[e][k].w ^ t8)) & 0xFFu)) & use[e][k];
              gs[e] += gdec(p[e][k].y); us[e] += gdec(p[e][k].w);
            }
          }
          if (__all(bad == 0) || n >= GSPIN) break;
          SLP(DLY_R7);
        }
#pragma unroll
        for (int e = 0; e < 2; ++e) {
          const float hv = gs[e] * __builtin_amdgcn_rcpf(1.0f + __expf(-gs[e])) * us[e];
          if ((v - 2) * 64 + lane + 128 * e < 160) *(LAS u16*)(L + hpa[e]) = (u16)pk(hv, hv);
        }
      }
      BAR();
      const int fe = lane & 31, fL = 8 * xg + (fe >> 2), frr = fe & 3;
      const int oidx = ((fL & 15) >> 2) * 576 + (qq < 8 ? 128 * (qq >> 1) + 8 * (4 * (fL >> 4) + frr) + 4 * (qq & 1) + (fL & 3) : 512 + 4 * (4 * (fL >> 4) + frr) + (fL & 3));
      unsigned xres = 0;
      if (v == 3 && qq < 9) xres = ((const GAS u16*)xin)[oidx];
      const int h5o = WS_H5 + slot(qq * 8 + xg, 16384) + v * 4096;
      int fina = LP_FIN + v * 128 + (lane & 7) * 16 + g * 4;
      asm volatile("" : "+v"(xres), "+v"(fina));
      RBAR();
      if (qq < 9) {
        SLP(DLY_H5);
        v4u d[4];
#pragma unroll
        for (int k = 0; k < 4; ++k) d[k] = hl4(rws, lane * 16, h5o + k * 1024);
        unsigned pend = 15u;
#pragma unroll 1
        for (int n = 0;; ++n) {
#pragma unroll
          for (int k = 0; k < 4; ++k)
            if (pend & (1u << k)) { if (__all((strip4(d[k], tb) & 0xFFu) == 0)) pend &= ~(1u << k); }
          if (pend == 0 || n >= GSPIN) break;
#pragma unroll
          for (int k = 0; k < 4; ++k)
            if (pend & (1u << k)) d[k] = hl4(rws, lane * 16, h5o + k * 1024);
        }
        f32x4 s = (__builtin_bit_cast(f32x4, d[0]) + __builtin_bit_cast(f32x4, d[1])) + (__builtin_bit_cast(f32x4, d[2]) + __builtin_bit_cast(f32x4, d[3]));
#pragma unroll
        for (int r = 0; r < 4; ++r) s[r] += dppz<0x128>(s[r]);
        const auto x01 = __builtin_amdgcn_permlane16_swap(__float_as_uint(s[0]), __float_as_uint(s[1]), false, false);
        const auto x23 = __builtin_amdgcn_permlane16_swap(__float_as_uint(s[2]), __float_as_uint(s[3]), false, false);
        const auto z = __builtin_amdgcn_permlane32_swap(__float_as_uint(__uint_as_float(x01[0]) + __uint_as_float(x01[1])),
                                                        __float_as_uint(__uint_as_float(x23[0]) + __uint_as_float(x23[1])), false, false);
        *(LAS float*)(L + fina) = __uint_as_float(z[0]) + __uint_as_float(z[1]);
        BAR();
        if (v == 3) {
          float y = bf2f(xres);
#pragma unroll
          for (int k = 0; k < 4; ++k) y += *(LAS float*)(L + LP_FIN + k * 128 + fe * 4);
          if (lane < 32) ((GAS u16*)out)[oidx] = (u16)pk(y, y);
          if (qq == 0 && xg == 0) st2(v2u{tlo, thi}, rws, lane == 0 ? 0 : OOB, WS_W);
        }
      }
    }
    return;
  }

  const int ab = xg >> 1;
  const int r32 = lane & 31, hh = lane >> 5;
  v2u wa_;
  if (w < 4) {
    const rsrc_t rc = mk(ckv, 4 * 4096 * 512 * 2);
    v2u wb_;
    asm volatile("buffer_load_dwordx2 %0, off, %1, %2 sc1" : "=v"(wb_) : "s"(rws), "s"(WS_W) : "memory");
    {
      int vo[4];
#pragma unroll
      for (int k = 0; k < 4; ++k) vo[k] = g * 1024 + ((i16 ^ ((g << 2) | k)) * 16);
      const int so = (idx * 128 + 32 * w) * 1024;
      SLP(DLY_C);
#pragma unroll
      for (int n = 0; n < 32; ++n) {
        if (n >= PACE_C) VMCNT(PACE_C - 1);
        dma16(rc, vo[(n >> 2) & 3], so + (n >> 2) * 4096 + (n & 3) * 256, lbase + (n & 3) * 32768 + (32 * w + 4 * (n >> 2)) * 256);
      }
    }
    RBAR();
    asm volatile("" : "+v"(wb_));
    wa_ = wb_;
  } else {
    wa_ = hl2(rws, 0, WS_W);
    if (w == 4) {
      v2u xr[9];
#pragma unroll
      for (int t = 0; t < 9; ++t) xr[t] = ld2<0>(rx, g * 1152 + i16 * 8, t * 128);
      const rsrc_t rq = mk(wq, 576 * 4096 * 2);
      const int vo = 2 * g * 8192 + i16 * 16, so = 8 * jq * 8192 + hd * 256;
      v4u a[18][2];
#pragma unroll
      for (int o = 0; o < 18; ++o) { a[o][0] = ld4<NT>(rq, vo, so + (32 * o) * 8192); a[o][1] = ld4<NT>(rq, vo, so + (32 * o + 1) * 8192); }
      SCHED();
      float s = 0.f, q = 0.f;
#pragma unroll
      for (int t = 0; t < 9; ++t) {
        *(LAS v2u*)(L + LA_QA + g * 1168 + i16 * 8 + t * 128) = xr[t];
        s = dot2(xr[t].x, 0x3F803F80u, s); q = dot2(xr[t].x, xr[t].x, q);
        s = dot2(xr[t].y, 0x3F803F80u, s); q = dot2(xr[t].y, xr[t].y, q);
      }
      FENCE();
      unsigned xp[18];
#pragma unroll
      for (int o = 0; o < 18; ++o) xp[o] = *(LAS unsigned*)(L + LA_QA + bj * 1168 + (4 * jq + 16 * o + g) * 4);
      const float mean = row_sum(s) * (1.0f / 576.0f), msq = row_sum(q) * (1.0f / 576.0f);
      const float rs0 = __builtin_amdgcn_rsqf(fmaxf(fmaf(-mean, mean, msq), 0.f) + 1e-5f);
      const float rstd = __uint_as_float(__builtin_amdgcn_ds_bpermute(bj * 64, __float_as_uint(rs0)));
      const float nb = __uint_as_float(__builtin_amdgcn_ds_bpermute(bj * 64, __float_as_uint(-mean * rs0)));
      v4u Bq[18];
#pragma unroll
      for (int o = 0; o < 18; ++o) {
        Bq[o] = bdiag(pk(fmaf(bf2f(xp[o]), rstd, nb), fmaf(__uint_as_float(xp[o] & 0xFFFF0000u), rstd, nb)), sel);
        asm volatile("" : "+v"(Bq[o]));
      }
      const u64 tg = (((u64)wa_.y << 32) | wa_.x) + 0x9E3779B97F4A7C15ull;
      unsigned ql = (unsigned)tg, q8 = (unsigned)(tg >> 32) & 0xFFu;
      asm volatile("" : "+v"(ql), "+v"(q8));
      SCHED();
      f32x4 qx = {0.f, 0.f, 0.f, 0.f}, qy = qx;
#pragma unroll
      for (int o = 0; o < 18; ++o) {
        swap24(a[o][0], a[o][1]);
        qx = mfma16(a[o][0], Bq[o], qx);
        qy = mfma16(a[o][1], Bq[o], qy);
      }
      const int po = WS_H1 + slot(idx, 4096);
      st4(v4u{ql, genc(qx[0], q8), ql, genc(qx[1], q8)}, rws, lane * 16, po);
      st4(v4u{ql, genc(qx[2], q8), ql, genc(qx[3], q8)}, rws, lane * 16, po + 1024);
      st4(v4u{ql, genc(qy[0], q8), ql, genc(qy[1], q8)}, rws, lane * 16, po + 2048);
      st4(v4u{ql, genc(qy[2], q8), ql, genc(qy[3], q8)}, rws, lane * 16, po + 3072);
    }
    RBAR();
  }
  MAKE_TAG(wa_);
  unsigned e4[4];
#pragma unroll
  for (int k = 0; k < 4; ++k) e4[k] = lsbtag(tlo, thi, 4 * i16 + k);
  if (w < 4) {
    v4u fa[32];
    {
      const int fr = ((r32 & 3) << 2) | ((r32 >> 2) & 3);
      const int ca = (32 * w + r32) * 256;
#define QK_READ(Q) _Pragma("unroll") for (int k = 0; k < 8; ++k) fa[(Q)*8 + k] = *(LAS v4u*)(L + (Q)*32768 + ca + (((2 * k + hh) ^ fr) * 16)); SCHED();
      VMCNT(0); QK_READ(0) QK_READ(1) QK_READ(2) QK_READ(3)
    }
    int qb[4];
#pragma unroll
    for (int k = 0; k < 4; ++k) { qb[k] = LA_QA + ((lane ^ ((2 * k + hh) & 7)) * 16); asm volatile("" : "+v"(qb[k])); }
    const int q4 = i16 >> 2, lowc = 2 * (g & 1) + (cp >> 1);
    int ta[4][2];
#pragma unroll
    for (int t = 0; t < 4; ++t)
#pragma unroll
      for (int t2 = 0; t2 < 2; ++t2) {
        ta[t][t2] = w * 32768 + (8 * t2 + 4 * hh + q4) * 256 + ((((t ^ q4) << 2) | (lowc ^ (2 * t2 + hh))) * 16) + 8 * (cp & 1);
        asm volatile("" : "+v"(ta[t][t2]));
      }
    int mxa = (r32 >> 4) * 320 + (r32 & 15) * 4, pba = LA_PB + lane * 16;
    unsigned e0 = lsbtag(tlo, thi, 4 * (r32 >> 1)), e1 = lsbtag(tlo, thi, 4 * (r32 >> 1) + 1), e2 = lsbtag(tlo, thi, 4 * (r32 >> 1) + 2), e3 = lsbtag(tlo, thi, 4 * (r32 >> 1) + 3);
    const int sp = idx & 31;
    int h3v = ((hh * 4 + (r32 & 1)) ^ (sp & 5)) * QB + (r32 >> 1) * 16 + WS_H3 + (sp * 16 + ab * 4 + w) * 256;
    asm volatile("" : "+v"(mxa), "+v"(pba), "+v"(e0), "+v"(e1), "+v"(e2), "+v"(e3), "+v"(h3v));
    BAR();
    v4u cb[32];
#define TR_READ(n) do { const v2u x0_ = __builtin_bit_cast(v2u, __builtin_amdgcn_ds_read_tr16_b64_v4i16((LAS s16x4*)(L + ta[(n) >> 3][0] + ((n) & 7) * 4096))); \
    const v2u x1_ = __builtin_bit_cast(v2u, __builtin_amdgcn_ds_read_tr16_b64_v4i16((LAS s16x4*)(L + ta[(n) >> 3][1] + ((n) & 7) * 4096)));                 \
    cb[n] = v4u{x0_.x, x0_.y, x1_.x, x1_.y}; } while (0)
    f32x16 sacc;
#pragma unroll
    for (int k = 0; k < 16; ++k) sacc[k] = 0.f;
    {
      v4u fb[32];
#pragma unroll
      for (int s = 0; s < 6; ++s) fb[s] = *(LAS v4u*)(L + qb[s & 3] + s * 1024);
      SCHED();
#pragma unroll
      for (int s = 0; s < 32; ++s) {
        if (s + 6 < 32) fb[s + 6] = *(LAS v4u*)(L + qb[(s + 6) & 3] + (s + 6) * 1024);
        if (s >= 26) TR_READ(s - 26);
        sacc = mfma32(fa[s], fb[s], sacc);
        SCHED();
      }
    }
    float mw = sacc[0];
#pragma unroll
    for (int k = 1; k < 16; ++k) mw = fmaxf(mw, sacc[k]);
    mw = x32_max(mw);
    asm volatile("s_waitcnt lgkmcnt(0)" ::: "memory");
    *(LAS float*)(L + mxa + w * 32768) = mw;
    BAR();
    const float mx = gdec(genc(fmaxf(fmaxf(*(LAS float*)(L + mxa), *(LAS float*)(L + mxa + 32768)), fmaxf(*(LAS float*)(L + mxa + 65536), *(LAS float*)(L + mxa + 98304))), 0u));
    float ls = 0.f;
#pragma unroll
    for (int k = 0; k < 16; ++k) { sacc[k] = __builtin_amdgcn_exp2f(sacc[k] - mx); ls += sacc[k]; }
#pragma unroll
    for (int s2 = 0; s2 < 2; ++s2)
      *(LAS v4u*)(L + pba + (2 * w + s2) * 1024) =
          v4u{pk(sacc[8 * s2], sacc[8 * s2 + 1]), pk(sacc[8 * s2 + 2], sacc[8 * s2 + 3]), pk(sacc[8 * s2 + 4], sacc[8 * s2 + 5]), pk(sacc[8 * s2 + 6], sacc[8 * s2 + 7])};
    ls = x32_sum(ls);
    *(LAS float*)(L + mxa + 640 + w * 32768) = ls;
    BAR();
    {
      v4u pf[8];
#pragma unroll
      for (int k = 0; k < 8; ++k) pf[k] = *(LAS v4u*)(L + pba + k * 1024);
      f32x4 l4;
      if (w == 0) l4 = f32x4{*(LAS float*)(L + mxa + 640), *(LAS float*)(L + mxa + 640 + 32768), *(LAS float*)(L + mxa + 640 + 65536), *(LAS float*)(L + mxa + 640 + 98304)};
      SCHED();
      f32x16 o[4];
      unsigned p01[16], pq[16];
      const bool odd = lane & 1;
#pragma unroll
      for (int t = 0; t < 4; ++t)
#pragma unroll
        for (int k = 0; k < 16; ++k) o[t][k] = 0.f;
#pragma unroll
      for (int n = 0; n < 32; ++n) {
        if (n + 6 < 32) TR_READ(n + 6);
        o[n >> 3] = mfma32(pf[n & 7], cb[n], o[n >> 3]);
        if (n == 0 && w == 0)
          st4(v4u{tlo, genc(mx, t8), tlo, genc((l4[0] + l4[1]) + (l4[2] + l4[3]), t8)}, rws, lane < 32 ? r32 * 16 : OOB, WS_ML + slot(idx, 512));
        if (n >= 17 && n < 32 && n != 24) {
          const int j = n < 24 ? n - 17 : n - 18;
          if (j < 8) { pq[2 * j] = pk7(o[0][2 * j], o[1][2 * j]); pq[2 * j + 1] = pk7(o[0][2 * j + 1], o[1][2 * j + 1]); asm volatile("" : "+v"(pq[2 * j]), "+v"(pq[2 * j + 1])); }
          if (j >= 6) {
            const int k = j - 6;
            p01[2 * k] = lsbput(odd ? pq[2 * k + 1] : pq[2 * k], e0);
            p01[2 * k + 1] = __builtin_amdgcn_update_dpp(0, lsbput(odd ? pq[2 * k] : pq[2 * k + 1], e2), 0xB1, 0xf, 0xf, true);
            asm volatile("" : "+v"(p01[2 * k]), "+v"(p01[2 * k + 1]));
          }
        }
        SCHED();
      }
#pragma unroll
      for (int k = 0; k < 8; ++k) {
        const unsigned pa = pk7(o[2][2 * k], o[3][2 * k]), pb = pk7(o[2][2 * k + 1], o[3][2 * k + 1]);
        st4(v4u{p01[2 * k], lsbput(odd ? pb : pa, e1), p01[2 * k + 1], (unsigned)__builtin_amdgcn_update_dpp(0, lsbput(odd ? pa : pb, e3), 0xB1, 0xf, 0xf, true)},
            rws, h3v, ((2 * (k & 1) + 8 * (k >> 1)) ^ (sp & 26)) * QB);
      }
    }
    RBAR();
    RBAR();
    RBAR();
  } else {
    const int v = w - 4;
    const rsrc_t rv = mk(wuv, 512 * 4096 * 2);
    v4u uw[8];
#define TL(i) do { uw[i] = ld4<NT>(rv, g * 32 * 8192 + i16 * 16, (128 * jq + 8 * v + ((i) ^ (hd & 1))) * 8192 + hd * 256); SCHED(); } while (0)
    {
      v4u qa[8];
      SLP(DLY_QA);
#pragma unroll
      for (int k = 0; k < 8; ++k) qa[k] = hl4(rws, lane * 16, WS_QA + ab * 2048 + slot(8 * v + k, 1024));
      SCHED();
#pragma unroll 1
      for (int n = 0;; ++n) {
        unsigned bad = 0;
#pragma unroll
        for (int k = 0; k < 8; ++k) bad |= strip4(qa[k], e4);
        if (__all((bad & LSBM) == 0) || n >= GSPIN) break;
        SLP(DLY_R2);
#pragma unroll
        for (int k = 0; k < 8; ++k) qa[k] = hl4(rws, lane * 16, WS_QA + ab * 2048 + slot(8 * v + k, 1024));
      }
#pragma unroll
      for (int k = 0; k < 8; ++k) *(LAS v4u*)(L + LA_QA + (lane >> 1) * 1024 + ((((lane & 1) * 32 + 8 * v + k) ^ (lane & 7)) * 16)) = qa[k];
    }
    BAR();
#pragma unroll
    for (int i = 0; i < 4; ++i) TL(i);
    RBAR();
#pragma unroll
    for (int i = 4; i < 8; ++i) TL(i);
    RBAR();
    const int e7 = lane & 7, q4 = i16 >> 2;
    int cta = LA_CTX + v * 288 + (32 * g + e7) * 2, ctr = LA_CTX + cp * 288 + g * 16;
    int h4a = ((2 * (g & 1) + (i16 & 1)) * 16 + (2 * (g >> 1) + (i16 >> 3)) * 4 + ((i16 >> 1) & 3)) * 16 + WS_H4 + slot(hd * 4 + v, 4096) + jq * 1024;
    const unsigned src = (lane & 8) ? 0x0302u : 0x0100u, s1 = (e7 & 1) ? ((src << 16) | 0x0C0Cu) : (0x0C0C0000u | src);
    unsigned s8[4];
    int uwa[4], bva[2][2];
#pragma unroll
    for (int k = 0; k < 4; ++k) {
      s8[k] = (e7 >> 1) == k ? s1 : 0x0C0C0C0Cu;
      uwa[k] = LA_UVW + (32 * v + g) * 256 + ((i16 ^ ((g << 2) | k)) * 16);
      bva[k >> 1][k & 1] = LA_UVW + (8 * g + 4 * (k & 1) + q4) * 256 + (((2 * (2 * v + (k >> 1)) + (cp >> 1)) ^ ((q4 << 2) | ((2 * g + (k & 1)) & 3))) * 16) + 8 * (cp & 1);
      asm volatile("" : "+v"(s8[k]), "+v"(uwa[k]), "+v"(bva[k >> 1][k & 1]));
    }
    asm volatile("" : "+v"(cta), "+v"(ctr), "+v"(h4a));
    const int h3l = ((hd & 3) ^ g) * QB + g * 4096 + i16 * 16;
#define ML_LOAD() hl4(rws, slot(r32, 512), WS_ML + v * 1024 + hd * 16)
#define ML_OK(V) __all((((V).x ^ tlo) | ((V).z ^ tlo) | ((((V).y ^ t8) | ((V).w ^ t8)) & 0xFFu)) == 0)
#define H3_LOADS() _Pragma("unroll") for (int k = 0; k < 8; ++k) d[k] = hl4(rws, h3l, WS_H3 + ((hd >> 2) ^ k) * 4 * QB + k * 16384 + (v * 4 + jq) * 256); SCHED()
    v4u d[8], bk[8];
    v4u ml;
    VMCNT(0);
    RBAR();
#pragma unroll
    for (int k = 0; k < 8; ++k) *(LAS v4u*)(L + uwa[k & 3] + k * 1024) = uw[k];
    BAR();
    v4u bv[2][4];
#pragma unroll
    for (int t = 0; t < 2; ++t)
#pragma unroll
      for (int ks = 0; ks < 4; ++ks) {
        const v2u x0 = __builtin_bit_cast(v2u, __builtin_amdgcn_ds_read_tr16_b64_v4i16((LAS s16x4*)(L + bva[t][0] + ks * 8192)));
        const v2u x1 = __builtin_bit_cast(v2u, __builtin_amdgcn_ds_read_tr16_b64_v4i16((LAS s16x4*)(L + bva[t][1] + ks * 8192)));
        bv[t][ks] = v4u{x0.x, x0.y, x1.x, x1.y};
      }
    SCHED();
    {
      SLP(DLY_M0);
      for (int n = 0;; ++n) {
        ml = ML_LOAD();
        if (ML_OK(ml) || n >= GSPIN) break;
        SLP(DLY_RM);
      }
      SLP(DLY_H3);
      H3_LOADS();
      const float m = gdec(ml.y), M = x16_max(row_max(m));
      const float e = __builtin_amdgcn_exp2f(m - M);
      const float al = e * __builtin_amdgcn_rcpf(x16_sum(row_sum(e * gdec(ml.w))));
      const unsigned ah = pk(al, al) & 0xFFFFu;
      const unsigned hl = ah | (pk(al - bf2f(ah), 0.f) << 16);
#pragma unroll
      for (int k = 0; k < 8; ++k) { bk[k] = bdiag(__builtin_amdgcn_ds_bpermute((4 * k + g) * 4, hl), s8); asm volatile("" : "+v"(bk[k])); }
      f32x4 c;
#pragma unroll 1
      for (int n = 0;; ++n) {
        unsigned bad = 0;
        c = f32x4{0.f, 0.f, 0.f, 0.f};
#pragma unroll
        for (int k = 0; k < 8; ++k) { bad |= strip4(d[k], e4); c = mfma16(d[k], bk[k], c); }
        if (__all((bad & LSBM) == 0) || n >= GSPIN) break;
#pragma unroll
        for (int k = 0; k < 8; ++k) d[k] = hl4(rws, h3l, WS_H3 + ((hd >> 2) ^ k) * 4 * QB + k * 16384 + (v * 4 + jq) * 256);
      }
#pragma unroll
      for (int r = 0; r < 4; ++r) {
        const float y = c[r] + dppz<0x128>(c[r]);
        *(LAS u16*)(L + cta + 16 * r) = (u16)pk(y, y);
      }
    }
    BAR();
    {
      v4u af[4];
#pragma unroll
      for (int ks = 0; ks < 4; ++ks) af[ks] = *(LAS v4u*)(L + ctr + 64 * ks);
      SCHED();
      f32x4 c0 = {0.f, 0.f, 0.f, 0.f}, c1 = c0;
#pragma unroll
      for (int ks = 0; ks < 4; ++ks) { c0 = mfma16(af[ks], bv[0][ks], c0); c1 = mfma16(af[ks], bv[1][ks], c1); }
      const f32x4 ct = g < 2 ? c0 : c1;
      const float x0 = (g & 1) ? ct[2] : ct[0], x1 = (g & 1) ? ct[3] : ct[1];
      const float n0 = dppz<0xB1>(x0), n1 = dppz<0xB1>(x1);
      st4(v4u{tlo, genc((lane & 1) ? n1 : x0, t8), tlo, genc((lane & 1) ? x1 : n0, t8)}, rws, h4a, 0);
    }
  }
}

extern "C" __global__ void fused_decode_end() {}

extern "C" void kernel_launch(void* const* d_in, const int* in_sizes, int n_in, void* d_out, int out_size, void* d_ws, size_t ws_size,
                              hipStream_t stream) {
  (void)hipFuncSetAttribute((const void*)fused_decode, hipFuncAttributeMaxDynamicSharedMemorySize, LDS_BYTES);
  fused_decode<<<256, 512, LDS_BYTES, stream>>>((const u16*)d_in[0], (const u16*)d_in[1], (const u16*)d_in[2], (const u16*)d_in[3],
                                                (const u16*)d_in[4], (const u16*)d_in[5], (const u16*)d_in[7], (const u16*)d_in[8],
                                                (const u16*)d_in[9], (u16*)d_out, (char*)d_ws);
}
```

```cpp
#define STAMPS
#define PWHAT 4
#define PB0 128
#define PB1 256
#define PW0 0
#define PW1 4
#define PK 6
#include <hip/hip_runtime.h>
#include <stdint.h>

typedef unsigned short u16;
typedef unsigned long long u64;
typedef unsigned v4u __attribute__((ext_vector_type(4)));
typedef unsigned v2u __attribute__((ext_vector_type(2)));
typedef float f32x2 __attribute__((ext_vector_type(2)));
typedef float f32x4 __attribute__((ext_vector_type(4)));
typedef float f32x16 __attribute__((ext_vector_type(16)));
typedef short s16x4 __attribute__((ext_vector_type(4)));
typedef __bf16 bf16x8 __attribute__((ext_vector_type(8)));
typedef __bf16 bf16x2 __attribute__((ext_vector_type(2)));
typedef __amdgpu_buffer_rsrc_t rsrc_t;
#define GAS __attribute__((address_space(1)))
#define LAS __attribute__((address_space(3)))
#define DI __device__ __forceinline__

#define NT_W 2
#define NT_WO 0
#define PACE_C 7
#define DLY_H1 2
#define KQA 31
#define PACE_C0 3
#define KC0 0
#define DLY_K 16
#define PACE_G 4
#define PACE_O 8
#define DLY_R1 7
#define DLY_C 40
#define DLY_Q 0
#define DLY_F 70
#define DLY_QA 8
#define ABL 0
#define KWO 16
#define PACE_T 4
#define PACE_K 4
#define PACE_G0 2
#define KG0 0
#define PACE_W2 6
#define W2_A 6
#define TL0 0
#define TL1 4
#define TL2 12
#define TL3 24
#define PFW 1
#define DLY_PF 24
#define PACE_PF 4
#define DLY_R4 2
#define DLY_R7 2
#define DLY_RM 4
#define MLFIRST 0
#define DLY_W2 1
#define DLY_M0 12
#define H4HINT 0
#define DLY_H4 12
#define PFWAVE 7
#define KC1 32
#define PACE_C1 12
#define WO_E 10
#define PT 9
#define T_LATE 400
#define T_H7 183
#define ROT 0
#define UKX 1
#define CLS 131072
#define WM (CLS / 131072)
#define DLY_R2 12
#define UK0 6
#define WO_A 9
#define WO_B 13
#define WO_C 12
#define DLY_H3 1
#define DLY_H7 22
#define DLY_H5 18

#define SLP(N) do { if (N) __builtin_amdgcn_s_sleep(N); } while (0)
#define TV(N) (N)
#define TUNE_PARAM
#define TUNE_ARG

constexpr int SC1 = 16;
constexpr int GSPIN = 1 << 13;

constexpr int QB = 131072, WIN = 16 * QB;
constexpr int WS_H1 = 0;
constexpr int WS_H4 = 32768;
constexpr int WS_QA = 65536;
constexpr int WS_ML = 73728;
constexpr int WS_W = 81920;
constexpr int WS_H7 = WIN;
constexpr int WS_H5 = 2 * WIN;
constexpr int WS_H3 = 3 * WIN;
DI constexpr int slot(int p, int size) { return (p & 15) * QB + (p >> 4) * size; }
DI constexpr int blk5(int n) { return (n & 1) * 8 * QB + (n >> 1) * 16384; }
constexpr int WS_END = WS_H3 + 128 * 32768;
constexpr int WS_STAMP = WS_END;
constexpr int OOB = (int)0x80000000;

constexpr int LP_TAG = 4992, LP_XP = 0, LP_XD = 5120, LP_QB = 15360, LP_QO = 16896, LP_RED = 18432, LP_HP = 34816, LP_B8 = 35840, LP_FIN = 39936, LP_UK = 40960;
constexpr int LA_QA = 131072, LA_MX = LA_QA;
constexpr int LA_PB = LA_QA + 1024, LA_SUM = LA_QA + 512, LA_CTX = LA_QA + 9216, LA_CT2 = LA_QA + 11264, LA_B8 = LA_QA + 16384,
              LA_FIN = LA_QA + 20480, LA_UVW = 0;
constexpr int LDS_BYTES = 163840;

DI rsrc_t mk(const void* p, int bytes) { return __builtin_amdgcn_make_buffer_rsrc((void*)p, (short)0, bytes, 0x00020000); }
template <int AUX> DI v4u ld4(rsrc_t r, int vo, int so) { return __builtin_amdgcn_raw_buffer_load_b128(r, vo, so, AUX); }
template <int AUX> DI v2u ld2(rsrc_t r, int vo, int so) { return __builtin_amdgcn_raw_buffer_load_b64(r, vo, so, AUX); }
DI v4u hl4(rsrc_t r, int vo, int so) { asm volatile("" : "+v"(vo)); return __builtin_amdgcn_raw_buffer_load_b128(r, vo, so, SC1); }
DI v2u hl2(rsrc_t r, int vo, int so) { asm volatile("" : "+v"(vo)); return __builtin_amdgcn_raw_buffer_load_b64(r, vo, so, SC1); }
DI void touch(rsrc_t r, int vo, int so) { v4u t; asm volatile("buffer_load_dwordx4 %0, %1, %2, %3 offen sc1\n\ts_waitcnt vmcnt(0)" : "=&v"(t) : "v"(vo), "s"(r), "s"(so) : "memory"); }
DI void st4(v4u v, rsrc_t r, int vo, int so) { __builtin_amdgcn_raw_buffer_store_b128(v, r, vo + so, 0, SC1); }
DI void st2(v2u v, rsrc_t r, int vo, int so) { __builtin_amdgcn_raw_buffer_store_b64(v, r, vo, so, SC1); }
DI void dma16(rsrc_t r, int vo, int so, unsigned lds) {
  asm volatile("s_mov_b32 m0, %0\n\ts_nop 0\n\tbuffer_load_dwordx4 %1, %2, %3 offen nt lds" ::"s"(lds), "v"(vo), "s"(r), "s"(so) : "memory");
}
#define VMCNT(n) asm volatile("s_waitcnt vmcnt(%0)" ::"n"(n) : "memory")
#define FENCE() asm volatile("" ::: "memory")
#define SCHED() __builtin_amdgcn_sched_barrier(0)

DI float bf2f(unsigned v) { return __uint_as_float(v << 16); }
DI unsigned pk(float a, float b) { f32x2 f = {a, b}; return __builtin_bit_cast(unsigned, __builtin_convertvector(f, bf16x2)); }
DI f32x4 mfma16(v4u a, v4u b, f32x4 c) {
  return __builtin_amdgcn_mfma_f32_16x16x32_bf16(__builtin_bit_cast(bf16x8, a), __builtin_bit_cast(bf16x8, b), c, 0, 0, 0);
}
DI f32x4 mfma16k16(v2u a, v2u b, f32x4 c) {
  return __builtin_amdgcn_mfma_f32_16x16x16bf16_1k(__builtin_bit_cast(s16x4, a), __builtin_bit_cast(s16x4, b), c, 0, 0, 0);
}
DI f32x16 mfma32(v4u a, v4u b, f32x16 c) {
  return __builtin_amdgcn_mfma_f32_32x32x16_bf16(__builtin_bit_cast(bf16x8, a), __builtin_bit_cast(bf16x8, b), c, 0, 0, 0);
}
template <int CTRL, int RM> DI float dppf(float old, float v) {
  return __builtin_bit_cast(float, __builtin_amdgcn_update_dpp(__builtin_bit_cast(int, old), __builtin_bit_cast(int, v), CTRL, RM, 0xf, false));
}
template <int CTRL> DI float dppz(float v) {
  return __builtin_bit_cast(float, __builtin_amdgcn_update_dpp(0, __builtin_bit_cast(int, v), CTRL, 0xf, 0xf, true));
}
DI float row_sum(float v) {
  v += dppz<0xB1>(v); v += dppz<0x4E>(v); v += dppz<0x124>(v); v += dppz<0x128>(v);
  return v;
}
DI float dot2(unsigned a, unsigned b, float c) {
  return __builtin_amdgcn_fdot2_f32_bf16(__builtin_bit_cast(bf16x2, a), __builtin_bit_cast(bf16x2, b), c, false);
}
DI float row_max(float v) {
  v = fmaxf(v, dppf<0xB1, 0xf>(v, v)); v = fmaxf(v, dppf<0x4E, 0xf>(v, v));
  v = fmaxf(v, dppf<0x124, 0xf>(v, v)); v = fmaxf(v, dppf<0x128, 0xf>(v, v));
  return v;
}
DI float x16_sum(float v) { auto r = __builtin_amdgcn_permlane16_swap(__float_as_uint(v), __float_as_uint(v), false, false); return __uint_as_float(r[0]) + __uint_as_float(r[1]); }
DI float x32_sum(float v) { auto r = __builtin_amdgcn_permlane32_swap(__float_as_uint(v), __float_as_uint(v), false, false); return __uint_as_float(r[0]) + __uint_as_float(r[1]); }
DI float x16_max(float v) { auto r = __builtin_amdgcn_permlane16_swap(__float_as_uint(v), __float_as_uint(v), false, false); return fmaxf(__uint_as_float(r[0]), __uint_as_float(r[1])); }
DI float x32_max(float v) { auto r = __builtin_amdgcn_permlane32_swap(__float_as_uint(v), __float_as_uint(v), false, false); return fmaxf(__uint_as_float(r[0]), __uint_as_float(r[1])); }

#define RBAR() asm volatile("s_barrier" ::: "memory")
#define BAR() asm volatile("s_waitcnt lgkmcnt(0)\n\ts_barrier" ::: "memory")
DI unsigned genc(float v, unsigned t8) { return ((__float_as_uint(v) + 0x80u) & 0xFFFFFF00u) | t8; }
DI float gdec(unsigned hi) { return __uint_as_float(hi & 0xFFFFFF00u); }

#define STAMP(k) do { stv[k] = __builtin_amdgcn_s_memtime(); } while (0)
#define STAMP_DECL u64 stv[12] = {0, 0, 0, 0, 0, 0, 0, 0, 0, 0, 0, 0}; const u64 strt0_ = __builtin_amdgcn_s_memrealtime()
#define STAMP_OUT() do { const u64 c1_ = __builtin_amdgcn_s_memtime(), r1_ = __builtin_amdgcn_s_memrealtime(); \
    if (lane == 0) { GAS u64* so_ = (GAS u64*)(ws + WS_STAMP) + ((size_t)blockIdx.x * 8 + w) * 16; \
    _Pragma("unroll") for (int k_ = 0; k_ < 12; ++k_) so_[k_] = stv[k_]; so_[12] = strt0_; so_[13] = c1_; so_[14] = r1_; } } while (0)

DI v4u bdiag(unsigned P, const unsigned (&s)[4]) {
  v4u b;
  b.x = __builtin_amdgcn_perm(P, P, s[0]); b.y = __builtin_amdgcn_perm(P, P, s[1]);
  b.z = __builtin_amdgcn_perm(P, P, s[2]); b.w = __builtin_amdgcn_perm(P, P, s[3]);
  return b;
}
DI void bdiag_sel(int cp, unsigned (&s)[4]) {
  const unsigned lo = (cp & 1) ? 0x01000C0Cu : 0x0C0C0100u, hi = (cp & 1) ? 0x03020C0Cu : 0x0C0C0302u, z = 0x0C0C0C0Cu;
  s[0] = cp < 2 ? lo : z; s[1] = cp < 2 ? z : lo; s[2] = cp < 2 ? hi : z; s[3] = cp < 2 ? z : hi;
}

DI unsigned lsbtag(unsigned tlo, unsigned thi, int p) { return (((p < 32 ? tlo : thi) >> (p & 31)) & 1u) | (((tlo ^ (unsigned)p) & 1u) << 16); }
constexpr unsigned LSBM = 0x00010001u;
DI unsigned lsbput(unsigned v, unsigned e) { return (v & ~LSBM) | e; }
DI unsigned strip4(v4u& d, const unsigned (&e)[4]) { d.x ^= e[0]; d.y ^= e[1]; d.z ^= e[2]; d.w ^= e[3]; return (d.x | d.y) | (d.z | d.w); }
DI void swap24(v4u& a, v4u& b) {
  unsigned az = a.z, aw = a.w, bx = b.x, by = b.y;
  asm volatile("v_swap_b32 %0, %2\n\tv_swap_b32 %1, %3\n\ts_nop 1" : "+v"(az), "+v"(aw), "+v"(bx), "+v"(by));
  a.z = az; a.w = aw; b.x = bx; b.y = by;
}
constexpr float LSB_COMP = 1.0028f;
DI v4u tag8(f32x4 c, const unsigned (&t)[4]) {
  return v4u{(__float_as_uint(c[0]) & 0xFFFFFF00u) | t[0], (__float_as_uint(c[1]) & 0xFFFFFF00u) | t[1],
             (__float_as_uint(c[2]) & 0xFFFFFF00u) | t[2], (__float_as_uint(c[3]) & 0xFFFFFF00u) | t[3]};
}
extern "C" __global__ void __launch_bounds__(512, 2)
fused_decode(const u16* __restrict__ xin, const u16* __restrict__ ckv, const u16* __restrict__ wq, const u16* __restrict__ wuk,
             const u16* __restrict__ wuv, const u16* __restrict__ wo, const u16* __restrict__ w1, const u16* __restrict__ w3,
             const u16* __restrict__ w2, u16* __restrict__ out, char* ws TUNE_PARAM) {
  extern __shared__ __attribute__((aligned(16))) char smem[];
  LAS char* L = (LAS char*)smem;
  const unsigned lbase = (unsigned)(size_t)smem;
  const int lane = threadIdx.x & 63, w = __builtin_amdgcn_readfirstlane(threadIdx.x >> 6);
  const int xg = blockIdx.x & 7, qd_ = blockIdx.x >> 3, qq = qd_ & 15, idx = xg * 16 + qq, hd = idx >> 2, jq = idx & 3;
  const int i16 = lane & 15, g = lane >> 4, bj = i16 >> 2, cp = lane & 3;
  STAMP_DECL;
  STAMP(0);
  if (w == PFWAVE) {
    u64 pc; unsigned back, len;
    asm volatile("s_getpc_b64 %0\n.Lpf_here:\n\ts_mov_b32 %1, .Lpf_here-fused_decode\n\ts_mov_b32 %2, fused_decode_end-fused_decode" : "=s"(pc), "=s"(back), "=s"(len));
    const rsrc_t rcode = mk((const void*)(pc - back), (int)min(len, 32768u));
    v4u dummy;
    asm volatile("s_nop 4\n\tbuffer_load_dwordx4 %0, %1, %2, 0 offen\n\ts_waitcnt vmcnt(0)" : "=&v"(dummy) : "v"(qd_ * 1024 + lane * 16), "s"(rcode) : "memory");
  }
  asm volatile("" ::"s"(xin), "s"(ckv), "s"(wq), "s"(wuk), "s"(wuv), "s"(wo), "s"(w1), "s"(w3), "s"(w2), "s"(out), "s"(ws));
  u64 t_start;
  asm volatile("s_memrealtime %0\n\ts_waitcnt lgkmcnt(0)" : "=s"(t_start) : "s"(ws) : "memory");
  const rsrc_t rws = mk(ws, WS_END);
  const rsrc_t rx = mk(xin, 4 * 576 * 2);
  unsigned sel[4];
  bdiag_sel(cp, sel);
#define MAKE_TAG(W) const u64 tag_ = (((u64)(W).y << 32) | (W).x) + 0x9E3779B97F4A7C15ull;  \
  const unsigned tlo = (unsigned)tag_, thi = (unsigned)(tag_ >> 32), t8 = thi & 0xFFu;        \
  const unsigned tb[4] = {tlo & 0xFFu, (tlo >> 8) & 0xFFu, (tlo >> 16) & 0xFFu, tlo >> 24}
  const int vo5 = (lane >> 3) * QB + (lane & 7) * 16;

  if (qd_ >= 16) {
    const int so5 = WS_H5 + idx * 128;
    if (w < 4) {
      v4u ka[2][4];
      v2u wv_;
      asm volatile("buffer_load_dwordx2 %0, off, %1, %2 sc1" : "=v"(wv_) : "s"(rws), "s"(WS_W) : "memory");
      {
        const rsrc_t rk = mk(wuk, 512 * 4096 * 2);
        const int ukx = UKX * 4 * (hd & 1);
        SLP(DLY_K);
#pragma unroll
        for (int n = 0; n < 8; ++n) {
          if (n >= PACE_K) VMCNT(PACE_K - 1);
          dma16(rk, g * 8192 + ((i16 ^ ((g << 2) | (n & 3))) * 16), (128 * jq + 32 * w + 4 * (n ^ ukx)) * 8192 + hd * 256, lbase + LP_UK + (8 * w + (n ^ ukx)) * 1024);
        }
        VMCNT(0);
        const int fr = ((i16 & 3) << 2) | (i16 >> 2);
#pragma unroll
        for (int t = 0; t < 2; ++t)
#pragma unroll
          for (int s = 0; s < 4; ++s) {
            ka[t][s] = *(LAS v4u*)(L + LP_UK + (32 * w + 16 * t + i16) * 256 + (((4 * s + g) ^ fr) * 16));
            asm volatile("" : "+v"(ka[t][s]));
          }
      }
      STAMP(1);
      asm volatile("" : "+v"(wv_));
      MAKE_TAG(wv_);
      unsigned eq[4];
#pragma unroll
      for (int k = 0; k < 4; ++k) { eq[k] = lsbtag(tlo, thi, 4 * i16 + k); asm volatile("" : "+v"(eq[k])); }
      SLP(DLY_H1);
      STAMP(2);
      {
        const int blk = w & 1, half = w >> 1;
        float q0, q1;
        for (int n = 0;; ++n) {
          v4u p[4];
#pragma unroll
          for (int jj = 0; jj < 4; ++jj) p[jj] = hl4(rws, lane * 16, WS_H1 + slot(hd * 4 + jj, 4096) + (blk * 2 + half) * 1024);
          unsigned bad = 0;
          q0 = 0.f; q1 = 0.f;
#pragma unroll
          for (int jj = 0; jj < 4; ++jj) {
            bad |= (p[jj].x ^ tlo) | (p[jj].z ^ tlo) | (((p[jj].y ^ t8) | (p[jj].w ^ t8)) & 0xFFu);
            q0 += gdec(p[jj].y); q1 += gdec(p[jj].w);
          }
          if (__all(bad == 0) || n >= GSPIN) break;
          SLP(DLY_R1);
        }
        const unsigned qp = pk(q0, q1);
        const int d0 = 32 * g + 16 * half + 4 * blk + cp;
        *(LAS u16*)(L + LP_QB + bj * 320 + d0 * 2) = (u16)qp;
        *(LAS u16*)(L + LP_QB + bj * 320 + (d0 + 8) * 2) = (u16)(qp >> 16);
      }
      STAMP(4);
      BAR();
      STAMP(5);
      {
        v4u qf[4];
#pragma unroll
        for (int s = 0; s < 4; ++s) qf[s] = *(LAS v4u*)(L + LP_QB + cp * 320 + s * 64 + g * 16);
        f32x4 c0 = {0.f, 0.f, 0.f, 0.f}, c1 = c0;
#pragma unroll
        for (int s = 0; s < 4; ++s) { c0 = mfma16(ka[0][s], qf[s], c0); c1 = mfma16(ka[1][s], qf[s], c1); }
        const float sc = 0.08838834764831845f * 1.4426950408889634f * LSB_COMP;
        if (i16 < 4) {
          *(LAS v2u*)(L + LP_QO + i16 * 256 + (32 * w + 4 * g) * 2) = v2u{pk(c0[0] * sc, c0[1] * sc), pk(c0[2] * sc, c0[3] * sc)};
          *(LAS v2u*)(L + LP_QO + i16 * 256 + (32 * w + 16 + 4 * g) * 2) = v2u{pk(c1[0] * sc, c1[1] * sc), pk(c1[2] * sc, c1[3] * sc)};
        }
      }
      BAR();
      if (w == 0) {
        const v4u v = *(LAS v4u*)(L + LP_QO + lane * 16);
        st4(v4u{lsbput(v.x, eq[0]), lsbput(v.y, eq[1]), lsbput(v.z, eq[2]), lsbput(v.w, eq[3])}, rws, g * 2048 + i16 * 16, WS_QA + slot(hd, 1024) + jq * 256);
      }
      STAMP(6);
      BAR();
      const rsrc_t r2 = mk(w2, 4608 * 576 * 2);
      const int so2 = (xg * 576 + 36 * qq) * 1152;
      const rsrc_t ro = mk(wo, 4096 * 576 * 2);
      const int soo = (hd * 128 + 32 * jq) * 1152;
      v4u a2[9][2];
      v2u a8[3][2];
      {
        const int vo4 = 2 * g * 1152 + i16 * 16, vo4m = g < 2 ? vo4 : OOB;
        const int vo8 = 2 * g * 1152 + i16 * 8 + 1024, vo8m = (g < 2 && w == 0) ? vo8 : OOB;
#pragma unroll
        for (int n = 0; n < 14 + WO_E; ++n) {
          if (n >= PACE_W2) VMCNT(PACE_W2 - 1);
          if (n == W2_A) { SCHED(); RBAR(); SLP(DLY_W2); }
          const int o = n >> 1, r = n & 1;
          if (n < 10) a2[o][r] = ld4<NT_W>(r2, o < 4 ? vo4 : vo4m, so2 + (8 * o + r) * 1152 + 256 * w);
          else if (n < 12) a8[0][r] = ld2<NT_W>(r2, vo8, so2 + (8 * w + r) * 1152);
          else if (n < 14) a8[1][r] = ld2<NT_W>(r2, vo8m, so2 + (32 + r) * 1152);
          else if (n < 22) a2[o - 2][r] = ld4<NT_W>(ro, vo4, soo + (8 * (o - 7) + r) * 1152 + 256 * w);
          else a8[2][r] = ld2<NT_W>(ro, vo8, soo + (8 * w + r) * 1152);
          SCHED();
        }
      }
      STAMP(7);
#pragma unroll
      for (int o = 0; o < 5; ++o) swap24(a2[o][0], a2[o][1]);
      STAMP(8);
      RBAR();
      STAMP(9);
      {
        const int vo4 = 2 * g * 1152 + i16 * 16, vo8 = 2 * g * 1152 + i16 * 8 + 1024;
#pragma unroll
        for (int n = 14 + WO_E; n < 24; ++n) {
          const int o = n >> 1, r = n & 1;
          if (n < 22) a2[o - 2][r] = ld4<NT_W>(ro, vo4, soo + (8 * (o - 7) + r) * 1152 + 256 * w);
          else a8[2][r] = ld2<NT_W>(ro, vo8, soo + (8 * w + r) * 1152);
        }
        SCHED();
        const v4u h4 = *(LAS v4u*)(L + LP_HP + (bj * 4 + g) * 48);
        const unsigned h5 = *(LAS unsigned*)(L + LP_HP + (bj * 4 + g) * 48 + 16);
        const unsigned hw = *(LAS unsigned*)(L + LP_HP + (bj * 4 + g) * 48 + 4 * w);
        f32x4 c8 = {0.f, 0.f, 0.f, 0.f}, cx = c8, cy = c8;
        c8 = mfma16(v4u{a8[0][0].x, a8[0][0].y, a8[0][1].x, a8[0][1].y}, bdiag(hw, sel), c8);
        c8 = mfma16(v4u{a8[1][0].x, a8[1][0].y, a8[1][1].x, a8[1][1].y}, bdiag(h5, sel), c8);
#pragma unroll
        for (int o = 0; o < 5; ++o) {
          const v4u B = bdiag(o == 0 ? h4.x : o == 1 ? h4.y : o == 2 ? h4.z : o == 3 ? h4.w : h5, sel);
          cx = mfma16(a2[o][0], B, cx);
          cy = mfma16(a2[o][1], B, cy);
        }
#pragma unroll
        for (int o = 5; o < 9; ++o) swap24(a2[o][0], a2[o][1]);
        asm volatile("" : "+v"(cx), "+v"(cy), "+v"(c8));
        int bpa = (bj * 16 + g) * 4;
        asm volatile("" : "+v"(bpa));
        STAMP(10);
        float y0, y1;
        for (int n = 0;; ++n) {
          v4u p[4];
#pragma unroll
          for (int jj = 0; jj < 4; ++jj) p[jj] = hl4(rws, lane * 16, WS_H4 + slot(idx, 4096) + jj * 1024);
          unsigned bad = 0;
          y0 = 0.f; y1 = 0.f;
#pragma unroll
          for (int jj = 0; jj < 4; ++jj) {
            bad |= (p[jj].x ^ tlo) | (p[jj].z ^ tlo) | (((p[jj].y ^ t8) | (p[jj].w ^ t8)) & 0xFFu);
            y0 += gdec(p[jj].y); y1 += gdec(p[jj].w);
          }
          stv[5] = stv[0] + (u64)(n + 1) * 2300;
          if (__all(bad == 0) || n >= GSPIN) break;
          SLP(DLY_R4);
        }
        STAMP(11);
        const unsigned yp = pk(y0, y1);
        const unsigned pw = __builtin_amdgcn_ds_bpermute(bpa + 16 * w, yp);
        unsigned po[4];
#pragma unroll
        for (int o = 0; o < 4; ++o) po[o] = __builtin_amdgcn_ds_bpermute(bpa + 16 * o, yp);
        c8 = mfma16(v4u{a8[2][0].x, a8[2][0].y, a8[2][1].x, a8[2][1].y}, bdiag(pw, sel), c8);
#pragma unroll
        for (int o = 0; o < 4; ++o) {
          const v4u B = bdiag(po[o], sel);
          cx = mfma16(a2[5 + o][0], B, cx);
          cy = mfma16(a2[5 + o][1], B, cy);
        }
        *(LAS f32x4*)(L + LP_B8 + w * 1024 + lane * 16) = c8;
        st4(tag8(cx, tb), rws, vo5, so5 + blk5(2 * w));
        st4(tag8(cy, tb), rws, vo5, so5 + blk5(2 * w + 1));
        BAR();
        if (w == 0) {
          f32x4 s = *(LAS f32x4*)(L + LP_B8 + lane * 16);
#pragma unroll
          for (int k = 1; k < 4; ++k) s += *(LAS f32x4*)(L + LP_B8 + k * 1024 + lane * 16);
          st4(tag8(s, tb), rws, vo5, so5 + blk5(8));
        }
      }
      STAMP(3);
      if (qq < 9) RBAR();
    } else {
      const int v = w - 4;
      unsigned xn[8];
      const v2u wv_ = hl2(rws, 0, WS_W);
      v4u pt1;
      if (PT & 1) asm volatile("buffer_load_dwordx4 %0, %1, %2, %3 offen sc1" : "=v"(pt1) : "v"(lane * 16), "s"(rws), "s"(WS_H1 + slot(idx, 4096) + v * 1024) : "memory");
      {
        const v4u x0 = ld4<0>(rx, lane * 16, v * 1152), x1 = ld4<0>(rx, lane < 8 ? lane * 16 : OOB, v * 1152 + 1024);
        float s = 0.f, q = 0.f;
#pragma unroll
        for (int k = 0; k < 4; ++k) {
          s = dot2(x0[k], 0x3F803F80u, s); q = dot2(x0[k], x0[k], q);
          s = dot2(x1[k], 0x3F803F80u, s); q = dot2(x1[k], x1[k], q);
        }
        const float mean = x32_sum(x16_sum(row_sum(s))) * (1.0f / 576.0f);
        const float msq = x32_sum(x16_sum(row_sum(q))) * (1.0f / 576.0f);
        const float rstd = __builtin_amdgcn_rsqf(fmaxf(fmaf(-mean, mean, msq), 0.f) + 1e-5f), nb = -mean * rstd;
#pragma unroll
        for (int k = 0; k < 4; ++k) {
          xn[k] = pk(fmaf(bf2f(x0[k]), rstd, nb), fmaf(__uint_as_float(x0[k] & 0xFFFF0000u), rstd, nb));
        }
#pragma unroll
        for (int k = 0; k < 4; ++k) xn[4 + k] = pk(fmaf(bf2f(x1[k]), rstd, nb), fmaf(__uint_as_float(x1[k] & 0xFFFF0000u), rstd, nb));
      }
      if (PT & 1) { VMCNT(0); asm volatile("" : "+v"(pt1)); }
#pragma unroll
      for (int hf = 0; hf < 2; ++hf) {
        if (hf == 0 || lane < 8) {
#pragma unroll
          for (int k = 0; k < 4; ++k) {
            const int o = lane + 64 * hf;
            *(LAS unsigned*)(L + LP_XD + ((v * 4 + ((2 * k) & 3)) * 148 + 2 * o + (k >> 1)) * 4) = __builtin_amdgcn_perm(0u, xn[4 * hf + k], 0x01000100u);
            *(LAS unsigned*)(L + LP_XD + ((v * 4 + ((2 * k + 1) & 3)) * 148 + 2 * o + (k >> 1)) * 4) = __builtin_amdgcn_perm(0u, xn[4 * hf + k], 0x03020302u);
          }
        }
      }
      STAMP(1);
      if (PT & 8) touch(rws, lane * 16, WS_H4 + slot(idx, 4096) + v * 1024);
      MAKE_TAG(wv_);
      const int U0 = (81 * qq) / 144, Qb = 144 * (U0 + 1);
#define QOF(j) (81 * qq + ((j) + ROT * xg >= 81 ? (j) + ROT * xg - 81 : (j) + ROT * xg))
      unsigned xd[21];
      const GAS char* wb = (const GAS char*)(i16 < 8 ? w1 : w3) + (xg * 576 * 2 + (i16 & 7) * 16 + g * 9216);
      const unsigned m0 = cp == 0 ? 0xFFFFu : cp == 1 ? 0xFFFF0000u : 0u, m1 = cp == 2 ? 0xFFFFu : cp == 3 ? 0xFFFF0000u : 0u;
      f32x4 acc[4];
#pragma unroll
      for (int k = 0; k < 4; ++k) acc[k] = f32x4{0.f, 0.f, 0.f, 0.f};
      v4u a[21];
        int off[2][3];
        unsigned use[2][3];
        int hpa[2];
#pragma unroll
        for (int e = 0; e < 2; ++e) {
          const int t = ((v - 2) & 1) * 64 + lane + 128 * e;
          const int cl = t >> 2, b = t & 3, f = 36 * qq + cl, u = f >> 6;
          const int r0 = (144 * u) / 81, r1 = (144 * u + 143) / 81;
#pragma unroll
          for (int k = 0; k < 3; ++k) {
            const int rr = r0 + k;
            const bool on = t < 144 && rr <= r1;
            off[e][k] = on ? slot(xg * 16 + rr, 8192) + (u - (81 * rr) / 144) * 4096 + (f & 63) * 64 + b * 16 : OOB;
            use[e][k] = on ? 0xFFFFFFFFu : 0u;
          }
          hpa[e] = LP_HP + ((b * 4 + ((cl >> 1) & 3)) * 12 + (cl >> 3)) * 4 + (cl & 1) * 2;
          asm volatile("" : "+v"(hpa[e]), "+v"(off[e][0]), "+v"(off[e][1]), "+v"(off[e][2]), "+v"(use[e][0]), "+v"(use[e][1]), "+v"(use[e][2]));
        }
      int pa = WS_H7 + slot(idx, 8192) + v * 4096 + (32 * ((lane >> 4) & 1) + 16 * (lane >> 5) + cp) * 64 + bj * 16;
      asm volatile("" : "+v"(pa));
      BAR();
      RBAR();
#pragma unroll
      for (int t2 = 0; t2 < 21; ++t2) {
        const int Q = QOF(min(4 * t2 + v, 80)), qk = Q >= Qb ? Q - Qb : Q - 144 * U0;
        xd[t2] = *(LAS unsigned*)(L + LP_XD + ((bj * 4 + g) * 148 + qk) * 4);
        asm volatile("" : "+v"(xd[t2]));
      }
      if ((PT & 16) && v >= 2) {
        v4u t[6];
#pragma unroll
        for (int k = 0; k < 6; ++k) t[k] = hl4(rws, off[k / 3][k % 3], WS_H7);
#pragma unroll
        for (int k = 0; k < 6; ++k) asm volatile("" ::"v"(t[k]));
      }
      if ((PT & 32) && v < 2 && qq < 9) {
        v4u t[8];
#pragma unroll
        for (int k = 0; k < 8; ++k) t[k] = hl4(rws, lane * 16, WS_H5 + slot(qq * 8 + xg, 16384) + (v * 8 + k) * 1024);
#pragma unroll
        for (int k = 0; k < 8; ++k) asm volatile("" ::"v"(t[k]));
      }
#pragma unroll 1
      for (int n = 0; n < 4096 && (unsigned)(__builtin_amdgcn_s_memrealtime() - t_start) < (unsigned)(TV(DLY_F) * 5); ++n) __builtin_amdgcn_s_sleep(1);
      STAMP(2);
#pragma unroll
      for (int t = -PACE_G; t < 21; ++t) {
        const int tl = t + PACE_G;
        if (tl < 21) {
          const int Q = QOF(4 * tl + v), u = Q >= Qb ? U0 + 1 : U0, qk = Q - 144 * u;
          if (tl < KG0 && tl >= PACE_G0) VMCNT(PACE_G0 - 1);
          if (tl < 20 || v == 0) {
            const GAS v4u* p = (const GAS v4u*)(wb + (unsigned)(qk * 4 * 9216 + u * 128));
            a[tl] = NT_W ? __builtin_nontemporal_load(p) : *p;
          } else a[tl] = v4u{0u, 0u, 0u, 0u};
          SCHED();
        }
        if (tl == 20) { STAMP(3); BAR(); }
        if (t >= 0) {
          const int Q = QOF(min(4 * t + v, 80));
          const v2u B = {xd[t] & m0, xd[t] & m1};
          if (Q < Qb) { acc[0] = mfma16k16(v2u{a[t].x, a[t].y}, B, acc[0]); acc[1] = mfma16k16(v2u{a[t].z, a[t].w}, B, acc[1]); }
          else        { acc[2] = mfma16k16(v2u{a[t].x, a[t].y}, B, acc[2]); acc[3] = mfma16k16(v2u{a[t].z, a[t].w}, B, acc[3]); }
          SCHED();
        }
      }
#pragma unroll
      for (int k = 0; k < 4; ++k) *(LAS f32x4*)(L + LP_RED + (v * 4 + k) * 1024 + lane * 16) = acc[k];
      STAMP(4);
      BAR();
      STAMP(9);
      if (v == 0 || (v == 1 && 81 * qq + 80 >= Qb)) {
#pragma unroll
        for (int hh = 0; hh < 2; ++hh) {
          f32x4 r = *(LAS f32x4*)(L + LP_RED + (2 * v + hh) * 1024 + lane * 16);
#pragma unroll
          for (int k = 1; k < 4; ++k) r += *(LAS f32x4*)(L + LP_RED + (k * 4 + 2 * v + hh) * 1024 + lane * 16);
#pragma unroll
          for (int k = 0; k < 2; ++k) {
            const auto x = __builtin_amdgcn_permlane32_swap(__float_as_uint(r[k]), __float_as_uint(r[2 + k]), false, false);
            st4(v4u{tlo, genc(__uint_as_float(x[0]), t8), tlo, genc(__uint_as_float(x[1]), t8)}, rws, pa + (8 * k + 4 * hh) * 64, 0);
          }
        }
      } else if (v >= 2) {
        SLP(DLY_H7);
#pragma unroll 1
        for (int n = 0; n < 4096 && (unsigned)(__builtin_amdgcn_s_memrealtime() - t_start) < (unsigned)(TV(T_H7) * 5); ++n) __builtin_amdgcn_s_sleep(1);
        STAMP(5);
        float gs[2], us[2];
        for (int n = 0;; ++n) {
          v4u p[2][3];
#pragma unroll
          for (int e = 0; e < 2; ++e)
#pragma unroll
            for (int k = 0; k < 3; ++k) p[e][k] = hl4(rws, off[e][k], WS_H7);
          unsigned bad = 0;
#pragma unroll
          for (int e = 0; e < 2; ++e) {
            gs[e] = 0.f; us[e] = 0.f;
#pragma unroll
            for (int k = 0; k < 3; ++k) {
              bad |= ((p[e][k].x ^ tlo) | (p[e][k].z ^ tlo) | (((p[e][k].y ^ t8) | (p[e][k].w ^ t8)) & 0xFFu)) & use[e][k];
              gs[e] += gdec(p[e][k].y); us[e] += gdec(p[e][k].w);
            }
          }
          stv[10] = stv[0] + (u64)(n + 1) * 2300;
          if (__all(bad == 0) || n >= GSPIN) break;
          SLP(DLY_R7);
        }
#pragma unroll
        for (int e = 0; e < 2; ++e) {
          const float hv = gs[e] / (1.0f + __expf(-gs[e])) * us[e];
          if ((v - 2) * 64 + lane + 128 * e < 160) *(LAS u16*)(L + hpa[e]) = (u16)pk(hv, hv);
        }
      }
      STAMP(6);
      BAR();
      const int fe = lane & 31, fL = 8 * xg + (fe >> 2), frr = fe & 3;
      const int oidx = ((fL & 15) >> 2) * 576 + (qq < 8 ? 128 * (qq >> 1) + 8 * (4 * (fL >> 4) + frr) + 4 * (qq & 1) + (fL & 3) : 512 + 4 * (4 * (fL >> 4) + frr) + (fL & 3));
      unsigned xres = 0;
      if (v == 3 && qq < 9) xres = ((const GAS u16*)xin)[oidx];
      const int h5o = WS_H5 + slot(qq * 8 + xg, 16384) + v * 4096;
      int fina = LP_FIN + v * 128 + (lane & 7) * 16 + g * 4;
      asm volatile("" : "+v"(xres), "+v"(fina));
      RBAR();
      STAMP(7);
      if (qq < 9) {
        SLP(DLY_H5);
        v4u d[4];
#pragma unroll
        for (int k = 0; k < 4; ++k) d[k] = hl4(rws, lane * 16, h5o + k * 1024);
        unsigned pend = 15u;
#pragma unroll 1
        for (int n = 0;; ++n) {
#pragma unroll
          for (int k = 0; k < 4; ++k)
            if (pend & (1u << k)) { if (__all((strip4(d[k], tb) & 0xFFu) == 0)) pend &= ~(1u << k); }
          if (pend == 0 || n >= GSPIN) break;
#pragma unroll
          for (int k = 0; k < 4; ++k)
            if (pend & (1u << k)) d[k] = hl4(rws, lane * 16, h5o + k * 1024);
        }
        f32x4 s = (__builtin_bit_cast(f32x4, d[0]) + __builtin_bit_cast(f32x4, d[1])) + (__builtin_bit_cast(f32x4, d[2]) + __builtin_bit_cast(f32x4, d[3]));
#pragma unroll
        for (int r = 0; r < 4; ++r) s[r] += dppz<0x128>(s[r]);
        const auto x01 = __builtin_amdgcn_permlane16_swap(__float_as_uint(s[0]), __float_as_uint(s[1]), false, false);
        const auto x23 = __builtin_amdgcn_permlane16_swap(__float_as_uint(s[2]), __float_as_uint(s[3]), false, false);
        const auto z = __builtin_amdgcn_permlane32_swap(__float_as_uint(__uint_as_float(x01[0]) + __uint_as_float(x01[1])),
                                                        __float_as_uint(__uint_as_float(x23[0]) + __uint_as_float(x23[1])), false, false);
        *(LAS float*)(L + fina) = __uint_as_float(z[0]) + __uint_as_float(z[1]);
        BAR();
        if (v == 3) {
          float y = bf2f(xres);
#pragma unroll
          for (int k = 0; k < 4; ++k) y += *(LAS float*)(L + LP_FIN + k * 128 + fe * 4);
          if (lane < 32) ((GAS u16*)out)[oidx] = (u16)pk(y, y);
          if (qq == 0 && xg == 0) st2(v2u{tlo, thi}, rws, lane == 0 ? 0 : OOB, WS_W);
        }
        STAMP(8);
      }
    }
    STAMP_OUT();
    return;
  }

  const int ab = xg >> 1;
  const int r32 = lane & 31, hh = lane >> 5;
  v2u wa_;
  if (w < 4) {
    const rsrc_t rc = mk(ckv, 4 * 4096 * 512 * 2);
    v2u wb_;
    asm volatile("buffer_load_dwordx2 %0, off, %1, %2 sc1" : "=v"(wb_) : "s"(rws), "s"(WS_W) : "memory");
    {
      int vo[4];
#pragma unroll
      for (int k = 0; k < 4; ++k) vo[k] = g * 1024 + ((i16 ^ ((g << 2) | k)) * 16);
      const int so = (idx * 128 + 32 * w) * 1024;
      SLP(DLY_C);
#pragma unroll
      for (int n = 0; n < 32; ++n) {
        if (n < KC0) { if (n >= PACE_C0) VMCNT(PACE_C0 - 1); }
        else if (n >= KC1) { if (n >= KC1 + PACE_C1 - PACE_C) VMCNT(PACE_C1 - 1); }
        else if (n >= PACE_C) VMCNT(PACE_C - 1);
        dma16(rc, vo[(n >> 2) & 3], so + (n >> 2) * 4096 + (n & 3) * 256, lbase + (n & 3) * 32768 + (32 * w + 4 * (n >> 2)) * 256);
        if (n == KQA) RBAR();
      }
    }
    asm volatile("" : "+v"(wb_));
    wa_ = wb_;
  } else {
    wa_ = hl2(rws, 0, WS_W);
    if ((PT & 2) && (w == 5 || w == 6)) touch(rws, lane * 16, WS_QA + ab * 2048 + slot(2 * qq + w - 5, 1024));
    if ((PT & 4) && w >= 5) {
      if (w == 7) touch(rws, lane < 32 ? slot(8 * qq + (lane >> 2), 512) + (lane & 3) * 16 : OOB, WS_ML + xg * 64);
    }
    if (w == 4) {
      v2u xr[9];
#pragma unroll
      for (int t = 0; t < 9; ++t) xr[t] = ld2<0>(rx, g * 1152 + i16 * 8, t * 128);
      const rsrc_t rq = mk(wq, 576 * 4096 * 2);
      const int vo = 2 * g * 8192 + i16 * 16, so = 8 * jq * 8192 + hd * 256;
      v4u a[18][2];
#pragma unroll
      for (int o = 0; o < 18; ++o) { a[o][0] = ld4<NT_W>(rq, vo, so + (32 * o) * 8192); a[o][1] = ld4<NT_W>(rq, vo, so + (32 * o + 1) * 8192); }
      SCHED();
      STAMP(7);
      float s = 0.f, q = 0.f;
#pragma unroll
      for (int t = 0; t < 9; ++t) {
        *(LAS v2u*)(L + LA_QA + g * 1168 + i16 * 8 + t * 128) = xr[t];
        s = dot2(xr[t].x, 0x3F803F80u, s); q = dot2(xr[t].x, xr[t].x, q);
        s = dot2(xr[t].y, 0x3F803F80u, s); q = dot2(xr[t].y, xr[t].y, q);
      }
      FENCE();
      unsigned xp[18];
#pragma unroll
      for (int o = 0; o < 18; ++o) xp[o] = *(LAS unsigned*)(L + LA_QA + bj * 1168 + (4 * jq + 16 * o + g) * 4);
      const float mean = row_sum(s) * (1.0f / 576.0f), msq = row_sum(q) * (1.0f / 576.0f);
      const float rs0 = __builtin_amdgcn_rsqf(fmaxf(fmaf(-mean, mean, msq), 0.f) + 1e-5f);
      const float rstd = __uint_as_float(__builtin_amdgcn_ds_bpermute(bj * 64, __float_as_uint(rs0)));
      const float nb = __uint_as_float(__builtin_amdgcn_ds_bpermute(bj * 64, __float_as_uint(-mean * rs0)));
      v4u Bq[18];
#pragma unroll
      for (int o = 0; o < 18; ++o) {
        Bq[o] = bdiag(pk(fmaf(bf2f(xp[o]), rstd, nb), fmaf(__uint_as_float(xp[o] & 0xFFFF0000u), rstd, nb)), sel);
        asm volatile("" : "+v"(Bq[o]));
      }
      const u64 tg = (((u64)wa_.y << 32) | wa_.x) + 0x9E3779B97F4A7C15ull;
      unsigned ql = (unsigned)tg, q8 = (unsigned)(tg >> 32) & 0xFFu;
      asm volatile("" : "+v"(ql), "+v"(q8));
      SCHED();
      STAMP(8);
      f32x4 qx = {0.f, 0.f, 0.f, 0.f}, qy = qx;
#pragma unroll
      for (int o = 0; o < 18; ++o) {
        swap24(a[o][0], a[o][1]);
        qx = mfma16(a[o][0], Bq[o], qx);
        qy = mfma16(a[o][1], Bq[o], qy);
      }
      const int po = WS_H1 + slot(idx, 4096);
      st4(v4u{ql, genc(qx[0], q8), ql, genc(qx[1], q8)}, rws, lane * 16, po);
      st4(v4u{ql, genc(qx[2], q8), ql, genc(qx[3], q8)}, rws, lane * 16, po + 1024);
      st4(v4u{ql, genc(qy[0], q8), ql, genc(qy[1], q8)}, rws, lane * 16, po + 2048);
      st4(v4u{ql, genc(qy[2], q8), ql, genc(qy[3], q8)}, rws, lane * 16, po + 3072);
      STAMP(9);
    }
    RBAR();
  }
  MAKE_TAG(wa_);
  unsigned e4[4];
#pragma unroll
  for (int k = 0; k < 4; ++k) e4[k] = lsbtag(tlo, thi, 4 * i16 + k);
  if (w < 4) {
    v4u fa[32];
    {
      const int fr = ((r32 & 3) << 2) | ((r32 >> 2) & 3);
      const int ca = (32 * w + r32) * 256;
#define QK_READ(Q) _Pragma("unroll") for (int k = 0; k < 8; ++k) fa[(Q)*8 + k] = *(LAS v4u*)(L + (Q)*32768 + ca + (((2 * k + hh) ^ fr) * 16)); SCHED();
      VMCNT(0); QK_READ(0) QK_READ(1) QK_READ(2) QK_READ(3)
    }
    int qb[4];
#pragma unroll
    for (int k = 0; k < 4; ++k) { qb[k] = LA_QA + ((lane ^ ((2 * k + hh) & 7)) * 16); asm volatile("" : "+v"(qb[k])); }
    const int q4 = i16 >> 2, lowc = 2 * (g & 1) + (cp >> 1);
    int ta[4][2];
#pragma unroll
    for (int t = 0; t < 4; ++t)
#pragma unroll
      for (int t2 = 0; t2 < 2; ++t2) {
        ta[t][t2] = w * 32768 + (8 * t2 + 4 * hh + q4) * 256 + ((((t ^ q4) << 2) | (lowc ^ (2 * t2 + hh))) * 16) + 8 * (cp & 1);
        asm volatile("" : "+v"(ta[t][t2]));
      }
    int mxa = (r32 >> 4) * 320 + (r32 & 15) * 4, pba = LA_PB + lane * 16;
    unsigned e0 = lsbtag(tlo, thi, 4 * (r32 >> 1)), e1 = lsbtag(tlo, thi, 4 * (r32 >> 1) + 1), e2 = lsbtag(tlo, thi, 4 * (r32 >> 1) + 2), e3 = lsbtag(tlo, thi, 4 * (r32 >> 1) + 3);
    const int sp = idx & 31;
    int h3v = ((hh * 4 + (r32 & 1)) ^ (sp & 5)) * QB + (r32 >> 1) * 16 + WS_H3 + (sp * 16 + ab * 4 + w) * 256;
    asm volatile("" : "+v"(mxa), "+v"(pba), "+v"(e0), "+v"(e1), "+v"(e2), "+v"(e3), "+v"(h3v));
    STAMP(2);
    BAR();
    STAMP(3);
    v4u cb[32];
#define TR_READ(n) do { const v2u x0_ = __builtin_bit_cast(v2u, __builtin_amdgcn_ds_read_tr16_b64_v4i16((LAS s16x4*)(L + ta[(n) >> 3][0] + ((n) & 7) * 4096))); \
    const v2u x1_ = __builtin_bit_cast(v2u, __builtin_amdgcn_ds_read_tr16_b64_v4i16((LAS s16x4*)(L + ta[(n) >> 3][1] + ((n) & 7) * 4096)));                 \
    cb[n] = v4u{x0_.x, x0_.y, x1_.x, x1_.y}; } while (0)
    f32x16 sacc;
#pragma unroll
    for (int k = 0; k < 16; ++k) sacc[k] = 0.f;
    {
      v4u fb[32];
#pragma unroll
      for (int s = 0; s < 6; ++s) fb[s] = *(LAS v4u*)(L + qb[s & 3] + s * 1024);
      SCHED();
#pragma unroll
      for (int s = 0; s < 32; ++s) {
        if (s + 6 < 32) fb[s + 6] = *(LAS v4u*)(L + qb[(s + 6) & 3] + (s + 6) * 1024);
        if (s >= 26) TR_READ(s - 26);
        sacc = mfma32(fa[s], fb[s], sacc);
        SCHED();
      }
    }
    float mw = sacc[0];
#pragma unroll
    for (int k = 1; k < 16; ++k) mw = fmaxf(mw, sacc[k]);
    mw = x32_max(mw);
    asm volatile("s_waitcnt lgkmcnt(0)" ::: "memory");
    *(LAS float*)(L + mxa + w * 32768) = mw;
    STAMP(4);
    BAR();
    const float mx = fmaxf(fmaxf(*(LAS float*)(L + mxa), *(LAS float*)(L + mxa + 32768)), fmaxf(*(LAS float*)(L + mxa + 65536), *(LAS float*)(L + mxa + 98304)));
    float ls = 0.f;
#pragma unroll
    for (int k = 0; k < 16; ++k) { sacc[k] = __builtin_amdgcn_exp2f(sacc[k] - mx); ls += sacc[k]; }
#pragma unroll
    for (int s2 = 0; s2 < 2; ++s2)
      *(LAS v4u*)(L + pba + (2 * w + s2) * 1024) =
          v4u{pk(sacc[8 * s2], sacc[8 * s2 + 1]), pk(sacc[8 * s2 + 2], sacc[8 * s2 + 3]), pk(sacc[8 * s2 + 4], sacc[8 * s2 + 5]), pk(sacc[8 * s2 + 6], sacc[8 * s2 + 7])};
    ls = x32_sum(ls);
    *(LAS float*)(L + mxa + 640 + w * 32768) = ls;
    BAR();
    STAMP(5);
    {
      v4u pf[8];
#pragma unroll
      for (int k = 0; k < 8; ++k) pf[k] = *(LAS v4u*)(L + pba + k * 1024);
      f32x4 l4;
      if (w == 0) l4 = f32x4{*(LAS float*)(L + mxa + 640), *(LAS float*)(L + mxa + 640 + 32768), *(LAS float*)(L + mxa + 640 + 65536), *(LAS float*)(L + mxa + 640 + 98304)};
      SCHED();
      f32x16 o[4];
      unsigned p01[16], pq[16];
      const bool odd = lane & 1;
#pragma unroll
      for (int t = 0; t < 4; ++t)
#pragma unroll
        for (int k = 0; k < 16; ++k) o[t][k] = 0.f;
#pragma unroll
      for (int n = 0; n < 32; ++n) {
        if (n + 6 < 32) TR_READ(n + 6);
        o[n >> 3] = mfma32(pf[n & 7], cb[n], o[n >> 3]);
        if (n == 0 && w == 0)
          st4(v4u{tlo, genc(mx, t8), tlo, genc((l4[0] + l4[1]) + (l4[2] + l4[3]), t8)}, rws, lane < 32 ? r32 * 16 : OOB, WS_ML + slot(idx, 512));
        if (n >= 17 && n < 32 && n != 24) {
          const int j = n < 24 ? n - 17 : n - 18;
          if (j < 8) { pq[2 * j] = pk(o[0][2 * j], o[1][2 * j]); pq[2 * j + 1] = pk(o[0][2 * j + 1], o[1][2 * j + 1]); asm volatile("" : "+v"(pq[2 * j]), "+v"(pq[2 * j + 1])); }
          if (j >= 6) {
            const int k = j - 6;
            p01[2 * k] = lsbput(odd ? pq[2 * k + 1] : pq[2 * k], e0);
            p01[2 * k + 1] = __builtin_amdgcn_update_dpp(0, lsbput(odd ? pq[2 * k] : pq[2 * k + 1], e2), 0xB1, 0xf, 0xf, true);
            asm volatile("" : "+v"(p01[2 * k]), "+v"(p01[2 * k + 1]));
          }
        }
        SCHED();
      }
      STAMP(1);
#pragma unroll
      for (int k = 0; k < 8; ++k) {
        const unsigned pa = pk(o[2][2 * k], o[3][2 * k]), pb = pk(o[2][2 * k + 1], o[3][2 * k + 1]);
        st4(v4u{p01[2 * k], lsbput(odd ? pb : pa, e1), p01[2 * k + 1], (unsigned)__builtin_amdgcn_update_dpp(0, lsbput(odd ? pa : pb, e3), 0xB1, 0xf, 0xf, true)},
            rws, h3v, ((2 * (k & 1) + 8 * (k >> 1)) ^ (sp & 26)) * QB);
      }
    }
    STAMP(6);
    RBAR();
    RBAR();
    RBAR();
  } else {
    const int v = w - 4;
    STAMP(1);
    const rsrc_t rv = mk(wuv, 512 * 4096 * 2);
    v4u uw[8];
#define TL(i) do { uw[i] = ld4<NT_W>(rv, g * 32 * 8192 + i16 * 16, (128 * jq + 8 * v + ((i) ^ (hd & 1))) * 8192 + hd * 256); SCHED(); } while (0)
    {
      v4u qa[8];
      SLP(DLY_QA);
#pragma unroll
      for (int k = 0; k < 8; ++k) qa[k] = hl4(rws, lane * 16, WS_QA + ab * 2048 + slot(8 * v + k, 1024));
      SCHED();
#pragma unroll
      for (int i = 0; i < TL0; ++i) TL(i);
#pragma unroll 1
      for (int n = 0;; ++n) {
        unsigned bad = 0;
#pragma unroll
        for (int k = 0; k < 8; ++k) bad |= strip4(qa[k], e4);
        if (n == 0 && w != 4) { STAMP(7); }
        if (w != 4) stv[8] = stv[0] + (u64)(n + 1) * 2300;
        if (__all((bad & LSBM) == 0) || n >= GSPIN) break;
        SLP(DLY_R2);
#pragma unroll
        for (int k = 0; k < 8; ++k) qa[k] = hl4(rws, lane * 16, WS_QA + ab * 2048 + slot(8 * v + k, 1024));
      }
#pragma unroll
      for (int k = 0; k < 8; ++k) *(LAS v4u*)(L + LA_QA + (lane >> 1) * 1024 + ((((lane & 1) * 32 + 8 * v + k) ^ (lane & 7)) * 16)) = qa[k];
    }
    STAMP(2);
    BAR();
#pragma unroll
    for (int i = TL0; i < TL1; ++i) TL(i);
    RBAR();
#pragma unroll
    for (int i = TL1; i < 8; ++i) TL(i);
    RBAR();
    const int e7 = lane & 7, q4 = i16 >> 2;
    int cta = LA_CTX + v * 288 + (32 * g + e7) * 2, ctr = LA_CTX + cp * 288 + g * 16;
    int h4a = ((2 * (g & 1) + (i16 & 1)) * 16 + (2 * (g >> 1) + (i16 >> 3)) * 4 + ((i16 >> 1) & 3)) * 16 + WS_H4 + slot(hd * 4 + v, 4096) + jq * 1024;
    const unsigned src = (lane & 8) ? 0x0302u : 0x0100u, s1 = (e7 & 1) ? ((src << 16) | 0x0C0Cu) : (0x0C0C0000u | src);
    unsigned s8[4];
    int uwa[4], bva[2][2];
#pragma unroll
    for (int k = 0; k < 4; ++k) {
      s8[k] = (e7 >> 1) == k ? s1 : 0x0C0C0C0Cu;
      uwa[k] = LA_UVW + (32 * v + g) * 256 + ((i16 ^ ((g << 2) | k)) * 16);
      bva[k >> 1][k & 1] = LA_UVW + (8 * g + 4 * (k & 1) + q4) * 256 + (((2 * (2 * v + (k >> 1)) + (cp >> 1)) ^ ((q4 << 2) | ((2 * g + (k & 1)) & 3))) * 16) + 8 * (cp & 1);
      asm volatile("" : "+v"(s8[k]), "+v"(uwa[k]), "+v"(bva[k >> 1][k & 1]));
    }
    asm volatile("" : "+v"(cta), "+v"(ctr), "+v"(h4a));
    const int h3l = ((hd & 3) ^ g) * QB + g * 4096 + i16 * 16;
#define ML_LOAD() hl4(rws, slot(r32, 512), WS_ML + v * 1024 + hd * 16)
#define ML_OK(V) __all((((V).x ^ tlo) | ((V).z ^ tlo) | ((((V).y ^ t8) | ((V).w ^ t8)) & 0xFFu)) == 0)
#define H3_LOADS() _Pragma("unroll") for (int k = 0; k < 8; ++k) d[k] = hl4(rws, h3l, WS_H3 + ((hd >> 2) ^ k) * 4 * QB + k * 16384 + (v * 4 + jq) * 256); SCHED()
    v4u d[8], bk[8];
    v4u ml;
    VMCNT(0);
    RBAR();
    const bool early = (unsigned)(__builtin_amdgcn_s_memrealtime() - t_start) > (unsigned)(TV(T_LATE) * 5);
    if (early) { ml = ML_LOAD(); H3_LOADS(); }
#pragma unroll
    for (int k = 0; k < 8; ++k) *(LAS v4u*)(L + uwa[k & 3] + k * 1024) = uw[k];
    BAR();
    v4u bv[2][4];
#pragma unroll
    for (int t = 0; t < 2; ++t)
#pragma unroll
      for (int ks = 0; ks < 4; ++ks) {
        const v2u x0 = __builtin_bit_cast(v2u, __builtin_amdgcn_ds_read_tr16_b64_v4i16((LAS s16x4*)(L + bva[t][0] + ks * 8192)));
        const v2u x1 = __builtin_bit_cast(v2u, __builtin_amdgcn_ds_read_tr16_b64_v4i16((LAS s16x4*)(L + bva[t][1] + ks * 8192)));
        bv[t][ks] = v4u{x0.x, x0.y, x1.x, x1.y};
      }
    SCHED();
    STAMP(4);
    {
      if (early) {
#pragma unroll 1
        for (int n = 0; n < GSPIN && !ML_OK(ml); ++n) ml = ML_LOAD();
      } else {
        SLP(DLY_M0);
        for (int n = 0;; ++n) {
          ml = ML_LOAD();
          if (ML_OK(ml) || n >= GSPIN) break;
          SLP(DLY_RM);
        }
        SLP(DLY_H3);
        H3_LOADS();
      }
      STAMP(5);
      const float m = gdec(ml.y), M = x16_max(row_max(m));
      const float e = __builtin_amdgcn_exp2f(m - M);
      const float al = e * LSB_COMP * __builtin_amdgcn_rcpf(x16_sum(row_sum(e * gdec(ml.w))));
      const unsigned ah = pk(al, al) & 0xFFFFu;
      const unsigned hl = ah | (pk(al - bf2f(ah), 0.f) << 16);
#pragma unroll
      for (int k = 0; k < 8; ++k) { bk[k] = bdiag(__builtin_amdgcn_ds_bpermute((4 * k + g) * 4, hl), s8); asm volatile("" : "+v"(bk[k])); }
      STAMP(3);
      f32x4 c;
#pragma unroll 1
      for (int n = 0;; ++n) {
        unsigned bad = 0;
        c = f32x4{0.f, 0.f, 0.f, 0.f};
#pragma unroll
        for (int k = 0; k < 8; ++k) { bad |= strip4(d[k], e4); c = mfma16(d[k], bk[k], c); }
        stv[10] = stv[0] + (u64)(n + 1) * 2300;
        if (__all((bad & LSBM) == 0) || n >= GSPIN) break;
#pragma unroll
        for (int k = 0; k < 8; ++k) d[k] = hl4(rws, h3l, WS_H3 + ((hd >> 2) ^ k) * 4 * QB + k * 16384 + (v * 4 + jq) * 256);
      }
      STAMP(6);
#pragma unroll
      for (int r = 0; r < 4; ++r) {
        const float y = c[r] + dppz<0x128>(c[r]);
        *(LAS u16*)(L + cta + 16 * r) = (u16)pk(y, y);
      }
    }
    BAR();
    {
      v4u af[4];
#pragma unroll
      for (int ks = 0; ks < 4; ++ks) af[ks] = *(LAS v4u*)(L + ctr + 64 * ks);
      SCHED();
      f32x4 c0 = {0.f, 0.f, 0.f, 0.f}, c1 = c0;
#pragma unroll
      for (int ks = 0; ks < 4; ++ks) { c0 = mfma16(af[ks], bv[0][ks], c0); c1 = mfma16(af[ks], bv[1][ks], c1); }
      const f32x4 ct = g < 2 ? c0 : c1;
      const float x0 = (g & 1) ? ct[2] : ct[0], x1 = (g & 1) ? ct[3] : ct[1];
      const float n0 = dppz<0xB1>(x0), n1 = dppz<0xB1>(x1);
      st4(v4u{tlo, genc((lane & 1) ? n1 : x0, t8), tlo, genc((lane & 1) ? x1 : n0, t8)}, rws, h4a, 0);
    }
    STAMP(11);
    VMCNT(0);
    STAMP(2);
  }
  STAMP_OUT();
}

extern "C" __global__ void fused_decode_end() {}

__global__ void k_spin(char* ws){
  const u64* o=(const u64*)(ws+WS_STAMP);
  int t=threadIdx.x;
  u64 mn=~0ull,mxe=0; double a=0,sum=0; int cnt=0;
  __shared__ u64 s0[256],s2[256]; __shared__ double s3[256],s4[256]; __shared__ int s5[256];
  for(int i=t;i<2048;i+=256){ u64 st=o[i*16+12], en=o[i*16+14]; mn=st<mn?st:mn; mxe=en>mxe?en:mxe; }
  s0[t]=mn; s2[t]=mxe; __syncthreads();
  for(int i=0;i<256;i++){ mn=s0[i]<mn?s0[i]:mn; mxe=s2[i]>mxe?s2[i]:mxe; }
  for(int i=t;i<2048;i+=256){ int blk=i>>3,w=i&7; if(blk>=PB0&&blk<PB1&&w>=PW0&&w<PW1&&o[i*16+PK]){ double v=(double)(o[i*16+12]-mn)+(double)(o[i*16+PK]-o[i*16])/22.8; a=v>a?v:a; sum+=v; cnt++; } }
  s3[t]=a;s4[t]=sum;s5[t]=cnt; __syncthreads();
  if(t==0){ for(int i=1;i<256;i++){ a=s3[i]>a?s3[i]:a; sum+=s4[i]; cnt+=s5[i]; }
    double T=(PWHAT==0)?(double)(mxe-mn):(PWHAT==2)?0.0:(PWHAT==3)?a:sum/(cnt?cnt:1);
    u64 TT=(u64)(T*100.0); if(TT>1000000) TT=1000000;
    u64 t0=__builtin_amdgcn_s_memrealtime();
    while(__builtin_amdgcn_s_memrealtime()-t0 < TT) __builtin_amdgcn_s_sleep(8);
  }
}
extern "C" void kernel_launch(void* const* d_in, const int* in_sizes, int n_in, void* d_out, int out_size, void* d_ws, size_t ws_size,
                              hipStream_t stream) {
  (void)hipFuncSetAttribute((const void*)fused_decode, hipFuncAttributeMaxDynamicSharedMemorySize, LDS_BYTES);
  fused_decode<<<256, 512, LDS_BYTES, stream>>>((const u16*)d_in[0], (const u16*)d_in[1], (const u16*)d_in[2], (const u16*)d_in[3],
                                                (const u16*)d_in[4], (const u16*)d_in[5], (const u16*)d_in[7], (const u16*)d_in[8],
                                                (const u16*)d_in[9], (u16*)d_out, (char*)d_ws TUNE_ARG);
  k_spin<<<1,256,0,stream>>>((char*)d_ws);
}
```

```cpp
#include <hip/hip_runtime.h>
#include <stdint.h>

typedef unsigned short u16;
typedef unsigned long long u64;
typedef unsigned v4u __attribute__((ext_vector_type(4)));
typedef unsigned v2u __attribute__((ext_vector_type(2)));
typedef float f32x2 __attribute__((ext_vector_type(2)));
typedef float f32x4 __attribute__((ext_vector_type(4)));
typedef float f32x16 __attribute__((ext_vector_type(16)));
typedef short s16x4 __attribute__((ext_vector_type(4)));
typedef __bf16 bf16x8 __attribute__((ext_vector_type(8)));
typedef __bf16 bf16x2 __attribute__((ext_vector_type(2)));
typedef __amdgpu_buffer_rsrc_t rsrc_t;
#define GAS __attribute__((address_space(1)))
#define LAS __attribute__((address_space(3)))
#define DI __device__ __forceinline__

constexpr int NT = 2;
constexpr int PACE_C = 7;
constexpr int PACE_K = 4;
constexpr int PACE_G = 4;
constexpr int PACE_W = 6;
#define DLY_C 40
#define DLY_K 12
#define DLY_H1 2
#define DLY_R1 7
#define DLY_QA 8
#define DLY_R2 12
#define DLY_M0 12
#define DLY_RM 4
#define DLY_H3 1
#define DLY_H7 22
#define DLY_R7 2
#define DLY_R4 2
#define DLY_H5 18
constexpr unsigned T_F = 350, T_H7 = 915;
#define SLP(N) __builtin_amdgcn_s_sleep(N)

constexpr int SC1 = 16;
constexpr int GSPIN = 1 << 13;

constexpr int QB = 131072, WIN = 16 * QB;
constexpr int WS_H1 = 0;
constexpr int WS_H4 = 32768;
constexpr int WS_QA = 65536;
constexpr int WS_ML = 73728;
constexpr int WS_W = 81920;
constexpr int WS_H7 = WIN;
constexpr int WS_H5 = 2 * WIN;
constexpr int WS_H3 = 3 * WIN;
DI constexpr int slot(int p, int size) { return (p & 15) * QB + (p >> 4) * size; }
DI constexpr int blk5(int n) { return (n & 1) * 8 * QB + (n >> 1) * 16384; }
constexpr int WS_END = WS_H3 + 128 * 32768;
constexpr int OOB = (int)0x80000000;

constexpr int LP_XD = 5120, LP_QB = 15360, LP_QO = 16896, LP_RED = 18432, LP_HP = 34816, LP_B8 = 35840, LP_FIN = 39936, LP_UK = 40960;
constexpr int LA_QA = 131072, LA_PB = LA_QA + 1024, LA_CTX = LA_QA + 9216, LA_UVW = 0;
constexpr int LDS_BYTES = 163840;

DI rsrc_t mk(const void* p, int bytes) { return __builtin_amdgcn_make_buffer_rsrc((void*)p, (short)0, bytes, 0x00020000); }
template <int AUX> DI v4u ld4(rsrc_t r, int vo, int so) { return __builtin_amdgcn_raw_buffer_load_b128(r, vo, so, AUX); }
template <int AUX> DI v2u ld2(rsrc_t r, int vo, int so) { return __builtin_amdgcn_raw_buffer_load_b64(r, vo, so, AUX); }
DI v4u hl4(rsrc_t r, int vo, int so) { asm volatile("" : "+v"(vo)); return __builtin_amdgcn_raw_buffer_load_b128(r, vo, so, SC1); }
DI v2u hl2(rsrc_t r, int vo, int so) { asm volatile("" : "+v"(vo)); return __builtin_amdgcn_raw_buffer_load_b64(r, vo, so, SC1); }
DI void touch(rsrc_t r, int vo, int so) { v4u t; asm volatile("buffer_load_dwordx4 %0, %1, %2, %3 offen sc1\n\ts_waitcnt vmcnt(0)" : "=&v"(t) : "v"(vo), "s"(r), "s"(so) : "memory"); }
DI void st4(v4u v, rsrc_t r, int vo, int so) { __builtin_amdgcn_raw_buffer_store_b128(v, r, vo + so, 0, SC1); }
DI void st2(v2u v, rsrc_t r, int vo, int so) { __builtin_amdgcn_raw_buffer_store_b64(v, r, vo, so, SC1); }
DI void dma16(rsrc_t r, int vo, int so, unsigned lds) {
  asm volatile("s_mov_b32 m0, %0\n\ts_nop 0\n\tbuffer_load_dwordx4 %1, %2, %3 offen nt lds" ::"s"(lds), "v"(vo), "s"(r), "s"(so) : "memory");
}
#define VMCNT(n) asm volatile("s_waitcnt vmcnt(%0)" ::"n"(n) : "memory")
#define FENCE() asm volatile("" ::: "memory")
#define SCHED() __builtin_amdgcn_sched_barrier(0)

DI float bf2f(unsigned v) { return __uint_as_float(v << 16); }
DI unsigned pk(float a, float b) { f32x2 f = {a, b}; return __builtin_bit_cast(unsigned, __builtin_convertvector(f, bf16x2)); }
DI f32x4 mfma16(v4u a, v4u b, f32x4 c) {
  return __builtin_amdgcn_mfma_f32_16x16x32_bf16(__builtin_bit_cast(bf16x8, a), __builtin_bit_cast(bf16x8, b), c, 0, 0, 0);
}
DI f32x4 mfma16k16(v2u a, v2u b, f32x4 c) {
  return __builtin_amdgcn_mfma_f32_16x16x16bf16_1k(__builtin_bit_cast(s16x4, a), __builtin_bit_cast(s16x4, b), c, 0, 0, 0);
}
DI f32x16 mfma32(v4u a, v4u b, f32x16 c) {
  return __builtin_amdgcn_mfma_f32_32x32x16_bf16(__builtin_bit_cast(bf16x8, a), __builtin_bit_cast(bf16x8, b), c, 0, 0, 0);
}
template <int CTRL, int RM> DI float dppf(float old, float v) {
  return __builtin_bit_cast(float, __builtin_amdgcn_update_dpp(__builtin_bit_cast(int, old), __builtin_bit_cast(int, v), CTRL, RM, 0xf, false));
}
template <int CTRL> DI float dppz(float v) {
  return __builtin_bit_cast(float, __builtin_amdgcn_update_dpp(0, __builtin_bit_cast(int, v), CTRL, 0xf, 0xf, true));
}
DI float row_sum(float v) {
  v += dppz<0xB1>(v); v += dppz<0x4E>(v); v += dppz<0x124>(v); v += dppz<0x128>(v);
  return v;
}
DI float dot2(unsigned a, unsigned b, float c) {
  return __builtin_amdgcn_fdot2_f32_bf16(__builtin_bit_cast(bf16x2, a), __builtin_bit_cast(bf16x2, b), c, false);
}
DI float row_max(float v) {
  v = fmaxf(v, dppf<0xB1, 0xf>(v, v)); v = fmaxf(v, dppf<0x4E, 0xf>(v, v));
  v = fmaxf(v, dppf<0x124, 0xf>(v, v)); v = fmaxf(v, dppf<0x128, 0xf>(v, v));
  return v;
}
DI float x16_sum(float v) { auto r = __builtin_amdgcn_permlane16_swap(__float_as_uint(v), __float_as_uint(v), false, false); return __uint_as_float(r[0]) + __uint_as_float(r[1]); }
DI float x32_sum(float v) { auto r = __builtin_amdgcn_permlane32_swap(__float_as_uint(v), __float_as_uint(v), false, false); return __uint_as_float(r[0]) + __uint_as_float(r[1]); }
DI float x16_max(float v) { auto r = __builtin_amdgcn_permlane16_swap(__float_as_uint(v), __float_as_uint(v), false, false); return fmaxf(__uint_as_float(r[0]), __uint_as_float(r[1])); }
DI float x32_max(float v) { auto r = __builtin_amdgcn_permlane32_swap(__float_as_uint(v), __float_as_uint(v), false, false); return fmaxf(__uint_as_float(r[0]), __uint_as_float(r[1])); }

#define RBAR() asm volatile("s_barrier" ::: "memory")
#define BAR() asm volatile("s_waitcnt lgkmcnt(0)\n\ts_barrier" ::: "memory")
DI unsigned genc(float v, unsigned t8) { return ((__float_as_uint(v) + 0x80u) & 0xFFFFFF00u) | t8; }
DI float gdec(unsigned hi) { return __uint_as_float(hi & 0xFFFFFF00u); }

DI void wait_clock(u64 t0, unsigned ticks) {
#pragma unroll 1
  for (int n = 0; n < 4096 && (unsigned)(__builtin_amdgcn_s_memrealtime() - t0) < ticks; ++n) __builtin_amdgcn_s_sleep(1);
}

DI v4u bdiag(unsigned P, const unsigned (&s)[4]) {
  v4u b;
  b.x = __builtin_amdgcn_perm(P, P, s[0]); b.y = __builtin_amdgcn_perm(P, P, s[1]);
  b.z = __builtin_amdgcn_perm(P, P, s[2]); b.w = __builtin_amdgcn_perm(P, P, s[3]);
  return b;
}
DI void bdiag_sel(int cp, unsigned (&s)[4]) {
  const unsigned lo = (cp & 1) ? 0x01000C0Cu : 0x0C0C0100u, hi = (cp & 1) ? 0x03020C0Cu : 0x0C0C0302u, z = 0x0C0C0C0Cu;
  s[0] = cp < 2 ? lo : z; s[1] = cp < 2 ? z : lo; s[2] = cp < 2 ? hi : z; s[3] = cp < 2 ? z : hi;
}

DI unsigned lsbtag(unsigned tlo, unsigned thi, int p) { return (((p < 32 ? tlo : thi) >> (p & 31)) & 1u) | (((tlo ^ (unsigned)p) & 1u) << 16); }
constexpr unsigned LSBM = 0x00010001u;
DI unsigned pk7(float a, float b) { return pk(__uint_as_float(__float_as_uint(a) + 0x8000u), __uint_as_float(__float_as_uint(b) + 0x8000u)); }
DI unsigned lsbput(unsigned v, unsigned e) { return (v & ~LSBM) | e; }
DI unsigned strip4(v4u& d, const unsigned (&e)[4]) { d.x ^= e[0]; d.y ^= e[1]; d.z ^= e[2]; d.w ^= e[3]; return (d.x | d.y) | (d.z | d.w); }
DI void swap24(v4u& a, v4u& b) {
  unsigned az = a.z, aw = a.w, bx = b.x, by = b.y;
  asm volatile("v_swap_b32 %0, %2\n\tv_swap_b32 %1, %3\n\ts_nop 1" : "+v"(az), "+v"(aw), "+v"(bx), "+v"(by));
  a.z = az; a.w = aw; b.x = bx; b.y = by;
}
DI v4u tag8(f32x4 c, const unsigned (&t)[4]) {
  return v4u{(__float_as_uint(c[0]) & 0xFFFFFF00u) | t[0], (__float_as_uint(c[1]) & 0xFFFFFF00u) | t[1],
             (__float_as_uint(c[2]) & 0xFFFFFF00u) | t[2], (__float_as_uint(c[3]) & 0xFFFFFF00u) | t[3]};
}
extern "C" __global__ void __launch_bounds__(512, 2)
fused_decode(const u16* __restrict__ xin, const u16* __restrict__ ckv, const u16* __restrict__ wq, const u16* __restrict__ wuk,
             const u16* __restrict__ wuv, const u16* __restrict__ wo, const u16* __restrict__ w1, const u16* __restrict__ w3,
             const u16* __restrict__ w2, u16* __restrict__ out, char* ws) {
  extern __shared__ __attribute__((aligned(16))) char smem[];
  LAS char* L = (LAS char*)smem;
  const unsigned lbase = (unsigned)(size_t)smem;
  const int lane = threadIdx.x & 63, w = __builtin_amdgcn_readfirstlane(threadIdx.x >> 6);
  const int xg = blockIdx.x & 7, qd_ = blockIdx.x >> 3, qq = qd_ & 15, idx = xg * 16 + qq, hd = idx >> 2, jq = idx & 3;
  const int i16 = lane & 15, g = lane >> 4, bj = i16 >> 2, cp = lane & 3;
  if (w == 7) {
    u64 pc; unsigned back, len;
    asm volatile("s_getpc_b64 %0\n.Lpf_here:\n\ts_mov_b32 %1, .Lpf_here-fused_decode\n\ts_mov_b32 %2, fused_decode_end-fused_decode" : "=s"(pc), "=s"(back), "=s"(len));
    const rsrc_t rcode = mk((const void*)(pc - back), (int)min(len, 32768u));
    v4u dummy;
    asm volatile("s_nop 4\n\tbuffer_load_dwordx4 %0, %1, %2, 0 offen\n\ts_waitcnt vmcnt(0)" : "=&v"(dummy) : "v"(qd_ * 1024 + lane * 16), "s"(rcode) : "memory");
  }
  asm volatile("" ::"s"(xin), "s"(ckv), "s"(wq), "s"(wuk), "s"(wuv), "s"(wo), "s"(w1), "s"(w3), "s"(w2), "s"(out), "s"(ws));
  u64 t_start;
  asm volatile("s_memrealtime %0\n\ts_waitcnt lgkmcnt(0)" : "=s"(t_start) : "s"(ws) : "memory");
  const rsrc_t rws = mk(ws, WS_END);
  const rsrc_t rx = mk(xin, 4 * 576 * 2);
  if (w == 6) {
    unsigned t0, t1, t2;
    asm volatile("buffer_load_dword %0, %3, %5, 0 offen\n\tbuffer_load_dword %1, %4, %5, 0 offen\n\tbuffer_load_dword %2, off, %6, 0\n\ts_waitcnt vmcnt(0)"
                 : "=&v"(t0), "=&v"(t1), "=&v"(t2) : "v"(lane * QB + QB - 64), "v"((64 + lane) * QB + QB - 64), "s"(rws), "s"(mk(out, 4 * 576 * 2)) : "memory");
  }
  unsigned sel[4];
  bdiag_sel(cp, sel);
#define MAKE_TAG(W) const u64 tag_ = (((u64)(W).y << 32) | (W).x) + 0x9E3779B97F4A7C15ull;  \
  const unsigned tlo = (unsigned)tag_, thi = (unsigned)(tag_ >> 32), t8 = thi & 0xFFu;        \
  const unsigned tb[4] = {tlo & 0xFFu, (tlo >> 8) & 0xFFu, (tlo >> 16) & 0xFFu, tlo >> 24}
  const int vo5 = (lane >> 3) * QB + (lane & 7) * 16;

  if (qd_ >= 16) {
    const int so5 = WS_H5 + idx * 128;
    if (w < 4) {
      v4u ka[2][4];
      v2u wv_;
      asm volatile("buffer_load_dwordx2 %0, off, %1, %2 sc1" : "=v"(wv_) : "s"(rws), "s"(WS_W) : "memory");
      {
        const rsrc_t rk = mk(wuk, 512 * 4096 * 2);
        const int ukx = 4 * (hd & 1);
        SLP(DLY_K);
#pragma unroll
        for (int n = 0; n < 8; ++n) {
          if (n >= PACE_K) VMCNT(PACE_K - 1);
          dma16(rk, g * 8192 + ((i16 ^ ((g << 2) | (n & 3))) * 16), (128 * jq + 32 * w + 4 * (n ^ ukx)) * 8192 + hd * 256, lbase + LP_UK + (8 * w + (n ^ ukx)) * 1024);
        }
        VMCNT(0);
        const int fr = ((i16 & 3) << 2) | (i16 >> 2);
#pragma unroll
        for (int t = 0; t < 2; ++t)
#pragma unroll
          for (int s = 0; s < 4; ++s) {
            ka[t][s] = *(LAS v4u*)(L + LP_UK + (32 * w + 16 * t + i16) * 256 + (((4 * s + g) ^ fr) * 16));
            asm volatile("" : "+v"(ka[t][s]));
          }
      }
      asm volatile("" : "+v"(wv_));
      MAKE_TAG(wv_);
      unsigned eq[4];
#pragma unroll
      for (int k = 0; k < 4; ++k) { eq[k] = lsbtag(tlo, thi, 4 * i16 + k); asm volatile("" : "+v"(eq[k])); }
      SLP(DLY_H1);
      {
        const int blk = w & 1, half = w >> 1;
        float q0, q1;
        for (int n = 0;; ++n) {
          v4u p[4];
#pragma unroll
          for (int jj = 0; jj < 4; ++jj) p[jj] = hl4(rws, lane * 16, WS_H1 + slot(hd * 4 + jj, 4096) + (blk * 2 + half) * 1024);
          unsigned bad = 0;
          q0 = 0.f; q1 = 0.f;
#pragma unroll
          for (int jj = 0; jj < 4; ++jj) {
            bad |= (p[jj].x ^ tlo) | (p[jj].z ^ tlo) | (((p[jj].y ^ t8) | (p[jj].w ^ t8)) & 0xFFu);
            q0 += gdec(p[jj].y); q1 += gdec(p[jj].w);
          }
          if (__all(bad == 0) || n >= GSPIN) break;
          SLP(DLY_R1);
        }
        const unsigned qp = pk(q0, q1);
        const int d0 = 32 * g + 16 * half + 4 * blk + cp;
        *(LAS u16*)(L + LP_QB + bj * 320 + d0 * 2) = (u16)qp;
        *(LAS u16*)(L + LP_QB + bj * 320 + (d0 + 8) * 2) = (u16)(qp >> 16);
      }
      BAR();
      {
        v4u qf[4];
#pragma unroll
        for (int s = 0; s < 4; ++s) qf[s] = *(LAS v4u*)(L + LP_QB + cp * 320 + s * 64 + g * 16);
        f32x4 c0 = {0.f, 0.f, 0.f, 0.f}, c1 = c0;
#pragma unroll
        for (int s = 0; s < 4; ++s) { c0 = mfma16(ka[0][s], qf[s], c0); c1 = mfma16(ka[1][s], qf[s], c1); }
        const float sc = 0.08838834764831845f * 1.4426950408889634f;
        if (i16 < 4) {
          *(LAS v2u*)(L + LP_QO + i16 * 256 + (32 * w + 4 * g) * 2) = v2u{pk7(c0[0] * sc, c0[1] * sc), pk7(c0[2] * sc, c0[3] * sc)};
          *(LAS v2u*)(L + LP_QO + i16 * 256 + (32 * w + 16 + 4 * g) * 2) = v2u{pk7(c1[0] * sc, c1[1] * sc), pk7(c1[2] * sc, c1[3] * sc)};
        }
      }
      BAR();
      if (w == 0) {
        const v4u v = *(LAS v4u*)(L + LP_QO + lane * 16);
        st4(v4u{lsbput(v.x, eq[0]), lsbput(v.y, eq[1]), lsbput(v.z, eq[2]), lsbput(v.w, eq[3])}, rws, g * 2048 + i16 * 16, WS_QA + slot(hd, 1024) + jq * 256);
      }
      BAR();
      const rsrc_t r2 = mk(w2, 4608 * 576 * 2);
      const int so2 = (xg * 576 + 36 * qq) * 1152;
      const rsrc_t ro = mk(wo, 4096 * 576 * 2);
      const int soo = (hd * 128 + 32 * jq) * 1152;
      v4u a2[9][2];
      v2u a8[3][2];
      {
        const int vo4 = 2 * g * 1152 + i16 * 16, vo4m = g < 2 ? vo4 : OOB;
        const int vo8 = 2 * g * 1152 + i16 * 8 + 1024, vo8m = (g < 2 && w == 0) ? vo8 : OOB;
#pragma unroll
        for (int n = 0; n < 24; ++n) {
          if (n >= PACE_W) VMCNT(PACE_W - 1);
          if (n == PACE_W) { SCHED(); RBAR(); SLP(1); }
          const int o = n >> 1, r = n & 1;
          if (n < 10) a2[o][r] = ld4<NT>(r2, o < 4 ? vo4 : vo4m, so2 + (8 * o + r) * 1152 + 256 * w);
          else if (n < 12) a8[0][r] = ld2<NT>(r2, vo8, so2 + (8 * w + r) * 1152);
          else if (n < 14) a8[1][r] = ld2<NT>(r2, vo8m, so2 + (32 + r) * 1152);
          else if (n < 22) a2[o - 2][r] = ld4<NT>(ro, vo4, soo + (8 * (o - 7) + r) * 1152 + 256 * w);
          else a8[2][r] = ld2<NT>(ro, vo8, soo + (8 * w + r) * 1152);
          SCHED();
        }
      }
#pragma unroll
      for (int o = 0; o < 5; ++o) swap24(a2[o][0], a2[o][1]);
      RBAR();
      {
        const v4u h4 = *(LAS v4u*)(L + LP_HP + (bj * 4 + g) * 48);
        const unsigned h5 = *(LAS unsigned*)(L + LP_HP + (bj * 4 + g) * 48 + 16);
        const unsigned hw = *(LAS unsigned*)(L + LP_HP + (bj * 4 + g) * 48 + 4 * w);
        f32x4 c8 = {0.f, 0.f, 0.f, 0.f}, cx = c8, cy = c8;
        c8 = mfma16(v4u{a8[0][0].x, a8[0][0].y, a8[0][1].x, a8[0][1].y}, bdiag(hw, sel), c8);
        c8 = mfma16(v4u{a8[1][0].x, a8[1][0].y, a8[1][1].x, a8[1][1].y}, bdiag(h5, sel), c8);
#pragma unroll
        for (int o = 0; o < 5; ++o) {
          const v4u B = bdiag(o == 0 ? h4.x : o == 1 ? h4.y : o == 2 ? h4.z : o == 3 ? h4.w : h5, sel);
          cx = mfma16(a2[o][0], B, cx);
          cy = mfma16(a2[o][1], B, cy);
        }
#pragma unroll
        for (int o = 5; o < 9; ++o) swap24(a2[o][0], a2[o][1]);
        asm volatile("" : "+v"(cx), "+v"(cy), "+v"(c8));
        int bpa = (bj * 16 + g) * 4;
        asm volatile("" : "+v"(bpa));
        float y0, y1;
        for (int n = 0;; ++n) {
          v4u p[4];
#pragma unroll
          for (int jj = 0; jj < 4; ++jj) p[jj] = hl4(rws, lane * 16, WS_H4 + slot(idx, 4096) + jj * 1024);
          unsigned bad = 0;
          y0 = 0.f; y1 = 0.f;
#pragma unroll
          for (int jj = 0; jj < 4; ++jj) {
            bad |= (p[jj].x ^ tlo) | (p[jj].z ^ tlo) | (((p[jj].y ^ t8) | (p[jj].w ^ t8)) & 0xFFu);
            y0 += gdec(p[jj].y); y1 += gdec(p[jj].w);
          }
          if (__all(bad == 0) || n >= GSPIN) break;
          SLP(DLY_R4);
        }
        const unsigned yp = pk(y0, y1);
        const unsigned pw = __builtin_amdgcn_ds_bpermute(bpa + 16 * w, yp);
        unsigned po[4];
#pragma unroll
        for (int o = 0; o < 4; ++o) po[o] = __builtin_amdgcn_ds_bpermute(bpa + 16 * o, yp);
        c8 = mfma16(v4u{a8[2][0].x, a8[2][0].y, a8[2][1].x, a8[2][1].y}, bdiag(pw, sel), c8);
#pragma unroll
        for (int o = 0; o < 4; ++o) {
          const v4u B = bdiag(po[o], sel);
          cx = mfma16(a2[5 + o][0], B, cx);
          cy = mfma16(a2[5 + o][1], B, cy);
        }
        *(LAS f32x4*)(L + LP_B8 + w * 1024 + lane * 16) = c8;
        st4(tag8(cx, tb), rws, vo5, so5 + blk5(2 * w));
        st4(tag8(cy, tb), rws, vo5, so5 + blk5(2 * w + 1));
        BAR();
        if (w == 0) {
          f32x4 s = *(LAS f32x4*)(L + LP_B8 + lane * 16);
#pragma unroll
          for (int k = 1; k < 4; ++k) s += *(LAS f32x4*)(L + LP_B8 + k * 1024 + lane * 16);
          st4(tag8(s, tb), rws, vo5, so5 + blk5(8));
        }
      }
      if (qq < 9) RBAR();
    } else {
      const int v = w - 4;
      unsigned xn[8];
      const v2u wv_ = hl2(rws, 0, WS_W);
      v4u pt1;
      asm volatile("buffer_load_dwordx4 %0, %1, %2, %3 offen sc1" : "=v"(pt1) : "v"(lane * 16), "s"(rws), "s"(WS_H1 + slot(idx, 4096) + v * 1024) : "memory");
      {
        const v4u x0 = ld4<0>(rx, lane * 16, v * 1152), x1 = ld4<0>(rx, lane < 8 ? lane * 16 : OOB, v * 1152 + 1024);
        float s = 0.f, q = 0.f;
#pragma unroll
        for (int k = 0; k < 4; ++k) {
          s = dot2(x0[k], 0x3F803F80u, s); q = dot2(x0[k], x0[k], q);
          s = dot2(x1[k], 0x3F803F80u, s); q = dot2(x1[k], x1[k], q);
        }
        const float mean = x32_sum(x16_sum(row_sum(s))) * (1.0f / 576.0f);
        const float msq = x32_sum(x16_sum(row_sum(q))) * (1.0f / 576.0f);
        const float rstd = __builtin_amdgcn_rsqf(fmaxf(fmaf(-mean, mean, msq), 0.f) + 1e-5f), nb = -mean * rstd;
#pragma unroll
        for (int k = 0; k < 4; ++k) {
          xn[k] = pk(fmaf(bf2f(x0[k]), rstd, nb), fmaf(__uint_as_float(x0[k] & 0xFFFF0000u), rstd, nb));
        }
#pragma unroll
        for (int k = 0; k < 4; ++k) xn[4 + k] = pk(fmaf(bf2f(x1[k]), rstd, nb), fmaf(__uint_as_float(x1[k] & 0xFFFF0000u), rstd, nb));
      }
      VMCNT(0);
      asm volatile("" : "+v"(pt1));
#pragma unroll
      for (int hf = 0; hf < 2; ++hf) {
        if (hf == 0 || lane < 8) {
#pragma unroll
          for (int k = 0; k < 4; ++k) {
            const int o = lane + 64 * hf;
            *(LAS unsigned*)(L + LP_XD + ((v * 4 + ((2 * k) & 3)) * 148 + 2 * o + (k >> 1)) * 4) = __builtin_amdgcn_perm(0u, xn[4 * hf + k], 0x01000100u);
            *(LAS unsigned*)(L + LP_XD + ((v * 4 + ((2 * k + 1) & 3)) * 148 + 2 * o + (k >> 1)) * 4) = __builtin_amdgcn_perm(0u, xn[4 * hf + k], 0x03020302u);
          }
        }
      }
      touch(rws, lane * 16, WS_H4 + slot(idx, 4096) + v * 1024);
      MAKE_TAG(wv_);
      const int U0 = (81 * qq) / 144, Qb = 144 * (U0 + 1);
      unsigned xd[21];
      const GAS char* wb = (const GAS char*)(i16 < 8 ? w1 : w3) + (xg * 576 * 2 + (i16 & 7) * 16 + g * 9216);
      const unsigned m0 = cp == 0 ? 0xFFFFu : cp == 1 ? 0xFFFF0000u : 0u, m1 = cp == 2 ? 0xFFFFu : cp == 3 ? 0xFFFF0000u : 0u;
      f32x4 acc[4];
#pragma unroll
      for (int k = 0; k < 4; ++k) acc[k] = f32x4{0.f, 0.f, 0.f, 0.f};
      v4u a[21];
        int off[2][3];
        unsigned use[2][3];
        int hpa[2];
#pragma unroll
        for (int e = 0; e < 2; ++e) {
          const int t = ((v - 2) & 1) * 64 + lane + 128 * e;
          const int cl = t >> 2, b = t & 3, f = 36 * qq + cl, u = f >> 6;
          const int r0 = (144 * u) / 81, r1 = (144 * u + 143) / 81;
#pragma unroll
          for (int k = 0; k < 3; ++k) {
            const int rr = r0 + k;
            const bool on = t < 144 && rr <= r1;
            off[e][k] = on ? slot(xg * 16 + rr, 8192) + (u - (81 * rr) / 144) * 4096 + (f & 63) * 64 + b * 16 : OOB;
            use[e][k] = on ? 0xFFFFFFFFu : 0u;
          }
          hpa[e] = LP_HP + ((b * 4 + ((cl >> 1) & 3)) * 12 + (cl >> 3)) * 4 + (cl & 1) * 2;
          asm volatile("" : "+v"(hpa[e]), "+v"(off[e][0]), "+v"(off[e][1]), "+v"(off[e][2]), "+v"(use[e][0]), "+v"(use[e][1]), "+v"(use[e][2]));
        }
      int pa = WS_H7 + slot(idx, 8192) + v * 4096 + (32 * ((lane >> 4) & 1) + 16 * (lane >> 5) + cp) * 64 + bj * 16;
      asm volatile("" : "+v"(pa));
      BAR();
      RBAR();
#pragma unroll
      for (int t2 = 0; t2 < 21; ++t2) {
        const int Q = 81 * qq + min(4 * t2 + v, 80), qk = Q >= Qb ? Q - Qb : Q - 144 * U0;
        xd[t2] = *(LAS unsigned*)(L + LP_XD + ((bj * 4 + g) * 148 + qk) * 4);
        asm volatile("" : "+v"(xd[t2]));
      }
      wait_clock(t_start, T_F);
#pragma unroll
      for (int t = -PACE_G; t < 21; ++t) {
        const int tl = t + PACE_G;
        if (tl < 21) {
          const int Q = 81 * qq + 4 * tl + v, u = Q >= Qb ? U0 + 1 : U0, qk = Q - 144 * u;
          if (tl < 20 || v == 0) {
            const GAS v4u* p = (const GAS v4u*)(wb + (unsigned)(qk * 4 * 9216 + u * 128));
            a[tl] = __builtin_nontemporal_load(p);
          } else a[tl] = v4u{0u, 0u, 0u, 0u};
          SCHED();
        }
        if (tl == 20) BAR();
        if (t >= 0) {
          const int Q = 81 * qq + min(4 * t + v, 80);
          const v2u B = {xd[t] & m0, xd[t] & m1};
          if (Q < Qb) { acc[0] = mfma16k16(v2u{a[t].x, a[t].y}, B, acc[0]); acc[1] = mfma16k16(v2u{a[t].z, a[t].w}, B, acc[1]); }
          else        { acc[2] = mfma16k16(v2u{a[t].x, a[t].y}, B, acc[2]); acc[3] = mfma16k16(v2u{a[t].z, a[t].w}, B, acc[3]); }
          SCHED();
        }
      }
#pragma unroll
      for (int k = 0; k < 4; ++k) *(LAS f32x4*)(L + LP_RED + (v * 4 + k) * 1024 + lane * 16) = acc[k];
      BAR();
      if (v == 0 || (v == 1 && 81 * qq + 80 >= Qb)) {
#pragma unroll
        for (int hh = 0; hh < 2; ++hh) {
          f32x4 r = *(LAS f32x4*)(L + LP_RED + (2 * v + hh) * 1024 + lane * 16);
#pragma unroll
          for (int k = 1; k < 4; ++k) r += *(LAS f32x4*)(L + LP_RED + (k * 4 + 2 * v + hh) * 1024 + lane * 16);
#pragma unroll
          for (int k = 0; k < 2; ++k) {
            const auto x = __builtin_amdgcn_permlane32_swap(__float_as_uint(r[k]), __float_as_uint(r[2 + k]), false, false);
            st4(v4u{tlo, genc(__uint_as_float(x[0]), t8), tlo, genc(__uint_as_float(x[1]), t8)}, rws, pa + (8 * k + 4 * hh) * 64, 0);
          }
        }
      } else if (v >= 2) {
        SLP(DLY_H7);
        wait_clock(t_start, T_H7);
        float gs[2], us[2];
        for (int n = 0;; ++n) {
          v4u p[2][3];
#pragma unroll
          for (int e = 0; e < 2; ++e)
#pragma unroll
            for (int k = 0; k < 3; ++k) p[e][k] = hl4(rws, off[e][k], WS_H7);
          unsigned bad = 0;
#pragma unroll
          for (int e = 0; e < 2; ++e) {
            gs[e] = 0.f; us[e] = 0.f;
#pragma unroll
            for (int k = 0; k < 3; ++k) {
              bad |= ((p[e][k].x ^ tlo) | (p[e][k].z ^ tlo) | (((p[e][k].y ^ t8) | (p[e][k].w ^ t8)) & 0xFFu)) & use[e][k];
              gs[e] += gdec(p[e][k].y); us[e] += gdec(p[e][k].w);
            }
          }
          if (__all(bad == 0) || n >= GSPIN) break;
          SLP(DLY_R7);
        }
#pragma unroll
        for (int e = 0; e < 2; ++e) {
          const float hv = gs[e] * __builtin_amdgcn_rcpf(1.0f + __expf(-gs[e])) * us[e];
          if ((v - 2) * 64 + lane + 128 * e < 160) *(LAS u16*)(L + hpa[e]) = (u16)pk(hv, hv);
        }
      }
      BAR();
      const int fe = lane & 31, fL = 8 * xg + (fe >> 2), frr = fe & 3;
      const int oidx = ((fL & 15) >> 2) * 576 + (qq < 8 ? 128 * (qq >> 1) + 8 * (4 * (fL >> 4) + frr) + 4 * (qq & 1) + (fL & 3) : 512 + 4 * (4 * (fL >> 4) + frr) + (fL & 3));
      unsigned xres = 0;
      if (v == 3 && qq < 9) xres = ((const GAS u16*)xin)[oidx];
      const int h5o = WS_H5 + slot(qq * 8 + xg, 16384) + v * 4096;
      int fina = LP_FIN + v * 128 + (lane & 7) * 16 + g * 4;
      asm volatile("" : "+v"(xres), "+v"(fina));
      RBAR();
      if (qq < 9) {
        SLP(DLY_H5);
        v4u d[4];
#pragma unroll
        for (int k = 0; k < 4; ++k) d[k] = hl4(rws, lane * 16, h5o + k * 1024);
        unsigned pend = 15u;
#pragma unroll 1
        for (int n = 0;; ++n) {
#pragma unroll
          for (int k = 0; k < 4; ++k)
            if (pend & (1u << k)) { if (__all((strip4(d[k], tb) & 0xFFu) == 0)) pend &= ~(1u << k); }
          if (pend == 0 || n >= GSPIN) break;
#pragma unroll
          for (int k = 0; k < 4; ++k)
            if (pend & (1u << k)) d[k] = hl4(rws, lane * 16, h5o + k * 1024);
        }
        f32x4 s = (__builtin_bit_cast(f32x4, d[0]) + __builtin_bit_cast(f32x4, d[1])) + (__builtin_bit_cast(f32x4, d[2]) + __builtin_bit_cast(f32x4, d[3]));
#pragma unroll
        for (int r = 0; r < 4; ++r) s[r] += dppz<0x128>(s[r]);
        const auto x01 = __builtin_amdgcn_permlane16_swap(__float_as_uint(s[0]), __float_as_uint(s[1]), false, false);
        const auto x23 = __builtin_amdgcn_permlane16_swap(__float_as_uint(s[2]), __float_as_uint(s[3]), false, false);
        const auto z = __builtin_amdgcn_permlane32_swap(__float_as_uint(__uint_as_float(x01[0]) + __uint_as_float(x01[1])),
                                                        __float_as_uint(__uint_as_float(x23[0]) + __uint_as_float(x23[1])), false, false);
        *(LAS float*)(L + fina) = __uint_as_float(z[0]) + __uint_as_float(z[1]);
        BAR();
        if (v == 3) {
          float y = bf2f(xres);
#pragma unroll
          for (int k = 0; k < 4; ++k) y += *(LAS float*)(L + LP_FIN + k * 128 + fe * 4);
          if (lane < 32) ((GAS u16*)out)[oidx] = (u16)pk(y, y);
          if (qq == 0 && xg == 0) st2(v2u{tlo, thi}, rws, lane == 0 ? 0 : OOB, WS_W);
        }
      }
    }
    return;
  }

  const int ab = xg >> 1;
  const int r32 = lane & 31, hh = lane >> 5;
  v2u wa_;
  if (w < 4) {
    const rsrc_t rc = mk(ckv, 4 * 4096 * 512 * 2);
    v2u wb_;
    asm volatile("buffer_load_dwordx2 %0, off, %1, %2 sc1" : "=v"(wb_) : "s"(rws), "s"(WS_W) : "memory");
    {
      int vo[4];
#pragma unroll
      for (int k = 0; k < 4; ++k) vo[k] = g * 1024 + ((i16 ^ ((g << 2) | k)) * 16);
      const int so = (idx * 128 + 32 * w) * 1024;
      SLP(DLY_C);
#pragma unroll
      for (int n = 0; n < 32; ++n) {
        if (n >= PACE_C) VMCNT(PACE_C - 1);
        dma16(rc, vo[(n >> 2) & 3], so + (n >> 2) * 4096 + (n & 3) * 256, lbase + (n & 3) * 32768 + (32 * w + 4 * (n >> 2)) * 256);
      }
    }
    RBAR();
    asm volatile("" : "+v"(wb_));
    wa_ = wb_;
  } else {
    wa_ = hl2(rws, 0, WS_W);
    if (w == 4) {
      v2u xr[9];
#pragma unroll
      for (int t = 0; t < 9; ++t) xr[t] = ld2<0>(rx, g * 1152 + i16 * 8, t * 128);
      const rsrc_t rq = mk(wq, 576 * 4096 * 2);
      const int vo = 2 * g * 8192 + i16 * 16, so = 8 * jq * 8192 + hd * 256;
      v4u a[18][2];
#pragma unroll
      for (int o = 0; o < 18; ++o) { a[o][0] = ld4<NT>(rq, vo, so + (32 * o) * 8192); a[o][1] = ld4<NT>(rq, vo, so + (32 * o + 1) * 8192); }
      SCHED();
      float s = 0.f, q = 0.f;
#pragma unroll
      for (int t = 0; t < 9; ++t) {
        *(LAS v2u*)(L + LA_QA + g * 1168 + i16 * 8 + t * 128) = xr[t];
        s = dot2(xr[t].x, 0x3F803F80u, s); q = dot2(xr[t].x, xr[t].x, q);
        s = dot2(xr[t].y, 0x3F803F80u, s); q = dot2(xr[t].y, xr[t].y, q);
      }
      FENCE();
      unsigned xp[18];
#pragma unroll
      for (int o = 0; o < 18; ++o) xp[o] = *(LAS unsigned*)(L + LA_QA + bj * 1168 + (4 * jq + 16 * o + g) * 4);
      const float mean = row_sum(s) * (1.0f / 576.0f), msq = row_sum(q) * (1.0f / 576.0f);
      const float rs0 = __builtin_amdgcn_rsqf(fmaxf(fmaf(-mean, mean, msq), 0.f) + 1e-5f);
      const float rstd = __uint_as_float(__builtin_amdgcn_ds_bpermute(bj * 64, __float_as_uint(rs0)));
      const float nb = __uint_as_float(__builtin_amdgcn_ds_bpermute(bj * 64, __float_as_uint(-mean * rs0)));
      v4u Bq[18];
#pragma unroll
      for (int o = 0; o < 18; ++o) {
        Bq[o] = bdiag(pk(fmaf(bf2f(xp[o]), rstd, nb), fmaf(__uint_as_float(xp[o] & 0xFFFF0000u), rstd, nb)), sel);
        asm volatile("" : "+v"(Bq[o]));
      }
      const u64 tg = (((u64)wa_.y << 32) | wa_.x) + 0x9E3779B97F4A7C15ull;
      unsigned ql = (unsigned)tg, q8 = (unsigned)(tg >> 32) & 0xFFu;
      asm volatile("" : "+v"(ql), "+v"(q8));
      SCHED();
      f32x4 qx = {0.f, 0.f, 0.f, 0.f}, qy = qx;
#pragma unroll
      for (int o = 0; o < 18; ++o) {
        swap24(a[o][0], a[o][1]);
        qx = mfma16(a[o][0], Bq[o], qx);
        qy = mfma16(a[o][1], Bq[o], qy);
      }
      const int po = WS_H1 + slot(idx, 4096);
      st4(v4u{ql, genc(qx[0], q8), ql, genc(qx[1], q8)}, rws, lane * 16, po);
      st4(v4u{ql, genc(qx[2], q8), ql, genc(qx[3], q8)}, rws, lane * 16, po + 1024);
      st4(v4u{ql, genc(qy[0], q8), ql, genc(qy[1], q8)}, rws, lane * 16, po + 2048);
      st4(v4u{ql, genc(qy[2], q8), ql, genc(qy[3], q8)}, rws, lane * 16, po + 3072);
    }
    RBAR();
  }
  MAKE_TAG(wa_);
  unsigned e4[4];
#pragma unroll
  for (int k = 0; k < 4; ++k) e4[k] = lsbtag(tlo, thi, 4 * i16 + k);
  if (w < 4) {
    v4u fa[32];
    {
      const int fr = ((r32 & 3) << 2) | ((r32 >> 2) & 3);
      const int ca = (32 * w + r32) * 256;
#define QK_READ(Q) _Pragma("unroll") for (int k = 0; k < 8; ++k) fa[(Q)*8 + k] = *(LAS v4u*)(L + (Q)*32768 + ca + (((2 * k + hh) ^ fr) * 16)); SCHED();
      VMCNT(0); QK_READ(0) QK_READ(1) QK_READ(2) QK_READ(3)
    }
    int qb[4];
#pragma unroll
    for (int k = 0; k < 4; ++k) { qb[k] = LA_QA + ((lane ^ ((2 * k + hh) & 7)) * 16); asm volatile("" : "+v"(qb[k])); }
    const int q4 = i16 >> 2, lowc = 2 * (g & 1) + (cp >> 1);
    int ta[4][2];
#pragma unroll
    for (int t = 0; t < 4; ++t)
#pragma unroll
      for (int t2 = 0; t2 < 2; ++t2) {
        ta[t][t2] = w * 32768 + (8 * t2 + 4 * hh + q4) * 256 + ((((t ^ q4) << 2) | (lowc ^ (2 * t2 + hh))) * 16) + 8 * (cp & 1);
        asm volatile("" : "+v"(ta[t][t2]));
      }
    int mxa = (r32 >> 4) * 320 + (r32 & 15) * 4, pba = LA_PB + lane * 16;
    unsigned e0 = lsbtag(tlo, thi, 4 * (r32 >> 1)), e1 = lsbtag(tlo, thi, 4 * (r32 >> 1) + 1), e2 = lsbtag(tlo, thi, 4 * (r32 >> 1) + 2), e3 = lsbtag(tlo, thi, 4 * (r32 >> 1) + 3);
    const int sp = idx & 31;
    int h3v = ((hh * 4 + (r32 & 1)) ^ (sp & 5)) * QB + (r32 >> 1) * 16 + WS_H3 + (sp * 16 + ab * 4 + w) * 256;
    asm volatile("" : "+v"(mxa), "+v"(pba), "+v"(e0), "+v"(e1), "+v"(e2), "+v"(e3), "+v"(h3v));
    BAR();
    v4u cb[32];
#define TR_READ(n) do { const v2u x0_ = __builtin_bit_cast(v2u, __builtin_amdgcn_ds_read_tr16_b64_v4i16((LAS s16x4*)(L + ta[(n) >> 3][0] + ((n) & 7) * 4096))); \
    const v2u x1_ = __builtin_bit_cast(v2u, __builtin_amdgcn_ds_read_tr16_b64_v4i16((LAS s16x4*)(L + ta[(n) >> 3][1] + ((n) & 7) * 4096)));                 \
    cb[n] = v4u{x0_.x, x0_.y, x1_.x, x1_.y}; } while (0)
    f32x16 sacc;
#pragma unroll
    for (int k = 0; k < 16; ++k) sacc[k] = 0.f;
    {
      v4u fb[32];
#pragma unroll
      for (int s = 0; s < 6; ++s) fb[s] = *(LAS v4u*)(L + qb[s & 3] + s * 1024);
      SCHED();
#pragma unroll
      for (int s = 0; s < 32; ++s) {
        if (s + 6 < 32) fb[s + 6] = *(LAS v4u*)(L + qb[(s + 6) & 3] + (s + 6) * 1024);
        if (s >= 26) TR_READ(s - 26);
        sacc = mfma32(fa[s], fb[s], sacc);
        SCHED();
      }
    }
    float mw = sacc[0];
#pragma unroll
    for (int k = 1; k < 16; ++k) mw = fmaxf(mw, sacc[k]);
    mw = x32_max(mw);
    asm volatile("s_waitcnt lgkmcnt(0)" ::: "memory");
    *(LAS float*)(L + mxa + w * 32768) = mw;
    BAR();
    const float mx = gdec(genc(fmaxf(fmaxf(*(LAS float*)(L + mxa), *(LAS float*)(L + mxa + 32768)), fmaxf(*(LAS float*)(L + mxa + 65536), *(LAS float*)(L + mxa + 98304))), 0u));
    float ls = 0.f;
#pragma unroll
    for (int k = 0; k < 16; ++k) { sacc[k] = __builtin_amdgcn_exp2f(sacc[k] - mx); ls += sacc[k]; }
#pragma unroll
    for (int s2 = 0; s2 < 2; ++s2)
      *(LAS v4u*)(L + pba + (2 * w + s2) * 1024) =
          v4u{pk(sacc[8 * s2], sacc[8 * s2 + 1]), pk(sacc[8 * s2 + 2], sacc[8 * s2 + 3]), pk(sacc[8 * s2 + 4], sacc[8 * s2 + 5]), pk(sacc[8 * s2 + 6], sacc[8 * s2 + 7])};
    ls = x32_sum(ls);
    *(LAS float*)(L + mxa + 640 + w * 32768) = ls;
    BAR();
    {
      v4u pf[8];
#pragma unroll
      for (int k = 0; k < 8; ++k) pf[k] = *(LAS v4u*)(L + pba + k * 1024);
      f32x4 l4;
      if (w == 0) l4 = f32x4{*(LAS float*)(L + mxa + 640), *(LAS float*)(L + mxa + 640 + 32768), *(LAS float*)(L + mxa + 640 + 65536), *(LAS float*)(L + mxa + 640 + 98304)};
      SCHED();
      f32x16 o[4];
      unsigned p01[16], pq[16];
      const bool odd = lane & 1;
#pragma unroll
      for (int t = 0; t < 4; ++t)
#pragma unroll
        for (int k = 0; k < 16; ++k) o[t][k] = 0.f;
#pragma unroll
      for (int n = 0; n < 32; ++n) {
        if (n + 6 < 32) TR_READ(n + 6);
        o[n >> 3] = mfma32(pf[n & 7], cb[n], o[n >> 3]);
        if (n == 0 && w == 0)
          st4(v4u{tlo, genc(mx, t8), tlo, genc((l4[0] + l4[1]) + (l4[2] + l4[3]), t8)}, rws, lane < 32 ? r32 * 16 : OOB, WS_ML + slot(idx, 512));
        if (n >= 17 && n < 32 && n != 24) {
          const int j = n < 24 ? n - 17 : n - 18;
          if (j < 8) { pq[2 * j] = pk7(o[0][2 * j], o[1][2 * j]); pq[2 * j + 1] = pk7(o[0][2 * j + 1], o[1][2 * j + 1]); asm volatile("" : "+v"(pq[2 * j]), "+v"(pq[2 * j + 1])); }
          if (j >= 6) {
            const int k = j - 6;
            p01[2 * k] = lsbput(odd ? pq[2 * k + 1] : pq[2 * k], e0);
            p01[2 * k + 1] = __builtin_amdgcn_update_dpp(0, lsbput(odd ? pq[2 * k] : pq[2 * k + 1], e2), 0xB1, 0xf, 0xf, true);
            asm volatile("" : "+v"(p01[2 * k]), "+v"(p01[2 * k + 1]));
          }
        }
        SCHED();
      }
#pragma unroll
      for (int k = 0; k < 8; ++k) {
        const unsigned pa = pk7(o[2][2 * k], o[3][2 * k]), pb = pk7(o[2][2 * k + 1], o[3][2 * k + 1]);
        st4(v4u{p01[2 * k], lsbput(odd ? pb : pa, e1), p01[2 * k + 1], (unsigned)__builtin_amdgcn_update_dpp(0, lsbput(odd ? pa : pb, e3), 0xB1, 0xf, 0xf, true)},
            rws, h3v, ((2 * (k & 1) + 8 * (k >> 1)) ^ (sp & 26)) * QB);
      }
    }
    RBAR();
    RBAR();
    RBAR();
  } else {
    const int v = w - 4;
    const rsrc_t rv = mk(wuv, 512 * 4096 * 2);
    v4u uw[8];
#define TL(i) do { uw[i] = ld4<NT>(rv, g * 32 * 8192 + i16 * 16, (128 * jq + 8 * v + ((i) ^ (hd & 1))) * 8192 + hd * 256); SCHED(); } while (0)
    {
      v4u qa[8];
      SLP(DLY_QA);
#pragma unroll
      for (int k = 0; k < 8; ++k) qa[k] = hl4(rws, lane * 16, WS_QA + ab * 2048 + slot(8 * v + k, 1024));
      SCHED();
#pragma unroll 1
      for (int n = 0;; ++n) {
        unsigned bad = 0;
#pragma unroll
        for (int k = 0; k < 8; ++k) bad |= strip4(qa[k], e4);
        if (__all((bad & LSBM) == 0) || n >= GSPIN) break;
        SLP(DLY_R2);
#pragma unroll
        for (int k = 0; k < 8; ++k) qa[k] = hl4(rws, lane * 16, WS_QA + ab * 2048 + slot(8 * v + k, 1024));
      }
#pragma unroll
      for (int k = 0; k < 8; ++k) *(LAS v4u*)(L + LA_QA + (lane >> 1) * 1024 + ((((lane & 1) * 32 + 8 * v + k) ^ (lane & 7)) * 16)) = qa[k];
    }
    BAR();
#pragma unroll
    for (int i = 0; i < 4; ++i) TL(i);
    RBAR();
#pragma unroll
    for (int i = 4; i < 8; ++i) TL(i);
    RBAR();
    const int e7 = lane & 7, q4 = i16 >> 2;
    int cta = LA_CTX + v * 288 + (32 * g + e7) * 2, ctr = LA_CTX + cp * 288 + g * 16;
    int h4a = ((2 * (g & 1) + (i16 & 1)) * 16 + (2 * (g >> 1) + (i16 >> 3)) * 4 + ((i16 >> 1) & 3)) * 16 + WS_H4 + slot(hd * 4 + v, 4096) + jq * 1024;
    const unsigned src = (lane & 8) ? 0x0302u : 0x0100u, s1 = (e7 & 1) ? ((src << 16) | 0x0C0Cu) : (0x0C0C0000u | src);
    unsigned s8[4];
    int uwa[4], bva[2][2];
#pragma unroll
    for (int k = 0; k < 4; ++k) {
      s8[k] = (e7 >> 1) == k ? s1 : 0x0C0C0C0Cu;
      uwa[k] = LA_UVW + (32 * v + g) * 256 + ((i16 ^ ((g << 2) | k)) * 16);
      bva[k >> 1][k & 1] = LA_UVW + (8 * g + 4 * (k & 1) + q4) * 256 + (((2 * (2 * v + (k >> 1)) + (cp >> 1)) ^ ((q4 << 2) | ((2 * g + (k & 1)) & 3))) * 16) + 8 * (cp & 1);
      asm volatile("" : "+v"(s8[k]), "+v"(uwa[k]), "+v"(bva[k >> 1][k & 1]));
    }
    asm volatile("" : "+v"(cta), "+v"(ctr), "+v"(h4a));
    const int h3l = ((hd & 3) ^ g) * QB + g * 4096 + i16 * 16;
#define ML_LOAD() hl4(rws, slot(r32, 512), WS_ML + v * 1024 + hd * 16)
#define ML_OK(V) __all((((V).x ^ tlo) | ((V).z ^ tlo) | ((((V).y ^ t8) | ((V).w ^ t8)) & 0xFFu)) == 0)
#define H3_LOADS() _Pragma("unroll") for (int k = 0; k < 8; ++k) d[k] = hl4(rws, h3l, WS_H3 + ((hd >> 2) ^ k) * 4 * QB + k * 16384 + (v * 4 + jq) * 256); SCHED()
    v4u d[8], bk[8];
    v4u ml;
    VMCNT(0);
    RBAR();
#pragma unroll
    for (int k = 0; k < 8; ++k) *(LAS v4u*)(L + uwa[k & 3] + k * 1024) = uw[k];
    BAR();
    v4u bv[2][4];
#pragma unroll
    for (int t = 0; t < 2; ++t)
#pragma unroll
      for (int ks = 0; ks < 4; ++ks) {
        const v2u x0 = __builtin_bit_cast(v2u, __builtin_amdgcn_ds_read_tr16_b64_v4i16((LAS s16x4*)(L + bva[t][0] + ks * 8192)));
        const v2u x1 = __builtin_bit_cast(v2u, __builtin_amdgcn_ds_read_tr16_b64_v4i16((LAS s16x4*)(L + bva[t][1] + ks * 8192)));
        bv[t][ks] = v4u{x0.x, x0.y, x1.x, x1.y};
      }
    SCHED();
    {
      SLP(DLY_M0);
      for (int n = 0;; ++n) {
        ml = ML_LOAD();
        if (ML_OK(ml) || n >= GSPIN) break;
        SLP(DLY_RM);
      }
      SLP(DLY_H3);
      H3_LOADS();
      const float m = gdec(ml.y), M = x16_max(row_max(m));
      const float e = __builtin_amdgcn_exp2f(m - M);
      const float al = e * __builtin_amdgcn_rcpf(x16_sum(row_sum(e * gdec(ml.w))));
      const unsigned ah = pk(al, al) & 0xFFFFu;
      const unsigned hl = ah | (pk(al - bf2f(ah), 0.f) << 16);
#pragma unroll
      for (int k = 0; k < 8; ++k) { bk[k] = bdiag(__builtin_amdgcn_ds_bpermute((4 * k + g) * 4, hl), s8); asm volatile("" : "+v"(bk[k])); }
      f32x4 c;
#pragma unroll 1
      for (int n = 0;; ++n) {
        unsigned bad = 0;
        c = f32x4{0.f, 0.f, 0.f, 0.f};
#pragma unroll
        for (int k = 0; k < 8; ++k) { bad |= strip4(d[k], e4); c = mfma16(d[k], bk[k], c); }
        if (__all((bad & LSBM) == 0) || n >= GSPIN) break;
#pragma unroll
        for (int k = 0; k < 8; ++k) d[k] = hl4(rws, h3l, WS_H3 + ((hd >> 2) ^ k) * 4 * QB + k * 16384 + (v * 4 + jq) * 256);
      }
#pragma unroll
      for (int r = 0; r < 4; ++r) {
        const float y = c[r] + dppz<0x128>(c[r]);
        *(LAS u16*)(L + cta + 16 * r) = (u16)pk(y, y);
      }
    }
    BAR();
    {
      v4u af[4];
#pragma unroll
      for (int ks = 0; ks < 4; ++ks) af[ks] = *(LAS v4u*)(L + ctr + 64 * ks);
      SCHED();
      f32x4 c0 = {0.f, 0.f, 0.f, 0.f}, c1 = c0;
#pragma unroll
      for (int ks = 0; ks < 4; ++ks) { c0 = mfma16(af[ks], bv[0][ks], c0); c1 = mfma16(af[ks], bv[1][ks], c1); }
      const f32x4 ct = g < 2 ? c0 : c1;
      const float x0 = (g & 1) ? ct[2] : ct[0], x1 = (g & 1) ? ct[3] : ct[1];
      const float n0 = dppz<0xB1>(x0), n1 = dppz<0xB1>(x1);
      st4(v4u{tlo, genc((lane & 1) ? n1 : x0, t8), tlo, genc((lane & 1) ? x1 : n0, t8)}, rws, h4a, 0);
    }
  }
}

extern "C" __global__ void fused_decode_end() {}

extern "C" void kernel_launch(void* const* d_in, const int* in_sizes, int n_in, void* d_out, int out_size, void* d_ws, size_t ws_size,
                              hipStream_t stream) {
  (void)hipFuncSetAttribute((const void*)fused_decode, hipFuncAttributeMaxDynamicSharedMemorySize, LDS_BYTES);
  fused_decode<<<256, 512, LDS_BYTES, stream>>>((const u16*)d_in[0], (const u16*)d_in[1], (const u16*)d_in[2], (const u16*)d_in[3],
                                                (const u16*)d_in[4], (const u16*)d_in[5], (const u16*)d_in[7], (const u16*)d_in[8],
                                                (const u16*)d_in[9], (u16*)d_out, (char*)d_ws);
}
```

```cpp
#include <hip/hip_runtime.h>
#include <stdint.h>

typedef unsigned short u16;
typedef unsigned long long u64;
typedef unsigned v4u __attribute__((ext_vector_type(4)));
typedef unsigned v2u __attribute__((ext_vector_type(2)));
typedef float f32x2 __attribute__((ext_vector_type(2)));
typedef float f32x4 __attribute__((ext_vector_type(4)));
typedef float f32x16 __attribute__((ext_vector_type(16)));
typedef short s16x4 __attribute__((ext_vector_type(4)));
typedef __bf16 bf16x8 __attribute__((ext_vector_type(8)));
typedef __bf16 bf16x2 __attribute__((ext_vector_type(2)));
typedef __amdgpu_buffer_rsrc_t rsrc_t;
#define GAS __attribute__((address_space(1)))
#define LAS __attribute__((address_space(3)))
#define DI __device__ __forceinline__

constexpr int NT = 2;
constexpr int PACE_C = 7;
constexpr int PACE_K = 4;
constexpr int PACE_G = 4;
constexpr int PACE_W = 6;
#define DLY_C 40
#define DLY_K 12
#define DLY_H1 2
#define DLY_R1 7
#define DLY_QA 8
#define DLY_R2 12
#define DLY_M0 12
#define DLY_RM 4
#define DLY_H3 1
#define DLY_H7 22
#define DLY_R7 2
#define DLY_R4 2
#define DLY_H5 18
constexpr unsigned T_F = 350, T_H7 = 915;
#define SLP(N) __builtin_amdgcn_s_sleep(N)

constexpr int SC1 = 16;
constexpr int GSPIN = 1 << 13;

constexpr int QB = 131072, WIN = 16 * QB;
constexpr int WS_H1 = 0;
constexpr int WS_H4 = 32768;
constexpr int WS_QA = 65536;
constexpr int WS_ML = 73728;
constexpr int WS_W = 81920;
constexpr int WS_H7 = WIN;
constexpr int WS_H5 = 2 * WIN;
constexpr int WS_H3 = 3 * WIN;
DI constexpr int slot(int p, int size) { return (p & 15) * QB + (p >> 4) * size; }
DI constexpr int blk5(int n) { return (n & 1) * 8 * QB + (n >> 1) * 16384; }
constexpr int WS_END = WS_H3 + 128 * 32768;
constexpr int OOB = (int)0x80000000;

constexpr int LP_XD = 5120, LP_QB = 15360, LP_QO = 16896, LP_RED = 18432, LP_HP = 34816, LP_B8 = 35840, LP_FIN = 39936, LP_UK = 40960;
constexpr int LA_QA = 131072, LA_PB = LA_QA + 1024, LA_CTX = LA_QA + 9216, LA_UVW = 0;
constexpr int LDS_BYTES = 163840;

DI rsrc_t mk(const void* p, int bytes) { return __builtin_amdgcn_make_buffer_rsrc((void*)p, (short)0, bytes, 0x00020000); }
template <int AUX> DI v4u ld4(rsrc_t r, int vo, int so) { return __builtin_amdgcn_raw_buffer_load_b128(r, vo, so, AUX); }
template <int AUX> DI v2u ld2(rsrc_t r, int vo, int so) { return __builtin_amdgcn_raw_buffer_load_b64(r, vo, so, AUX); }
DI v4u hl4(rsrc_t r, int vo, int so) { asm volatile("" : "+v"(vo)); return __builtin_amdgcn_raw_buffer_load_b128(r, vo, so, SC1); }
DI v2u hl2(rsrc_t r, int vo, int so) { asm volatile("" : "+v"(vo)); return __builtin_amdgcn_raw_buffer_load_b64(r, vo, so, SC1); }
DI void touch(rsrc_t r, int vo, int so) { v4u t; asm volatile("buffer_load_dwordx4 %0, %1, %2, %3 offen sc1\n\ts_waitcnt vmcnt(0)" : "=&v"(t) : "v"(vo), "s"(r), "s"(so) : "memory"); }
DI void st4(v4u v, rsrc_t r, int vo, int so) { __builtin_amdgcn_raw_buffer_store_b128(v, r, vo + so, 0, SC1); }
DI void st2(v2u v, rsrc_t r, int vo, int so) { __builtin_amdgcn_raw_buffer_store_b64(v, r, vo, so, SC1); }
DI void dma16(rsrc_t r, int vo, int so, unsigned lds) {
  asm volatile("s_mov_b32 m0, %0\n\ts_nop 0\n\tbuffer_load_dwordx4 %1, %2, %3 offen nt lds" ::"s"(lds), "v"(vo), "s"(r), "s"(so) : "memory");
}
#define VMCNT(n) asm volatile("s_waitcnt vmcnt(%0)" ::"n"(n) : "memory")
#define FENCE() asm volatile("" ::: "memory")
#define SCHED() __builtin_amdgcn_sched_barrier(0)

DI float bf2f(unsigned v) { return __uint_as_float(v << 16); }
DI unsigned pk(float a, float b) { f32x2 f = {a, b}; return __builtin_bit_cast(unsigned, __builtin_convertvector(f, bf16x2)); }
DI f32x4 mfma16(v4u a, v4u b, f32x4 c) {
  return __builtin_amdgcn_mfma_f32_16x16x32_bf16(__builtin_bit_cast(bf16x8, a), __builtin_bit_cast(bf16x8, b), c, 0, 0, 0);
}
DI f32x4 mfma16k16(v2u a, v2u b, f32x4 c) {
  return __builtin_amdgcn_mfma_f32_16x16x16bf16_1k(__builtin_bit_cast(s16x4, a), __builtin_bit_cast(s16x4, b), c, 0, 0, 0);
}
DI f32x16 mfma32(v4u a, v4u b, f32x16 c) {
  return __builtin_amdgcn_mfma_f32_32x32x16_bf16(__builtin_bit_cast(bf16x8, a), __builtin_bit_cast(bf16x8, b), c, 0, 0, 0);
}
template <int CTRL, int RM> DI float dppf(float old, float v) {
  return __builtin_bit_cast(float, __builtin_amdgcn_update_dpp(__builtin_bit_cast(int, old), __builtin_bit_cast(int, v), CTRL, RM, 0xf, false));
}
template <int CTRL> DI float dppz(float v) {
  return __builtin_bit_cast(float, __builtin_amdgcn_update_dpp(0, __builtin_bit_cast(int, v), CTRL, 0xf, 0xf, true));
}
DI float row_sum(float v) {
  v += dppz<0xB1>(v); v += dppz<0x4E>(v); v += dppz<0x124>(v); v += dppz<0x128>(v);
  return v;
}
DI float dot2(unsigned a, unsigned b, float c) {
  return __builtin_amdgcn_fdot2_f32_bf16(__builtin_bit_cast(bf16x2, a), __builtin_bit_cast(bf16x2, b), c, false);
}
DI float row_max(float v) {
  v = fmaxf(v, dppf<0xB1, 0xf>(v, v)); v = fmaxf(v, dppf<0x4E, 0xf>(v, v));
  v = fmaxf(v, dppf<0x124, 0xf>(v, v)); v = fmaxf(v, dppf<0x128, 0xf>(v, v));
  return v;
}
DI float x16_sum(float v) { auto r = __builtin_amdgcn_permlane16_swap(__float_as_uint(v), __float_as_uint(v), false, false); return __uint_as_float(r[0]) + __uint_as_float(r[1]); }
DI float x32_sum(float v) { auto r = __builtin_amdgcn_permlane32_swap(__float_as_uint(v), __float_as_uint(v), false, false); return __uint_as_float(r[0]) + __uint_as_float(r[1]); }
DI float x16_max(float v) { auto r = __builtin_amdgcn_permlane16_swap(__float_as_uint(v), __float_as_uint(v), false, false); return fmaxf(__uint_as_float(r[0]), __uint_as_float(r[1])); }
DI float x32_max(float v) { auto r = __builtin_amdgcn_permlane32_swap(__float_as_uint(v), __float_as_uint(v), false, false); return fmaxf(__uint_as_float(r[0]), __uint_as_float(r[1])); }

#define RBAR() asm volatile("s_barrier" ::: "memory")
#define BAR() asm volatile("s_waitcnt lgkmcnt(0)\n\ts_barrier" ::: "memory")
DI unsigned genc(float v, unsigned t8) { return ((__float_as_uint(v) + 0x80u) & 0xFFFFFF00u) | t8; }
DI float gdec(unsigned hi) { return __uint_as_float(hi & 0xFFFFFF00u); }

DI void wait_clock(u64 t0, unsigned ticks) {
#pragma unroll 1
  for (int n = 0; n < 4096 && (unsigned)(__builtin_amdgcn_s_memrealtime() - t0) < ticks; ++n) __builtin_amdgcn_s_sleep(1);
}

DI v4u bdiag(unsigned P, const unsigned (&s)[4]) {
  v4u b;
  b.x = __builtin_amdgcn_perm(P, P, s[0]); b.y = __builtin_amdgcn_perm(P, P, s[1]);
  b.z = __builtin_amdgcn_perm(P, P, s[2]); b.w = __builtin_amdgcn_perm(P, P, s[3]);
  return b;
}
DI void bdiag_sel(int cp, unsigned (&s)[4]) {
  const unsigned lo = (cp & 1) ? 0x01000C0Cu : 0x0C0C0100u, hi = (cp & 1) ? 0x03020C0Cu : 0x0C0C0302u, z = 0x0C0C0C0Cu;
  s[0] = cp < 2 ? lo : z; s[1] = cp < 2 ? z : lo; s[2] = cp < 2 ? hi : z; s[3] = cp < 2 ? z : hi;
}

DI unsigned lsbtag(unsigned tlo, unsigned thi, int p) { return (((p < 32 ? tlo : thi) >> (p & 31)) & 1u) | (((tlo ^ (unsigned)p) & 1u) << 16); }
constexpr unsigned LSBM = 0x00010001u;
DI unsigned pk7(float a, float b) { return pk(__uint_as_float(__float_as_uint(a) + 0x8000u), __uint_as_float(__float_as_uint(b) + 0x8000u)); }
DI unsigned lsbput(unsigned v, unsigned e) { return (v & ~LSBM) | e; }
DI unsigned strip4(v4u& d, const unsigned (&e)[4]) { d.x ^= e[0]; d.y ^= e[1]; d.z ^= e[2]; d.w ^= e[3]; return (d.x | d.y) | (d.z | d.w); }
DI void swap24(v4u& a, v4u& b) {
  unsigned az = a.z, aw = a.w, bx = b.x, by = b.y;
  asm volatile("v_swap_b32 %0, %2\n\tv_swap_b32 %1, %3\n\ts_nop 1" : "+v"(az), "+v"(aw), "+v"(bx), "+v"(by));
  a.z = az; a.w = aw; b.x = bx; b.y = by;
}
DI v4u tag8(f32x4 c, const unsigned (&t)[4]) {
  return v4u{(__float_as_uint(c[0]) & 0xFFFFFF00u) | t[0], (__float_as_uint(c[1]) & 0xFFFFFF00u) | t[1],
             (__float_as_uint(c[2]) & 0xFFFFFF00u) | t[2], (__float_as_uint(c[3]) & 0xFFFFFF00u) | t[3]};
}
extern "C" __global__ void __launch_bounds__(512, 2)
fused_decode(const u16* __restrict__ xin, const u16* __restrict__ ckv, const u16* __restrict__ wq, const u16* __restrict__ wuk,
             const u16* __restrict__ wuv, const u16* __restrict__ wo, const u16* __restrict__ w1, const u16* __restrict__ w3,
             const u16* __restrict__ w2, u16* __restrict__ out, char* ws) {
  extern __shared__ __attribute__((aligned(16))) char smem[];
  LAS char* L = (LAS char*)smem;
  const unsigned lbase = (unsigned)(size_t)smem;
  const int lane = threadIdx.x & 63, w = __builtin_amdgcn_readfirstlane(threadIdx.x >> 6);
  const int xg = blockIdx.x & 7, qd_ = blockIdx.x >> 3, qq = qd_ & 15, idx = xg * 16 + qq, hd = idx >> 2, jq = idx & 3;
  const int i16 = lane & 15, g = lane >> 4, bj = i16 >> 2, cp = lane & 3;
  if (w == 7) {
    u64 pc; unsigned back, len;
    asm volatile("s_getpc_b64 %0\n.Lpf_here:\n\ts_mov_b32 %1, .Lpf_here-fused_decode\n\ts_mov_b32 %2, fused_decode_end-fused_decode" : "=s"(pc), "=s"(back), "=s"(len));
    const rsrc_t rcode = mk((const void*)(pc - back), (int)min(len, 32768u));
    v4u dummy;
    asm volatile("s_nop 4\n\tbuffer_load_dwordx4 %0, %1, %2, 0 offen\n\ts_waitcnt vmcnt(0)" : "=&v"(dummy) : "v"(qd_ * 1024 + lane * 16), "s"(rcode) : "memory");
  }
  asm volatile("" ::"s"(xin), "s"(ckv), "s"(wq), "s"(wuk), "s"(wuv), "s"(wo), "s"(w1), "s"(w3), "s"(w2), "s"(out), "s"(ws));
  u64 t_start;
  asm volatile("s_memrealtime %0\n\ts_waitcnt lgkmcnt(0)" : "=s"(t_start) : "s"(ws) : "memory");
  const rsrc_t rws = mk(ws, WS_END);
  const rsrc_t rx = mk(xin, 4 * 576 * 2);
  unsigned sel[4];
  bdiag_sel(cp, sel);
#define MAKE_TAG(W) const u64 tag_ = (((u64)(W).y << 32) | (W).x) + 0x9E3779B97F4A7C15ull;  \
  const unsigned tlo = (unsigned)tag_, thi = (unsigned)(tag_ >> 32), t8 = thi & 0xFFu;        \
  const unsigned tb[4] = {tlo & 0xFFu, (tlo >> 8) & 0xFFu, (tlo >> 16) & 0xFFu, tlo >> 24}
  const int vo5 = (lane >> 3) * QB + (lane & 7) * 16;

  if (qd_ >= 16) {
    const int so5 = WS_H5 + idx * 128;
    if (w < 4) {
      v4u ka[2][4];
      v2u wv_;
      asm volatile("buffer_load_dwordx2 %0, off, %1, %2 sc1" : "=v"(wv_) : "s"(rws), "s"(WS_W) : "memory");
      {
        const rsrc_t rk = mk(wuk, 512 * 4096 * 2);
        const int ukx = 4 * (hd & 1);
        SLP(DLY_K);
#pragma unroll
        for (int n = 0; n < 8; ++n) {
          if (n >= PACE_K) VMCNT(PACE_K - 1);
          dma16(rk, g * 8192 + ((i16 ^ ((g << 2) | (n & 3))) * 16), (128 * jq + 32 * w + 4 * (n ^ ukx)) * 8192 + hd * 256, lbase + LP_UK + (8 * w + (n ^ ukx)) * 1024);
        }
        VMCNT(0);
        const int fr = ((i16 & 3) << 2) | (i16 >> 2);
#pragma unroll
        for (int t = 0; t < 2; ++t)
#pragma unroll
          for (int s = 0; s < 4; ++s) {
            ka[t][s] = *(LAS v4u*)(L + LP_UK + (32 * w + 16 * t + i16) * 256 + (((4 * s + g) ^ fr) * 16));
            asm volatile("" : "+v"(ka[t][s]));
          }
      }
      asm volatile("" : "+v"(wv_));
      MAKE_TAG(wv_);
      unsigned eq[4];
#pragma unroll
      for (int k = 0; k < 4; ++k) { eq[k] = lsbtag(tlo, thi, 4 * i16 + k); asm volatile("" : "+v"(eq[k])); }
      SLP(DLY_H1);
      {
        const int blk = w & 1, half = w >> 1;
        float q0, q1;
        for (int n = 0;; ++n) {
          v4u p[4];
#pragma unroll
          for (int jj = 0; jj < 4; ++jj) p[jj] = hl4(rws, lane * 16, WS_H1 + slot(hd * 4 + jj, 4096) + (blk * 2 + half) * 1024);
          unsigned bad = 0;
          q0 = 0.f; q1 = 0.f;
#pragma unroll
          for (int jj = 0; jj < 4; ++jj) {
            bad |= (p[jj].x ^ tlo) | (p[jj].z ^ tlo) | (((p[jj].y ^ t8) | (p[jj].w ^ t8)) & 0xFFu);
            q0 += gdec(p[jj].y); q1 += gdec(p[jj].w);
          }
          if (__all(bad == 0) || n >= GSPIN) break;
          SLP(DLY_R1);
        }
        const unsigned qp = pk(q0, q1);
        const int d0 = 32 * g + 16 * half + 4 * blk + cp;
        *(LAS u16*)(L + LP_QB + bj * 320 + d0 * 2) = (u16)qp;
        *(LAS u16*)(L + LP_QB + bj * 320 + (d0 + 8) * 2) = (u16)(qp >> 16);
      }
      BAR();
      {
        v4u qf[4];
#pragma unroll
        for (int s = 0; s < 4; ++s) qf[s] = *(LAS v4u*)(L + LP_QB + cp * 320 + s * 64 + g * 16);
        f32x4 c0 = {0.f, 0.f, 0.f, 0.f}, c1 = c0;
#pragma unroll
        for (int s = 0; s < 4; ++s) { c0 = mfma16(ka[0][s], qf[s], c0); c1 = mfma16(ka[1][s], qf[s], c1); }
        const float sc = 0.08838834764831845f * 1.4426950408889634f;
        if (i16 < 4) {
          *(LAS v2u*)(L + LP_QO + i16 * 256 + (32 * w + 4 * g) * 2) = v2u{pk7(c0[0] * sc, c0[1] * sc), pk7(c0[2] * sc, c0[3] * sc)};
          *(LAS v2u*)(L + LP_QO + i16 * 256 + (32 * w + 16 + 4 * g) * 2) = v2u{pk7(c1[0] * sc, c1[1] * sc), pk7(c1[2] * sc, c1[3] * sc)};
        }
      }
      BAR();
      if (w == 0) {
        const v4u v = *(LAS v4u*)(L + LP_QO + lane * 16);
        st4(v4u{lsbput(v.x, eq[0]), lsbput(v.y, eq[1]), lsbput(v.z, eq[2]), lsbput(v.w, eq[3])}, rws, g * 2048 + i16 * 16, WS_QA + slot(hd, 1024) + jq * 256);
      }
      BAR();
      const rsrc_t r2 = mk(w2, 4608 * 576 * 2);
      const int so2 = (xg * 576 + 36 * qq) * 1152;
      const rsrc_t ro = mk(wo, 4096 * 576 * 2);
      const int soo = (hd * 128 + 32 * jq) * 1152;
      v4u a2[9][2];
      v2u a8[3][2];
      {
        const int vo4 = 2 * g * 1152 + i16 * 16, vo4m = g < 2 ? vo4 : OOB;
        const int vo8 = 2 * g * 1152 + i16 * 8 + 1024, vo8m = (g < 2 && w == 0) ? vo8 : OOB;
#pragma unroll
        for (int n = 0; n < 24; ++n) {
          if (n >= PACE_W) VMCNT(PACE_W - 1);
          if (n == PACE_W) { SCHED(); RBAR(); SLP(1); }
          const int o = n >> 1, r = n & 1;
          if (n < 10) a2[o][r] = ld4<NT>(r2, o < 4 ? vo4 : vo4m, so2 + (8 * o + r) * 1152 + 256 * w);
          else if (n < 12) a8[0][r] = ld2<NT>(r2, vo8, so2 + (8 * w + r) * 1152);
          else if (n < 14) a8[1][r] = ld2<NT>(r2, vo8m, so2 + (32 + r) * 1152);
          else if (n < 22) a2[o - 2][r] = ld4<NT>(ro, vo4, soo + (8 * (o - 7) + r) * 1152 + 256 * w);
          else a8[2][r] = ld2<NT>(ro, vo8, soo + (8 * w + r) * 1152);
          SCHED();
        }
      }
#pragma unroll
      for (int o = 0; o < 5; ++o) swap24(a2[o][0], a2[o][1]);
      RBAR();
      {
        const v4u h4 = *(LAS v4u*)(L + LP_HP + (bj * 4 + g) * 48);
        const unsigned h5 = *(LAS unsigned*)(L + LP_HP + (bj * 4 + g) * 48 + 16);
        const unsigned hw = *(LAS unsigned*)(L + LP_HP + (bj * 4 + g) * 48 + 4 * w);
        f32x4 c8 = {0.f, 0.f, 0.f, 0.f}, cx = c8, cy = c8;
        c8 = mfma16(v4u{a8[0][0].x, a8[0][0].y, a8[0][1].x, a8[0][1].y}, bdiag(hw, sel), c8);
        c8 = mfma16(v4u{a8[1][0].x, a8[1][0].y, a8[1][1].x, a8[1][1].y}, bdiag(h5, sel), c8);
#pragma unroll
        for (int o = 0; o < 5; ++o) {
          const v4u B = bdiag(o == 0 ? h4.x : o == 1 ? h4.y : o == 2 ? h4.z : o == 3 ? h4.w : h5, sel);
          cx = mfma16(a2[o][0], B, cx);
          cy = mfma16(a2[o][1], B, cy);
        }
#pragma unroll
        for (int o = 5; o < 9; ++o) swap24(a2[o][0], a2[o][1]);
        asm volatile("" : "+v"(cx), "+v"(cy), "+v"(c8));
        int bpa = (bj * 16 + g) * 4;
        asm volatile("" : "+v"(bpa));
        float y0, y1;
        for (int n = 0;; ++n) {
          v4u p[4];
#pragma unroll
          for (int jj = 0; jj < 4; ++jj) p[jj] = hl4(rws, lane * 16, WS_H4 + slot(idx, 4096) + jj * 1024);
          unsigned bad = 0;
          y0 = 0.f; y1 = 0.f;
#pragma unroll
          for (int jj = 0; jj < 4; ++jj) {
            bad |= (p[jj].x ^ tlo) | (p[jj].z ^ tlo) | (((p[jj].y ^ t8) | (p[jj].w ^ t8)) & 0xFFu);
            y0 += gdec(p[jj].y); y1 += gdec(p[jj].w);
          }
          if (__all(bad == 0) || n >= GSPIN) break;
          SLP(DLY_R4);
        }
        const unsigned yp = pk(y0, y1);
        const unsigned pw = __builtin_amdgcn_ds_bpermute(bpa + 16 * w, yp);
        unsigned po[4];
#pragma unroll
        for (int o = 0; o < 4; ++o) po[o] = __builtin_amdgcn_ds_bpermute(bpa + 16 * o, yp);
        c8 = mfma16(v4u{a8[2][0].x, a8[2][0].y, a8[2][1].x, a8[2][1].y}, bdiag(pw, sel), c8);
#pragma unroll
        for (int o = 0; o < 4; ++o) {
          const v4u B = bdiag(po[o], sel);
          cx = mfma16(a2[5 + o][0], B, cx);
          cy = mfma16(a2[5 + o][1], B, cy);
        }
        *(LAS f32x4*)(L + LP_B8 + w * 1024 + lane * 16) = c8;
        st4(tag8(cx, tb), rws, vo5, so5 + blk5(2 * w));
        st4(tag8(cy, tb), rws, vo5, so5 + blk5(2 * w + 1));
        BAR();
        if (w == 0) {
          f32x4 s = *(LAS f32x4*)(L + LP_B8 + lane * 16);
#pragma unroll
          for (int k = 1; k < 4; ++k) s += *(LAS f32x4*)(L + LP_B8 + k * 1024 + lane * 16);
          st4(tag8(s, tb), rws, vo5, so5 + blk5(8));
        }
      }
      if (qq < 9) RBAR();
    } else {
      const int v = w - 4;
      unsigned xn[8];
      const v2u wv_ = hl2(rws, 0, WS_W);
      v4u pt1;
      asm volatile("buffer_load_dwordx4 %0, %1, %2, %3 offen sc1" : "=v"(pt1) : "v"(lane * 16), "s"(rws), "s"(WS_H1 + slot(idx, 4096) + v * 1024) : "memory");
      {
        const v4u x0 = ld4<0>(rx, lane * 16, v * 1152), x1 = ld4<0>(rx, lane < 8 ? lane * 16 : OOB, v * 1152 + 1024);
        float s = 0.f, q = 0.f;
#pragma unroll
        for (int k = 0; k < 4; ++k) {
          s = dot2(x0[k], 0x3F803F80u, s); q = dot2(x0[k], x0[k], q);
          s = dot2(x1[k], 0x3F803F80u, s); q = dot2(x1[k], x1[k], q);
        }
        const float mean = x32_sum(x16_sum(row_sum(s))) * (1.0f / 576.0f);
        const float msq = x32_sum(x16_sum(row_sum(q))) * (1.0f / 576.0f);
        const float rstd = __builtin_amdgcn_rsqf(fmaxf(fmaf(-mean, mean, msq), 0.f) + 1e-5f), nb = -mean * rstd;
#pragma unroll
        for (int k = 0; k < 4; ++k) {
          xn[k] = pk(fmaf(bf2f(x0[k]), rstd, nb), fmaf(__uint_as_float(x0[k] & 0xFFFF0000u), rstd, nb));
        }
#pragma unroll
        for (int k = 0; k < 4; ++k) xn[4 + k] = pk(fmaf(bf2f(x1[k]), rstd, nb), fmaf(__uint_as_float(x1[k] & 0xFFFF0000u), rstd, nb));
      }
      VMCNT(0);
      asm volatile("" : "+v"(pt1));
#pragma unroll
      for (int hf = 0; hf < 2; ++hf) {
        if (hf == 0 || lane < 8) {
#pragma unroll
          for (int k = 0; k < 4; ++k) {
            const int o = lane + 64 * hf;
            *(LAS unsigned*)(L + LP_XD + ((v * 4 + ((2 * k) & 3)) * 148 + 2 * o + (k >> 1)) * 4) = __builtin_amdgcn_perm(0u, xn[4 * hf + k], 0x01000100u);
            *(LAS unsigned*)(L + LP_XD + ((v * 4 + ((2 * k + 1) & 3)) * 148 + 2 * o + (k >> 1)) * 4) = __builtin_amdgcn_perm(0u, xn[4 * hf + k], 0x03020302u);
          }
        }
      }
      touch(rws, lane * 16, WS_H4 + slot(idx, 4096) + v * 1024);
      MAKE_TAG(wv_);
      const int U0 = (81 * qq) / 144, Qb = 144 * (U0 + 1);
      unsigned xd[21];
      const GAS char* wb = (const GAS char*)(i16 < 8 ? w1 : w3) + (xg * 576 * 2 + (i16 & 7) * 16 + g * 9216);
      const unsigned m0 = cp == 0 ? 0xFFFFu : cp == 1 ? 0xFFFF0000u : 0u, m1 = cp == 2 ? 0xFFFFu : cp == 3 ? 0xFFFF0000u : 0u;
      f32x4 acc[4];
#pragma unroll
      for (int k = 0; k < 4; ++k) acc[k] = f32x4{0.f, 0.f, 0.f, 0.f};
      v4u a[21];
        int off[2][3];
        unsigned use[2][3];
        int hpa[2];
#pragma unroll
        for (int e = 0; e < 2; ++e) {
          const int t = ((v - 2) & 1) * 64 + lane + 128 * e;
          const int cl = t >> 2, b = t & 3, f = 36 * qq + cl, u = f >> 6;
          const int r0 = (144 * u) / 81, r1 = (144 * u + 143) / 81;
#pragma unroll
          for (int k = 0; k < 3; ++k) {
            const int rr = r0 + k;
            const bool on = t < 144 && rr <= r1;
            off[e][k] = on ? slot(xg * 16 + rr, 8192) + (u - (81 * rr) / 144) * 4096 + (f & 63) * 64 + b * 16 : OOB;
            use[e][k] = on ? 0xFFFFFFFFu : 0u;
          }
          hpa[e] = LP_HP + ((b * 4 + ((cl >> 1) & 3)) * 12 + (cl >> 3)) * 4 + (cl & 1) * 2;
          asm volatile("" : "+v"(hpa[e]), "+v"(off[e][0]), "+v"(off[e][1]), "+v"(off[e][2]), "+v"(use[e][0]), "+v"(use[e][1]), "+v"(use[e][2]));
        }
      int pa = WS_H7 + slot(idx, 8192) + v * 4096 + (32 * ((lane >> 4) & 1) + 16 * (lane >> 5) + cp) * 64 + bj * 16;
      asm volatile("" : "+v"(pa));
      BAR();
      RBAR();
#pragma unroll
      for (int t2 = 0; t2 < 21; ++t2) {
        const int Q = 81 * qq + min(4 * t2 + v, 80), qk = Q >= Qb ? Q - Qb : Q - 144 * U0;
        xd[t2] = *(LAS unsigned*)(L + LP_XD + ((bj * 4 + g) * 148 + qk) * 4);
        asm volatile("" : "+v"(xd[t2]));
      }
      wait_clock(t_start, T_F);
#pragma unroll
      for (int t = -PACE_G; t < 21; ++t) {
        const int tl = t + PACE_G;
        if (tl < 21) {
          const int Q = 81 * qq + 4 * tl + v, u = Q >= Qb ? U0 + 1 : U0, qk = Q - 144 * u;
          if (tl < 20 || v == 0) {
            const GAS v4u* p = (const GAS v4u*)(wb + (unsigned)(qk * 4 * 9216 + u * 128));
            a[tl] = __builtin_nontemporal_load(p);
          } else a[tl] = v4u{0u, 0u, 0u, 0u};
          SCHED();
        }
        if (tl == 20) BAR();
        if (t >= 0) {
          const int Q = 81 * qq + min(4 * t + v, 80);
          const v2u B = {xd[t] & m0, xd[t] & m1};
          if (Q < Qb) { acc[0] = mfma16k16(v2u{a[t].x, a[t].y}, B, acc[0]); acc[1] = mfma16k16(v2u{a[t].z, a[t].w}, B, acc[1]); }
          else        { acc[2] = mfma16k16(v2u{a[t].x, a[t].y}, B, acc[2]); acc[3] = mfma16k16(v2u{a[t].z, a[t].w}, B, acc[3]); }
          SCHED();
        }
      }
#pragma unroll
      for (int k = 0; k < 4; ++k) *(LAS f32x4*)(L + LP_RED + (v * 4 + k) * 1024 + lane * 16) = acc[k];
      BAR();
      if (v == 0 || (v == 1 && 81 * qq + 80 >= Qb)) {
#pragma unroll
        for (int hh = 0; hh < 2; ++hh) {
          f32x4 r = *(LAS f32x4*)(L + LP_RED + (2 * v + hh) * 1024 + lane * 16);
#pragma unroll
          for (int k = 1; k < 4; ++k) r += *(LAS f32x4*)(L + LP_RED + (k * 4 + 2 * v + hh) * 1024 + lane * 16);
#pragma unroll
          for (int k = 0; k < 2; ++k) {
            const auto x = __builtin_amdgcn_permlane32_swap(__float_as_uint(r[k]), __float_as_uint(r[2 + k]), false, false);
            st4(v4u{tlo, genc(__uint_as_float(x[0]), t8), tlo, genc(__uint_as_float(x[1]), t8)}, rws, pa + (8 * k + 4 * hh) * 64, 0);
          }
        }
      } else if (v >= 2) {
        SLP(DLY_H7);
        wait_clock(t_start, T_H7);
        float gs[2], us[2];
        for (int n = 0;; ++n) {
          v4u p[2][3];
#pragma unroll
          for (int e = 0; e < 2; ++e)
#pragma unroll
            for (int k = 0; k < 3; ++k) p[e][k] = hl4(rws, off[e][k], WS_H7);
          unsigned bad = 0;
#pragma unroll
          for (int e = 0; e < 2; ++e) {
            gs[e] = 0.f; us[e] = 0.f;
#pragma unroll
            for (int k = 0; k < 3; ++k) {
              bad |= ((p[e][k].x ^ tlo) | (p[e][k].z ^ tlo) | (((p[e][k].y ^ t8) | (p[e][k].w ^ t8)) & 0xFFu)) & use[e][k];
              gs[e] += gdec(p[e][k].y); us[e] += gdec(p[e][k].w);
            }
          }
          if (__all(bad == 0) || n >= GSPIN) break;
          SLP(DLY_R7);
        }
#pragma unroll
        for (int e = 0; e < 2; ++e) {
          const float hv = gs[e] * __builtin_amdgcn_rcpf(1.0f + __expf(-gs[e])) * us[e];
          if ((v - 2) * 64 + lane + 128 * e < 160) *(LAS u16*)(L + hpa[e]) = (u16)pk(hv, hv);
        }
      }
      BAR();
      const int fe = lane & 31, fL = 8 * xg + (fe >> 2), frr = fe & 3;
      const int oidx = ((fL & 15) >> 2) * 576 + (qq < 8 ? 128 * (qq >> 1) + 8 * (4 * (fL >> 4) + frr) + 4 * (qq & 1) + (fL & 3) : 512 + 4 * (4 * (fL >> 4) + frr) + (fL & 3));
      unsigned xres = 0;
      if (v == 3 && qq < 9) xres = ((const GAS u16*)xin)[oidx];
      const int h5o = WS_H5 + slot(qq * 8 + xg, 16384) + v * 4096;
      int fina = LP_FIN + v * 128 + (lane & 7) * 16 + g * 4;
      asm volatile("" : "+v"(xres), "+v"(fina));
      RBAR();
      if (qq < 9) {
        SLP(DLY_H5);
        v4u d[4];
#pragma unroll
        for (int k = 0; k < 4; ++k) d[k] = hl4(rws, lane * 16, h5o + k * 1024);
        unsigned pend = 15u;
#pragma unroll 1
        for (int n = 0;; ++n) {
#pragma unroll
          for (int k = 0; k < 4; ++k)
            if (pend & (1u << k)) { if (__all((strip4(d[k], tb) & 0xFFu) == 0)) pend &= ~(1u << k); }
          if (pend == 0 || n >= GSPIN) break;
#pragma unroll
          for (int k = 0; k < 4; ++k)
            if (pend & (1u << k)) d[k] = hl4(rws, lane * 16, h5o + k * 1024);
        }
        f32x4 s = (__builtin_bit_cast(f32x4, d[0]) + __builtin_bit_cast(f32x4, d[1])) + (__builtin_bit_cast(f32x4, d[2]) + __builtin_bit_cast(f32x4, d[3]));
#pragma unroll
        for (int r = 0; r < 4; ++r) s[r] += dppz<0x128>(s[r]);
        const auto x01 = __builtin_amdgcn_permlane16_swap(__float_as_uint(s[0]), __float_as_uint(s[1]), false, false);
        const auto x23 = __builtin_amdgcn_permlane16_swap(__float_as_uint(s[2]), __float_as_uint(s[3]), false, false);
        const auto z = __builtin_amdgcn_permlane32_swap(__float_as_uint(__uint_as_float(x01[0]) + __uint_as_float(x01[1])),
                                                        __float_as_uint(__uint_as_float(x23[0]) + __uint_as_float(x23[1])), false, false);
        *(LAS float*)(L + fina) = __uint_as_float(z[0]) + __uint_as_float(z[1]);
        BAR();
        if (v == 3) {
          float y = bf2f(xres);
#pragma unroll
          for (int k = 0; k < 4; ++k) y += *(LAS float*)(L + LP_FIN + k * 128 + fe * 4);
          if (lane < 32) ((GAS u16*)out)[oidx] = (u16)pk(y, y);
          if (qq == 0 && xg == 0) st2(v2u{tlo, thi}, rws, lane == 0 ? 0 : OOB, WS_W);
        }
      }
    }
    return;
  }

  const int ab = xg >> 1;
  const int r32 = lane & 31, hh = lane >> 5;
  v2u wa_;
  if (w < 4) {
    const rsrc_t rc = mk(ckv, 4 * 4096 * 512 * 2);
    v2u wb_;
    asm volatile("buffer_load_dwordx2 %0, off, %1, %2 sc1" : "=v"(wb_) : "s"(rws), "s"(WS_W) : "memory");
    {
      int vo[4];
#pragma unroll
      for (int k = 0; k < 4; ++k) vo[k] = g * 1024 + ((i16 ^ ((g << 2) | k)) * 16);
      const int so = (idx * 128 + 32 * w) * 1024;
      SLP(DLY_C);
#pragma unroll
      for (int n = 0; n < 32; ++n) {
        if (n >= PACE_C) VMCNT(PACE_C - 1);
        dma16(rc, vo[(n >> 2) & 3], so + (n >> 2) * 4096 + (n & 3) * 256, lbase + (n & 3) * 32768 + (32 * w + 4 * (n >> 2)) * 256);
      }
    }
    RBAR();
    asm volatile("" : "+v"(wb_));
    wa_ = wb_;
  } else {
    wa_ = hl2(rws, 0, WS_W);
    if (w == 4) {
      v2u xr[9];
#pragma unroll
      for (int t = 0; t < 9; ++t) xr[t] = ld2<0>(rx, g * 1152 + i16 * 8, t * 128);
      const rsrc_t rq = mk(wq, 576 * 4096 * 2);
      const int vo = 2 * g * 8192 + i16 * 16, so = 8 * jq * 8192 + hd * 256;
      v4u a[18][2];
#pragma unroll
      for (int o = 0; o < 18; ++o) { a[o][0] = ld4<NT>(rq, vo, so + (32 * o) * 8192); a[o][1] = ld4<NT>(rq, vo, so + (32 * o + 1) * 8192); }
      SCHED();
      float s = 0.f, q = 0.f;
#pragma unroll
      for (int t = 0; t < 9; ++t) {
        *(LAS v2u*)(L + LA_QA + g * 1168 + i16 * 8 + t * 128) = xr[t];
        s = dot2(xr[t].x, 0x3F803F80u, s); q = dot2(xr[t].x, xr[t].x, q);
        s = dot2(xr[t].y, 0x3F803F80u, s); q = dot2(xr[t].y, xr[t].y, q);
      }
      FENCE();
      unsigned xp[18];
#pragma unroll
      for (int o = 0; o < 18; ++o) xp[o] = *(LAS unsigned*)(L + LA_QA + bj * 1168 + (4 * jq + 16 * o + g) * 4);
      const float mean = row_sum(s) * (1.0f / 576.0f), msq = row_sum(q) * (1.0f / 576.0f);
      const float rs0 = __builtin_amdgcn_rsqf(fmaxf(fmaf(-mean, mean, msq), 0.f) + 1e-5f);
      const float rstd = __uint_as_float(__builtin_amdgcn_ds_bpermute(bj * 64, __float_as_uint(rs0)));
      const float nb = __uint_as_float(__builtin_amdgcn_ds_bpermute(bj * 64, __float_as_uint(-mean * rs0)));
      v4u Bq[18];
#pragma unroll
      for (int o = 0; o < 18; ++o) {
        Bq[o] = bdiag(pk(fmaf(bf2f(xp[o]), rstd, nb), fmaf(__uint_as_float(xp[o] & 0xFFFF0000u), rstd, nb)), sel);
        asm volatile("" : "+v"(Bq[o]));
      }
      const u64 tg = (((u64)wa_.y << 32) | wa_.x) + 0x9E3779B97F4A7C15ull;
      unsigned ql = (unsigned)tg, q8 = (unsigned)(tg >> 32) & 0xFFu;
      asm volatile("" : "+v"(ql), "+v"(q8));
      SCHED();
      f32x4 qx = {0.f, 0.f, 0.f, 0.f}, qy = qx;
#pragma unroll
      for (int o = 0; o < 18; ++o) {
        swap24(a[o][0], a[o][1]);
        qx = mfma16(a[o][0], Bq[o], qx);
        qy = mfma16(a[o][1], Bq[o], qy);
      }
      const int po = WS_H1 + slot(idx, 4096);
      st4(v4u{ql, genc(qx[0], q8), ql, genc(qx[1], q8)}, rws, lane * 16, po);
      st4(v4u{ql, genc(qx[2], q8), ql, genc(qx[3], q8)}, rws, lane * 16, po + 1024);
      st4(v4u{ql, genc(qy[0], q8), ql, genc(qy[1], q8)}, rws, lane * 16, po + 2048);
      st4(v4u{ql, genc(qy[2], q8), ql, genc(qy[3], q8)}, rws, lane * 16, po + 3072);
    }
    RBAR();
  }
  MAKE_TAG(wa_);
  unsigned e4[4];
#pragma unroll
  for (int k = 0; k < 4; ++k) e4[k] = lsbtag(tlo, thi, 4 * i16 + k);
  if (w < 4) {
    v4u fa[32];
    {
      const int fr = ((r32 & 3) << 2) | ((r32 >> 2) & 3);
      const int ca = (32 * w + r32) * 256;
#define QK_READ(Q) _Pragma("unroll") for (int k = 0; k < 8; ++k) fa[(Q)*8 + k] = *(LAS v4u*)(L + (Q)*32768 + ca + (((2 * k + hh) ^ fr) * 16)); SCHED();
      VMCNT(0); QK_READ(0) QK_READ(1) QK_READ(2) QK_READ(3)
    }
    int qb[4];
#pragma unroll
    for (int k = 0; k < 4; ++k) { qb[k] = LA_QA + ((lane ^ ((2 * k + hh) & 7)) * 16); asm volatile("" : "+v"(qb[k])); }
    const int q4 = i16 >> 2, lowc = 2 * (g & 1) + (cp >> 1);
    int ta[4][2];
#pragma unroll
    for (int t = 0; t < 4; ++t)
#pragma unroll
      for (int t2 = 0; t2 < 2; ++t2) {
        ta[t][t2] = w * 32768 + (8 * t2 + 4 * hh + q4) * 256 + ((((t ^ q4) << 2) | (lowc ^ (2 * t2 + hh))) * 16) + 8 * (cp & 1);
        asm volatile("" : "+v"(ta[t][t2]));
      }
    int mxa = (r32 >> 4) * 320 + (r32 & 15) * 4, pba = LA_PB + lane * 16;
    unsigned e0 = lsbtag(tlo, thi, 4 * (r32 >> 1)), e1 = lsbtag(tlo, thi, 4 * (r32 >> 1) + 1), e2 = lsbtag(tlo, thi, 4 * (r32 >> 1) + 2), e3 = lsbtag(tlo, thi, 4 * (r32 >> 1) + 3);
    const int sp = idx & 31;
    int h3v = ((hh * 4 + (r32 & 1)) ^ (sp & 5)) * QB + (r32 >> 1) * 16 + WS_H3 + (sp * 16 + ab * 4 + w) * 256;
    asm volatile("" : "+v"(mxa), "+v"(pba), "+v"(e0), "+v"(e1), "+v"(e2), "+v"(e3), "+v"(h3v));
    BAR();
    v4u cb[32];
#define TR_READ(n) do { const v2u x0_ = __builtin_bit_cast(v2u, __builtin_amdgcn_ds_read_tr16_b64_v4i16((LAS s16x4*)(L + ta[(n) >> 3][0] + ((n) & 7) * 4096))); \
    const v2u x1_ = __builtin_bit_cast(v2u, __builtin_amdgcn_ds_read_tr16_b64_v4i16((LAS s16x4*)(L + ta[(n) >> 3][1] + ((n) & 7) * 4096)));                 \
    cb[n] = v4u{x0_.x, x0_.y, x1_.x, x1_.y}; } while (0)
    f32x16 sacc;
#pragma unroll
    for (int k = 0; k < 16; ++k) sacc[k] = 0.f;
    {
      v4u fb[32];
#pragma unroll
      for (int s = 0; s < 6; ++s) fb[s] = *(LAS v4u*)(L + qb[s & 3] + s * 1024);
      SCHED();
#pragma unroll
      for (int s = 0; s < 32; ++s) {
        if (s + 6 < 32) fb[s + 6] = *(LAS v4u*)(L + qb[(s + 6) & 3] + (s + 6) * 1024);
        if (s >= 26) TR_READ(s - 26);
        sacc = mfma32(fa[s], fb[s], sacc);
        SCHED();
      }
    }
    float mw = sacc[0];
#pragma unroll
    for (int k = 1; k < 16; ++k) mw = fmaxf(mw, sacc[k]);
    mw = x32_max(mw);
    asm volatile("s_waitcnt lgkmcnt(0)" ::: "memory");
    *(LAS float*)(L + mxa + w * 32768) = mw;
    BAR();
    const float mx = gdec(genc(fmaxf(fmaxf(*(LAS float*)(L + mxa), *(LAS float*)(L + mxa + 32768)), fmaxf(*(LAS float*)(L + mxa + 65536), *(LAS float*)(L + mxa + 98304))), 0u));
    float ls = 0.f;
#pragma unroll
    for (int k = 0; k < 16; ++k) { sacc[k] = __builtin_amdgcn_exp2f(sacc[k] - mx); ls += sacc[k]; }
#pragma unroll
    for (int s2 = 0; s2 < 2; ++s2)
      *(LAS v4u*)(L + pba + (2 * w + s2) * 1024) =
          v4u{pk(sacc[8 * s2], sacc[8 * s2 + 1]), pk(sacc[8 * s2 + 2], sacc[8 * s2 + 3]), pk(sacc[8 * s2 + 4], sacc[8 * s2 + 5]), pk(sacc[8 * s2 + 6], sacc[8 * s2 + 7])};
    ls = x32_sum(ls);
    *(LAS float*)(L + mxa + 640 + w * 32768) = ls;
    BAR();
    {
      v4u pf[8];
#pragma unroll
      for (int k = 0; k < 8; ++k) pf[k] = *(LAS v4u*)(L + pba + k * 1024);
      f32x4 l4;
      if (w == 0) l4 = f32x4{*(LAS float*)(L + mxa + 640), *(LAS float*)(L + mxa + 640 + 32768), *(LAS float*)(L + mxa + 640 + 65536), *(LAS float*)(L + mxa + 640 + 98304)};
      SCHED();
      f32x16 o[4];
      unsigned p01[16], pq[16];
      const bool odd = lane & 1;
#pragma unroll
      for (int t = 0; t < 4; ++t)
#pragma unroll
        for (int k = 0; k < 16; ++k) o[t][k] = 0.f;
#pragma unroll
      for (int n = 0; n < 32; ++n) {
        if (n + 6 < 32) TR_READ(n + 6);
        o[n >> 3] = mfma32(pf[n & 7], cb[n], o[n >> 3]);
        if (n == 0 && w == 0)
          st4(v4u{tlo, genc(mx, t8), tlo, genc((l4[0] + l4[1]) + (l4[2] + l4[3]), t8)}, rws, lane < 32 ? r32 * 16 : OOB, WS_ML + slot(idx, 512));
        if (n >= 17 && n < 32 && n != 24) {
          const int j = n < 24 ? n - 17 : n - 18;
          if (j < 8) { pq[2 * j] = pk7(o[0][2 * j], o[1][2 * j]); pq[2 * j + 1] = pk7(o[0][2 * j + 1], o[1][2 * j + 1]); asm volatile("" : "+v"(pq[2 * j]), "+v"(pq[2 * j + 1])); }
          if (j >= 6) {
            const int k = j - 6;
            p01[2 * k] = lsbput(odd ? pq[2 * k + 1] : pq[2 * k], e0);
            p01[2 * k + 1] = __builtin_amdgcn_update_dpp(0, lsbput(odd ? pq[2 * k] : pq[2 * k + 1], e2), 0xB1, 0xf, 0xf, true);
            asm volatile("" : "+v"(p01[2 * k]), "+v"(p01[2 * k + 1]));
          }
        }
        SCHED();
      }
#pragma unroll
      for (int k = 0; k < 8; ++k) {
        const unsigned pa = pk7(o[2][2 * k], o[3][2 * k]), pb = pk7(o[2][2 * k + 1], o[3][2 * k + 1]);
        st4(v4u{p01[2 * k], lsbput(odd ? pb : pa, e1), p01[2 * k + 1], (unsigned)__builtin_amdgcn_update_dpp(0, lsbput(odd ? pa : pb, e3), 0xB1, 0xf, 0xf, true)},
            rws, h3v, ((2 * (k & 1) + 8 * (k >> 1)) ^ (sp & 26)) * QB);
      }
    }
    RBAR();
    RBAR();
    RBAR();
  } else {
    const int v = w - 4;
    const rsrc_t rv = mk(wuv, 512 * 4096 * 2);
    v4u uw[8];
#define TL(i) do { uw[i] = ld4<NT>(rv, g * 32 * 8192 + i16 * 16, (128 * jq + 8 * v + ((i) ^ (hd & 1))) * 8192 + hd * 256); SCHED(); } while (0)
    {
      v4u qa[8];
      SLP(DLY_QA);
#pragma unroll
      for (int k = 0; k < 8; ++k) qa[k] = hl4(rws, lane * 16, WS_QA + ab * 2048 + slot(8 * v + k, 1024));
      SCHED();
#pragma unroll 1
      for (int n = 0;; ++n) {
        unsigned bad = 0;
#pragma unroll
        for (int k = 0; k < 8; ++k) bad |= strip4(qa[k], e4);
        if (__all((bad & LSBM) == 0) || n >= GSPIN) break;
        SLP(DLY_R2);
#pragma unroll
        for (int k = 0; k < 8; ++k) qa[k] = hl4(rws, lane * 16, WS_QA + ab * 2048 + slot(8 * v + k, 1024));
      }
#pragma unroll
      for (int k = 0; k < 8; ++k) *(LAS v4u*)(L + LA_QA + (lane >> 1) * 1024 + ((((lane & 1) * 32 + 8 * v + k) ^ (lane & 7)) * 16)) = qa[k];
    }
    BAR();
#pragma unroll
    for (int i = 0; i < 4; ++i) TL(i);
    RBAR();
#pragma unroll
    for (int i = 4; i < 8; ++i) TL(i);
    RBAR();
    const int e7 = lane & 7, q4 = i16 >> 2;
    int cta = LA_CTX + v * 288 + (32 * g + e7) * 2, ctr = LA_CTX + cp * 288 + g * 16;
    int h4a = ((2 * (g & 1) + (i16 & 1)) * 16 + (2 * (g >> 1) + (i16 >> 3)) * 4 + ((i16 >> 1) & 3)) * 16 + WS_H4 + slot(hd * 4 + v, 4096) + jq * 1024;
    const unsigned src = (lane & 8) ? 0x0302u : 0x0100u, s1 = (e7 & 1) ? ((src << 16) | 0x0C0Cu) : (0x0C0C0000u | src);
    unsigned s8[4];
    int uwa[4], bva[2][2];
#pragma unroll
    for (int k = 0; k < 4; ++k) {
      s8[k] = (e7 >> 1) == k ? s1 : 0x0C0C0C0Cu;
      uwa[k] = LA_UVW + (32 * v + g) * 256 + ((i16 ^ ((g << 2) | k)) * 16);
      bva[k >> 1][k & 1] = LA_UVW + (8 * g + 4 * (k & 1) + q4) * 256 + (((2 * (2 * v + (k >> 1)) + (cp >> 1)) ^ ((q4 << 2) | ((2 * g + (k & 1)) & 3))) * 16) + 8 * (cp & 1);
      asm volatile("" : "+v"(s8[k]), "+v"(uwa[k]), "+v"(bva[k >> 1][k & 1]));
    }
    asm volatile("" : "+v"(cta), "+v"(ctr), "+v"(h4a));
    const int h3l = ((hd & 3) ^ g) * QB + g * 4096 + i16 * 16;
#define ML_LOAD() hl4(rws, slot(r32, 512), WS_ML + v * 1024 + hd * 16)
#define ML_OK(V) __all((((V).x ^ tlo) | ((V).z ^ tlo) | ((((V).y ^ t8) | ((V).w ^ t8)) & 0xFFu)) == 0)
#define H3_LOADS() _Pragma("unroll") for (int k = 0; k < 8; ++k) d[k] = hl4(rws, h3l, WS_H3 + ((hd >> 2) ^ k) * 4 * QB + k * 16384 + (v * 4 + jq) * 256); SCHED()
    v4u d[8], bk[8];
    v4u ml;
    VMCNT(0);
    RBAR();
#pragma unroll
    for (int k = 0; k < 8; ++k) *(LAS v4u*)(L + uwa[k & 3] + k * 1024) = uw[k];
    BAR();
    v4u bv[2][4];
#pragma unroll
    for (int t = 0; t < 2; ++t)
#pragma unroll
      for (int ks = 0; ks < 4; ++ks) {
        const v2u x0 = __builtin_bit_cast(v2u, __builtin_amdgcn_ds_read_tr16_b64_v4i16((LAS s16x4*)(L + bva[t][0] + ks * 8192)));
        const v2u x1 = __builtin_bit_cast(v2u, __builtin_amdgcn_ds_read_tr16_b64_v4i16((LAS s16x4*)(L + bva[t][1] + ks * 8192)));
        bv[t][ks] = v4u{x0.x, x0.y, x1.x, x1.y};
      }
    SCHED();
    {
      SLP(DLY_M0);
      for (int n = 0;; ++n) {
        ml = ML_LOAD();
        if (ML_OK(ml) || n >= GSPIN) break;
        SLP(DLY_RM);
      }
      SLP(DLY_H3);
      H3_LOADS();
      const float m = gdec(ml.y), M = x16_max(row_max(m));
      const float e = __builtin_amdgcn_exp2f(m - M);
      const float al = e * __builtin_amdgcn_rcpf(x16_sum(row_sum(e * gdec(ml.w))));
      const unsigned ah = pk(al, al) & 0xFFFFu;
      const unsigned hl = ah | (pk(al - bf2f(ah), 0.f) << 16);
#pragma unroll
      for (int k = 0; k < 8; ++k) { bk[k] = bdiag(__builtin_amdgcn_ds_bpermute((4 * k + g) * 4, hl), s8); asm volatile("" : "+v"(bk[k])); }
      f32x4 c;
#pragma unroll 1
      for (int n = 0;; ++n) {
        unsigned bad = 0;
        c = f32x4{0.f, 0.f, 0.f, 0.f};
#pragma unroll
        for (int k = 0; k < 8; ++k) { bad |= strip4(d[k], e4); c = mfma16(d[k], bk[k], c); }
        if (__all((bad & LSBM) == 0) || n >= GSPIN) break;
#pragma unroll
        for (int k = 0; k < 8; ++k) d[k] = hl4(rws, h3l, WS_H3 + ((hd >> 2) ^ k) * 4 * QB + k * 16384 + (v * 4 + jq) * 256);
      }
#pragma unroll
      for (int r = 0; r < 4; ++r) {
        const float y = c[r] + dppz<0x128>(c[r]);
        *(LAS u16*)(L + cta + 16 * r) = (u16)pk(y, y);
      }
    }
    BAR();
    {
      v4u af[4];
#pragma unroll
      for (int ks = 0; ks < 4; ++ks) af[ks] = *(LAS v4u*)(L + ctr + 64 * ks);
      SCHED();
      f32x4 c0 = {0.f, 0.f, 0.f, 0.f}, c1 = c0;
#pragma unroll
      for (int ks = 0; ks < 4; ++ks) { c0 = mfma16(af[ks], bv[0][ks], c0); c1 = mfma16(af[ks], bv[1][ks], c1); }
      const f32x4 ct = g < 2 ? c0 : c1;
      const float x0 = (g & 1) ? ct[2] : ct[0], x1 = (g & 1) ? ct[3] : ct[1];
      const float n0 = dppz<0xB1>(x0), n1 = dppz<0xB1>(x1);
      st4(v4u{tlo, genc((lane & 1) ? n1 : x0, t8), tlo, genc((lane & 1) ? x1 : n0, t8)}, rws, h4a, 0);
    }
  }
}

extern "C" __global__ void fused_decode_end() {}

extern "C" void kernel_launch(void* const* d_in, const int* in_sizes, int n_in, void* d_out, int out_size, void* d_ws, size_t ws_size,
                              hipStream_t stream) {
  (void)hipFuncSetAttribute((const void*)fused_decode, hipFuncAttributeMaxDynamicSharedMemorySize, LDS_BYTES);
  fused_decode<<<256, 512, LDS_BYTES, stream>>>((const u16*)d_in[0], (const u16*)d_in[1], (const u16*)d_in[2], (const u16*)d_in[3],
                                                (const u16*)d_in[4], (const u16*)d_in[5], (const u16*)d_in[7], (const u16*)d_in[8],
                                                (const u16*)d_in[9], (u16*)d_out, (char*)d_ws);
}
```
